```python
import torch

def _softmax(x, axis=-1):
    return torch.softmax(x, dim=axis)
def _gelu(x):
    return torch.nn.functional.gelu(x, approximate="tanh")
def _silu(x):
    return torch.nn.functional.silu(x)
def _sigmoid(x):
    return torch.sigmoid(x)
def _layernorm(x, axis=-1, eps=1e-5):
    m = x.mean(dim=axis, keepdim=True)
    v = x.var(dim=axis, keepdim=True, unbiased=False)
    return (x - m) / torch.sqrt(v + eps)
def _scatter_add(tgt, idx, src, axis=0):
    return tgt.clone().index_add_(axis, idx, src)
def _segment_sum(src, seg, n):
    out = torch.zeros((n,) + src.shape[1:], dtype=src.dtype)
    return out.index_add_(0, seg, src)
def _pad(x, pw):
    flat = tuple(v for pair in reversed(pw) for v in pair)
    return torch.nn.functional.pad(x, flat)

kernel_name = 'concat_heads__gqa_block_cached__mha_block_cached__near_ridge'

def setup_inputs():

    _w = (lambda a: (lambda t: t.to(torch.bfloat16) if t.is_floating_point() else t)(torch.as_tensor(a)))
    def _A_setup():
        B, Sq, D = 16, 64, 512
        Hq, Hkv, Sk, Dh = 32, 8, 512, 128
        assert Hq % Hkv == 0, (Hq, Hkv)
        return {
            "x": _w((lambda *s: torch.randn(*s, dtype=torch.bfloat16) * 0.8)(B, Sq, D)),
            "k_cache": _w((lambda *s: torch.randn(*s, dtype=torch.bfloat16) * 0.8)(B, Hkv, Sk, Dh)),
            "v_cache": _w((lambda *s: torch.randn(*s, dtype=torch.bfloat16) * 0.05)(B, Hkv, Sk, Dh)),
            "wq": _w((lambda *s: torch.randn(*s, dtype=torch.bfloat16) * 0.8)(D, Hq * Dh) * (D ** -0.5)),
            "wo": _w((lambda *s: torch.randn(*s, dtype=torch.bfloat16) * 0.05)(Hq * Dh, D) * ((Hq * Dh) ** -0.5)),
        }
    _A_in = _A_setup()

    def _B_setup():
        B, Sq, D = 16, 64, 512
        H, Sk, Dh = 32, 512, 128
        return {
            "x": _w((lambda *s: torch.randn(*s, dtype=torch.bfloat16) * 0.8)(B, Sq, D)),
            "k_cache": _w((lambda *s: torch.randn(*s, dtype=torch.bfloat16) * 0.8)(B, H, Sk, Dh)),
            "v_cache": _w((lambda *s: torch.randn(*s, dtype=torch.bfloat16) * 0.05)(B, H, Sk, Dh)),
            "wq": _w((lambda *s: torch.randn(*s, dtype=torch.bfloat16) * 0.8)(D, H * Dh) * (D ** -0.5)),
            "wo": _w((lambda *s: torch.randn(*s, dtype=torch.bfloat16) * 0.05)(H * Dh, D) * ((H * Dh) ** -0.5)),
        }
    _B_in = _B_setup()

    return {
        "A_x": _A_in["x"],
        "A_k_cache": _A_in["k_cache"],
        "A_v_cache": _A_in["v_cache"],
        "A_wq": _A_in["wq"],
        "A_wo": _A_in["wo"],
        "B_x": _B_in["x"],
        "B_k_cache": _B_in["k_cache"],
        "B_v_cache": _B_in["v_cache"],
        "B_wq": _B_in["wq"],
        "B_wo": _B_in["wo"],
    }

def reference(A_x, A_k_cache, A_v_cache, A_wq, A_wo, B_x, B_k_cache, B_v_cache, B_wq, B_wo):
    def _A_ref(x, k_cache, v_cache, wq, wo):
        B, Sq, D = x.shape
        Hq, Dh = 32, 128
        Hkv = k_cache.shape[1]
        rep = Hq // Hkv
        q = (lambda x, *d: x.permute(*d))((x @ wq).reshape(B, Sq, Hq, Dh), 0, 2, 1, 3)
        k = (lambda x, n, axis: torch.repeat_interleave(x, n, dim=axis))(k_cache, rep, axis=1)
        v = (lambda x, n, axis: torch.repeat_interleave(x, n, dim=axis))(v_cache, rep, axis=1)
        scores = torch.einsum("bhqd,bhkd->bhqk", q, k) * (Dh ** -0.5)
        p = _softmax(scores, axis=-1)
        o = torch.einsum("bhqk,bhkd->bhqd", p, v)
        return (lambda x, *d: x.permute(*d))(o, 0, 2, 1, 3).reshape(B, Sq, Hq * Dh) @ wo

    def _B_ref(x, k_cache, v_cache, wq, wo):
        B, Sq, D = x.shape
        H, Dh = 32, 128
        q = (lambda x, *d: x.permute(*d))((x @ wq).reshape(B, Sq, H, Dh), 0, 2, 1, 3)
        scores = torch.einsum("bhqd,bhkd->bhqk", q, k_cache) * (Dh ** -0.5)
        p = _softmax(scores, axis=-1)
        o = torch.einsum("bhqk,bhkd->bhqd", p, v_cache)
        return (lambda x, *d: x.permute(*d))(o, 0, 2, 1, 3).reshape(B, Sq, H * Dh) @ wo

    a = _A_ref(A_x, A_k_cache, A_v_cache, A_wq, A_wo)
    b = _B_ref(B_x, B_k_cache, B_v_cache, B_wq, B_wo)
    return torch.concatenate([a, b], axis=-1)
```

```cpp
#include <hip/hip_runtime.h>
#include <hip/hip_bf16.h>
#include <utility>

typedef __bf16 bf16x8 __attribute__((ext_vector_type(8)));
typedef __bf16 bf16x4 __attribute__((ext_vector_type(4)));
typedef __bf16 bf16x2 __attribute__((ext_vector_type(2)));
typedef float f32x16 __attribute__((ext_vector_type(16)));
typedef float f32x2 __attribute__((ext_vector_type(2)));
typedef unsigned u32x4 __attribute__((ext_vector_type(4)));
#define LAS __attribute__((address_space(3)))
#define DI __device__ __forceinline__
typedef __amdgpu_buffer_rsrc_t srd_t;
template <int... Is> using iseq = std::integer_sequence<int, Is...>;
template <int N> using make_iseq = std::make_integer_sequence<int, N>;

#define KV_NT 1
#define W_NT 0

constexpr unsigned XIMG = 0, RING = 65536, SLOT = 8192, NSLOT = 3, RINGW = NSLOT * SLOT;
constexpr unsigned LDS_BYTES = RING + 4 * RINGW;
constexpr float SC = 0.12751743082f;
constexpr float THR_RAW = 8.0f / SC;
constexpr unsigned BIAS = 3072;

#define GAP() __builtin_amdgcn_sched_barrier(0)
#define STAMP(i) do {} while (0)
DI unsigned rfl(unsigned x) { return __builtin_amdgcn_readfirstlane(x); }
DI unsigned cvtpk(float lo, float hi) { f32x2 v = {lo, hi}; bf16x2 b = __builtin_convertvector(v, bf16x2); return __builtin_bit_cast(unsigned, b); }
DI float max3f(float a, float b, float c) { float r; asm("v_max3_f32 %0, %1, %2, %3" : "=v"(r) : "v"(a), "v"(b), "v"(c)); return r; }
DI float max2f(float a, float b) { float r; asm("v_max_f32_e32 %0, %1, %2" : "=v"(r) : "v"(a), "v"(b)); return r; }
DI float fadd_s(float a, float b) { float r; asm("v_add_f32_e32 %0, %1, %2" : "=v"(r) : "v"(a), "v"(b)); return r; }
DI float ffma_s(float a, float b, float c) { float r; asm("v_fma_f32 %0, %1, %2, %3" : "=v"(r) : "v"(a), "s"(b), "v"(c)); return r; }
DI bf16x8 pack8(const f32x16& x, int s, float sc) {
  u32x4 w = {cvtpk(x[8 * s] * sc, x[8 * s + 1] * sc), cvtpk(x[8 * s + 2] * sc, x[8 * s + 3] * sc), cvtpk(x[8 * s + 4] * sc, x[8 * s + 5] * sc), cvtpk(x[8 * s + 6] * sc, x[8 * s + 7] * sc)};
  return __builtin_bit_cast(bf16x8, w);
}
DI bf16x8 pack8n(const f32x16& x, int s) {
  u32x4 w = {cvtpk(x[8 * s], x[8 * s + 1]), cvtpk(x[8 * s + 2], x[8 * s + 3]), cvtpk(x[8 * s + 4], x[8 * s + 5]), cvtpk(x[8 * s + 6], x[8 * s + 7])};
  return __builtin_bit_cast(bf16x8, w);
}
template <bool Z> DI void mm_vvv(f32x16& c, const bf16x8& a, const bf16x8& b) {
  if constexpr (Z) asm volatile("v_mfma_f32_32x32x16_bf16 %0, %1, %2, 0" : "=&v"(c) : "v"(a), "v"(b));
  else             asm volatile("v_mfma_f32_32x32x16_bf16 %0, %1, %2, %0" : "+v"(c) : "v"(a), "v"(b));
}
template <bool Z> DI void mm_vva(f32x16& c, const bf16x8& a, const bf16x8& b) {
  if constexpr (Z) asm volatile("v_mfma_f32_32x32x16_bf16 %0, %1, %2, 0" : "=&v"(c) : "v"(a), "a"(b));
  else             asm volatile("v_mfma_f32_32x32x16_bf16 %0, %1, %2, %0" : "+v"(c) : "v"(a), "a"(b));
}
DI void mm_avv(f32x16& c, const bf16x8& a, const bf16x8& b) {
  asm volatile("v_mfma_f32_32x32x16_bf16 %0, %1, %2, %0" : "+a"(c) : "v"(a), "v"(b));
}
DI void set_m0(unsigned v) { asm volatile("s_mov_b32 m0, %0" :: "s"(v) : "memory"); }
template <int NT, int P> DI void dma_p(unsigned voff, srd_t srd, unsigned soff) {
  if constexpr (NT) asm volatile("buffer_load_dwordx4 %0, %1, %2 offen offset:%3 nt lds" :: "v"(voff), "s"(srd), "s"(soff), "i"(1024 * P) : "memory");
  else              asm volatile("buffer_load_dwordx4 %0, %1, %2 offen offset:%3 lds" :: "v"(voff), "s"(srd), "s"(soff), "i"(1024 * P) : "memory");
}
struct Src { unsigned v[4]; srd_t srd; };
template <int NT> DI void dma_unit(const Src& s, unsigned soff, unsigned ldsaddr) {
  asm volatile("s_mov_b32 m0, %0\n\ts_nop 0" :: "s"(ldsaddr) : "memory");
  dma_p<NT, 0>(s.v[0], s.srd, soff); dma_p<NT, 1>(s.v[1], s.srd, soff); dma_p<NT, 2>(s.v[2], s.srd, soff); dma_p<NT, 3>(s.v[3], s.srd, soff);
}
template <int N> DI void wait_vm() { asm volatile("s_waitcnt vmcnt(%0)" :: "i"(N) : "memory"); }
template <int N> DI void wait_vm_lgkm0() { asm volatile("" ::: "memory"); __builtin_amdgcn_s_waitcnt((N & 15) | ((N >> 4) << 14) | 0x70); asm volatile("" ::: "memory"); }
DI void wait_lgkm0() { asm volatile("" ::: "memory"); __builtin_amdgcn_s_waitcnt(0xC07F); asm volatile("" ::: "memory"); }
DI bf16x8 lds128(unsigned a) { return *(const LAS bf16x8*)(size_t)(a); }
DI bf16x8 lds_tr(unsigned a0, unsigned a1) {
  bf16x4 lo = __builtin_amdgcn_ds_read_tr16_b64_v4bf16((LAS bf16x4*)(size_t)(a0));
  bf16x4 hi = __builtin_amdgcn_ds_read_tr16_b64_v4bf16((LAS bf16x4*)(size_t)(a1));
  return __builtin_shufflevector(lo, hi, 0, 1, 2, 3, 4, 5, 6, 7);
}
DI unsigned next_slot(unsigned s) { return s == (NSLOT - 1) * SLOT ? 0u : s + SLOT; }
DI unsigned ximg_off(unsigned c) { return (c >> 2) * 16384u + (c & 3u) * 512u; }
DI void mk_src(Src& s, const char* base, unsigned bytes, unsigned stride, unsigned srow, unsigned c0, unsigned c1) {
  s.srd = __builtin_amdgcn_make_buffer_rsrc((void*)(base - BIAS), (short)0, (int)(bytes + BIAS), 0x00020000);
  s.v[0] = srow * stride + c0 + BIAS;
  s.v[1] = srow * stride + c0 + 128u + (BIAS - 1024u);
  s.v[2] = srow * stride + c1 + 8u * stride + (BIAS - 2048u);
  s.v[3] = srow * stride + c1 + 8u * stride + 128u + (BIAS - 3072u);
}

struct UF { bf16x8 a[4]; bf16x8 b[2]; };
DI void unit_read(UF& f, unsigned wa, unsigned xa) {
  f.b[0] = lds128(xa); f.b[1] = lds128(xa + 8192u);
#pragma unroll
  for (int db = 0; db < 4; ++db) f.a[db] = lds_tr(wa + db * 512u, wa + db * 512u + 256u);
}
template <bool Z, bool RD, bool DM, int NT, int VM>
DI void proj_unit(f32x16 (&acc)[4][2], const UF& cur, UF& nxt, unsigned wa, unsigned xa, const Src& src, unsigned soff, unsigned ldsaddr) {
  if constexpr (RD) wait_vm_lgkm0<VM>(); else wait_lgkm0();
  if constexpr (DM) set_m0(ldsaddr);
  GAP();
  mm_vvv<Z>(acc[0][0], cur.a[0], cur.b[0]);
  if constexpr (RD) { nxt.b[0] = lds128(xa); nxt.a[0] = lds_tr(wa, wa + 256u); }
  GAP();
  mm_vvv<Z>(acc[0][1], cur.a[0], cur.b[1]);
  if constexpr (RD) nxt.b[1] = lds128(xa + 8192u);
  if constexpr (DM) dma_p<NT, 0>(src.v[0], src.srd, soff);
  GAP();
  mm_vvv<Z>(acc[1][0], cur.a[1], cur.b[0]);
  if constexpr (RD) nxt.a[1] = lds_tr(wa + 512u, wa + 768u);
  GAP();
  mm_vvv<Z>(acc[1][1], cur.a[1], cur.b[1]);
  if constexpr (RD) nxt.a[2] = lds_tr(wa + 1024u, wa + 1280u);
  if constexpr (DM) dma_p<NT, 1>(src.v[1], src.srd, soff);
  GAP();
  mm_vvv<Z>(acc[2][0], cur.a[2], cur.b[0]);
  if constexpr (RD) nxt.a[3] = lds_tr(wa + 1536u, wa + 1792u);
  GAP();
  mm_vvv<Z>(acc[2][1], cur.a[2], cur.b[1]);
  if constexpr (DM) dma_p<NT, 2>(src.v[2], src.srd, soff);
  GAP();
  mm_vvv<Z>(acc[3][0], cur.a[3], cur.b[0]);
  GAP();
  mm_vvv<Z>(acc[3][1], cur.a[3], cur.b[1]);
  if constexpr (DM) dma_p<NT, 3>(src.v[3], src.srd, soff);
  GAP();
}
struct PC { unsigned ring, slot, tbW, rb0, rb1, ximg, rot; };
template <bool Z, bool LAST, bool DM, int NT, int VM0, int VM1>
DI void proj_elem(f32x16 (&acc)[4][2], UF& f0, UF& f1, PC& c, unsigned e, const Src& src, unsigned soff, unsigned ustep) {
  const unsigned cur = c.ring + c.slot;
  const unsigned xo = c.ximg + ximg_off((e + c.rot) & 15u);
  proj_unit<Z, true, DM, NT, VM0>(acc, f0, f1, cur + 4096u + c.tbW, xo + c.rb1, src, soff, cur);
  c.slot = next_slot(c.slot);
  const unsigned xn = c.ximg + ximg_off((e + 1u + c.rot) & 15u);
  proj_unit<false, !LAST, DM, NT, VM1>(acc, f1, f0, c.ring + c.slot + c.tbW, xn + c.rb0, src, soff + ustep, cur + 4096u);
}

struct AC {
  f32x16 o[4][2];
  bf16x8 q[2][8];
  bf16x8 kf[8], vf[4][2], pf[2][2];
  float m[2], mc[2], l[2], f[2];
  float t0[2], t1[2];
  bool pend;
};
template <int QB, int J> DI void sm_exp2(f32x16 (&S)[2], AC& c) {
  float a, b;
  asm volatile("v_fma_f32 %0, %4, %2, %3\n\tv_fma_f32 %1, %5, %2, %3\n\tv_exp_f32_e32 %0, %0\n\tv_exp_f32_e32 %1, %1" : "=&v"(a), "=v"(b) : "s"(SC), "v"(c.mc[QB]), "v"(S[QB][J]), "v"(S[QB][J + 1]));
  S[QB][J] = a; S[QB][J + 1] = b;
}
template <int QB, int S_, int P> DI void sm_pack(f32x16 (&S)[2], AC& c) {
  u32x4 w = __builtin_bit_cast(u32x4, c.pf[QB][S_]);
  unsigned r;
  asm volatile("v_cvt_pk_bf16_f32 %0, %3, %4\n\tv_add_f32_e32 %1, %1, %3\n\tv_add_f32_e32 %2, %2, %4" : "=&v"(r), "+v"(c.t0[QB]), "+v"(c.t1[QB]) : "v"(S[QB][8 * S_ + 2 * P]), "v"(S[QB][8 * S_ + 2 * P + 1]));
  w[P] = r;
  c.pf[QB][S_] = __builtin_bit_cast(bf16x8, w);
}
template <bool QK, bool DM, int NT, int G>
DI void gapA(AC& c, f32x16 (&S)[2], f32x16 (&Sn)[2], unsigned va, const Src& src, unsigned soff, unsigned ustep, unsigned ldsaddr) {
  if constexpr (G == 1 && DM) { wait_lgkm0(); }
  if constexpr (DM && (G == 1 || G == 5)) set_m0(ldsaddr + (G == 5 ? 4096u : 0u));
  if constexpr (QK) mm_vva<(G < 2)>(Sn[G & 1], c.kf[G >> 1], c.q[G & 1][G >> 1]);
  if constexpr (DM && G >= 1 && G <= 8) { if constexpr (!QK) asm volatile("s_nop 0"); dma_p<NT, (G - 1) & 3>(src.v[(G - 1) & 3], src.srd, soff + (G >= 5 ? ustep : 0u)); }
  if constexpr (G == 8) wait_vm<16>();
  if constexpr (G >= 8) { constexpr int i = G - 8, s = i >> 2, db = i & 3; c.vf[db][s] = lds_tr(va + s * 4096u + db * 512u, va + s * 4096u + db * 512u + 2048u); }
  if constexpr (G < 8) {
    sm_exp2<1, 2 * G>(S, c);
    sm_pack<0, (G >> 2), (G & 3)>(S, c);
  } else {
    constexpr int i = G - 8;
    sm_pack<1, (i >> 2), (i & 3)>(S, c);
  }
  GAP();
}
template <bool QK, bool DM, int NT, int... Gs>
DI void phaseA_(AC& c, f32x16 (&S)[2], f32x16 (&Sn)[2], unsigned va, const Src& src, unsigned soff, unsigned ustep, unsigned ldsaddr, iseq<Gs...>) {
  (gapA<QK, DM, NT, Gs>(c, S, Sn, va, src, soff, ustep, ldsaddr), ...);
}
template <bool SM, bool KR, int NT, int G>
DI void gapB(AC& c, f32x16 (&Sn)[2], unsigned ka0, unsigned ka1, const Src& src, unsigned soff, unsigned ustep, unsigned ldsaddr, float (&mx)[2][2]) {
  if constexpr (G == 0) asm volatile("s_nop 1");
  if constexpr (G == 1) wait_lgkm0();
  if constexpr (G == 1 || G == 5) set_m0(ldsaddr + (G == 5 ? 4096u : 0u));
  { constexpr int s = G >> 3, db = (G >> 1) & 3, qb = G & 1; mm_avv(c.o[db][qb], c.vf[db][s], c.pf[qb][s]); }
  if constexpr (G >= 1 && G <= 8) dma_p<NT, (G - 1) & 3>(src.v[(G - 1) & 3], src.srd, soff + (G >= 5 ? ustep : 0u));
  if constexpr (KR && G == 8) wait_vm<16>();
  if constexpr (KR && G >= 8) { constexpr int s = G - 8; c.kf[s] = lds128(((s & 1) ? ka1 : ka0) + (s >> 1) * 512u); }
  if constexpr (SM) {
    if constexpr (G >= 2 && G <= 5) {
      constexpr int q = (G - 2) >> 1, r = 8 * ((G - 2) & 1);
      if constexpr (r == 0)
        asm volatile("v_max3_f32 %0, %2, %3, %4\n\tv_max3_f32 %1, %5, %6, %7\n\tv_max3_f32 %0, %0, %8, %9\n\tv_max3_f32 %1, %1, %10, %11"
                     : "=&v"(mx[q][0]), "=&v"(mx[q][1]) : "v"(Sn[q][0]), "v"(Sn[q][1]), "v"(Sn[q][2]), "v"(Sn[q][3]), "v"(Sn[q][4]), "v"(Sn[q][5]), "v"(Sn[q][6]), "v"(Sn[q][7]), "v"(Sn[q][8]), "v"(Sn[q][9]));
      else
        asm volatile("v_max3_f32 %0, %0, %2, %3\n\tv_max3_f32 %1, %1, %4, %5\n\tv_max3_f32 %0, %0, %6, %7\n\tv_max_f32_e32 %0, %0, %1\n\tv_mov_b32_e32 %1, %0"
                     : "+v"(mx[q][0]), "+v"(mx[q][1]) : "v"(Sn[q][10]), "v"(Sn[q][11]), "v"(Sn[q][12]), "v"(Sn[q][13]), "v"(Sn[q][14]), "v"(Sn[q][15]));
    }
    if constexpr (G == 6) {
      asm volatile("v_permlane32_swap_b32_e32 %0, %1\n\tv_permlane32_swap_b32_e32 %2, %3\n\tv_max3_f32 %0, %0, %1, %4\n\tv_max3_f32 %2, %2, %3, %5"
                   : "+v"(mx[0][0]), "+v"(mx[0][1]), "+v"(mx[1][0]), "+v"(mx[1][1]) : "v"(c.m[0]), "v"(c.m[1]));
    }
    if constexpr (G == 7) {
      const float d0 = c.m[0] - mx[0][0], d1 = c.m[1] - mx[1][0];
      c.pend = __builtin_amdgcn_ballot_w64(fminf(d0, d1) < -THR_RAW) != 0;
      c.f[0] = d0; c.f[1] = d1;
      c.m[0] = c.pend ? mx[0][0] : c.m[0]; c.m[1] = c.pend ? mx[1][0] : c.m[1];
      c.mc[0] = -c.m[0] * SC; c.mc[1] = -c.m[1] * SC;
      asm volatile("" : "+v"(c.mc[0]), "+v"(c.mc[1]));
    }
    if constexpr (G >= 8) sm_exp2<0, 2 * (G - 8)>(Sn, c);
  }
  GAP();
}
template <bool SM, bool KR, int NT, int... Gs>
DI void phaseB_(AC& c, f32x16 (&Sn)[2], unsigned ka0, unsigned ka1, const Src& src, unsigned soff, unsigned ustep, unsigned ldsaddr, iseq<Gs...>) {
  float mx[2][2];
  (gapB<SM, KR, NT, Gs>(c, Sn, ka0, ka1, src, soff, ustep, ldsaddr, mx), ...);
  if constexpr (SM) {
    if (__builtin_expect(c.pend, 0)) {
      asm volatile("s_nop 7\n\ts_nop 7" ::: "memory");
#pragma unroll
      for (int q = 0; q < 2; ++q) {
        const float f = __builtin_amdgcn_exp2f(c.f[q] * SC);
        c.t0[q] *= f; c.t1[q] *= f;
#pragma unroll
        for (int db = 0; db < 4; ++db) {
          asm volatile("" : "+a"(c.o[db][q]));
          c.o[db][q] *= f;
          asm volatile("" : "+a"(c.o[db][q]));
        }
      }
      asm volatile("s_nop 1" ::: "memory");
    }
  }
}

__global__ __launch_bounds__(256, 1)
void fused_attn(const char* __restrict__ Ax, const char* __restrict__ Ak, const char* __restrict__ Av, const char* __restrict__ Awq, const char* __restrict__ Awo,
                const char* __restrict__ Bx, const char* __restrict__ Bk, const char* __restrict__ Bv, const char* __restrict__ Bwq, const char* __restrict__ Bwo,
                char* __restrict__ part) {
  extern __shared__ __attribute__((aligned(16))) char lds[];
  STAMP(0);
  const unsigned lane = threadIdx.x & 63u, w = rfl(threadIdx.x >> 6);
  const unsigned bid = blockIdx.x, xg = bid & 7u, rr = bid >> 3, isA = rr >> 4, b = rr & 15u, h = xg * 4u + w;
  const unsigned ldsb = (unsigned)(size_t)lds;
  const unsigned r32 = lane & 31u, hh = lane >> 5;
  const char* xp  = (isA ? Ax : Bx) + (size_t)b * 65536u + w * 256u;
  const char* wqp = (isA ? Awq : Bwq) + h * 256u;
  const char* wop = (isA ? Awo : Bwo) + (size_t)xg * 524288u + w * 256u;
  const size_t kvo = isA ? (size_t)(b * 8u + xg) * 131072u : (size_t)(b * 32u + h) * 131072u;
  const char* kp = (isA ? Ak : Bk) + kvo;
  const char* vp = (isA ? Av : Bv) + kvo;
  const unsigned srow = (lane >> 2) & 7u, scol = 64u * hh + 16u * (lane & 3u);
  const unsigned sx0 = 64u * hh + 16u * ((lane & 3u) ^ (srow >> 2)), sx1 = 64u * hh + 16u * ((lane & 3u) ^ (2u | (srow >> 2)));
  PC pc;
  pc.ring = rfl(ldsb + RING + w * RINGW); pc.slot = 0; pc.ximg = rfl(ldsb + XIMG); pc.rot = b;
  Src sQ;
  {
    Src sX; mk_src(sX, xp, 63u * 1024u + 256u, 1024u, srow, sx0, sx1);
#pragma unroll
    for (int u = 0; u < 4; ++u) dma_unit<W_NT>(sX, u * 16384u, pc.ximg + w * 16384u + u * 4096u);
    mk_src(sQ, wqp, 511u * 8192u + 256u, 8192u, srow, scol, scol);
#pragma unroll
    for (int e = 0; e < 3; ++e) {
      const unsigned so = ((e + b) & 15u) * (32u * 8192u);
      dma_unit<W_NT>(sQ, so, pc.ring + e * SLOT); dma_unit<W_NT>(sQ, so + 16u * 8192u, pc.ring + e * SLOT + 4096u);
    }
  }
  Src sK, sV;
  mk_src(sK, kp, 131072u, 256u, srow, sx0, sx1);
  mk_src(sV, vp, 131072u, 256u, srow, scol, scol);
  pc.rb0 = 2048u * (r32 >> 3) + 64u * (r32 & 7u) + 16u * (hh ^ ((r32 >> 2) & 3u));
  pc.rb1 = 2048u * (r32 >> 3) + 64u * (r32 & 7u) + 16u * ((2u + hh) ^ ((r32 >> 2) & 3u));
  const unsigned p4 = lane & 3u, rev = ((p4 & 1u) << 1) | (p4 >> 1);
  const unsigned tb = ((lane >> 2) & 3u) * 64u + ((lane >> 4) & 1u) * 32u + rev * 8u;
  pc.tbW = tb + hh * 2048u;
  const unsigned tbV = tb + hh * 256u;

  AC ac;
  {
    f32x16 acc[4][2];
    UF f0, f1;
    wait_vm<20>();
    __builtin_amdgcn_s_barrier();
    STAMP(1);
    unit_read(f0, pc.ring + pc.tbW, pc.ximg + ximg_off(b) + pc.rb0);
    proj_elem<true, false, true, W_NT, 16, 16>(acc, f0, f1, pc, 0u, sQ, ((3u + b) & 15u) * (32u * 8192u), 16u * 8192u);
    for (unsigned e = 1; e < 13; ++e)
      proj_elem<false, false, true, W_NT, 16, 16>(acc, f0, f1, pc, e, sQ, ((e + 3u + b) & 15u) * (32u * 8192u), 16u * 8192u);
    for (unsigned e = 13; e < 15; ++e)
      proj_elem<false, false, true, KV_NT, 16, 16>(acc, f0, f1, pc, e, sK, (e - 13u) * 8192u, 4096u);
    proj_elem<false, true, true, KV_NT, 16, 16>(acc, f0, f1, pc, 15u, sV, 0u, 4096u);
    asm volatile("s_nop 7\n\ts_nop 7" ::: "memory");
    GAP();
    STAMP(2);
#pragma unroll
    for (int db = 0; db < 4; ++db)
#pragma unroll
      for (int qb = 0; qb < 2; ++qb) {
        ac.q[qb][2 * db] = pack8n(acc[db][qb], 0); ac.q[qb][2 * db + 1] = pack8n(acc[db][qb], 1);
        asm volatile("" : "+a"(ac.q[qb][2 * db]), "+a"(ac.q[qb][2 * db + 1]));
      }
  }
#pragma unroll
  for (int i = 0; i < 4; ++i) { ac.o[i][0] = f32x16{}; ac.o[i][1] = f32x16{}; asm volatile("" : "+a"(ac.o[i][0]), "+a"(ac.o[i][1])); }
  f32x16 SA[2], SB[2];
  {
    wait_vm<16>();
    unsigned cur = pc.ring + pc.slot;
#pragma unroll
    for (int s = 0; s < 8; ++s) ac.kf[s] = lds128(cur + ((s & 1) ? pc.rb1 : pc.rb0) + (s >> 1) * 512u);
    wait_lgkm0();
    GAP();
    dma_unit<KV_NT>(sK, 2u * 8192u, cur); dma_unit<KV_NT>(sK, 2u * 8192u + 4096u, cur + 4096u);
    mm_vva<true>(SA[0], ac.kf[0], ac.q[0][0]); mm_vva<true>(SA[1], ac.kf[0], ac.q[1][0]);
#pragma unroll
    for (int s = 1; s < 8; ++s) { mm_vva<false>(SA[0], ac.kf[s], ac.q[0][s]); mm_vva<false>(SA[1], ac.kf[s], ac.q[1][s]); }
    pc.slot = next_slot(pc.slot);
    cur = pc.ring + pc.slot;
    wait_vm<16>();
    GAP();
#pragma unroll
    for (int s = 0; s < 8; ++s) ac.kf[s] = lds128(cur + ((s & 1) ? pc.rb1 : pc.rb0) + (s >> 1) * 512u);
    asm volatile("s_nop 7\n\ts_nop 7" ::: "memory");
    GAP();
#pragma unroll
    for (int q = 0; q < 2; ++q) {
      float mx = SA[q][0];
#pragma unroll
      for (int i = 1; i < 16; ++i) mx = max2f(mx, SA[q][i]);
      auto pr = __builtin_amdgcn_permlane32_swap(__float_as_uint(mx), __float_as_uint(mx), false, false);
      ac.m[q] = max2f(__uint_as_float(pr[0]), __uint_as_float(pr[1]));
      ac.mc[q] = -ac.m[q] * SC; ac.t0[q] = 0.f; ac.t1[q] = 0.f;
    }
#pragma unroll
    for (int j = 0; j < 16; ++j) SA[0][j] = __builtin_amdgcn_exp2f(ffma_s(SA[0][j], SC, ac.mc[0]));
    GAP();
  }
  Src sO;
  mk_src(sO, wop, 511u * 1024u + 256u, 1024u, srow, scol, scol);
  {
    Src sB = sK;
    unsigned sof = 3u * 8192u;
    for (unsigned i = 0; i < 14; i += 2) {
      unsigned fill = pc.ring + pc.slot; pc.slot = next_slot(pc.slot);
      phaseA_<true, true, KV_NT>(ac, SA, SB, pc.ring + pc.slot + tbV, sV, (i + 1u) * 8192u, 4096u, fill, make_iseq<16>{});
      fill = pc.ring + pc.slot; pc.slot = next_slot(pc.slot);
      phaseB_<true, true, KV_NT>(ac, SB, pc.ring + pc.slot + pc.rb0, pc.ring + pc.slot + pc.rb1, sK, (i + 3u) * 8192u, 4096u, fill, make_iseq<16>{});
      fill = pc.ring + pc.slot; pc.slot = next_slot(pc.slot);
      phaseA_<true, true, KV_NT>(ac, SB, SA, pc.ring + pc.slot + tbV, sV, (i + 2u) * 8192u, 4096u, fill, make_iseq<16>{});
      sof = (i + 4u) * 8192u;
      if (i == 12) { sB = sV; sof = 15u * 8192u; }
      fill = pc.ring + pc.slot; pc.slot = next_slot(pc.slot);
      phaseB_<true, true, KV_NT>(ac, SA, pc.ring + pc.slot + pc.rb0, pc.ring + pc.slot + pc.rb1, sB, sof, 4096u, fill, make_iseq<16>{});
    }
    unsigned fill = pc.ring + pc.slot; pc.slot = next_slot(pc.slot);
    phaseA_<true, true, W_NT>(ac, SA, SB, pc.ring + pc.slot + tbV, sO, (b & 15u) * (32u * 1024u), 16u * 1024u, fill, make_iseq<16>{});
    fill = pc.ring + pc.slot; pc.slot = next_slot(pc.slot);
    phaseB_<true, false, W_NT>(ac, SB, 0u, 0u, sO, ((b + 1u) & 15u) * (32u * 1024u), 16u * 1024u, fill, make_iseq<16>{});
    phaseA_<false, false, W_NT>(ac, SB, SA, pc.ring + pc.slot + tbV, sO, 0u, 0u, 0u, make_iseq<16>{});
    fill = pc.ring + pc.slot; pc.slot = next_slot(pc.slot);
    phaseB_<false, false, W_NT>(ac, SA, 0u, 0u, sO, ((b + 2u) & 15u) * (32u * 1024u), 16u * 1024u, fill, make_iseq<16>{});
  }
  asm volatile("s_nop 7\n\ts_nop 7" : "+a"(ac.o[0][0]), "+a"(ac.o[0][1]), "+a"(ac.o[1][0]), "+a"(ac.o[1][1]), "+a"(ac.o[2][0]), "+a"(ac.o[2][1]), "+a"(ac.o[3][0]), "+a"(ac.o[3][1]) :: "memory");
  STAMP(3);
  __builtin_amdgcn_s_barrier();
  GAP();
#pragma unroll
  for (int qb = 0; qb < 2; ++qb) {
    const float ls = ac.t0[qb] + ac.t1[qb];
    auto pr = __builtin_amdgcn_permlane32_swap(__float_as_uint(ls), __float_as_uint(ls), false, false);
    const float inv = 1.0f / (__uint_as_float(pr[0]) + __uint_as_float(pr[1]));
#pragma unroll
    for (int db = 0; db < 4; ++db)
#pragma unroll
      for (int s = 0; s < 2; ++s)
        *(LAS bf16x8*)(size_t)(pc.ximg + w * 16384u + qb * 8192u + db * 512u + (s ? pc.rb1 : pc.rb0)) = pack8(ac.o[db][qb], s, inv);
  }
  wait_vm_lgkm0<20>();
  __builtin_amdgcn_s_barrier();
  GAP();
  STAMP(4);
  {
    f32x16 acc[4][2];
    UF f0, f1;
    unit_read(f0, pc.ring + pc.slot + pc.tbW, pc.ximg + ximg_off(b) + pc.rb0);
    proj_elem<true, false, true, W_NT, 16, 16>(acc, f0, f1, pc, 0u, sO, ((3u + b) & 15u) * (32u * 1024u), 16u * 1024u);
    for (unsigned e = 1; e < 13; ++e)
      proj_elem<false, false, true, W_NT, 16, 16>(acc, f0, f1, pc, e, sO, ((e + 3u + b) & 15u) * (32u * 1024u), 16u * 1024u);
    proj_elem<false, false, false, W_NT, 16, 12>(acc, f0, f1, pc, 13u, sO, 0u, 0u);
    proj_elem<false, false, false, W_NT, 8, 4>(acc, f0, f1, pc, 14u, sO, 0u, 0u);
    proj_elem<false, true, false, W_NT, 0, 0>(acc, f0, f1, pc, 15u, sO, 0u, 0u);
    asm volatile("s_nop 7\n\ts_nop 7" ::: "memory");
    GAP();
    STAMP(5);
    char* pp = part + ((size_t)(rr * 8u + xg) * 65536u + w * 16384u + lane * 16u);
#pragma unroll
    for (int nb = 0; nb < 4; ++nb)
#pragma unroll
      for (int qb = 0; qb < 2; ++qb)
#pragma unroll
        for (int s = 0; s < 2; ++s)
          *(bf16x8*)(pp + ((((nb >> 1) * 2 + qb) * 2 + (nb & 1)) * 2 + s) * 1024) = pack8n(acc[nb][qb], s);
  }
}

__global__ __launch_bounds__(256) void reduce_out(const char* __restrict__ part, char* __restrict__ out) {
  const unsigned t = blockIdx.x * 256u + threadIdx.x, g = t >> 12, u = t & 4095u;
  const char* pp = part + (size_t)g * 8u * 65536u + u * 16u;
  u32x4 v[8];
#pragma unroll
  for (int x = 0; x < 8; ++x) v[x] = *(const u32x4*)(pp + (size_t)x * 65536u);
  float s[8];
#pragma unroll
  for (int j = 0; j < 4; ++j) {
    float lo = 0.f, hi = 0.f;
#pragma unroll
    for (int x = 0; x < 8; ++x) { lo += __uint_as_float(v[x][j] << 16); hi += __uint_as_float(v[x][j] & 0xffff0000u); }
    s[2 * j] = lo; s[2 * j + 1] = hi;
  }
  const unsigned lane = u & 63u, sp = (u >> 6) & 1u, nb0 = (u >> 7) & 1u, qb = (u >> 8) & 1u, nb1 = (u >> 9) & 1u, w = u >> 10;
  const unsigned q = 32u * qb + (lane & 31u), n = 128u * w + 64u * nb1 + 32u * nb0 + 16u * sp + 8u * (lane >> 5);
  const unsigned isA = g >> 4, b = g & 15u;
  u32x4 o = {cvtpk(s[0], s[1]), cvtpk(s[2], s[3]), cvtpk(s[4], s[5]), cvtpk(s[6], s[7])};
  *(u32x4*)(out + ((size_t)(b * 64u + q) * 1024u + (isA ? 0u : 512u) + n) * 2u) = o;
}

extern "C" void kernel_launch(void* const* d_in, const int* in_sizes, int n_in, void* d_out, int out_size, void* d_ws, size_t ws_size, hipStream_t stream) {
  (void)in_sizes; (void)n_in; (void)out_size; (void)ws_size;
  (void)hipFuncSetAttribute((const void*)fused_attn, hipFuncAttributeMaxDynamicSharedMemorySize, (int)LDS_BYTES);
  fused_attn<<<256, 256, LDS_BYTES, stream>>>((const char*)d_in[0], (const char*)d_in[1], (const char*)d_in[2], (const char*)d_in[3], (const char*)d_in[4],
                                              (const char*)d_in[5], (const char*)d_in[6], (const char*)d_in[7], (const char*)d_in[8], (const char*)d_in[9], (char*)d_ws);
  reduce_out<<<512, 256, 0, stream>>>((const char*)d_ws, (char*)d_out);
}
```

```cpp
#include <hip/hip_runtime.h>
#include <hip/hip_bf16.h>
#include <utility>

typedef __bf16 bf16x8 __attribute__((ext_vector_type(8)));
typedef __bf16 bf16x4 __attribute__((ext_vector_type(4)));
typedef __bf16 bf16x2 __attribute__((ext_vector_type(2)));
typedef float f32x16 __attribute__((ext_vector_type(16)));
typedef float f32x2 __attribute__((ext_vector_type(2)));
typedef unsigned u32x4 __attribute__((ext_vector_type(4)));
#define LAS __attribute__((address_space(3)))
#define DI __device__ __forceinline__
typedef __amdgpu_buffer_rsrc_t srd_t;
template <int... Is> using iseq = std::integer_sequence<int, Is...>;
template <int N> using make_iseq = std::make_integer_sequence<int, N>;

#define KV_NT 1
#define W_NT 0
#define PF_N 0
#define PF_POL ""
#define SLEEP_A 32
#define SLEEP_B 4
#define ORDER 0
#define X_NT 0

constexpr unsigned SLOT = 8192, REG = 5 * SLOT, LDS_BYTES = 4 * REG;
constexpr unsigned HOFF = 32u << 20;
constexpr float SC = 0.12751743082f;
constexpr float THR_RAW = 8.0f / SC;

#define NO_REDUCE_STAMPS
#define GAP() __builtin_amdgcn_sched_barrier(0)
#define STAMP(i) do {} while (0)
#define PINS(x) asm volatile("" : "+s"(x))
#define PINV(x) asm volatile("" : "+v"(x))
DI unsigned rfl(unsigned x) { return __builtin_amdgcn_readfirstlane(x); }
DI unsigned cvtpk(float lo, float hi) { f32x2 v = {lo, hi}; bf16x2 b = __builtin_convertvector(v, bf16x2); return __builtin_bit_cast(unsigned, b); }
DI bf16x8 pack8(const f32x16& x, int s, float sc) {
  u32x4 w = {cvtpk(x[8 * s] * sc, x[8 * s + 1] * sc), cvtpk(x[8 * s + 2] * sc, x[8 * s + 3] * sc), cvtpk(x[8 * s + 4] * sc, x[8 * s + 5] * sc), cvtpk(x[8 * s + 6] * sc, x[8 * s + 7] * sc)};
  return __builtin_bit_cast(bf16x8, w);
}
DI bf16x8 pack8n(const f32x16& x, int s) {
  u32x4 w = {cvtpk(x[8 * s], x[8 * s + 1]), cvtpk(x[8 * s + 2], x[8 * s + 3]), cvtpk(x[8 * s + 4], x[8 * s + 5]), cvtpk(x[8 * s + 6], x[8 * s + 7])};
  return __builtin_bit_cast(bf16x8, w);
}
template <bool Z> DI void mm_vvv(f32x16& c, const bf16x8& a, const bf16x8& b) {
  if constexpr (Z) asm volatile("v_mfma_f32_32x32x16_bf16 %0, %1, %2, 0" : "=&v"(c) : "v"(a), "v"(b));
  else             asm volatile("v_mfma_f32_32x32x16_bf16 %0, %1, %2, %0" : "+v"(c) : "v"(a), "v"(b));
}
#define AMF(x) x
#define ASM_(x) x
template <bool Z> DI void mm_vva(f32x16& c, const bf16x8& a, const bf16x8& b) {
  if constexpr (Z) asm volatile(AMF("v_mfma_f32_32x32x16_bf16 %0, %1, %2, 0") : "=&v"(c) : "v"(a), "a"(b));
  else             asm volatile(AMF("v_mfma_f32_32x32x16_bf16 %0, %1, %2, %0") : "+v"(c) : "v"(a), "a"(b));
}
DI void mm_avv(f32x16& c, const bf16x8& a, const bf16x8& b) {
  asm volatile(AMF("v_mfma_f32_32x32x16_bf16 %0, %1, %2, %0") : "+a"(c) : "v"(a), "v"(b));
}
DI void set_m0(unsigned v) { asm volatile("s_mov_b32 m0, %0" :: "s"(v) : "memory"); }
template <int NT, int P> DI void dma_p(unsigned voff, srd_t srd, unsigned soff) {
  if constexpr (NT) asm volatile("buffer_load_dwordx4 %0, %1, %2 offen offset:%3 nt lds" :: "v"(voff), "s"(srd), "s"(soff), "i"(1024 * P) : "memory");
  else              asm volatile("buffer_load_dwordx4 %0, %1, %2 offen offset:%3 lds" :: "v"(voff), "s"(srd), "s"(soff), "i"(1024 * P) : "memory");
}
struct Src { unsigned v[4]; srd_t srd; };
template <int NT> DI void dma_unit(const Src& s, unsigned soff, unsigned ldsaddr) {
  asm volatile("s_mov_b32 m0, %0\n\ts_nop 0" :: "s"(ldsaddr) : "memory");
  dma_p<NT, 0>(s.v[0], s.srd, soff); dma_p<NT, 1>(s.v[1], s.srd, soff); dma_p<NT, 2>(s.v[2], s.srd, soff); dma_p<NT, 3>(s.v[3], s.srd, soff);
}
template <int NT> DI void dma_elem(const Src& s, unsigned soff, unsigned ustep, unsigned ldsaddr) { dma_unit<NT>(s, soff, ldsaddr); dma_unit<NT>(s, soff + ustep, ldsaddr + 4096u); }
template <int N> DI void wait_vm() { asm volatile("s_waitcnt vmcnt(%0)" :: "i"(N) : "memory"); }
template <int N> DI void wait_vm_lgkm0() { asm volatile("" ::: "memory"); __builtin_amdgcn_s_waitcnt((N & 15) | ((N >> 4) << 14) | 0x70); asm volatile("" ::: "memory"); }
DI void wait_lgkm0() { asm volatile("" ::: "memory"); __builtin_amdgcn_s_waitcnt(0xC07F); asm volatile("" ::: "memory"); }
DI void barrier() { asm volatile("s_barrier" ::: "memory"); }
DI bf16x8 lds128(unsigned a) { return *(const LAS bf16x8*)(size_t)(a); }
DI bf16x8 lds_tr(unsigned a0, unsigned a1) {
  bf16x4 lo = __builtin_amdgcn_ds_read_tr16_b64_v4bf16((LAS bf16x4*)(size_t)(a0));
  bf16x4 hi = __builtin_amdgcn_ds_read_tr16_b64_v4bf16((LAS bf16x4*)(size_t)(a1));
  return __builtin_shufflevector(lo, hi, 0, 1, 2, 3, 4, 5, 6, 7);
}
template <bool XOR> DI void mk_src(Src& s, const char* base, unsigned bytes, unsigned stride, unsigned lane) {
  s.srd = __builtin_amdgcn_make_buffer_rsrc((void*)base, (short)0, (int)bytes, 0x00020000);
  const unsigned row = (lane >> 2) & 3u, cb = lane >> 4, sl = lane & 3u;
#pragma unroll
  for (int p = 0; p < 4; ++p) s.v[p] = row * stride + cb * 64u + ((XOR ? (sl ^ p) : sl) << 4) + (4u * stride - 1024u) * p;
}

struct UF { bf16x8 a[4]; bf16x8 b[2]; };
struct PC {
  unsigned reg, ldsb, slot, rot, e;
  unsigned tbW, rb0, rb1;
  unsigned vW, xa;
  unsigned fill, soff;
};
DI unsigned chunk_of(unsigned e, unsigned rot) { return ((e + (rot & 12u)) & 12u) | ((e + rot) & 3u); }
DI unsigned ksoff(unsigned m, unsigned rk) { return ((m + rk) & 15u) * 8192u; }
DI unsigned ksoff_oob(unsigned m, unsigned rk) { return m < 16u ? ((m + rk) & 15u) * 8192u : 0x100000u; }
DI unsigned ximg_off(unsigned c) { return (c >> 2) * REG + (c & 3u) * 256u; }
DI unsigned next3(unsigned s) { return s == 4u * SLOT ? 2u * SLOT : s + SLOT; }
DI unsigned next5(unsigned s) { return s == 4u * SLOT ? 0u : s + SLOT; }
DI void unit_read(UF& f, unsigned wa, unsigned xa) {
  f.b[0] = lds128(xa); f.b[1] = lds128(xa + 8192u);
#pragma unroll
  for (int db = 0; db < 4; ++db) f.a[db] = lds_tr(wa + db * 256u, wa + db * 256u + 1024u);
}
template <int S, bool Z, bool RD, bool DM, int NT, int VM, bool BAR, bool XT, unsigned USTEP, unsigned CSTEP>
DI void proj_unit(f32x16 (&acc)[4][2], const UF& cur, UF& nxt, PC& c, const Src& src, const Src& xs, unsigned xsoff, unsigned xdst, bool expl, unsigned nsoff) {
  if constexpr (RD) wait_vm_lgkm0<VM>(); else wait_lgkm0();
  if constexpr (BAR) barrier();
  const unsigned wa = c.vW + (S == 0 ? 4096u : 0u), xa = c.xa, fill = c.fill, soff = c.soff;
  if constexpr (XT) set_m0(xdst); else if constexpr (DM) set_m0(fill);
  GAP();
  mm_vvv<Z>(acc[0][0], cur.a[0], cur.b[0]);
  if constexpr (RD) { nxt.b[0] = lds128(xa); nxt.a[0] = lds_tr(wa, wa + 1024u); }
  if constexpr (XT) dma_p<X_NT, 0>(xs.v[0], xs.srd, xsoff);
  GAP();
  mm_vvv<Z>(acc[0][1], cur.a[0], cur.b[1]);
  if constexpr (RD) nxt.b[1] = lds128(xa + 8192u);
  if constexpr (XT) dma_p<X_NT, 1>(xs.v[1], xs.srd, xsoff); else if constexpr (DM) dma_p<NT, 0>(src.v[0], src.srd, soff);
  GAP();
  mm_vvv<Z>(acc[1][0], cur.a[1], cur.b[0]);
  if constexpr (RD) nxt.a[1] = lds_tr(wa + 256u, wa + 1280u);
  if constexpr (XT) dma_p<X_NT, 2>(xs.v[2], xs.srd, xsoff);
  GAP();
  mm_vvv<Z>(acc[1][1], cur.a[1], cur.b[1]);
  if constexpr (RD) nxt.a[2] = lds_tr(wa + 512u, wa + 1536u);
  if constexpr (XT) { dma_p<X_NT, 3>(xs.v[3], xs.srd, xsoff); set_m0(fill); } else if constexpr (DM) dma_p<NT, 1>(src.v[1], src.srd, soff);
  GAP();
  mm_vvv<Z>(acc[2][0], cur.a[2], cur.b[0]);
  if constexpr (RD) nxt.a[3] = lds_tr(wa + 768u, wa + 1792u);
  if constexpr (XT) dma_p<NT, 0>(src.v[0], src.srd, soff);
  GAP();
  mm_vvv<Z>(acc[2][1], cur.a[2], cur.b[1]);
  if constexpr (XT) dma_p<NT, 1>(src.v[1], src.srd, soff); else if constexpr (DM) dma_p<NT, 2>(src.v[2], src.srd, soff);
  if constexpr (S == 0) {
    PINS(c.slot);
    c.slot = next3(c.slot);
    c.vW = c.reg + c.slot + c.tbW;
    PINV(c.vW);
  } else {
    PINS(c.e);
    c.e += 1u;
  }
  GAP();
  mm_vvv<Z>(acc[3][0], cur.a[3], cur.b[0]);
  if constexpr (XT) dma_p<NT, 2>(src.v[2], src.srd, soff);
  if constexpr (S == 0) {
    PINS(c.e);
    const unsigned xo = c.ldsb + ximg_off(chunk_of(c.e + 1u, c.rot));
    c.xa = xo + c.rb0;
    PINV(c.xa);
  } else {
    c.xa = xa ^ 32u;
    PINV(c.xa);
  }
  GAP();
  mm_vvv<Z>(acc[3][1], cur.a[3], cur.b[1]);
  if constexpr (DM) dma_p<NT, 3>(src.v[3], src.srd, soff);
  if constexpr (S == 0) { c.fill = fill + 4096u; c.soff = soff + USTEP; }
  else { c.fill = c.reg + c.slot; c.soff = expl ? nsoff : chunk_of(c.e + 3u, c.rot) * CSTEP; }
  PINS(c.fill); PINS(c.soff);
  GAP();
}
template <bool Z, bool LAST, bool DM, int NT, int VM0, int VM1, bool BAR1, bool XT0, bool XT1, unsigned USTEP, unsigned CSTEP>
DI void proj_elem(f32x16 (&acc)[4][2], UF& f0, UF& f1, PC& c, const Src& src, const Src& xs, unsigned xso0, unsigned xd0, unsigned xso1, unsigned xd1, bool expl, unsigned nsoff) {
  proj_unit<0, Z, true, DM, NT, VM0, false, XT0, USTEP, CSTEP>(acc, f0, f1, c, src, xs, xso0, xd0, false, 0u);
  proj_unit<1, false, !LAST, DM, NT, VM1, BAR1, XT1, USTEP, CSTEP>(acc, f1, f0, c, src, xs, xso1, xd1, expl, nsoff);
}

struct AC {
  f32x16 o[4][2];
  bf16x8 q[2][8];
  bf16x8 kf[8], vf[4][2], pf[2][2];
  float m[2], mc[2], f[2];
  float t0[2], t1[2];
  bool pend;
};
template <int QB, int J> DI void sm_exp2(f32x16 (&S)[2], AC& c) {
  float a, b;
  asm volatile(ASM_("v_fma_f32 %0, %4, %2, %3\n\tv_fma_f32 %1, %5, %2, %3\n\tv_exp_f32_e32 %0, %0\n\tv_exp_f32_e32 %1, %1") : "=&v"(a), "=v"(b) : "s"(SC), "v"(c.mc[QB]), "v"(S[QB][J]), "v"(S[QB][J + 1]));
  S[QB][J] = a; S[QB][J + 1] = b;
}
template <int QB, int S_, int P> DI void sm_pack(f32x16 (&S)[2], AC& c) {
  u32x4 w = __builtin_bit_cast(u32x4, c.pf[QB][S_]);
  unsigned r;
  asm volatile(ASM_("v_cvt_pk_bf16_f32 %0, %3, %4\n\tv_add_f32_e32 %1, %1, %3\n\tv_add_f32_e32 %2, %2, %4") : "=&v"(r), "+v"(c.t0[QB]), "+v"(c.t1[QB]) : "v"(S[QB][8 * S_ + 2 * P]), "v"(S[QB][8 * S_ + 2 * P + 1]));
  w[P] = r;
  c.pf[QB][S_] = __builtin_bit_cast(bf16x8, w);
}
template <int DM, int G> DI void mem_pre(unsigned ldsaddr) {
  if constexpr (G == 1 && DM != 0) wait_lgkm0();
  if constexpr (DM == 1 && (G == 1 || G == 5)) set_m0(ldsaddr + (G == 5 ? 4096u : 0u));
  if constexpr (DM == 2 && G == 9) set_m0(ldsaddr);
}
template <int DM, int NT, int VM, bool RD, int G> DI void mem_post(const Src& src, unsigned soff, unsigned ustep) {
  if constexpr (DM == 1 && G >= 1 && G <= 8) dma_p<NT, (G - 1) & 3>(src.v[(G - 1) & 3], src.srd, soff + (G >= 5 ? ustep : 0u));
  if constexpr (DM == 2 && (G == 9 || G == 10)) dma_p<NT, G - 9>(src.v[G - 9], src.srd, soff);
  if constexpr (G == 8 && (RD || DM == 2) && VM < 63) wait_vm<VM>();
  if constexpr (G == 8 && DM == 2) barrier();
}
template <bool QK, int DM, int NT, int VM, int G>
DI void gapA(AC& c, f32x16 (&S)[2], f32x16 (&Sn)[2], unsigned va, const Src& src, unsigned soff, unsigned ustep, unsigned ldsaddr) {
  mem_pre<DM, G>(ldsaddr);
  if constexpr (QK) mm_vva<(G < 2)>(Sn[G & 1], c.kf[G >> 1], c.q[G & 1][G >> 1]); else if constexpr (DM != 0) asm volatile("s_nop 0");
  mem_post<DM, NT, VM, true, G>(src, soff, ustep);
  if constexpr (G >= 8) { constexpr int i = G - 8, s = i >> 2, db = i & 3; c.vf[db][s] = lds_tr(va + s * 4096u + db * 256u, va + s * 4096u + db * 256u + 2048u); }
  if constexpr (G < 8) {
    sm_exp2<1, 2 * G>(S, c);
    sm_pack<0, (G >> 2), (G & 3)>(S, c);
  } else {
    constexpr int i = G - 8;
    sm_pack<1, (i >> 2), (i & 3)>(S, c);
  }
  GAP();
}
template <bool QK, int DM, int NT, int VM, int... Gs>
DI void phaseA_(AC& c, f32x16 (&S)[2], f32x16 (&Sn)[2], unsigned va, const Src& src, unsigned soff, unsigned ustep, unsigned ldsaddr, iseq<Gs...>) {
  (gapA<QK, DM, NT, VM, Gs>(c, S, Sn, va, src, soff, ustep, ldsaddr), ...);
}
template <bool QK, int DM, int NT, int VM>
DI void phaseA(AC& c, f32x16 (&S)[2], f32x16 (&Sn)[2], unsigned va, const Src& src, unsigned soff, unsigned ustep, unsigned ldsaddr) {
  phaseA_<QK, DM, NT, VM>(c, S, Sn, va, src, soff, ustep, ldsaddr, make_iseq<16>{});
}
template <bool SM, bool KR, int DM, int NT, int VM, int G>
DI void gapB(AC& c, f32x16 (&Sn)[2], unsigned ka0, unsigned ka1, const Src& src, unsigned soff, unsigned ustep, unsigned ldsaddr, float (&mx)[2][2]) {
  if constexpr (G == 0) asm volatile("s_nop 1");
  mem_pre<DM, G>(ldsaddr);
  { constexpr int s = G >> 3, db = (G >> 1) & 3, qb = G & 1; mm_avv(c.o[db][qb], c.vf[db][s], c.pf[qb][s]); }
  mem_post<DM, NT, VM, KR, G>(src, soff, ustep);
  if constexpr (KR && G >= 8) { constexpr int s = G - 8; c.kf[s] = lds128(((s & 1) ? ka1 : ka0) + (s >> 1) * 256u); }
  if constexpr (SM) {
    if constexpr (G >= 2 && G <= 5) {
      constexpr int q = (G - 2) >> 1, r = 8 * ((G - 2) & 1);
      if constexpr (r == 0)
        asm volatile("v_max3_f32 %0, %2, %3, %4\n\tv_max3_f32 %1, %5, %6, %7\n\tv_max3_f32 %0, %0, %8, %9\n\tv_max3_f32 %1, %1, %10, %11"
                     : "=&v"(mx[q][0]), "=&v"(mx[q][1]) : "v"(Sn[q][0]), "v"(Sn[q][1]), "v"(Sn[q][2]), "v"(Sn[q][3]), "v"(Sn[q][4]), "v"(Sn[q][5]), "v"(Sn[q][6]), "v"(Sn[q][7]), "v"(Sn[q][8]), "v"(Sn[q][9]));
      else
        asm volatile("v_max3_f32 %0, %0, %2, %3\n\tv_max3_f32 %1, %1, %4, %5\n\tv_max3_f32 %0, %0, %6, %7\n\tv_max_f32_e32 %0, %0, %1\n\tv_mov_b32_e32 %1, %0"
                     : "+v"(mx[q][0]), "+v"(mx[q][1]) : "v"(Sn[q][10]), "v"(Sn[q][11]), "v"(Sn[q][12]), "v"(Sn[q][13]), "v"(Sn[q][14]), "v"(Sn[q][15]));
    }
    if constexpr (G == 6) {
      asm volatile("v_permlane32_swap_b32_e32 %0, %1\n\tv_permlane32_swap_b32_e32 %2, %3\n\tv_max3_f32 %0, %0, %1, %4\n\tv_max3_f32 %2, %2, %3, %5"
                   : "+v"(mx[0][0]), "+v"(mx[0][1]), "+v"(mx[1][0]), "+v"(mx[1][1]) : "v"(c.m[0]), "v"(c.m[1]));
    }
    if constexpr (G == 7) {
      const float d0 = c.m[0] - mx[0][0], d1 = c.m[1] - mx[1][0];
      c.pend = __builtin_amdgcn_ballot_w64(fminf(d0, d1) < -THR_RAW) != 0;
      c.f[0] = d0; c.f[1] = d1;
      c.m[0] = c.pend ? mx[0][0] : c.m[0]; c.m[1] = c.pend ? mx[1][0] : c.m[1];
      c.mc[0] = -c.m[0] * SC; c.mc[1] = -c.m[1] * SC;
      asm volatile("" : "+v"(c.mc[0]), "+v"(c.mc[1]));
    }
    if constexpr (G >= 8) sm_exp2<0, 2 * (G - 8)>(Sn, c);
  }
  GAP();
}
template <bool SM, bool KR, int DM, int NT, int VM, int... Gs>
DI void phaseB_(AC& c, f32x16 (&Sn)[2], unsigned ka0, unsigned ka1, const Src& src, unsigned soff, unsigned ustep, unsigned ldsaddr, iseq<Gs...>) {
  float mx[2][2];
  (gapB<SM, KR, DM, NT, VM, Gs>(c, Sn, ka0, ka1, src, soff, ustep, ldsaddr, mx), ...);
  if constexpr (SM) {
    if (__builtin_expect(c.pend, 0)) {
      asm volatile("s_nop 7\n\ts_nop 7" ::: "memory");
#pragma unroll
      for (int q = 0; q < 2; ++q) {
        const float f = __builtin_amdgcn_exp2f(c.f[q] * SC);
        c.t0[q] *= f; c.t1[q] *= f;
#pragma unroll
        for (int db = 0; db < 4; ++db) {
          asm volatile("" : "+a"(c.o[db][q]));
          c.o[db][q] *= f;
          asm volatile("" : "+a"(c.o[db][q]));
        }
      }
      asm volatile("s_nop 1" ::: "memory");
    }
  }
}
template <bool SM, bool KR, int DM, int NT, int VM>
DI void phaseB(AC& c, f32x16 (&Sn)[2], unsigned ka0, unsigned ka1, const Src& src, unsigned soff, unsigned ustep, unsigned ldsaddr) {
  phaseB_<SM, KR, DM, NT, VM>(c, Sn, ka0, ka1, src, soff, ustep, ldsaddr, make_iseq<16>{});
}
DI void k_read(AC& ac, unsigned ka0, unsigned ka1) {
#pragma unroll
  for (int s = 0; s < 8; ++s) ac.kf[s] = lds128(((s & 1) ? ka1 : ka0) + (s >> 1) * 256u);
}
DI void qk0(AC& ac, f32x16 (&SA)[2]) {
  mm_vva<true>(SA[0], ac.kf[0], ac.q[0][0]); mm_vva<true>(SA[1], ac.kf[0], ac.q[1][0]);
#pragma unroll
  for (int s = 1; s < 8; ++s) { mm_vva<false>(SA[0], ac.kf[s], ac.q[0][s]); mm_vva<false>(SA[1], ac.kf[s], ac.q[1][s]); }
}
template <int... Js> DI void exp_first(f32x16 (&SA)[2], AC& ac, iseq<Js...>) { (sm_exp2<0, 2 * Js>(SA, ac), ...); }
DI void sm_first(AC& ac, f32x16 (&SA)[2]) {
  asm volatile("s_nop 7\n\ts_nop 7" : "+v"(SA[0]), "+v"(SA[1]) :: "memory");
  GAP();
#pragma unroll
  for (int q = 0; q < 2; ++q) {
    float mx = SA[q][0];
#pragma unroll
    for (int i = 1; i < 16; ++i) mx = fmaxf(mx, SA[q][i]);
    auto pr = __builtin_amdgcn_permlane32_swap(__float_as_uint(mx), __float_as_uint(mx), false, false);
    ac.m[q] = fmaxf(__uint_as_float(pr[0]), __uint_as_float(pr[1]));
    ac.mc[q] = -ac.m[q] * SC; ac.t0[q] = 0.f; ac.t1[q] = 0.f;
  }
  exp_first(SA, ac, make_iseq<8>{});
  GAP();
}

struct Stash { u32x4 p[64]; };
template <int I> DI void stash_load1(Stash& st, const unsigned (&vk)[2], unsigned vv, srd_t sk, srd_t sv, unsigned rk, unsigned wo_) {
  constexpr int m = I >> 2, isV = (I >> 1) & 1, pc_ = I & 1;
  const unsigned so = ksoff(m, rk) + wo_;
  if constexpr (I < 56) asm volatile("buffer_load_dwordx4 %0, %1, %2, %3 offen offset:%4 nt" : "=a"(st.p[I]) : "v"(isV ? vv : vk[pc_]), "s"(isV ? sv : sk), "s"(so), "i"(1024 * pc_) : "memory");
  else                  asm volatile("buffer_load_dwordx4 %0, %1, %2, %3 offen offset:%4 nt" : "=v"(st.p[I]) : "v"(isV ? vv : vk[pc_]), "s"(isV ? sv : sk), "s"(so), "i"(1024 * pc_) : "memory");
}
template <int... Is> DI void stash_load(Stash& st, const unsigned (&vk)[2], unsigned vv, srd_t sk, srd_t sv, unsigned rk, unsigned wo_, iseq<Is...>) { (stash_load1<Is>(st, vk, vv, sk, sv, rk, wo_), ...); }
template <int I> DI void stash_write1(const Stash& st, const unsigned (&base)[3]) {
  constexpr int m = I >> 2, isV = (I >> 1) & 1, pc_ = I & 1, off = (2 * (m % 10) + isV) * 8192 + pc_ * 1024;
  if constexpr (I < 56) asm volatile("ds_write_b128 %0, %1 offset:%2" :: "v"(base[off >> 16]), "a"(st.p[I]), "i"(off & 65535) : "memory");
  else                  asm volatile("ds_write_b128 %0, %1 offset:%2" :: "v"(base[off >> 16]), "v"(st.p[I]), "i"(off & 65535) : "memory");
}
template <int B, int... Is> DI void stash_write(const Stash& st, const unsigned (&base)[3], iseq<Is...>) { (stash_write1<B + Is>(st, base), ...); }

template <bool isA>
DI void body(const char* __restrict__ X, const char* __restrict__ K, const char* __restrict__ V, const char* __restrict__ Wq, const char* __restrict__ Wo, char* __restrict__ part,
             const char* __restrict__ PK, const char* __restrict__ PV, char* __restrict__ out) {
  extern __shared__ __attribute__((aligned(16))) char lds[];
  STAMP(0);
  const unsigned lane = threadIdx.x & 63u, w = rfl(threadIdx.x >> 6);
  const unsigned bid = blockIdx.x, xg = bid & 7u, rr = bid >> 3, b = rr & 15u, h = xg * 4u + w;
  const unsigned r32 = lane & 31u, hh = lane >> 5;
  const char* xp  = X + (size_t)b * 65536u + w * 16384u;
  const char* wqp = Wq + h * 256u;
  const char* wop = Wo + (size_t)xg * 524288u + w * 256u;
  const size_t kvo = isA ? (size_t)(b * 8u + xg) * 131072u : (size_t)(b * 32u + h) * 131072u;
  const char* kp = K + kvo;
  const char* vp = V + kvo;
  const unsigned rk = isA ? b : b + 4u * w;
  PC pc;
  pc.ldsb = rfl((unsigned)(size_t)lds); pc.reg = pc.ldsb + w * REG; pc.rot = b;
  const srd_t srdH = __builtin_amdgcn_make_buffer_rsrc((void*)(part + HOFF), (short)0, 4096 + 256 * 128, 0x00020000);
  const unsigned tok_off = rr * 128u, flg_off = 4096u + rr * 1024u;
  const unsigned l8 = (lane & 7u) * 4u;
  unsigned entry_flags;
  asm volatile("buffer_load_dword %0, %1, %2, %3 offen sc1" : "=v"(entry_flags) : "v"(l8), "s"(srdH), "s"(flg_off + xg * 128u) : "memory");
  Src sQ, sX, sK, sV, sO;
  mk_src<true>(sK, kp, 131072u, 256u, lane);
  mk_src<false>(sV, vp, 131072u, 256u, lane);
  mk_src<true>(sX, xp, 16384u, 1024u, lane);
  mk_src<false>(sQ, wqp, 511u * 8192u + 256u, 8192u, lane);
  const unsigned p0 = b >> 2;
  Stash st;
  unsigned pf_dummy = 0;
  auto early = [&]() {
    const unsigned vk[2] = {(w & 1u) ? sK.v[2] : sK.v[0], (w & 1u) ? sK.v[3] : sK.v[1]};
    stash_load(st, vk, sV.v[0], sK.srd, sV.srd, rk, w * 2048u, make_iseq<64>{});
  };
  constexpr bool FLAT = isA && ORDER == 1;
  if constexpr (isA && ORDER == 0) early();
  dma_unit<X_NT>(sX, p0 * 256u, pc.ldsb + p0 * REG + w * 4096u);
  dma_elem<W_NT>(sQ, chunk_of(0u, b) * (32u * 8192u), 16u * 8192u, pc.reg + 4u * SLOT);
  dma_elem<W_NT>(sQ, chunk_of(1u, b) * (32u * 8192u), 16u * 8192u, pc.reg + 2u * SLOT);
  if constexpr (FLAT) {
    dma_elem<W_NT>(sQ, chunk_of(2u, b) * (32u * 8192u), 16u * 8192u, pc.reg + 3u * SLOT);
#pragma unroll
    for (unsigned i = 1; i < 4; ++i) { const unsigned p = (p0 + i) & 3u; dma_unit<X_NT>(sX, p * 256u, pc.ldsb + p * REG + w * 4096u); }
    early();
  }
  mk_src<false>(sO, wop, 511u * 1024u + 256u, 1024u, lane);
  pc.rb0 = 1024u * (r32 >> 2) + 64u * (r32 & 3u) + 16u * (hh ^ ((r32 >> 2) & 3u));
  pc.rb1 = pc.rb0 ^ 32u;
  const unsigned p4 = lane & 3u, rev = ((p4 & 1u) << 1) | (p4 >> 1);
  const unsigned tb = ((lane >> 2) & 3u) * 64u + ((lane >> 4) & 1u) * 32u + rev * 8u;
  pc.tbW = tb + hh * 2048u;
  const unsigned tbV = tb + hh * 1024u;

  const unsigned sb0 = pc.ldsb + w * 2048u + lane * 16u;
  const unsigned sbase[3] = {sb0, sb0 + 65536u, sb0 + 131072u};
  AC ac;
  {
    f32x16 acc[4][2];
    UF f0, f1;
    asm volatile("s_waitcnt vmcnt(12)" : "+v"(entry_flags) :: "memory");
    if (w == 0) {
      const unsigned long long mk = __builtin_amdgcn_ballot_w64(((entry_flags >> (lane & 7u)) & 1u) != 0u);
      const unsigned tok = 0x5a5a5a00u | (~(unsigned)mk & 0xffu);
      if (lane == 0) asm volatile("buffer_store_dword %0, off, %1, %2 sc1" :: "v"(tok), "s"(srdH), "s"(tok_off + xg * 4u) : "memory");
    }
    barrier();
    STAMP(1);
    pc.e = 0; pc.slot = 4u * SLOT; pc.vW = pc.reg + pc.slot + pc.tbW;
    const unsigned xo = pc.ldsb + ximg_off(chunk_of(0u, b));
    unit_read(f0, pc.vW, xo + pc.rb0);
    if constexpr (!FLAT) dma_elem<W_NT>(sQ, chunk_of(2u, b) * (32u * 8192u), 16u * 8192u, pc.reg + 3u * SLOT);
    pc.xa = xo + pc.rb1; pc.fill = pc.reg + pc.slot; pc.soff = chunk_of(3u, b) * (32u * 8192u);
    constexpr unsigned US = 16u * 8192u, CS = 32u * 8192u;
    const unsigned p1 = (p0 + 1u) & 3u, p2 = (p0 + 2u) & 3u, p3 = (p0 + 3u) & 3u, xw = pc.ldsb + w * 4096u;
    if constexpr (FLAT) {
      proj_elem<true,  false, true, W_NT, 16, 16, false, false, false, US, CS>(acc, f0, f1, pc, sQ, sX, 0u, 0u, 0u, 0u, false, 0u);
    } else {
    proj_elem<true,  false, true, W_NT, 16, 20, false, true,  true,  US, CS>(acc, f0, f1, pc, sQ, sX, p1 * 256u, xw + p1 * REG, p2 * 256u, xw + p2 * REG, false, 0u);
    STAMP(10);
    proj_elem<false, false, true, W_NT, 24, 28, false, true,  false, US, CS>(acc, f0, f1, pc, sQ, sX, p3 * 256u, xw + p3 * REG, 0u, 0u, false, 0u);
    STAMP(11);
    proj_elem<false, false, true, W_NT, 28, 24, false, false, false, US, CS>(acc, f0, f1, pc, sQ, sX, 0u, 0u, 0u, 0u, false, 0u);
    proj_elem<false, false, true, W_NT, 20, 16, true,  false, false, US, CS>(acc, f0, f1, pc, sQ, sX, 0u, 0u, 0u, 0u, false, 0u);
    }
    STAMP(8);
    const unsigned first_tail = isA ? chunk_of(0u, b) * (32u * 1024u) : ksoff(0u, rk);
    for (unsigned e = FLAT ? 1 : 4; e < 13; ++e)
      proj_elem<false, false, true, W_NT, 16, 16, false, false, false, US, CS>(acc, f0, f1, pc, sQ, sX, 0u, 0u, 0u, 0u, e == 12, first_tail);
    STAMP(9);
    if constexpr (isA) {
      proj_elem<false, false, false, W_NT, 16, 12, false, false, false, US, CS>(acc, f0, f1, pc, sQ, sX, 0u, 0u, 0u, 0u, false, 0u);
      proj_elem<false, false, false, W_NT, 8, 4, false, false, false, US, CS>(acc, f0, f1, pc, sQ, sX, 0u, 0u, 0u, 0u, false, 0u);
      proj_elem<false, true, false, W_NT, 0, 0, false, false, false, US, CS>(acc, f0, f1, pc, sQ, sX, 0u, 0u, 0u, 0u, false, 0u);
    } else {
      proj_elem<false, false, true, KV_NT, 16, 16, false, false, false, 4096u, CS>(acc, f0, f1, pc, sK, sX, 0u, 0u, 0u, 0u, true, ksoff(1u, rk));
      proj_elem<false, false, true, KV_NT, 16, 16, false, false, false, 4096u, CS>(acc, f0, f1, pc, sK, sX, 0u, 0u, 0u, 0u, true, ksoff(0u, rk));
      proj_elem<false, true,  true, KV_NT, 16, 16, false, false, false, 4096u, CS>(acc, f0, f1, pc, sV, sX, 0u, 0u, 0u, 0u, true, 0u);
    }
    barrier();
    STAMP(2);
    if constexpr (isA) {
      asm volatile("" :: "v"(pf_dummy));
      stash_write<0>(st, sbase, make_iseq<40>{});
    } else {
      dma_elem<KV_NT>(sK, ksoff(2u, rk), 4096u, pc.reg);
      dma_elem<KV_NT>(sV, ksoff(1u, rk), 4096u, pc.reg + SLOT);
    }
    asm volatile("s_nop 7\n\ts_nop 7" : "+v"(acc[0][0]), "+v"(acc[0][1]), "+v"(acc[1][0]), "+v"(acc[1][1]), "+v"(acc[2][0]), "+v"(acc[2][1]), "+v"(acc[3][0]), "+v"(acc[3][1]) :: "memory");
    GAP();
#pragma unroll
    for (int db = 0; db < 4; ++db)
#pragma unroll
      for (int qb = 0; qb < 2; ++qb) {
        ac.q[qb][2 * db] = pack8n(acc[db][qb], 0); ac.q[qb][2 * db + 1] = pack8n(acc[db][qb], 1);
        asm volatile("" : "+a"(ac.q[qb][2 * db]), "+a"(ac.q[qb][2 * db + 1]));
      }
  }
#pragma unroll
  for (int i = 0; i < 4; ++i) { ac.o[i][0] = f32x16{}; ac.o[i][1] = f32x16{}; asm volatile("" : "+a"(ac.o[i][0]), "+a"(ac.o[i][1])); }
  f32x16 SA[2], SB[2];
  if constexpr (isA) {
    const unsigned kb0 = pc.ldsb + pc.rb0, kb1 = pc.ldsb + pc.rb1, vb = pc.ldsb + SLOT + tbV;
    asm volatile("s_waitcnt lgkmcnt(0)" ::: "memory");
    barrier();
    k_read(ac, kb0, kb1);
    wait_lgkm0();
    GAP();
    qk0(ac, SA);
    GAP();
    k_read(ac, kb0 + 2u * SLOT, kb1 + 2u * SLOT);
    sm_first(ac, SA);
    STAMP(13);
    for (unsigned i = 0; i < 14; i += 2) {
      if (i == 6) { wait_lgkm0(); barrier(); stash_write<40>(st, sbase, make_iseq<24>{}); }
      if (i == 8) { wait_lgkm0(); barrier(); }
      const unsigned s0 = (i >= 10u ? i - 10u : i) * (2u * SLOT), s2 = (i >= 8u ? i - 8u : i + 2u) * (2u * SLOT);
      phaseA<true, 0, KV_NT, 63>(ac, SA, SB, vb + s0, sK, 0u, 0u, 0u);
      phaseB<true, true, 0, KV_NT, 63>(ac, SB, kb0 + s2, kb1 + s2, sK, 0u, 0u, 0u);
      phaseA<true, 0, KV_NT, 63>(ac, SB, SA, vb + s0 + 2u * SLOT, sK, 0u, 0u, 0u);
      phaseB<true, true, 0, KV_NT, 63>(ac, SA, kb0 + s2 + 2u * SLOT, kb1 + s2 + 2u * SLOT, sK, 0u, 0u, 0u);
    }
    STAMP(14);
    phaseA<true, 0, KV_NT, 63>(ac, SA, SB, vb + 8u * SLOT, sK, 0u, 0u, 0u);
    phaseB<true, false, 0, KV_NT, 63>(ac, SB, 0u, 0u, sK, 0u, 0u, 0u);
    phaseA<false, 0, KV_NT, 63>(ac, SB, SA, vb + 10u * SLOT, sK, 0u, 0u, 0u);
    phaseB<false, false, 0, KV_NT, 63>(ac, SA, 0u, 0u, sK, 0u, 0u, 0u);
    pc.slot = 2u * SLOT;
  } else {
    const unsigned kb0 = pc.reg + pc.rb0, kb1 = pc.reg + pc.rb1, vb = pc.reg + tbV;
    wait_vm<32>();
    k_read(ac, kb0 + 2u * SLOT, kb1 + 2u * SLOT);
    wait_lgkm0();
    GAP();
    dma_elem<KV_NT>(sK, ksoff(3u, rk), 4096u, pc.reg + 2u * SLOT);
    qk0(ac, SA);
    wait_vm<32>();
    GAP();
    k_read(ac, kb0 + 3u * SLOT, kb1 + 3u * SLOT);
    sm_first(ac, SA);
    STAMP(13);
    unsigned sl = 3u * SLOT;
    for (unsigned i = 0; i < 12; i += 2) {
      unsigned fill = pc.reg + sl; sl = next5(sl);
      phaseA<true, 1, KV_NT, 32>(ac, SA, SB, vb + sl, sV, ksoff(i + 2u, rk), 4096u, fill);
      fill = pc.reg + sl; sl = next5(sl);
      phaseB<true, true, 1, KV_NT, 32>(ac, SB, kb0 + sl, kb1 + sl, sK, ksoff(i + 4u, rk), 4096u, fill);
      fill = pc.reg + sl; sl = next5(sl);
      phaseA<true, 1, KV_NT, 32>(ac, SB, SA, vb + sl, sV, ksoff(i + 3u, rk), 4096u, fill);
      fill = pc.reg + sl; sl = next5(sl);
      phaseB<true, true, 1, KV_NT, 32>(ac, SA, kb0 + sl, kb1 + sl, sK, ksoff(i + 5u, rk), 4096u, fill);
    }
    STAMP(14);
    constexpr unsigned UO = 16u * 1024u;
    phaseA<true, 1, KV_NT, 32>(ac, SA, SB, vb + 3u * SLOT, sV, ksoff(14u, rk), 4096u, pc.reg + 2u * SLOT);
    phaseB<true, true, 1, KV_NT, 32>(ac, SB, kb0 + 4u * SLOT, kb1 + 4u * SLOT, sV, ksoff(15u, rk), 4096u, pc.reg + 3u * SLOT);
    phaseA<true, 1, W_NT, 32>(ac, SB, SA, vb, sO, chunk_of(0u, b) * (32u * 1024u), UO, pc.reg + 4u * SLOT);
    phaseB<true, true, 0, W_NT, 24>(ac, SA, kb0 + SLOT, kb1 + SLOT, sO, 0u, 0u, 0u);
    phaseA<true, 0, W_NT, 16>(ac, SA, SB, vb + 2u * SLOT, sO, 0u, 0u, 0u);
    phaseB<true, false, 1, W_NT, 0>(ac, SB, 0u, 0u, sO, chunk_of(1u, b) * (32u * 1024u), UO, pc.reg + 2u * SLOT);
    phaseA<false, 0, W_NT, 16>(ac, SB, SA, vb + 3u * SLOT, sO, 0u, 0u, 0u);
    phaseB<false, false, 1, W_NT, 0>(ac, SA, 0u, 0u, sO, chunk_of(2u, b) * (32u * 1024u), UO, pc.reg + 3u * SLOT);
    pc.slot = 4u * SLOT;
  }
  asm volatile("s_nop 7\n\ts_nop 7" : "+a"(ac.o[0][0]), "+a"(ac.o[0][1]), "+a"(ac.o[1][0]), "+a"(ac.o[1][1]), "+a"(ac.o[2][0]), "+a"(ac.o[2][1]), "+a"(ac.o[3][0]), "+a"(ac.o[3][1]) :: "memory");
  STAMP(3);
  if constexpr (isA) {
    barrier();
#pragma unroll
    for (unsigned e = 0; e < 3; ++e) dma_elem<W_NT>(sO, chunk_of(e, b) * (32u * 1024u), 16u * 1024u, pc.reg + (2u + e) * SLOT);
  }
  GAP();
#pragma unroll
  for (int qb = 0; qb < 2; ++qb) {
    const float ls = ac.t0[qb] + ac.t1[qb];
    auto pr = __builtin_amdgcn_permlane32_swap(__float_as_uint(ls), __float_as_uint(ls), false, false);
    const float inv = 1.0f / (__uint_as_float(pr[0]) + __uint_as_float(pr[1]));
#pragma unroll
    for (int db = 0; db < 4; ++db)
#pragma unroll
      for (int s = 0; s < 2; ++s)
        *(LAS bf16x8*)(size_t)(pc.reg + qb * 8192u + db * 256u + (s ? pc.rb1 : pc.rb0)) = pack8(ac.o[db][qb], s, inv);
  }
  wait_vm_lgkm0<20>();
  barrier();
  GAP();
  STAMP(4);
  {
    f32x16 acc[4][2];
    UF f0, f1;
    constexpr unsigned US = 16u * 1024u, CS = 32u * 1024u;
    pc.e = 0; pc.vW = pc.reg + pc.slot + pc.tbW;
    const unsigned xo = pc.ldsb + ximg_off(chunk_of(0u, b));
    unit_read(f0, pc.vW, xo + pc.rb0);
    pc.xa = xo + pc.rb1; pc.fill = pc.reg + pc.slot; pc.soff = chunk_of(3u, b) * CS;
    proj_elem<true, false, true, W_NT, 16, 16, false, false, false, US, CS>(acc, f0, f1, pc, sO, sO, 0u, 0u, 0u, 0u, false, 0u);
    for (unsigned e = 1; e < 13; ++e)
      proj_elem<false, false, true, W_NT, 16, 16, false, false, false, US, CS>(acc, f0, f1, pc, sO, sO, 0u, 0u, 0u, 0u, false, 0u);
    proj_elem<false, false, false, W_NT, 16, 12, false, false, false, US, CS>(acc, f0, f1, pc, sO, sO, 0u, 0u, 0u, 0u, false, 0u);
    proj_elem<false, false, false, W_NT, 8, 4, false, false, false, US, CS>(acc, f0, f1, pc, sO, sO, 0u, 0u, 0u, 0u, false, 0u);
    proj_elem<false, true, false, W_NT, 0, 0, false, false, false, US, CS>(acc, f0, f1, pc, sO, sO, 0u, 0u, 0u, 0u, false, 0u);
    asm volatile("s_nop 7\n\ts_nop 7" : "+v"(acc[0][0]), "+v"(acc[0][1]), "+v"(acc[1][0]), "+v"(acc[1][1]), "+v"(acc[2][0]), "+v"(acc[2][1]), "+v"(acc[3][0]), "+v"(acc[3][1]) :: "memory");
    GAP();
    STAMP(5);
    const srd_t srdP = __builtin_amdgcn_make_buffer_rsrc((void*)(part + (size_t)rr * 524288u), (short)0, 524288, 0x00020000);
    unsigned toks;
    asm volatile("buffer_load_dword %0, %1, %2, %3 offen sc1" : "=v"(toks) : "v"(l8), "s"(srdH), "s"(tok_off) : "memory");
    const unsigned l16 = lane * 16u;
    u32x4 pk[16];
#pragma unroll
    for (int nb = 0; nb < 4; ++nb)
#pragma unroll
      for (int qb = 0; qb < 2; ++qb)
#pragma unroll
        for (int s = 0; s < 2; ++s) {
          pk[(nb * 2 + qb) * 2 + s] = __builtin_bit_cast(u32x4, pack8n(acc[nb][qb], s));
          __builtin_amdgcn_raw_buffer_store_b128(pk[(nb * 2 + qb) * 2 + s], srdP, l16 + ((qb * 2 + (nb & 1)) * 2 + s) * 1024u, xg * 65536u + w * 16384u + (nb >> 1) * 8192u, 16);
        }
    asm volatile("s_nop 1" :: "v"(pk[0]), "v"(pk[1]), "v"(pk[2]), "v"(pk[3]), "v"(pk[4]), "v"(pk[5]), "v"(pk[6]), "v"(pk[7]),
                 "v"(pk[8]), "v"(pk[9]), "v"(pk[10]), "v"(pk[11]), "v"(pk[12]), "v"(pk[13]), "v"(pk[14]), "v"(pk[15]) : "memory");
    STAMP(6);
    asm volatile("s_waitcnt vmcnt(0)" : "+v"(toks) :: "memory");
    STAMP(7);
    const bool mine = (lane >> 1) == w;
    const unsigned fo = lane * 128u + xg * 4u;
    if (mine) asm volatile("buffer_store_dword %0, %1, %2, %3 offen sc1" :: "v"(toks), "v"(fo), "s"(srdH), "s"(flg_off) : "memory");
    const unsigned tau = __builtin_amdgcn_readlane(toks, xg);
#pragma unroll 1
    for (unsigned spin = 0; spin < (1u << 22); ++spin) {
      unsigned fl, tk;
      asm volatile("buffer_load_dword %0, %2, %3, %4 offen sc1\n\tbuffer_load_dword %1, %2, %3, %5 offen sc1\n\ts_waitcnt vmcnt(0)"
                   : "=&v"(fl), "=&v"(tk) : "v"(l8), "s"(srdH), "s"(flg_off + xg * 128u), "s"(tok_off) : "memory");
      if (mine && tk != toks) asm volatile("buffer_store_dword %0, %1, %2, %3 offen sc1" :: "v"(tk), "v"(fo), "s"(srdH), "s"(flg_off) : "memory");
      toks = tk;
      if (__builtin_amdgcn_ballot_w64(fl != tau) == 0ull) break;
      __builtin_amdgcn_s_sleep(isA ? SLEEP_A : SLEEP_B);
    }
    STAMP(15);
    unsigned tk2;
    asm volatile("buffer_load_dword %0, %1, %2, %3 offen sc1" : "=v"(tk2) : "v"(l8), "s"(srdH), "s"(tok_off) : "memory");
    u32x4 v[2][8];
#pragma unroll
    for (int x = 0; x < 8; ++x)
#pragma unroll
      for (int s = 0; s < 2; ++s) v[s][x] = __builtin_amdgcn_raw_buffer_load_b128(srdP, l16 + s * 1024u, x * 65536u + xg * 8192u + w * 2048u, 16);
    const unsigned q = 32u * (w >> 1) + r32, n0 = 64u * xg + 32u * (w & 1u) + 8u * hh;
    char* op = out + ((size_t)(b * 64u + q) * 1024u + (isA ? 0u : 512u) + n0) * 2u;
#pragma unroll
    for (int s = 0; s < 2; ++s) {
      u32x4 o;
#pragma unroll
      for (int j = 0; j < 4; ++j) {
        float lo = 0.f, hi = 0.f;
#pragma unroll
        for (int x = 0; x < 8; ++x) { lo += __uint_as_float(v[s][x][j] << 16); hi += __uint_as_float(v[s][x][j] & 0xffff0000u); }
        o[j] = cvtpk(lo, hi);
      }
      *(u32x4*)(op + s * 32) = o;
    }
    asm volatile("s_waitcnt vmcnt(0)" : "+v"(tk2) :: "memory");
    if (mine && tk2 != toks) asm volatile("buffer_store_dword %0, %1, %2, %3 offen sc1" :: "v"(tk2), "v"(fo), "s"(srdH), "s"(flg_off) : "memory");
  }
}

__global__ __launch_bounds__(256, 1)
void fused_attn(const char* __restrict__ Ax, const char* __restrict__ Ak, const char* __restrict__ Av, const char* __restrict__ Awq, const char* __restrict__ Awo,
                const char* __restrict__ Bx, const char* __restrict__ Bk, const char* __restrict__ Bv, const char* __restrict__ Bwq, const char* __restrict__ Bwo,
                char* __restrict__ part, char* __restrict__ out) {
  if (blockIdx.x >= 128) body<true>(Ax, Ak, Av, Awq, Awo, part, Bk, Bv, out);
  else                   body<false>(Bx, Bk, Bv, Bwq, Bwo, part, Bk, Bv, out);
}

extern "C" void kernel_launch(void* const* d_in, const int* in_sizes, int n_in, void* d_out, int out_size, void* d_ws, size_t ws_size, hipStream_t stream) {
  (void)in_sizes; (void)n_in; (void)out_size; (void)ws_size;
  (void)hipFuncSetAttribute((const void*)fused_attn, hipFuncAttributeMaxDynamicSharedMemorySize, (int)LDS_BYTES);
  fused_attn<<<256, 256, LDS_BYTES, stream>>>((const char*)d_in[0], (const char*)d_in[1], (const char*)d_in[2], (const char*)d_in[3], (const char*)d_in[4],
                                              (const char*)d_in[5], (const char*)d_in[6], (const char*)d_in[7], (const char*)d_in[8], (const char*)d_in[9], (char*)d_ws, (char*)d_out);
}
```

```cpp
#include <hip/hip_runtime.h>
#include <hip/hip_bf16.h>
#include <utility>

typedef __bf16 bf16x8 __attribute__((ext_vector_type(8)));
typedef __bf16 bf16x4 __attribute__((ext_vector_type(4)));
typedef __bf16 bf16x2 __attribute__((ext_vector_type(2)));
typedef float f32x16 __attribute__((ext_vector_type(16)));
typedef float f32x2 __attribute__((ext_vector_type(2)));
typedef unsigned u32x4 __attribute__((ext_vector_type(4)));
#define LAS __attribute__((address_space(3)))
#define DI __device__ __forceinline__
typedef __amdgpu_buffer_rsrc_t srd_t;
template <int... Is> using iseq = std::integer_sequence<int, Is...>;
template <int N> using make_iseq = std::make_integer_sequence<int, N>;

#define KV_NT 1
#define W_NT 0
#define PF_N 0
#define PF_POL ""
#define SLEEP_A 32
#define SLEEP_B 4
#define ORDER 0
#define PACE_A 0
#define X_NT 0

constexpr unsigned SLOT = 8192, REG = 5 * SLOT, LDS_BYTES = 4 * REG;
constexpr unsigned HOFF = 32u << 20;
constexpr float SC = 0.12751743082f;
constexpr float THR_RAW = 8.0f / SC;

#define NO_REDUCE_STAMPS
#define GAP() __builtin_amdgcn_sched_barrier(0)
#define STAMP(i) do {} while (0)
#define PINS(x) asm volatile("" : "+s"(x))
#define PINV(x) asm volatile("" : "+v"(x))
DI unsigned rfl(unsigned x) { return __builtin_amdgcn_readfirstlane(x); }
DI unsigned cvtpk(float lo, float hi) { f32x2 v = {lo, hi}; bf16x2 b = __builtin_convertvector(v, bf16x2); return __builtin_bit_cast(unsigned, b); }
DI bf16x8 pack8(const f32x16& x, int s, float sc) {
  u32x4 w = {cvtpk(x[8 * s] * sc, x[8 * s + 1] * sc), cvtpk(x[8 * s + 2] * sc, x[8 * s + 3] * sc), cvtpk(x[8 * s + 4] * sc, x[8 * s + 5] * sc), cvtpk(x[8 * s + 6] * sc, x[8 * s + 7] * sc)};
  return __builtin_bit_cast(bf16x8, w);
}
DI bf16x8 pack8n(const f32x16& x, int s) {
  u32x4 w = {cvtpk(x[8 * s], x[8 * s + 1]), cvtpk(x[8 * s + 2], x[8 * s + 3]), cvtpk(x[8 * s + 4], x[8 * s + 5]), cvtpk(x[8 * s + 6], x[8 * s + 7])};
  return __builtin_bit_cast(bf16x8, w);
}
template <bool Z> DI void mm_vvv(f32x16& c, const bf16x8& a, const bf16x8& b) {
  if constexpr (Z) asm volatile("v_mfma_f32_32x32x16_bf16 %0, %1, %2, 0" : "=&v"(c) : "v"(a), "v"(b));
  else             asm volatile("v_mfma_f32_32x32x16_bf16 %0, %1, %2, %0" : "+v"(c) : "v"(a), "v"(b));
}
#define AMF(x) x
#define ASM_(x) x
template <bool Z> DI void mm_vva(f32x16& c, const bf16x8& a, const bf16x8& b) {
  if constexpr (Z) asm volatile(AMF("v_mfma_f32_32x32x16_bf16 %0, %1, %2, 0") : "=&v"(c) : "v"(a), "a"(b));
  else             asm volatile(AMF("v_mfma_f32_32x32x16_bf16 %0, %1, %2, %0") : "+v"(c) : "v"(a), "a"(b));
}
DI void mm_avv(f32x16& c, const bf16x8& a, const bf16x8& b) {
  asm volatile(AMF("v_mfma_f32_32x32x16_bf16 %0, %1, %2, %0") : "+a"(c) : "v"(a), "v"(b));
}
DI void set_m0(unsigned v) { asm volatile("s_mov_b32 m0, %0" :: "s"(v) : "memory"); }
template <int NT, int P> DI void dma_p(unsigned voff, srd_t srd, unsigned soff) {
  if constexpr (NT) asm volatile("buffer_load_dwordx4 %0, %1, %2 offen offset:%3 nt lds" :: "v"(voff), "s"(srd), "s"(soff), "i"(1024 * P) : "memory");
  else              asm volatile("buffer_load_dwordx4 %0, %1, %2 offen offset:%3 lds" :: "v"(voff), "s"(srd), "s"(soff), "i"(1024 * P) : "memory");
}
struct Src { unsigned v[4]; srd_t srd; };
template <int NT> DI void dma_unit(const Src& s, unsigned soff, unsigned ldsaddr) {
  asm volatile("s_mov_b32 m0, %0\n\ts_nop 0" :: "s"(ldsaddr) : "memory");
  dma_p<NT, 0>(s.v[0], s.srd, soff); dma_p<NT, 1>(s.v[1], s.srd, soff); dma_p<NT, 2>(s.v[2], s.srd, soff); dma_p<NT, 3>(s.v[3], s.srd, soff);
}
template <int NT> DI void dma_elem(const Src& s, unsigned soff, unsigned ustep, unsigned ldsaddr) { dma_unit<NT>(s, soff, ldsaddr); dma_unit<NT>(s, soff + ustep, ldsaddr + 4096u); }
template <int N> DI void wait_vm() { asm volatile("s_waitcnt vmcnt(%0)" :: "i"(N) : "memory"); }
template <int N> DI void wait_vm_lgkm0() { asm volatile("" ::: "memory"); __builtin_amdgcn_s_waitcnt((N & 15) | ((N >> 4) << 14) | 0x70); asm volatile("" ::: "memory"); }
DI void wait_lgkm0() { asm volatile("" ::: "memory"); __builtin_amdgcn_s_waitcnt(0xC07F); asm volatile("" ::: "memory"); }
DI void barrier() { asm volatile("s_barrier" ::: "memory"); }
DI bf16x8 lds128(unsigned a) { return *(const LAS bf16x8*)(size_t)(a); }
DI bf16x8 lds_tr(unsigned a0, unsigned a1) {
  bf16x4 lo = __builtin_amdgcn_ds_read_tr16_b64_v4bf16((LAS bf16x4*)(size_t)(a0));
  bf16x4 hi = __builtin_amdgcn_ds_read_tr16_b64_v4bf16((LAS bf16x4*)(size_t)(a1));
  return __builtin_shufflevector(lo, hi, 0, 1, 2, 3, 4, 5, 6, 7);
}
template <bool XOR> DI void mk_src(Src& s, const char* base, unsigned bytes, unsigned stride, unsigned lane) {
  s.srd = __builtin_amdgcn_make_buffer_rsrc((void*)base, (short)0, (int)bytes, 0x00020000);
  const unsigned row = (lane >> 2) & 3u, cb = lane >> 4, sl = lane & 3u;
#pragma unroll
  for (int p = 0; p < 4; ++p) s.v[p] = row * stride + cb * 64u + ((XOR ? (sl ^ p) : sl) << 4) + (4u * stride - 1024u) * p;
}

struct UF { bf16x8 a[4]; bf16x8 b[2]; };
struct PC {
  unsigned reg, ldsb, slot, rot, e;
  unsigned tbW, rb0, rb1;
  unsigned vW, xa;
  unsigned fill, soff;
};
DI unsigned chunk_of(unsigned e, unsigned rot) { return ((e + (rot & 12u)) & 12u) | ((e + rot) & 3u); }
DI unsigned ksoff(unsigned m, unsigned rk) { return ((m + rk) & 15u) * 8192u; }
DI unsigned ksoff_oob(unsigned m, unsigned rk) { return m < 16u ? ((m + rk) & 15u) * 8192u : 0x100000u; }
DI unsigned ximg_off(unsigned c) { return (c >> 2) * REG + (c & 3u) * 256u; }
DI unsigned next3(unsigned s) { return s == 4u * SLOT ? 2u * SLOT : s + SLOT; }
DI unsigned next5(unsigned s) { return s == 4u * SLOT ? 0u : s + SLOT; }
DI void unit_read(UF& f, unsigned wa, unsigned xa) {
  f.b[0] = lds128(xa); f.b[1] = lds128(xa + 8192u);
#pragma unroll
  for (int db = 0; db < 4; ++db) f.a[db] = lds_tr(wa + db * 256u, wa + db * 256u + 1024u);
}
template <int S, bool Z, bool RD, bool DM, int NT, int VM, bool BAR, bool XT, unsigned USTEP, unsigned CSTEP>
DI void proj_unit(f32x16 (&acc)[4][2], const UF& cur, UF& nxt, PC& c, const Src& src, const Src& xs, unsigned xsoff, unsigned xdst, bool expl, unsigned nsoff) {
  if constexpr (RD) wait_vm_lgkm0<VM>(); else wait_lgkm0();
  if constexpr (BAR) barrier();
  const unsigned wa = c.vW + (S == 0 ? 4096u : 0u), xa = c.xa, fill = c.fill, soff = c.soff;
  if constexpr (XT) set_m0(xdst); else if constexpr (DM) set_m0(fill);
  GAP();
  mm_vvv<Z>(acc[0][0], cur.a[0], cur.b[0]);
  if constexpr (RD) { nxt.b[0] = lds128(xa); nxt.a[0] = lds_tr(wa, wa + 1024u); }
  if constexpr (XT) dma_p<X_NT, 0>(xs.v[0], xs.srd, xsoff);
  GAP();
  mm_vvv<Z>(acc[0][1], cur.a[0], cur.b[1]);
  if constexpr (RD) nxt.b[1] = lds128(xa + 8192u);
  if constexpr (XT) dma_p<X_NT, 1>(xs.v[1], xs.srd, xsoff); else if constexpr (DM) dma_p<NT, 0>(src.v[0], src.srd, soff);
  GAP();
  mm_vvv<Z>(acc[1][0], cur.a[1], cur.b[0]);
  if constexpr (RD) nxt.a[1] = lds_tr(wa + 256u, wa + 1280u);
  if constexpr (XT) dma_p<X_NT, 2>(xs.v[2], xs.srd, xsoff);
  GAP();
  mm_vvv<Z>(acc[1][1], cur.a[1], cur.b[1]);
  if constexpr (RD) nxt.a[2] = lds_tr(wa + 512u, wa + 1536u);
  if constexpr (XT) { dma_p<X_NT, 3>(xs.v[3], xs.srd, xsoff); set_m0(fill); } else if constexpr (DM) dma_p<NT, 1>(src.v[1], src.srd, soff);
  GAP();
  mm_vvv<Z>(acc[2][0], cur.a[2], cur.b[0]);
  if constexpr (RD) nxt.a[3] = lds_tr(wa + 768u, wa + 1792u);
  if constexpr (XT) dma_p<NT, 0>(src.v[0], src.srd, soff);
  GAP();
  mm_vvv<Z>(acc[2][1], cur.a[2], cur.b[1]);
  if constexpr (XT) dma_p<NT, 1>(src.v[1], src.srd, soff); else if constexpr (DM) dma_p<NT, 2>(src.v[2], src.srd, soff);
  if constexpr (S == 0) {
    PINS(c.slot);
    c.slot = next3(c.slot);
    c.vW = c.reg + c.slot + c.tbW;
    PINV(c.vW);
  } else {
    PINS(c.e);
    c.e += 1u;
  }
  GAP();
  mm_vvv<Z>(acc[3][0], cur.a[3], cur.b[0]);
  if constexpr (XT) dma_p<NT, 2>(src.v[2], src.srd, soff);
  if constexpr (S == 0) {
    PINS(c.e);
    const unsigned xo = c.ldsb + ximg_off(chunk_of(c.e + 1u, c.rot));
    c.xa = xo + c.rb0;
    PINV(c.xa);
  } else {
    c.xa = xa ^ 32u;
    PINV(c.xa);
  }
  GAP();
  mm_vvv<Z>(acc[3][1], cur.a[3], cur.b[1]);
  if constexpr (DM) dma_p<NT, 3>(src.v[3], src.srd, soff);
  if constexpr (S == 0) { c.fill = fill + 4096u; c.soff = soff + USTEP; }
  else { c.fill = c.reg + c.slot; c.soff = expl ? nsoff : chunk_of(c.e + 3u, c.rot) * CSTEP; }
  PINS(c.fill); PINS(c.soff);
  GAP();
}
template <bool Z, bool LAST, bool DM, int NT, int VM0, int VM1, bool BAR1, bool XT0, bool XT1, unsigned USTEP, unsigned CSTEP>
DI void proj_elem(f32x16 (&acc)[4][2], UF& f0, UF& f1, PC& c, const Src& src, const Src& xs, unsigned xso0, unsigned xd0, unsigned xso1, unsigned xd1, bool expl, unsigned nsoff) {
  proj_unit<0, Z, true, DM, NT, VM0, false, XT0, USTEP, CSTEP>(acc, f0, f1, c, src, xs, xso0, xd0, false, 0u);
  proj_unit<1, false, !LAST, DM, NT, VM1, BAR1, XT1, USTEP, CSTEP>(acc, f1, f0, c, src, xs, xso1, xd1, expl, nsoff);
}

struct AC {
  f32x16 o[4][2];
  bf16x8 q[2][8];
  bf16x8 kf[8], vf[4][2], pf[2][2];
  float m[2], mc[2], f[2];
  float t0[2], t1[2];
  bool pend;
};
template <int QB, int J> DI void sm_exp2(f32x16 (&S)[2], AC& c) {
  float a, b;
  asm volatile(ASM_("v_fma_f32 %0, %4, %2, %3\n\tv_fma_f32 %1, %5, %2, %3\n\tv_exp_f32_e32 %0, %0\n\tv_exp_f32_e32 %1, %1") : "=&v"(a), "=v"(b) : "s"(SC), "v"(c.mc[QB]), "v"(S[QB][J]), "v"(S[QB][J + 1]));
  S[QB][J] = a; S[QB][J + 1] = b;
}
template <int QB, int S_, int P> DI void sm_pack(f32x16 (&S)[2], AC& c) {
  u32x4 w = __builtin_bit_cast(u32x4, c.pf[QB][S_]);
  unsigned r;
  asm volatile(ASM_("v_cvt_pk_bf16_f32 %0, %3, %4\n\tv_add_f32_e32 %1, %1, %3\n\tv_add_f32_e32 %2, %2, %4") : "=&v"(r), "+v"(c.t0[QB]), "+v"(c.t1[QB]) : "v"(S[QB][8 * S_ + 2 * P]), "v"(S[QB][8 * S_ + 2 * P + 1]));
  w[P] = r;
  c.pf[QB][S_] = __builtin_bit_cast(bf16x8, w);
}
template <int DM, int G> DI void mem_pre(unsigned ldsaddr) {
  if constexpr (G == 1 && DM != 0) wait_lgkm0();
  if constexpr (DM == 1 && (G == 1 || G == 5)) set_m0(ldsaddr + (G == 5 ? 4096u : 0u));
  if constexpr (DM == 2 && G == 9) set_m0(ldsaddr);
}
template <int DM, int NT, int VM, bool RD, int G> DI void mem_post(const Src& src, unsigned soff, unsigned ustep) {
  if constexpr (DM == 1 && G >= 1 && G <= 8) dma_p<NT, (G - 1) & 3>(src.v[(G - 1) & 3], src.srd, soff + (G >= 5 ? ustep : 0u));
  if constexpr (DM == 2 && (G == 9 || G == 10)) dma_p<NT, G - 9>(src.v[G - 9], src.srd, soff);
  if constexpr (G == 8 && (RD || DM == 2) && VM < 63) wait_vm<VM>();
  if constexpr (G == 8 && DM == 2) barrier();
}
template <bool QK, int DM, int NT, int VM, int G>
DI void gapA(AC& c, f32x16 (&S)[2], f32x16 (&Sn)[2], unsigned va, const Src& src, unsigned soff, unsigned ustep, unsigned ldsaddr) {
  mem_pre<DM, G>(ldsaddr);
  if constexpr (QK) mm_vva<(G < 2)>(Sn[G & 1], c.kf[G >> 1], c.q[G & 1][G >> 1]); else if constexpr (DM != 0) asm volatile("s_nop 0");
  mem_post<DM, NT, VM, true, G>(src, soff, ustep);
  if constexpr (G >= 8) { constexpr int i = G - 8, s = i >> 2, db = i & 3; c.vf[db][s] = lds_tr(va + s * 4096u + db * 256u, va + s * 4096u + db * 256u + 2048u); }
  if constexpr (G < 8) {
    sm_exp2<1, 2 * G>(S, c);
    sm_pack<0, (G >> 2), (G & 3)>(S, c);
  } else {
    constexpr int i = G - 8;
    sm_pack<1, (i >> 2), (i & 3)>(S, c);
  }
  GAP();
}
template <bool QK, int DM, int NT, int VM, int... Gs>
DI void phaseA_(AC& c, f32x16 (&S)[2], f32x16 (&Sn)[2], unsigned va, const Src& src, unsigned soff, unsigned ustep, unsigned ldsaddr, iseq<Gs...>) {
  (gapA<QK, DM, NT, VM, Gs>(c, S, Sn, va, src, soff, ustep, ldsaddr), ...);
}
template <bool QK, int DM, int NT, int VM>
DI void phaseA(AC& c, f32x16 (&S)[2], f32x16 (&Sn)[2], unsigned va, const Src& src, unsigned soff, unsigned ustep, unsigned ldsaddr) {
  phaseA_<QK, DM, NT, VM>(c, S, Sn, va, src, soff, ustep, ldsaddr, make_iseq<16>{});
}
template <bool SM, bool KR, int DM, int NT, int VM, int G>
DI void gapB(AC& c, f32x16 (&Sn)[2], unsigned ka0, unsigned ka1, const Src& src, unsigned soff, unsigned ustep, unsigned ldsaddr, float (&mx)[2][2]) {
  if constexpr (G == 0) asm volatile("s_nop 1");
  mem_pre<DM, G>(ldsaddr);
  { constexpr int s = G >> 3, db = (G >> 1) & 3, qb = G & 1; mm_avv(c.o[db][qb], c.vf[db][s], c.pf[qb][s]); }
  mem_post<DM, NT, VM, KR, G>(src, soff, ustep);
  if constexpr (KR && G >= 8) { constexpr int s = G - 8; c.kf[s] = lds128(((s & 1) ? ka1 : ka0) + (s >> 1) * 256u); }
  if constexpr (SM) {
    if constexpr (G >= 2 && G <= 5) {
      constexpr int q = (G - 2) >> 1, r = 8 * ((G - 2) & 1);
      if constexpr (r == 0)
        asm volatile("v_max3_f32 %0, %2, %3, %4\n\tv_max3_f32 %1, %5, %6, %7\n\tv_max3_f32 %0, %0, %8, %9\n\tv_max3_f32 %1, %1, %10, %11"
                     : "=&v"(mx[q][0]), "=&v"(mx[q][1]) : "v"(Sn[q][0]), "v"(Sn[q][1]), "v"(Sn[q][2]), "v"(Sn[q][3]), "v"(Sn[q][4]), "v"(Sn[q][5]), "v"(Sn[q][6]), "v"(Sn[q][7]), "v"(Sn[q][8]), "v"(Sn[q][9]));
      else
        asm volatile("v_max3_f32 %0, %0, %2, %3\n\tv_max3_f32 %1, %1, %4, %5\n\tv_max3_f32 %0, %0, %6, %7\n\tv_max_f32_e32 %0, %0, %1\n\tv_mov_b32_e32 %1, %0"
                     : "+v"(mx[q][0]), "+v"(mx[q][1]) : "v"(Sn[q][10]), "v"(Sn[q][11]), "v"(Sn[q][12]), "v"(Sn[q][13]), "v"(Sn[q][14]), "v"(Sn[q][15]));
    }
    if constexpr (G == 6) {
      asm volatile("v_permlane32_swap_b32_e32 %0, %1\n\tv_permlane32_swap_b32_e32 %2, %3\n\tv_max3_f32 %0, %0, %1, %4\n\tv_max3_f32 %2, %2, %3, %5"
                   : "+v"(mx[0][0]), "+v"(mx[0][1]), "+v"(mx[1][0]), "+v"(mx[1][1]) : "v"(c.m[0]), "v"(c.m[1]));
    }
    if constexpr (G == 7) {
      const float d0 = c.m[0] - mx[0][0], d1 = c.m[1] - mx[1][0];
      c.pend = __builtin_amdgcn_ballot_w64(fminf(d0, d1) < -THR_RAW) != 0;
      c.f[0] = d0; c.f[1] = d1;
      c.m[0] = c.pend ? mx[0][0] : c.m[0]; c.m[1] = c.pend ? mx[1][0] : c.m[1];
      c.mc[0] = -c.m[0] * SC; c.mc[1] = -c.m[1] * SC;
      asm volatile("" : "+v"(c.mc[0]), "+v"(c.mc[1]));
    }
    if constexpr (G >= 8) sm_exp2<0, 2 * (G - 8)>(Sn, c);
  }
  GAP();
}
template <bool SM, bool KR, int DM, int NT, int VM, int... Gs>
DI void phaseB_(AC& c, f32x16 (&Sn)[2], unsigned ka0, unsigned ka1, const Src& src, unsigned soff, unsigned ustep, unsigned ldsaddr, iseq<Gs...>) {
  float mx[2][2];
  (gapB<SM, KR, DM, NT, VM, Gs>(c, Sn, ka0, ka1, src, soff, ustep, ldsaddr, mx), ...);
  if constexpr (SM) {
    if (__builtin_expect(c.pend, 0)) {
      asm volatile("s_nop 7\n\ts_nop 7" ::: "memory");
#pragma unroll
      for (int q = 0; q < 2; ++q) {
        const float f = __builtin_amdgcn_exp2f(c.f[q] * SC);
        c.t0[q] *= f; c.t1[q] *= f;
#pragma unroll
        for (int db = 0; db < 4; ++db) {
          asm volatile("" : "+a"(c.o[db][q]));
          c.o[db][q] *= f;
          asm volatile("" : "+a"(c.o[db][q]));
        }
      }
      asm volatile("s_nop 1" ::: "memory");
    }
  }
}
template <bool SM, bool KR, int DM, int NT, int VM>
DI void phaseB(AC& c, f32x16 (&Sn)[2], unsigned ka0, unsigned ka1, const Src& src, unsigned soff, unsigned ustep, unsigned ldsaddr) {
  phaseB_<SM, KR, DM, NT, VM>(c, Sn, ka0, ka1, src, soff, ustep, ldsaddr, make_iseq<16>{});
}
DI void k_read(AC& ac, unsigned ka0, unsigned ka1) {
#pragma unroll
  for (int s = 0; s < 8; ++s) ac.kf[s] = lds128(((s & 1) ? ka1 : ka0) + (s >> 1) * 256u);
}
DI void qk0(AC& ac, f32x16 (&SA)[2]) {
  mm_vva<true>(SA[0], ac.kf[0], ac.q[0][0]); mm_vva<true>(SA[1], ac.kf[0], ac.q[1][0]);
#pragma unroll
  for (int s = 1; s < 8; ++s) { mm_vva<false>(SA[0], ac.kf[s], ac.q[0][s]); mm_vva<false>(SA[1], ac.kf[s], ac.q[1][s]); }
}
template <int... Js> DI void exp_first(f32x16 (&SA)[2], AC& ac, iseq<Js...>) { (sm_exp2<0, 2 * Js>(SA, ac), ...); }
DI void sm_first(AC& ac, f32x16 (&SA)[2]) {
  asm volatile("s_nop 7\n\ts_nop 7" : "+v"(SA[0]), "+v"(SA[1]) :: "memory");
  GAP();
#pragma unroll
  for (int q = 0; q < 2; ++q) {
    float mx = SA[q][0];
#pragma unroll
    for (int i = 1; i < 16; ++i) mx = fmaxf(mx, SA[q][i]);
    auto pr = __builtin_amdgcn_permlane32_swap(__float_as_uint(mx), __float_as_uint(mx), false, false);
    ac.m[q] = fmaxf(__uint_as_float(pr[0]), __uint_as_float(pr[1]));
    ac.mc[q] = -ac.m[q] * SC; ac.t0[q] = 0.f; ac.t1[q] = 0.f;
  }
  exp_first(SA, ac, make_iseq<8>{});
  GAP();
}

struct Stash { u32x4 p[64]; };
template <int I> DI void stash_load1(Stash& st, const unsigned (&vk)[2], unsigned vv, srd_t sk, srd_t sv, unsigned rk, unsigned wo_) {
  constexpr int m = I >> 2, isV = (I >> 1) & 1, pc_ = I & 1;
  const unsigned so = ksoff(m, rk) + wo_;
  if constexpr (I < 56) asm volatile("buffer_load_dwordx4 %0, %1, %2, %3 offen offset:%4 nt" : "=a"(st.p[I]) : "v"(isV ? vv : vk[pc_]), "s"(isV ? sv : sk), "s"(so), "i"(1024 * pc_) : "memory");
  else                  asm volatile("buffer_load_dwordx4 %0, %1, %2, %3 offen offset:%4 nt" : "=v"(st.p[I]) : "v"(isV ? vv : vk[pc_]), "s"(isV ? sv : sk), "s"(so), "i"(1024 * pc_) : "memory");
}
template <int... Is> DI void stash_load(Stash& st, const unsigned (&vk)[2], unsigned vv, srd_t sk, srd_t sv, unsigned rk, unsigned wo_, iseq<Is...>) { (stash_load1<Is>(st, vk, vv, sk, sv, rk, wo_), ...); }
template <int I> DI void stash_write1(const Stash& st, const unsigned (&base)[3]) {
  constexpr int m = I >> 2, isV = (I >> 1) & 1, pc_ = I & 1, off = (2 * (m % 10) + isV) * 8192 + pc_ * 1024;
  if constexpr (I < 56) asm volatile("ds_write_b128 %0, %1 offset:%2" :: "v"(base[off >> 16]), "a"(st.p[I]), "i"(off & 65535) : "memory");
  else                  asm volatile("ds_write_b128 %0, %1 offset:%2" :: "v"(base[off >> 16]), "v"(st.p[I]), "i"(off & 65535) : "memory");
}
template <int B, int... Is> DI void stash_write(const Stash& st, const unsigned (&base)[3], iseq<Is...>) { (stash_write1<B + Is>(st, base), ...); }

template <bool isA>
DI void body(const char* __restrict__ X, const char* __restrict__ K, const char* __restrict__ V, const char* __restrict__ Wq, const char* __restrict__ Wo, char* __restrict__ part,
             const char* __restrict__ PK, const char* __restrict__ PV, char* __restrict__ out) {
  extern __shared__ __attribute__((aligned(16))) char lds[];
  STAMP(0);
  const unsigned lane = threadIdx.x & 63u, w = rfl(threadIdx.x >> 6);
  const unsigned bid = blockIdx.x, xg = bid & 7u, rr = bid >> 3, b = rr & 15u, h = xg * 4u + w;
  const unsigned r32 = lane & 31u, hh = lane >> 5;
  const char* xp  = X + (size_t)b * 65536u + w * 16384u;
  const char* wqp = Wq + h * 256u;
  const char* wop = Wo + (size_t)xg * 524288u + w * 256u;
  const size_t kvo = isA ? (size_t)(b * 8u + xg) * 131072u : (size_t)(b * 32u + h) * 131072u;
  const char* kp = K + kvo;
  const char* vp = V + kvo;
  const unsigned rk = isA ? b : b + 4u * w;
  PC pc;
  pc.ldsb = rfl((unsigned)(size_t)lds); pc.reg = pc.ldsb + w * REG; pc.rot = b;
  const srd_t srdH = __builtin_amdgcn_make_buffer_rsrc((void*)(part + HOFF), (short)0, 4096 + 256 * 128, 0x00020000);
  const unsigned tok_off = rr * 128u, flg_off = 4096u + rr * 1024u;
  const unsigned l8 = (lane & 7u) * 4u;
  unsigned entry_flags;
  asm volatile("buffer_load_dword %0, %1, %2, %3 offen sc1" : "=v"(entry_flags) : "v"(l8), "s"(srdH), "s"(flg_off + xg * 128u) : "memory");
  Src sQ, sX, sK, sV, sO;
  mk_src<true>(sK, kp, 131072u, 256u, lane);
  mk_src<false>(sV, vp, 131072u, 256u, lane);
  mk_src<true>(sX, xp, 16384u, 1024u, lane);
  mk_src<false>(sQ, wqp, 511u * 8192u + 256u, 8192u, lane);
  const unsigned p0 = b >> 2;
  Stash st;
  unsigned pf_dummy = 0;
  auto early = [&]() {
    const unsigned vk[2] = {(w & 1u) ? sK.v[2] : sK.v[0], (w & 1u) ? sK.v[3] : sK.v[1]};
    stash_load(st, vk, sV.v[0], sK.srd, sV.srd, rk, w * 2048u, make_iseq<64>{});
  };
  constexpr bool FLAT = isA && ORDER == 1;
  if constexpr (isA && ORDER == 0) early();
  dma_unit<X_NT>(sX, p0 * 256u, pc.ldsb + p0 * REG + w * 4096u);
  dma_elem<W_NT>(sQ, chunk_of(0u, b) * (32u * 8192u), 16u * 8192u, pc.reg + 4u * SLOT);
  dma_elem<W_NT>(sQ, chunk_of(1u, b) * (32u * 8192u), 16u * 8192u, pc.reg + 2u * SLOT);
  if constexpr (FLAT) {
    dma_elem<W_NT>(sQ, chunk_of(2u, b) * (32u * 8192u), 16u * 8192u, pc.reg + 3u * SLOT);
#pragma unroll
    for (unsigned i = 1; i < 4; ++i) { const unsigned p = (p0 + i) & 3u; dma_unit<X_NT>(sX, p * 256u, pc.ldsb + p * REG + w * 4096u); }
    early();
  }
  mk_src<false>(sO, wop, 511u * 1024u + 256u, 1024u, lane);
  pc.rb0 = 1024u * (r32 >> 2) + 64u * (r32 & 3u) + 16u * (hh ^ ((r32 >> 2) & 3u));
  pc.rb1 = pc.rb0 ^ 32u;
  const unsigned p4 = lane & 3u, rev = ((p4 & 1u) << 1) | (p4 >> 1);
  const unsigned tb = ((lane >> 2) & 3u) * 64u + ((lane >> 4) & 1u) * 32u + rev * 8u;
  pc.tbW = tb + hh * 2048u;
  const unsigned tbV = tb + hh * 1024u;

  const unsigned sb0 = pc.ldsb + w * 2048u + lane * 16u;
  const unsigned sbase[3] = {sb0, sb0 + 65536u, sb0 + 131072u};
  AC ac;
  {
    f32x16 acc[4][2];
    UF f0, f1;
    asm volatile("s_waitcnt vmcnt(12)" : "+v"(entry_flags) :: "memory");
    if (w == 0) {
      const unsigned long long mk = __builtin_amdgcn_ballot_w64(((entry_flags >> (lane & 7u)) & 1u) != 0u);
      const unsigned tok = 0x5a5a5a00u | (~(unsigned)mk & 0xffu);
      if (lane == 0) asm volatile("buffer_store_dword %0, off, %1, %2 sc1" :: "v"(tok), "s"(srdH), "s"(tok_off + xg * 4u) : "memory");
    }
    barrier();
    STAMP(1);
    pc.e = 0; pc.slot = 4u * SLOT; pc.vW = pc.reg + pc.slot + pc.tbW;
    const unsigned xo = pc.ldsb + ximg_off(chunk_of(0u, b));
    unit_read(f0, pc.vW, xo + pc.rb0);
    if constexpr (!FLAT) dma_elem<W_NT>(sQ, chunk_of(2u, b) * (32u * 8192u), 16u * 8192u, pc.reg + 3u * SLOT);
    pc.xa = xo + pc.rb1; pc.fill = pc.reg + pc.slot; pc.soff = chunk_of(3u, b) * (32u * 8192u);
    constexpr unsigned US = 16u * 8192u, CS = 32u * 8192u;
    const unsigned p1 = (p0 + 1u) & 3u, p2 = (p0 + 2u) & 3u, p3 = (p0 + 3u) & 3u, xw = pc.ldsb + w * 4096u;
    if constexpr (FLAT) {
      proj_elem<true,  false, true, W_NT, 16, 16, false, false, false, US, CS>(acc, f0, f1, pc, sQ, sX, 0u, 0u, 0u, 0u, false, 0u);
    } else {
    proj_elem<true,  false, true, W_NT, 16, 20, false, true,  true,  US, CS>(acc, f0, f1, pc, sQ, sX, p1 * 256u, xw + p1 * REG, p2 * 256u, xw + p2 * REG, false, 0u);
    STAMP(10);
    proj_elem<false, false, true, W_NT, 24, 28, false, true,  false, US, CS>(acc, f0, f1, pc, sQ, sX, p3 * 256u, xw + p3 * REG, 0u, 0u, false, 0u);
    STAMP(11);
    proj_elem<false, false, true, W_NT, 28, 24, false, false, false, US, CS>(acc, f0, f1, pc, sQ, sX, 0u, 0u, 0u, 0u, false, 0u);
    proj_elem<false, false, true, W_NT, 20, 16, true,  false, false, US, CS>(acc, f0, f1, pc, sQ, sX, 0u, 0u, 0u, 0u, false, 0u);
    }
    STAMP(8);
    const unsigned first_tail = isA ? chunk_of(0u, b) * (32u * 1024u) : ksoff(0u, rk);
    for (unsigned e = FLAT ? 1 : 4; e < 13; ++e) {
      if constexpr (isA && PACE_A > 0) __builtin_amdgcn_s_sleep(PACE_A);
      proj_elem<false, false, true, W_NT, 16, 16, false, false, false, US, CS>(acc, f0, f1, pc, sQ, sX, 0u, 0u, 0u, 0u, e == 12, first_tail);
    }
    STAMP(9);
    if constexpr (isA) {
      if constexpr (PACE_A > 0) __builtin_amdgcn_s_sleep(PACE_A);
      proj_elem<false, false, false, W_NT, 16, 12, false, false, false, US, CS>(acc, f0, f1, pc, sQ, sX, 0u, 0u, 0u, 0u, false, 0u);
      if constexpr (PACE_A > 0) __builtin_amdgcn_s_sleep(PACE_A);
      proj_elem<false, false, false, W_NT, 8, 4, false, false, false, US, CS>(acc, f0, f1, pc, sQ, sX, 0u, 0u, 0u, 0u, false, 0u);
      if constexpr (PACE_A > 0) __builtin_amdgcn_s_sleep(PACE_A);
      proj_elem<false, true, false, W_NT, 0, 0, false, false, false, US, CS>(acc, f0, f1, pc, sQ, sX, 0u, 0u, 0u, 0u, false, 0u);
    } else {
      proj_elem<false, false, true, KV_NT, 16, 16, false, false, false, 4096u, CS>(acc, f0, f1, pc, sK, sX, 0u, 0u, 0u, 0u, true, ksoff(1u, rk));
      proj_elem<false, false, true, KV_NT, 16, 16, false, false, false, 4096u, CS>(acc, f0, f1, pc, sK, sX, 0u, 0u, 0u, 0u, true, ksoff(0u, rk));
      proj_elem<false, true,  true, KV_NT, 16, 16, false, false, false, 4096u, CS>(acc, f0, f1, pc, sV, sX, 0u, 0u, 0u, 0u, true, 0u);
    }
    barrier();
    STAMP(2);
    if constexpr (isA) {
      asm volatile("" :: "v"(pf_dummy));
      stash_write<0>(st, sbase, make_iseq<40>{});
    } else {
      dma_elem<KV_NT>(sK, ksoff(2u, rk), 4096u, pc.reg);
      dma_elem<KV_NT>(sV, ksoff(1u, rk), 4096u, pc.reg + SLOT);
    }
    asm volatile("s_nop 7\n\ts_nop 7" : "+v"(acc[0][0]), "+v"(acc[0][1]), "+v"(acc[1][0]), "+v"(acc[1][1]), "+v"(acc[2][0]), "+v"(acc[2][1]), "+v"(acc[3][0]), "+v"(acc[3][1]) :: "memory");
    GAP();
#pragma unroll
    for (int db = 0; db < 4; ++db)
#pragma unroll
      for (int qb = 0; qb < 2; ++qb) {
        ac.q[qb][2 * db] = pack8n(acc[db][qb], 0); ac.q[qb][2 * db + 1] = pack8n(acc[db][qb], 1);
        asm volatile("" : "+a"(ac.q[qb][2 * db]), "+a"(ac.q[qb][2 * db + 1]));
      }
  }
#pragma unroll
  for (int i = 0; i < 4; ++i) { ac.o[i][0] = f32x16{}; ac.o[i][1] = f32x16{}; asm volatile("" : "+a"(ac.o[i][0]), "+a"(ac.o[i][1])); }
  f32x16 SA[2], SB[2];
  if constexpr (isA) {
    const unsigned kb0 = pc.ldsb + pc.rb0, kb1 = pc.ldsb + pc.rb1, vb = pc.ldsb + SLOT + tbV;
    asm volatile("s_waitcnt lgkmcnt(0)" ::: "memory");
    barrier();
    k_read(ac, kb0, kb1);
    wait_lgkm0();
    GAP();
    qk0(ac, SA);
    GAP();
    k_read(ac, kb0 + 2u * SLOT, kb1 + 2u * SLOT);
    sm_first(ac, SA);
    STAMP(13);
    for (unsigned i = 0; i < 14; i += 2) {
      if (i == 6) { wait_lgkm0(); barrier(); stash_write<40>(st, sbase, make_iseq<24>{}); }
      if (i == 8) { wait_lgkm0(); barrier(); }
      const unsigned s0 = (i >= 10u ? i - 10u : i) * (2u * SLOT), s2 = (i >= 8u ? i - 8u : i + 2u) * (2u * SLOT);
      phaseA<true, 0, KV_NT, 63>(ac, SA, SB, vb + s0, sK, 0u, 0u, 0u);
      phaseB<true, true, 0, KV_NT, 63>(ac, SB, kb0 + s2, kb1 + s2, sK, 0u, 0u, 0u);
      phaseA<true, 0, KV_NT, 63>(ac, SB, SA, vb + s0 + 2u * SLOT, sK, 0u, 0u, 0u);
      phaseB<true, true, 0, KV_NT, 63>(ac, SA, kb0 + s2 + 2u * SLOT, kb1 + s2 + 2u * SLOT, sK, 0u, 0u, 0u);
    }
    STAMP(14);
    phaseA<true, 0, KV_NT, 63>(ac, SA, SB, vb + 8u * SLOT, sK, 0u, 0u, 0u);
    phaseB<true, false, 0, KV_NT, 63>(ac, SB, 0u, 0u, sK, 0u, 0u, 0u);
    phaseA<false, 0, KV_NT, 63>(ac, SB, SA, vb + 10u * SLOT, sK, 0u, 0u, 0u);
    phaseB<false, false, 0, KV_NT, 63>(ac, SA, 0u, 0u, sK, 0u, 0u, 0u);
    pc.slot = 2u * SLOT;
  } else {
    const unsigned kb0 = pc.reg + pc.rb0, kb1 = pc.reg + pc.rb1, vb = pc.reg + tbV;
    wait_vm<32>();
    k_read(ac, kb0 + 2u * SLOT, kb1 + 2u * SLOT);
    wait_lgkm0();
    GAP();
    dma_elem<KV_NT>(sK, ksoff(3u, rk), 4096u, pc.reg + 2u * SLOT);
    qk0(ac, SA);
    wait_vm<32>();
    GAP();
    k_read(ac, kb0 + 3u * SLOT, kb1 + 3u * SLOT);
    sm_first(ac, SA);
    STAMP(13);
    unsigned sl = 3u * SLOT;
    for (unsigned i = 0; i < 12; i += 2) {
      unsigned fill = pc.reg + sl; sl = next5(sl);
      phaseA<true, 1, KV_NT, 32>(ac, SA, SB, vb + sl, sV, ksoff(i + 2u, rk), 4096u, fill);
      fill = pc.reg + sl; sl = next5(sl);
      phaseB<true, true, 1, KV_NT, 32>(ac, SB, kb0 + sl, kb1 + sl, sK, ksoff(i + 4u, rk), 4096u, fill);
      fill = pc.reg + sl; sl = next5(sl);
      phaseA<true, 1, KV_NT, 32>(ac, SB, SA, vb + sl, sV, ksoff(i + 3u, rk), 4096u, fill);
      fill = pc.reg + sl; sl = next5(sl);
      phaseB<true, true, 1, KV_NT, 32>(ac, SA, kb0 + sl, kb1 + sl, sK, ksoff(i + 5u, rk), 4096u, fill);
    }
    STAMP(14);
    constexpr unsigned UO = 16u * 1024u;
    phaseA<true, 1, KV_NT, 32>(ac, SA, SB, vb + 3u * SLOT, sV, ksoff(14u, rk), 4096u, pc.reg + 2u * SLOT);
    phaseB<true, true, 1, KV_NT, 32>(ac, SB, kb0 + 4u * SLOT, kb1 + 4u * SLOT, sV, ksoff(15u, rk), 4096u, pc.reg + 3u * SLOT);
    phaseA<true, 1, W_NT, 32>(ac, SB, SA, vb, sO, chunk_of(0u, b) * (32u * 1024u), UO, pc.reg + 4u * SLOT);
    phaseB<true, true, 0, W_NT, 24>(ac, SA, kb0 + SLOT, kb1 + SLOT, sO, 0u, 0u, 0u);
    phaseA<true, 0, W_NT, 16>(ac, SA, SB, vb + 2u * SLOT, sO, 0u, 0u, 0u);
    phaseB<true, false, 1, W_NT, 0>(ac, SB, 0u, 0u, sO, chunk_of(1u, b) * (32u * 1024u), UO, pc.reg + 2u * SLOT);
    phaseA<false, 0, W_NT, 16>(ac, SB, SA, vb + 3u * SLOT, sO, 0u, 0u, 0u);
    phaseB<false, false, 1, W_NT, 0>(ac, SA, 0u, 0u, sO, chunk_of(2u, b) * (32u * 1024u), UO, pc.reg + 3u * SLOT);
    pc.slot = 4u * SLOT;
  }
  asm volatile("s_nop 7\n\ts_nop 7" : "+a"(ac.o[0][0]), "+a"(ac.o[0][1]), "+a"(ac.o[1][0]), "+a"(ac.o[1][1]), "+a"(ac.o[2][0]), "+a"(ac.o[2][1]), "+a"(ac.o[3][0]), "+a"(ac.o[3][1]) :: "memory");
  STAMP(3);
  if constexpr (isA) {
    barrier();
#pragma unroll
    for (unsigned e = 0; e < 3; ++e) dma_elem<W_NT>(sO, chunk_of(e, b) * (32u * 1024u), 16u * 1024u, pc.reg + (2u + e) * SLOT);
  }
  GAP();
#pragma unroll
  for (int qb = 0; qb < 2; ++qb) {
    const float ls = ac.t0[qb] + ac.t1[qb];
    auto pr = __builtin_amdgcn_permlane32_swap(__float_as_uint(ls), __float_as_uint(ls), false, false);
    const float inv = 1.0f / (__uint_as_float(pr[0]) + __uint_as_float(pr[1]));
#pragma unroll
    for (int db = 0; db < 4; ++db)
#pragma unroll
      for (int s = 0; s < 2; ++s)
        *(LAS bf16x8*)(size_t)(pc.reg + qb * 8192u + db * 256u + (s ? pc.rb1 : pc.rb0)) = pack8(ac.o[db][qb], s, inv);
  }
  wait_vm_lgkm0<20>();
  barrier();
  GAP();
  STAMP(4);
  {
    f32x16 acc[4][2];
    UF f0, f1;
    constexpr unsigned US = 16u * 1024u, CS = 32u * 1024u;
    pc.e = 0; pc.vW = pc.reg + pc.slot + pc.tbW;
    const unsigned xo = pc.ldsb + ximg_off(chunk_of(0u, b));
    unit_read(f0, pc.vW, xo + pc.rb0);
    pc.xa = xo + pc.rb1; pc.fill = pc.reg + pc.slot; pc.soff = chunk_of(3u, b) * CS;
    proj_elem<true, false, true, W_NT, 16, 16, false, false, false, US, CS>(acc, f0, f1, pc, sO, sO, 0u, 0u, 0u, 0u, false, 0u);
    for (unsigned e = 1; e < 13; ++e)
      proj_elem<false, false, true, W_NT, 16, 16, false, false, false, US, CS>(acc, f0, f1, pc, sO, sO, 0u, 0u, 0u, 0u, false, 0u);
    proj_elem<false, false, false, W_NT, 16, 12, false, false, false, US, CS>(acc, f0, f1, pc, sO, sO, 0u, 0u, 0u, 0u, false, 0u);
    proj_elem<false, false, false, W_NT, 8, 4, false, false, false, US, CS>(acc, f0, f1, pc, sO, sO, 0u, 0u, 0u, 0u, false, 0u);
    proj_elem<false, true, false, W_NT, 0, 0, false, false, false, US, CS>(acc, f0, f1, pc, sO, sO, 0u, 0u, 0u, 0u, false, 0u);
    asm volatile("s_nop 7\n\ts_nop 7" : "+v"(acc[0][0]), "+v"(acc[0][1]), "+v"(acc[1][0]), "+v"(acc[1][1]), "+v"(acc[2][0]), "+v"(acc[2][1]), "+v"(acc[3][0]), "+v"(acc[3][1]) :: "memory");
    GAP();
    STAMP(5);
    const srd_t srdP = __builtin_amdgcn_make_buffer_rsrc((void*)(part + (size_t)rr * 524288u), (short)0, 524288, 0x00020000);
    unsigned toks;
    asm volatile("buffer_load_dword %0, %1, %2, %3 offen sc1" : "=v"(toks) : "v"(l8), "s"(srdH), "s"(tok_off) : "memory");
    const unsigned l16 = lane * 16u;
    u32x4 pk[16];
#pragma unroll
    for (int nb = 0; nb < 4; ++nb)
#pragma unroll
      for (int qb = 0; qb < 2; ++qb)
#pragma unroll
        for (int s = 0; s < 2; ++s) {
          pk[(nb * 2 + qb) * 2 + s] = __builtin_bit_cast(u32x4, pack8n(acc[nb][qb], s));
          __builtin_amdgcn_raw_buffer_store_b128(pk[(nb * 2 + qb) * 2 + s], srdP, l16 + ((qb * 2 + (nb & 1)) * 2 + s) * 1024u, xg * 65536u + w * 16384u + (nb >> 1) * 8192u, 16);
        }
    asm volatile("s_nop 1" :: "v"(pk[0]), "v"(pk[1]), "v"(pk[2]), "v"(pk[3]), "v"(pk[4]), "v"(pk[5]), "v"(pk[6]), "v"(pk[7]),
                 "v"(pk[8]), "v"(pk[9]), "v"(pk[10]), "v"(pk[11]), "v"(pk[12]), "v"(pk[13]), "v"(pk[14]), "v"(pk[15]) : "memory");
    STAMP(6);
    asm volatile("s_waitcnt vmcnt(0)" : "+v"(toks) :: "memory");
    STAMP(7);
    const bool mine = (lane >> 1) == w;
    const unsigned fo = lane * 128u + xg * 4u;
    if (mine) asm volatile("buffer_store_dword %0, %1, %2, %3 offen sc1" :: "v"(toks), "v"(fo), "s"(srdH), "s"(flg_off) : "memory");
    const unsigned tau = __builtin_amdgcn_readlane(toks, xg);
#pragma unroll 1
    for (unsigned spin = 0; spin < (1u << 22); ++spin) {
      unsigned fl, tk;
      asm volatile("buffer_load_dword %0, %2, %3, %4 offen sc1\n\tbuffer_load_dword %1, %2, %3, %5 offen sc1\n\ts_waitcnt vmcnt(0)"
                   : "=&v"(fl), "=&v"(tk) : "v"(l8), "s"(srdH), "s"(flg_off + xg * 128u), "s"(tok_off) : "memory");
      if (mine && tk != toks) asm volatile("buffer_store_dword %0, %1, %2, %3 offen sc1" :: "v"(tk), "v"(fo), "s"(srdH), "s"(flg_off) : "memory");
      toks = tk;
      if (__builtin_amdgcn_ballot_w64(fl != tau) == 0ull) break;
      __builtin_amdgcn_s_sleep(isA ? SLEEP_A : SLEEP_B);
    }
    STAMP(15);
    unsigned tk2;
    asm volatile("buffer_load_dword %0, %1, %2, %3 offen sc1" : "=v"(tk2) : "v"(l8), "s"(srdH), "s"(tok_off) : "memory");
    u32x4 v[2][8];
#pragma unroll
    for (int x = 0; x < 8; ++x)
#pragma unroll
      for (int s = 0; s < 2; ++s) v[s][x] = __builtin_amdgcn_raw_buffer_load_b128(srdP, l16 + s * 1024u, x * 65536u + xg * 8192u + w * 2048u, 16);
    const unsigned q = 32u * (w >> 1) + r32, n0 = 64u * xg + 32u * (w & 1u) + 8u * hh;
    char* op = out + ((size_t)(b * 64u + q) * 1024u + (isA ? 0u : 512u) + n0) * 2u;
#pragma unroll
    for (int s = 0; s < 2; ++s) {
      u32x4 o;
#pragma unroll
      for (int j = 0; j < 4; ++j) {
        float lo = 0.f, hi = 0.f;
#pragma unroll
        for (int x = 0; x < 8; ++x) { lo += __uint_as_float(v[s][x][j] << 16); hi += __uint_as_float(v[s][x][j] & 0xffff0000u); }
        o[j] = cvtpk(lo, hi);
      }
      *(u32x4*)(op + s * 32) = o;
    }
    asm volatile("s_waitcnt vmcnt(0)" : "+v"(tk2) :: "memory");
    if (mine && tk2 != toks) asm volatile("buffer_store_dword %0, %1, %2, %3 offen sc1" :: "v"(tk2), "v"(fo), "s"(srdH), "s"(flg_off) : "memory");
  }
}

__global__ __launch_bounds__(256, 1)
void fused_attn(const char* __restrict__ Ax, const char* __restrict__ Ak, const char* __restrict__ Av, const char* __restrict__ Awq, const char* __restrict__ Awo,
                const char* __restrict__ Bx, const char* __restrict__ Bk, const char* __restrict__ Bv, const char* __restrict__ Bwq, const char* __restrict__ Bwo,
                char* __restrict__ part, char* __restrict__ out) {
  if (blockIdx.x >= 128) body<true>(Ax, Ak, Av, Awq, Awo, part, Bk, Bv, out);
  else                   body<false>(Bx, Bk, Bv, Bwq, Bwo, part, Bk, Bv, out);
}

extern "C" void kernel_launch(void* const* d_in, const int* in_sizes, int n_in, void* d_out, int out_size, void* d_ws, size_t ws_size, hipStream_t stream) {
  (void)in_sizes; (void)n_in; (void)out_size; (void)ws_size;
  (void)hipFuncSetAttribute((const void*)fused_attn, hipFuncAttributeMaxDynamicSharedMemorySize, (int)LDS_BYTES);
  fused_attn<<<256, 256, LDS_BYTES, stream>>>((const char*)d_in[0], (const char*)d_in[1], (const char*)d_in[2], (const char*)d_in[3], (const char*)d_in[4],
                                              (const char*)d_in[5], (const char*)d_in[6], (const char*)d_in[7], (const char*)d_in[8], (const char*)d_in[9], (char*)d_ws, (char*)d_out);
}
```

```cpp
#include <hip/hip_runtime.h>
#include <hip/hip_bf16.h>
#include <utility>

typedef __bf16 bf16x8 __attribute__((ext_vector_type(8)));
typedef __bf16 bf16x4 __attribute__((ext_vector_type(4)));
typedef __bf16 bf16x2 __attribute__((ext_vector_type(2)));
typedef float f32x16 __attribute__((ext_vector_type(16)));
typedef float f32x2 __attribute__((ext_vector_type(2)));
typedef unsigned u32x4 __attribute__((ext_vector_type(4)));
#define LAS __attribute__((address_space(3)))
#define DI __device__ __forceinline__
typedef __amdgpu_buffer_rsrc_t srd_t;
template <int... Is> using iseq = std::integer_sequence<int, Is...>;
template <int N> using make_iseq = std::make_integer_sequence<int, N>;

#define KV_NT 1
#define W_NT 0
#define PF_N 0
#define PF_POL ""
#define SLEEP_A 32
#define SLEEP_B 4
#define ORDER 0
#define PACE_A 0
#define WARM_CODE 1
#define WARM_TLB 1
#define X_NT 0

constexpr unsigned SLOT = 8192, REG = 5 * SLOT, LDS_BYTES = 4 * REG;
constexpr unsigned HOFF = 32u << 20;
constexpr float SC = 0.12751743082f;
constexpr float THR_RAW = 8.0f / SC;

#define NO_REDUCE_STAMPS
#define GAP() __builtin_amdgcn_sched_barrier(0)
#define STAMP(i) do {} while (0)
#define PINS(x) asm volatile("" : "+s"(x))
#define PINV(x) asm volatile("" : "+v"(x))
DI unsigned rfl(unsigned x) { return __builtin_amdgcn_readfirstlane(x); }
DI unsigned cvtpk(float lo, float hi) { f32x2 v = {lo, hi}; bf16x2 b = __builtin_convertvector(v, bf16x2); return __builtin_bit_cast(unsigned, b); }
DI bf16x8 pack8(const f32x16& x, int s, float sc) {
  u32x4 w = {cvtpk(x[8 * s] * sc, x[8 * s + 1] * sc), cvtpk(x[8 * s + 2] * sc, x[8 * s + 3] * sc), cvtpk(x[8 * s + 4] * sc, x[8 * s + 5] * sc), cvtpk(x[8 * s + 6] * sc, x[8 * s + 7] * sc)};
  return __builtin_bit_cast(bf16x8, w);
}
DI bf16x8 pack8n(const f32x16& x, int s) {
  u32x4 w = {cvtpk(x[8 * s], x[8 * s + 1]), cvtpk(x[8 * s + 2], x[8 * s + 3]), cvtpk(x[8 * s + 4], x[8 * s + 5]), cvtpk(x[8 * s + 6], x[8 * s + 7])};
  return __builtin_bit_cast(bf16x8, w);
}
template <bool Z> DI void mm_vvv(f32x16& c, const bf16x8& a, const bf16x8& b) {
  if constexpr (Z) asm volatile("v_mfma_f32_32x32x16_bf16 %0, %1, %2, 0" : "=&v"(c) : "v"(a), "v"(b));
  else             asm volatile("v_mfma_f32_32x32x16_bf16 %0, %1, %2, %0" : "+v"(c) : "v"(a), "v"(b));
}
#define AMF(x) x
#define ASM_(x) x
template <bool Z> DI void mm_vva(f32x16& c, const bf16x8& a, const bf16x8& b) {
  if constexpr (Z) asm volatile(AMF("v_mfma_f32_32x32x16_bf16 %0, %1, %2, 0") : "=&v"(c) : "v"(a), "a"(b));
  else             asm volatile(AMF("v_mfma_f32_32x32x16_bf16 %0, %1, %2, %0") : "+v"(c) : "v"(a), "a"(b));
}
DI void mm_avv(f32x16& c, const bf16x8& a, const bf16x8& b) {
  asm volatile(AMF("v_mfma_f32_32x32x16_bf16 %0, %1, %2, %0") : "+a"(c) : "v"(a), "v"(b));
}
DI void set_m0(unsigned v) { asm volatile("s_mov_b32 m0, %0" :: "s"(v) : "memory"); }
template <int NT, int P> DI void dma_p(unsigned voff, srd_t srd, unsigned soff) {
  if constexpr (NT) asm volatile("buffer_load_dwordx4 %0, %1, %2 offen offset:%3 nt lds" :: "v"(voff), "s"(srd), "s"(soff), "i"(1024 * P) : "memory");
  else              asm volatile("buffer_load_dwordx4 %0, %1, %2 offen offset:%3 lds" :: "v"(voff), "s"(srd), "s"(soff), "i"(1024 * P) : "memory");
}
struct Src { unsigned v[4]; srd_t srd; };
template <int NT> DI void dma_unit(const Src& s, unsigned soff, unsigned ldsaddr) {
  asm volatile("s_mov_b32 m0, %0\n\ts_nop 0" :: "s"(ldsaddr) : "memory");
  dma_p<NT, 0>(s.v[0], s.srd, soff); dma_p<NT, 1>(s.v[1], s.srd, soff); dma_p<NT, 2>(s.v[2], s.srd, soff); dma_p<NT, 3>(s.v[3], s.srd, soff);
}
template <int NT> DI void dma_elem(const Src& s, unsigned soff, unsigned ustep, unsigned ldsaddr) { dma_unit<NT>(s, soff, ldsaddr); dma_unit<NT>(s, soff + ustep, ldsaddr + 4096u); }
template <int N> DI void wait_vm() { asm volatile("s_waitcnt vmcnt(%0)" :: "i"(N) : "memory"); }
template <int N> DI void wait_vm_lgkm0() { asm volatile("" ::: "memory"); __builtin_amdgcn_s_waitcnt((N & 15) | ((N >> 4) << 14) | 0x70); asm volatile("" ::: "memory"); }
DI void wait_lgkm0() { asm volatile("" ::: "memory"); __builtin_amdgcn_s_waitcnt(0xC07F); asm volatile("" ::: "memory"); }
DI void barrier() { asm volatile("s_barrier" ::: "memory"); }
DI bf16x8 lds128(unsigned a) { return *(const LAS bf16x8*)(size_t)(a); }
DI bf16x8 lds_tr(unsigned a0, unsigned a1) {
  bf16x4 lo = __builtin_amdgcn_ds_read_tr16_b64_v4bf16((LAS bf16x4*)(size_t)(a0));
  bf16x4 hi = __builtin_amdgcn_ds_read_tr16_b64_v4bf16((LAS bf16x4*)(size_t)(a1));
  return __builtin_shufflevector(lo, hi, 0, 1, 2, 3, 4, 5, 6, 7);
}
template <bool XOR> DI void mk_src(Src& s, const char* base, unsigned bytes, unsigned stride, unsigned lane) {
  s.srd = __builtin_amdgcn_make_buffer_rsrc((void*)base, (short)0, (int)bytes, 0x00020000);
  const unsigned row = (lane >> 2) & 3u, cb = lane >> 4, sl = lane & 3u;
#pragma unroll
  for (int p = 0; p < 4; ++p) s.v[p] = row * stride + cb * 64u + ((XOR ? (sl ^ p) : sl) << 4) + (4u * stride - 1024u) * p;
}

struct UF { bf16x8 a[4]; bf16x8 b[2]; };
struct PC {
  unsigned reg, ldsb, slot, rot, e;
  unsigned tbW, rb0, rb1;
  unsigned vW, xa;
  unsigned fill, soff;
};
DI unsigned chunk_of(unsigned e, unsigned rot) { return ((e + (rot & 12u)) & 12u) | ((e + rot) & 3u); }
DI unsigned ksoff(unsigned m, unsigned rk) { return ((m + rk) & 15u) * 8192u; }
DI unsigned ksoff_oob(unsigned m, unsigned rk) { return m < 16u ? ((m + rk) & 15u) * 8192u : 0x100000u; }
DI unsigned ximg_off(unsigned c) { return (c >> 2) * REG + (c & 3u) * 256u; }
DI unsigned next3(unsigned s) { return s == 4u * SLOT ? 2u * SLOT : s + SLOT; }
DI unsigned next5(unsigned s) { return s == 4u * SLOT ? 0u : s + SLOT; }
DI void unit_read(UF& f, unsigned wa, unsigned xa) {
  f.b[0] = lds128(xa); f.b[1] = lds128(xa + 8192u);
#pragma unroll
  for (int db = 0; db < 4; ++db) f.a[db] = lds_tr(wa + db * 256u, wa + db * 256u + 1024u);
}
template <int S, bool Z, bool RD, bool DM, int NT, int VM, bool BAR, bool XT, unsigned USTEP, unsigned CSTEP>
DI void proj_unit(f32x16 (&acc)[4][2], const UF& cur, UF& nxt, PC& c, const Src& src, const Src& xs, unsigned xsoff, unsigned xdst, bool expl, unsigned nsoff) {
  if constexpr (RD) wait_vm_lgkm0<VM>(); else wait_lgkm0();
  if constexpr (BAR) barrier();
  const unsigned wa = c.vW + (S == 0 ? 4096u : 0u), xa = c.xa, fill = c.fill, soff = c.soff;
  if constexpr (XT) set_m0(xdst); else if constexpr (DM) set_m0(fill);
  GAP();
  mm_vvv<Z>(acc[0][0], cur.a[0], cur.b[0]);
  if constexpr (RD) { nxt.b[0] = lds128(xa); nxt.a[0] = lds_tr(wa, wa + 1024u); }
  if constexpr (XT) dma_p<X_NT, 0>(xs.v[0], xs.srd, xsoff);
  GAP();
  mm_vvv<Z>(acc[0][1], cur.a[0], cur.b[1]);
  if constexpr (RD) nxt.b[1] = lds128(xa + 8192u);
  if constexpr (XT) dma_p<X_NT, 1>(xs.v[1], xs.srd, xsoff); else if constexpr (DM) dma_p<NT, 0>(src.v[0], src.srd, soff);
  GAP();
  mm_vvv<Z>(acc[1][0], cur.a[1], cur.b[0]);
  if constexpr (RD) nxt.a[1] = lds_tr(wa + 256u, wa + 1280u);
  if constexpr (XT) dma_p<X_NT, 2>(xs.v[2], xs.srd, xsoff);
  GAP();
  mm_vvv<Z>(acc[1][1], cur.a[1], cur.b[1]);
  if constexpr (RD) nxt.a[2] = lds_tr(wa + 512u, wa + 1536u);
  if constexpr (XT) { dma_p<X_NT, 3>(xs.v[3], xs.srd, xsoff); set_m0(fill); } else if constexpr (DM) dma_p<NT, 1>(src.v[1], src.srd, soff);
  GAP();
  mm_vvv<Z>(acc[2][0], cur.a[2], cur.b[0]);
  if constexpr (RD) nxt.a[3] = lds_tr(wa + 768u, wa + 1792u);
  if constexpr (XT) dma_p<NT, 0>(src.v[0], src.srd, soff);
  GAP();
  mm_vvv<Z>(acc[2][1], cur.a[2], cur.b[1]);
  if constexpr (XT) dma_p<NT, 1>(src.v[1], src.srd, soff); else if constexpr (DM) dma_p<NT, 2>(src.v[2], src.srd, soff);
  if constexpr (S == 0) {
    PINS(c.slot);
    c.slot = next3(c.slot);
    c.vW = c.reg + c.slot + c.tbW;
    PINV(c.vW);
  } else {
    PINS(c.e);
    c.e += 1u;
  }
  GAP();
  mm_vvv<Z>(acc[3][0], cur.a[3], cur.b[0]);
  if constexpr (XT) dma_p<NT, 2>(src.v[2], src.srd, soff);
  if constexpr (S == 0) {
    PINS(c.e);
    const unsigned xo = c.ldsb + ximg_off(chunk_of(c.e + 1u, c.rot));
    c.xa = xo + c.rb0;
    PINV(c.xa);
  } else {
    c.xa = xa ^ 32u;
    PINV(c.xa);
  }
  GAP();
  mm_vvv<Z>(acc[3][1], cur.a[3], cur.b[1]);
  if constexpr (DM) dma_p<NT, 3>(src.v[3], src.srd, soff);
  if constexpr (S == 0) { c.fill = fill + 4096u; c.soff = soff + USTEP; }
  else { c.fill = c.reg + c.slot; c.soff = expl ? nsoff : chunk_of(c.e + 3u, c.rot) * CSTEP; }
  PINS(c.fill); PINS(c.soff);
  GAP();
}
template <bool Z, bool LAST, bool DM, int NT, int VM0, int VM1, bool BAR1, bool XT0, bool XT1, unsigned USTEP, unsigned CSTEP>
DI void proj_elem(f32x16 (&acc)[4][2], UF& f0, UF& f1, PC& c, const Src& src, const Src& xs, unsigned xso0, unsigned xd0, unsigned xso1, unsigned xd1, bool expl, unsigned nsoff) {
  proj_unit<0, Z, true, DM, NT, VM0, false, XT0, USTEP, CSTEP>(acc, f0, f1, c, src, xs, xso0, xd0, false, 0u);
  proj_unit<1, false, !LAST, DM, NT, VM1, BAR1, XT1, USTEP, CSTEP>(acc, f1, f0, c, src, xs, xso1, xd1, expl, nsoff);
}

struct AC {
  f32x16 o[4][2];
  bf16x8 q[2][8];
  bf16x8 kf[8], vf[4][2], pf[2][2];
  float m[2], mc[2], f[2];
  float t0[2], t1[2];
  bool pend;
};
template <int QB, int J> DI void sm_exp2(f32x16 (&S)[2], AC& c) {
  float a, b;
  asm volatile(ASM_("v_fma_f32 %0, %4, %2, %3\n\tv_fma_f32 %1, %5, %2, %3\n\tv_exp_f32_e32 %0, %0\n\tv_exp_f32_e32 %1, %1") : "=&v"(a), "=v"(b) : "s"(SC), "v"(c.mc[QB]), "v"(S[QB][J]), "v"(S[QB][J + 1]));
  S[QB][J] = a; S[QB][J + 1] = b;
}
template <int QB, int S_, int P> DI void sm_pack(f32x16 (&S)[2], AC& c) {
  u32x4 w = __builtin_bit_cast(u32x4, c.pf[QB][S_]);
  unsigned r;
  asm volatile(ASM_("v_cvt_pk_bf16_f32 %0, %3, %4\n\tv_add_f32_e32 %1, %1, %3\n\tv_add_f32_e32 %2, %2, %4") : "=&v"(r), "+v"(c.t0[QB]), "+v"(c.t1[QB]) : "v"(S[QB][8 * S_ + 2 * P]), "v"(S[QB][8 * S_ + 2 * P + 1]));
  w[P] = r;
  c.pf[QB][S_] = __builtin_bit_cast(bf16x8, w);
}
template <int DM, int G> DI void mem_pre(unsigned ldsaddr) {
  if constexpr (G == 1 && DM != 0) wait_lgkm0();
  if constexpr (DM == 1 && (G == 1 || G == 5)) set_m0(ldsaddr + (G == 5 ? 4096u : 0u));
  if constexpr (DM == 2 && G == 9) set_m0(ldsaddr);
}
template <int DM, int NT, int VM, bool RD, int G> DI void mem_post(const Src& src, unsigned soff, unsigned ustep) {
  if constexpr (DM == 1 && G >= 1 && G <= 8) dma_p<NT, (G - 1) & 3>(src.v[(G - 1) & 3], src.srd, soff + (G >= 5 ? ustep : 0u));
  if constexpr (DM == 2 && (G == 9 || G == 10)) dma_p<NT, G - 9>(src.v[G - 9], src.srd, soff);
  if constexpr (G == 8 && (RD || DM == 2) && VM < 63) wait_vm<VM>();
  if constexpr (G == 8 && DM == 2) barrier();
}
template <bool QK, int DM, int NT, int VM, int G>
DI void gapA(AC& c, f32x16 (&S)[2], f32x16 (&Sn)[2], unsigned va, const Src& src, unsigned soff, unsigned ustep, unsigned ldsaddr) {
  mem_pre<DM, G>(ldsaddr);
  if constexpr (QK) mm_vva<(G < 2)>(Sn[G & 1], c.kf[G >> 1], c.q[G & 1][G >> 1]); else if constexpr (DM != 0) asm volatile("s_nop 0");
  mem_post<DM, NT, VM, true, G>(src, soff, ustep);
  if constexpr (G >= 8) { constexpr int i = G - 8, s = i >> 2, db = i & 3; c.vf[db][s] = lds_tr(va + s * 4096u + db * 256u, va + s * 4096u + db * 256u + 2048u); }
  if constexpr (G < 8) {
    sm_exp2<1, 2 * G>(S, c);
    sm_pack<0, (G >> 2), (G & 3)>(S, c);
  } else {
    constexpr int i = G - 8;
    sm_pack<1, (i >> 2), (i & 3)>(S, c);
  }
  GAP();
}
template <bool QK, int DM, int NT, int VM, int... Gs>
DI void phaseA_(AC& c, f32x16 (&S)[2], f32x16 (&Sn)[2], unsigned va, const Src& src, unsigned soff, unsigned ustep, unsigned ldsaddr, iseq<Gs...>) {
  (gapA<QK, DM, NT, VM, Gs>(c, S, Sn, va, src, soff, ustep, ldsaddr), ...);
}
template <bool QK, int DM, int NT, int VM>
DI void phaseA(AC& c, f32x16 (&S)[2], f32x16 (&Sn)[2], unsigned va, const Src& src, unsigned soff, unsigned ustep, unsigned ldsaddr) {
  phaseA_<QK, DM, NT, VM>(c, S, Sn, va, src, soff, ustep, ldsaddr, make_iseq<16>{});
}
template <bool SM, bool KR, int DM, int NT, int VM, int G>
DI void gapB(AC& c, f32x16 (&Sn)[2], unsigned ka0, unsigned ka1, const Src& src, unsigned soff, unsigned ustep, unsigned ldsaddr, float (&mx)[2][2]) {
  if constexpr (G == 0) asm volatile("s_nop 1");
  mem_pre<DM, G>(ldsaddr);
  { constexpr int s = G >> 3, db = (G >> 1) & 3, qb = G & 1; mm_avv(c.o[db][qb], c.vf[db][s], c.pf[qb][s]); }
  mem_post<DM, NT, VM, KR, G>(src, soff, ustep);
  if constexpr (KR && G >= 8) { constexpr int s = G - 8; c.kf[s] = lds128(((s & 1) ? ka1 : ka0) + (s >> 1) * 256u); }
  if constexpr (SM) {
    if constexpr (G >= 2 && G <= 5) {
      constexpr int q = (G - 2) >> 1, r = 8 * ((G - 2) & 1);
      if constexpr (r == 0)
        asm volatile("v_max3_f32 %0, %2, %3, %4\n\tv_max3_f32 %1, %5, %6, %7\n\tv_max3_f32 %0, %0, %8, %9\n\tv_max3_f32 %1, %1, %10, %11"
                     : "=&v"(mx[q][0]), "=&v"(mx[q][1]) : "v"(Sn[q][0]), "v"(Sn[q][1]), "v"(Sn[q][2]), "v"(Sn[q][3]), "v"(Sn[q][4]), "v"(Sn[q][5]), "v"(Sn[q][6]), "v"(Sn[q][7]), "v"(Sn[q][8]), "v"(Sn[q][9]));
      else
        asm volatile("v_max3_f32 %0, %0, %2, %3\n\tv_max3_f32 %1, %1, %4, %5\n\tv_max3_f32 %0, %0, %6, %7\n\tv_max_f32_e32 %0, %0, %1\n\tv_mov_b32_e32 %1, %0"
                     : "+v"(mx[q][0]), "+v"(mx[q][1]) : "v"(Sn[q][10]), "v"(Sn[q][11]), "v"(Sn[q][12]), "v"(Sn[q][13]), "v"(Sn[q][14]), "v"(Sn[q][15]));
    }
    if constexpr (G == 6) {
      asm volatile("v_permlane32_swap_b32_e32 %0, %1\n\tv_permlane32_swap_b32_e32 %2, %3\n\tv_max3_f32 %0, %0, %1, %4\n\tv_max3_f32 %2, %2, %3, %5"
                   : "+v"(mx[0][0]), "+v"(mx[0][1]), "+v"(mx[1][0]), "+v"(mx[1][1]) : "v"(c.m[0]), "v"(c.m[1]));
    }
    if constexpr (G == 7) {
      const float d0 = c.m[0] - mx[0][0], d1 = c.m[1] - mx[1][0];
      c.pend = __builtin_amdgcn_ballot_w64(fminf(d0, d1) < -THR_RAW) != 0;
      c.f[0] = d0; c.f[1] = d1;
      c.m[0] = c.pend ? mx[0][0] : c.m[0]; c.m[1] = c.pend ? mx[1][0] : c.m[1];
      c.mc[0] = -c.m[0] * SC; c.mc[1] = -c.m[1] * SC;
      asm volatile("" : "+v"(c.mc[0]), "+v"(c.mc[1]));
    }
    if constexpr (G >= 8) sm_exp2<0, 2 * (G - 8)>(Sn, c);
  }
  GAP();
}
template <bool SM, bool KR, int DM, int NT, int VM, int... Gs>
DI void phaseB_(AC& c, f32x16 (&Sn)[2], unsigned ka0, unsigned ka1, const Src& src, unsigned soff, unsigned ustep, unsigned ldsaddr, iseq<Gs...>) {
  float mx[2][2];
  (gapB<SM, KR, DM, NT, VM, Gs>(c, Sn, ka0, ka1, src, soff, ustep, ldsaddr, mx), ...);
  if constexpr (SM) {
    if (__builtin_expect(c.pend, 0)) {
      asm volatile("s_nop 7\n\ts_nop 7" ::: "memory");
#pragma unroll
      for (int q = 0; q < 2; ++q) {
        const float f = __builtin_amdgcn_exp2f(c.f[q] * SC);
        c.t0[q] *= f; c.t1[q] *= f;
#pragma unroll
        for (int db = 0; db < 4; ++db) {
          asm volatile("" : "+a"(c.o[db][q]));
          c.o[db][q] *= f;
          asm volatile("" : "+a"(c.o[db][q]));
        }
      }
      asm volatile("s_nop 1" ::: "memory");
    }
  }
}
template <bool SM, bool KR, int DM, int NT, int VM>
DI void phaseB(AC& c, f32x16 (&Sn)[2], unsigned ka0, unsigned ka1, const Src& src, unsigned soff, unsigned ustep, unsigned ldsaddr) {
  phaseB_<SM, KR, DM, NT, VM>(c, Sn, ka0, ka1, src, soff, ustep, ldsaddr, make_iseq<16>{});
}
DI void k_read(AC& ac, unsigned ka0, unsigned ka1) {
#pragma unroll
  for (int s = 0; s < 8; ++s) ac.kf[s] = lds128(((s & 1) ? ka1 : ka0) + (s >> 1) * 256u);
}
DI void qk0(AC& ac, f32x16 (&SA)[2]) {
  mm_vva<true>(SA[0], ac.kf[0], ac.q[0][0]); mm_vva<true>(SA[1], ac.kf[0], ac.q[1][0]);
#pragma unroll
  for (int s = 1; s < 8; ++s) { mm_vva<false>(SA[0], ac.kf[s], ac.q[0][s]); mm_vva<false>(SA[1], ac.kf[s], ac.q[1][s]); }
}
template <int... Js> DI void exp_first(f32x16 (&SA)[2], AC& ac, iseq<Js...>) { (sm_exp2<0, 2 * Js>(SA, ac), ...); }
DI void sm_first(AC& ac, f32x16 (&SA)[2]) {
  asm volatile("s_nop 7\n\ts_nop 7" : "+v"(SA[0]), "+v"(SA[1]) :: "memory");
  GAP();
#pragma unroll
  for (int q = 0; q < 2; ++q) {
    float mx = SA[q][0];
#pragma unroll
    for (int i = 1; i < 16; ++i) mx = fmaxf(mx, SA[q][i]);
    auto pr = __builtin_amdgcn_permlane32_swap(__float_as_uint(mx), __float_as_uint(mx), false, false);
    ac.m[q] = fmaxf(__uint_as_float(pr[0]), __uint_as_float(pr[1]));
    ac.mc[q] = -ac.m[q] * SC; ac.t0[q] = 0.f; ac.t1[q] = 0.f;
  }
  exp_first(SA, ac, make_iseq<8>{});
  GAP();
}

struct Stash { u32x4 p[64]; };
template <int I> DI void stash_load1(Stash& st, const unsigned (&vk)[2], unsigned vv, srd_t sk, srd_t sv, unsigned rk, unsigned wo_) {
  constexpr int m = I >> 2, isV = (I >> 1) & 1, pc_ = I & 1;
  const unsigned so = ksoff(m, rk) + wo_;
  if constexpr (I < 56) asm volatile("buffer_load_dwordx4 %0, %1, %2, %3 offen offset:%4 nt" : "=a"(st.p[I]) : "v"(isV ? vv : vk[pc_]), "s"(isV ? sv : sk), "s"(so), "i"(1024 * pc_) : "memory");
  else                  asm volatile("buffer_load_dwordx4 %0, %1, %2, %3 offen offset:%4 nt" : "=v"(st.p[I]) : "v"(isV ? vv : vk[pc_]), "s"(isV ? sv : sk), "s"(so), "i"(1024 * pc_) : "memory");
}
template <int... Is> DI void stash_load(Stash& st, const unsigned (&vk)[2], unsigned vv, srd_t sk, srd_t sv, unsigned rk, unsigned wo_, iseq<Is...>) { (stash_load1<Is>(st, vk, vv, sk, sv, rk, wo_), ...); }
template <int I> DI void stash_write1(const Stash& st, const unsigned (&base)[3]) {
  constexpr int m = I >> 2, isV = (I >> 1) & 1, pc_ = I & 1, off = (2 * (m % 10) + isV) * 8192 + pc_ * 1024;
  if constexpr (I < 56) asm volatile("ds_write_b128 %0, %1 offset:%2" :: "v"(base[off >> 16]), "a"(st.p[I]), "i"(off & 65535) : "memory");
  else                  asm volatile("ds_write_b128 %0, %1 offset:%2" :: "v"(base[off >> 16]), "v"(st.p[I]), "i"(off & 65535) : "memory");
}
template <int B, int... Is> DI void stash_write(const Stash& st, const unsigned (&base)[3], iseq<Is...>) { (stash_write1<B + Is>(st, base), ...); }

template <bool isA>
DI void body(const char* __restrict__ X, const char* __restrict__ K, const char* __restrict__ V, const char* __restrict__ Wq, const char* __restrict__ Wo, char* __restrict__ part,
             const char* __restrict__ PK, const char* __restrict__ PV, char* __restrict__ out, const char* code) {
  extern __shared__ __attribute__((aligned(16))) char lds[];
  STAMP(0);
  const unsigned lane = threadIdx.x & 63u, w = rfl(threadIdx.x >> 6);
  const unsigned bid = blockIdx.x, xg = bid & 7u, rr = bid >> 3, b = rr & 15u, h = xg * 4u + w;
  const unsigned r32 = lane & 31u, hh = lane >> 5;
  const char* xp  = X + (size_t)b * 65536u + w * 16384u;
  const char* wqp = Wq + h * 256u;
  const char* wop = Wo + (size_t)xg * 524288u + w * 256u;
  const size_t kvo = isA ? (size_t)(b * 8u + xg) * 131072u : (size_t)(b * 32u + h) * 131072u;
  const char* kp = K + kvo;
  const char* vp = V + kvo;
  const unsigned rk = isA ? b : b + 4u * w;
  PC pc;
  pc.ldsb = rfl((unsigned)(size_t)lds); pc.reg = pc.ldsb + w * REG; pc.rot = b;
  const srd_t srdH = __builtin_amdgcn_make_buffer_rsrc((void*)(part + HOFF), (short)0, 4096 + 256 * 128, 0x00020000);
  const unsigned tok_off = rr * 128u, flg_off = 4096u + rr * 1024u;
  const unsigned l8 = (lane & 7u) * 4u;
  unsigned warm_dummy = 0;
  if (w == 0 && lane < 14u) asm volatile("global_load_dword %0, %1, %2" : "+v"(warm_dummy) : "v"((rr * 14u + lane) * 128u), "s"(code) : "memory");
  unsigned entry_flags;
  asm volatile("buffer_load_dword %0, %1, %2, %3 offen sc1" : "=v"(entry_flags) : "v"(l8), "s"(srdH), "s"(flg_off + xg * 128u) : "memory");
  Src sQ, sX, sK, sV, sO;
  mk_src<true>(sK, kp, 131072u, 256u, lane);
  mk_src<false>(sV, vp, 131072u, 256u, lane);
  mk_src<true>(sX, xp, 16384u, 1024u, lane);
  mk_src<false>(sQ, wqp, 511u * 8192u + 256u, 8192u, lane);
  const unsigned p0 = b >> 2;
  Stash st;
  unsigned pf_dummy = 0;
  auto early = [&]() {
    const unsigned vk[2] = {(w & 1u) ? sK.v[2] : sK.v[0], (w & 1u) ? sK.v[3] : sK.v[1]};
    stash_load(st, vk, sV.v[0], sK.srd, sV.srd, rk, w * 2048u, make_iseq<64>{});
  };
  constexpr bool FLAT = isA && ORDER == 1;
  if constexpr (isA && ORDER == 0) early();
  dma_unit<X_NT>(sX, p0 * 256u, pc.ldsb + p0 * REG + w * 4096u);
  dma_elem<W_NT>(sQ, chunk_of(0u, b) * (32u * 8192u), 16u * 8192u, pc.reg + 4u * SLOT);
  dma_elem<W_NT>(sQ, chunk_of(1u, b) * (32u * 8192u), 16u * 8192u, pc.reg + 2u * SLOT);
  if constexpr (FLAT) {
    dma_elem<W_NT>(sQ, chunk_of(2u, b) * (32u * 8192u), 16u * 8192u, pc.reg + 3u * SLOT);
#pragma unroll
    for (unsigned i = 1; i < 4; ++i) { const unsigned p = (p0 + i) & 3u; dma_unit<X_NT>(sX, p * 256u, pc.ldsb + p * REG + w * 4096u); }
    early();
  }
  mk_src<false>(sO, wop, 511u * 1024u + 256u, 1024u, lane);
  {
    const char* tp = lane == 0 ? kp : lane == 1 ? kp + 65536 : lane == 2 ? vp : lane == 3 ? vp + 65536 : lane == 4 ? wop + w * 131072u : lane == 5 ? wop + w * 131072u + 65536u
                   : lane == 6 ? part + (size_t)rr * 524288u + w * 131072u : lane == 7 ? part + (size_t)rr * 524288u + w * 131072u + 65536u : out + (size_t)b * 131072u + (w & 1u) * 65536u;
    if (lane < 9u) asm volatile("global_load_dword %0, %1, off" : "+v"(warm_dummy) : "v"(tp) : "memory");
  }
  pc.rb0 = 1024u * (r32 >> 2) + 64u * (r32 & 3u) + 16u * (hh ^ ((r32 >> 2) & 3u));
  pc.rb1 = pc.rb0 ^ 32u;
  const unsigned p4 = lane & 3u, rev = ((p4 & 1u) << 1) | (p4 >> 1);
  const unsigned tb = ((lane >> 2) & 3u) * 64u + ((lane >> 4) & 1u) * 32u + rev * 8u;
  pc.tbW = tb + hh * 2048u;
  const unsigned tbV = tb + hh * 1024u;

  const unsigned sb0 = pc.ldsb + w * 2048u + lane * 16u;
  const unsigned sbase[3] = {sb0, sb0 + 65536u, sb0 + 131072u};
  AC ac;
  {
    f32x16 acc[4][2];
    UF f0, f1;
    asm volatile("s_waitcnt vmcnt(%1)" : "+v"(entry_flags) : "i"(12 + WARM_TLB) : "memory");
    if (w == 0) {
      const unsigned long long mk = __builtin_amdgcn_ballot_w64(((entry_flags >> (lane & 7u)) & 1u) != 0u);
      const unsigned tok = 0x5a5a5a00u | (~(unsigned)mk & 0xffu);
      if (lane == 0) asm volatile("buffer_store_dword %0, off, %1, %2 sc1" :: "v"(tok), "s"(srdH), "s"(tok_off + xg * 4u) : "memory");
    }
    barrier();
    STAMP(1);
    pc.e = 0; pc.slot = 4u * SLOT; pc.vW = pc.reg + pc.slot + pc.tbW;
    const unsigned xo = pc.ldsb + ximg_off(chunk_of(0u, b));
    unit_read(f0, pc.vW, xo + pc.rb0);
    if constexpr (!FLAT) dma_elem<W_NT>(sQ, chunk_of(2u, b) * (32u * 8192u), 16u * 8192u, pc.reg + 3u * SLOT);
    pc.xa = xo + pc.rb1; pc.fill = pc.reg + pc.slot; pc.soff = chunk_of(3u, b) * (32u * 8192u);
    constexpr unsigned US = 16u * 8192u, CS = 32u * 8192u;
    const unsigned p1 = (p0 + 1u) & 3u, p2 = (p0 + 2u) & 3u, p3 = (p0 + 3u) & 3u, xw = pc.ldsb + w * 4096u;
    if constexpr (FLAT) {
      proj_elem<true,  false, true, W_NT, 16, 16, false, false, false, US, CS>(acc, f0, f1, pc, sQ, sX, 0u, 0u, 0u, 0u, false, 0u);
    } else {
    proj_elem<true,  false, true, W_NT, 16, 20, false, true,  true,  US, CS>(acc, f0, f1, pc, sQ, sX, p1 * 256u, xw + p1 * REG, p2 * 256u, xw + p2 * REG, false, 0u);
    STAMP(10);
    proj_elem<false, false, true, W_NT, 24, 28, false, true,  false, US, CS>(acc, f0, f1, pc, sQ, sX, p3 * 256u, xw + p3 * REG, 0u, 0u, false, 0u);
    STAMP(11);
    proj_elem<false, false, true, W_NT, 28, 24, false, false, false, US, CS>(acc, f0, f1, pc, sQ, sX, 0u, 0u, 0u, 0u, false, 0u);
    proj_elem<false, false, true, W_NT, 20, 16, true,  false, false, US, CS>(acc, f0, f1, pc, sQ, sX, 0u, 0u, 0u, 0u, false, 0u);
    }
    STAMP(8);
    const unsigned first_tail = isA ? chunk_of(0u, b) * (32u * 1024u) : ksoff(0u, rk);
    for (unsigned e = FLAT ? 1 : 4; e < 13; ++e) {
      if constexpr (isA && PACE_A > 0) __builtin_amdgcn_s_sleep(PACE_A);
      proj_elem<false, false, true, W_NT, 16, 16, false, false, false, US, CS>(acc, f0, f1, pc, sQ, sX, 0u, 0u, 0u, 0u, e == 12, first_tail);
    }
    STAMP(9);
    if constexpr (isA) {
      if constexpr (PACE_A > 0) __builtin_amdgcn_s_sleep(PACE_A);
      proj_elem<false, false, false, W_NT, 16, 12, false, false, false, US, CS>(acc, f0, f1, pc, sQ, sX, 0u, 0u, 0u, 0u, false, 0u);
      if constexpr (PACE_A > 0) __builtin_amdgcn_s_sleep(PACE_A);
      proj_elem<false, false, false, W_NT, 8, 4, false, false, false, US, CS>(acc, f0, f1, pc, sQ, sX, 0u, 0u, 0u, 0u, false, 0u);
      if constexpr (PACE_A > 0) __builtin_amdgcn_s_sleep(PACE_A);
      proj_elem<false, true, false, W_NT, 0, 0, false, false, false, US, CS>(acc, f0, f1, pc, sQ, sX, 0u, 0u, 0u, 0u, false, 0u);
    } else {
      proj_elem<false, false, true, KV_NT, 16, 16, false, false, false, 4096u, CS>(acc, f0, f1, pc, sK, sX, 0u, 0u, 0u, 0u, true, ksoff(1u, rk));
      proj_elem<false, false, true, KV_NT, 16, 16, false, false, false, 4096u, CS>(acc, f0, f1, pc, sK, sX, 0u, 0u, 0u, 0u, true, ksoff(0u, rk));
      proj_elem<false, true,  true, KV_NT, 16, 16, false, false, false, 4096u, CS>(acc, f0, f1, pc, sV, sX, 0u, 0u, 0u, 0u, true, 0u);
    }
    asm volatile("" :: "v"(warm_dummy));
    barrier();
    STAMP(2);
    if constexpr (isA) {
      asm volatile("" :: "v"(pf_dummy));
      stash_write<0>(st, sbase, make_iseq<40>{});
    } else {
      dma_elem<KV_NT>(sK, ksoff(2u, rk), 4096u, pc.reg);
      dma_elem<KV_NT>(sV, ksoff(1u, rk), 4096u, pc.reg + SLOT);
    }
    asm volatile("s_nop 7\n\ts_nop 7" : "+v"(acc[0][0]), "+v"(acc[0][1]), "+v"(acc[1][0]), "+v"(acc[1][1]), "+v"(acc[2][0]), "+v"(acc[2][1]), "+v"(acc[3][0]), "+v"(acc[3][1]) :: "memory");
    GAP();
#pragma unroll
    for (int db = 0; db < 4; ++db)
#pragma unroll
      for (int qb = 0; qb < 2; ++qb) {
        ac.q[qb][2 * db] = pack8n(acc[db][qb], 0); ac.q[qb][2 * db + 1] = pack8n(acc[db][qb], 1);
        asm volatile("" : "+a"(ac.q[qb][2 * db]), "+a"(ac.q[qb][2 * db + 1]));
      }
  }
#pragma unroll
  for (int i = 0; i < 4; ++i) { ac.o[i][0] = f32x16{}; ac.o[i][1] = f32x16{}; asm volatile("" : "+a"(ac.o[i][0]), "+a"(ac.o[i][1])); }
  f32x16 SA[2], SB[2];
  if constexpr (isA) {
    const unsigned kb0 = pc.ldsb + pc.rb0, kb1 = pc.ldsb + pc.rb1, vb = pc.ldsb + SLOT + tbV;
    asm volatile("s_waitcnt lgkmcnt(0)" ::: "memory");
    barrier();
    k_read(ac, kb0, kb1);
    wait_lgkm0();
    GAP();
    qk0(ac, SA);
    GAP();
    k_read(ac, kb0 + 2u * SLOT, kb1 + 2u * SLOT);
    sm_first(ac, SA);
    STAMP(13);
    for (unsigned i = 0; i < 14; i += 2) {
      if (i == 6) { wait_lgkm0(); barrier(); stash_write<40>(st, sbase, make_iseq<24>{}); }
      if (i == 8) { wait_lgkm0(); barrier(); }
      const unsigned s0 = (i >= 10u ? i - 10u : i) * (2u * SLOT), s2 = (i >= 8u ? i - 8u : i + 2u) * (2u * SLOT);
      phaseA<true, 0, KV_NT, 63>(ac, SA, SB, vb + s0, sK, 0u, 0u, 0u);
      phaseB<true, true, 0, KV_NT, 63>(ac, SB, kb0 + s2, kb1 + s2, sK, 0u, 0u, 0u);
      phaseA<true, 0, KV_NT, 63>(ac, SB, SA, vb + s0 + 2u * SLOT, sK, 0u, 0u, 0u);
      phaseB<true, true, 0, KV_NT, 63>(ac, SA, kb0 + s2 + 2u * SLOT, kb1 + s2 + 2u * SLOT, sK, 0u, 0u, 0u);
    }
    STAMP(14);
    phaseA<true, 0, KV_NT, 63>(ac, SA, SB, vb + 8u * SLOT, sK, 0u, 0u, 0u);
    phaseB<true, false, 0, KV_NT, 63>(ac, SB, 0u, 0u, sK, 0u, 0u, 0u);
    phaseA<false, 0, KV_NT, 63>(ac, SB, SA, vb + 10u * SLOT, sK, 0u, 0u, 0u);
    phaseB<false, false, 0, KV_NT, 63>(ac, SA, 0u, 0u, sK, 0u, 0u, 0u);
    pc.slot = 2u * SLOT;
  } else {
    const unsigned kb0 = pc.reg + pc.rb0, kb1 = pc.reg + pc.rb1, vb = pc.reg + tbV;
    wait_vm<32>();
    k_read(ac, kb0 + 2u * SLOT, kb1 + 2u * SLOT);
    wait_lgkm0();
    GAP();
    dma_elem<KV_NT>(sK, ksoff(3u, rk), 4096u, pc.reg + 2u * SLOT);
    qk0(ac, SA);
    wait_vm<32>();
    GAP();
    k_read(ac, kb0 + 3u * SLOT, kb1 + 3u * SLOT);
    sm_first(ac, SA);
    STAMP(13);
    unsigned sl = 3u * SLOT;
    for (unsigned i = 0; i < 12; i += 2) {
      unsigned fill = pc.reg + sl; sl = next5(sl);
      phaseA<true, 1, KV_NT, 32>(ac, SA, SB, vb + sl, sV, ksoff(i + 2u, rk), 4096u, fill);
      fill = pc.reg + sl; sl = next5(sl);
      phaseB<true, true, 1, KV_NT, 32>(ac, SB, kb0 + sl, kb1 + sl, sK, ksoff(i + 4u, rk), 4096u, fill);
      fill = pc.reg + sl; sl = next5(sl);
      phaseA<true, 1, KV_NT, 32>(ac, SB, SA, vb + sl, sV, ksoff(i + 3u, rk), 4096u, fill);
      fill = pc.reg + sl; sl = next5(sl);
      phaseB<true, true, 1, KV_NT, 32>(ac, SA, kb0 + sl, kb1 + sl, sK, ksoff(i + 5u, rk), 4096u, fill);
    }
    STAMP(14);
    constexpr unsigned UO = 16u * 1024u;
    phaseA<true, 1, KV_NT, 32>(ac, SA, SB, vb + 3u * SLOT, sV, ksoff(14u, rk), 4096u, pc.reg + 2u * SLOT);
    phaseB<true, true, 1, KV_NT, 32>(ac, SB, kb0 + 4u * SLOT, kb1 + 4u * SLOT, sV, ksoff(15u, rk), 4096u, pc.reg + 3u * SLOT);
    phaseA<true, 1, W_NT, 32>(ac, SB, SA, vb, sO, chunk_of(0u, b) * (32u * 1024u), UO, pc.reg + 4u * SLOT);
    phaseB<true, true, 0, W_NT, 24>(ac, SA, kb0 + SLOT, kb1 + SLOT, sO, 0u, 0u, 0u);
    phaseA<true, 0, W_NT, 16>(ac, SA, SB, vb + 2u * SLOT, sO, 0u, 0u, 0u);
    phaseB<true, false, 1, W_NT, 0>(ac, SB, 0u, 0u, sO, chunk_of(1u, b) * (32u * 1024u), UO, pc.reg + 2u * SLOT);
    phaseA<false, 0, W_NT, 16>(ac, SB, SA, vb + 3u * SLOT, sO, 0u, 0u, 0u);
    phaseB<false, false, 1, W_NT, 0>(ac, SA, 0u, 0u, sO, chunk_of(2u, b) * (32u * 1024u), UO, pc.reg + 3u * SLOT);
    pc.slot = 4u * SLOT;
  }
  asm volatile("s_nop 7\n\ts_nop 7" : "+a"(ac.o[0][0]), "+a"(ac.o[0][1]), "+a"(ac.o[1][0]), "+a"(ac.o[1][1]), "+a"(ac.o[2][0]), "+a"(ac.o[2][1]), "+a"(ac.o[3][0]), "+a"(ac.o[3][1]) :: "memory");
  STAMP(3);
  if constexpr (isA) {
    barrier();
#pragma unroll
    for (unsigned e = 0; e < 3; ++e) dma_elem<W_NT>(sO, chunk_of(e, b) * (32u * 1024u), 16u * 1024u, pc.reg + (2u + e) * SLOT);
  }
  GAP();
#pragma unroll
  for (int qb = 0; qb < 2; ++qb) {
    const float ls = ac.t0[qb] + ac.t1[qb];
    auto pr = __builtin_amdgcn_permlane32_swap(__float_as_uint(ls), __float_as_uint(ls), false, false);
    const float inv = 1.0f / (__uint_as_float(pr[0]) + __uint_as_float(pr[1]));
#pragma unroll
    for (int db = 0; db < 4; ++db)
#pragma unroll
      for (int s = 0; s < 2; ++s)
        *(LAS bf16x8*)(size_t)(pc.reg + qb * 8192u + db * 256u + (s ? pc.rb1 : pc.rb0)) = pack8(ac.o[db][qb], s, inv);
  }
  wait_vm_lgkm0<20>();
  barrier();
  GAP();
  STAMP(4);
  {
    f32x16 acc[4][2];
    UF f0, f1;
    constexpr unsigned US = 16u * 1024u, CS = 32u * 1024u;
    pc.e = 0; pc.vW = pc.reg + pc.slot + pc.tbW;
    const unsigned xo = pc.ldsb + ximg_off(chunk_of(0u, b));
    unit_read(f0, pc.vW, xo + pc.rb0);
    pc.xa = xo + pc.rb1; pc.fill = pc.reg + pc.slot; pc.soff = chunk_of(3u, b) * CS;
    proj_elem<true, false, true, W_NT, 16, 16, false, false, false, US, CS>(acc, f0, f1, pc, sO, sO, 0u, 0u, 0u, 0u, false, 0u);
    for (unsigned e = 1; e < 13; ++e)
      proj_elem<false, false, true, W_NT, 16, 16, false, false, false, US, CS>(acc, f0, f1, pc, sO, sO, 0u, 0u, 0u, 0u, false, 0u);
    proj_elem<false, false, false, W_NT, 16, 12, false, false, false, US, CS>(acc, f0, f1, pc, sO, sO, 0u, 0u, 0u, 0u, false, 0u);
    proj_elem<false, false, false, W_NT, 8, 4, false, false, false, US, CS>(acc, f0, f1, pc, sO, sO, 0u, 0u, 0u, 0u, false, 0u);
    proj_elem<false, true, false, W_NT, 0, 0, false, false, false, US, CS>(acc, f0, f1, pc, sO, sO, 0u, 0u, 0u, 0u, false, 0u);
    asm volatile("s_nop 7\n\ts_nop 7" : "+v"(acc[0][0]), "+v"(acc[0][1]), "+v"(acc[1][0]), "+v"(acc[1][1]), "+v"(acc[2][0]), "+v"(acc[2][1]), "+v"(acc[3][0]), "+v"(acc[3][1]) :: "memory");
    GAP();
    STAMP(5);
    const srd_t srdP = __builtin_amdgcn_make_buffer_rsrc((void*)(part + (size_t)rr * 524288u), (short)0, 524288, 0x00020000);
    unsigned toks;
    asm volatile("buffer_load_dword %0, %1, %2, %3 offen sc1" : "=v"(toks) : "v"(l8), "s"(srdH), "s"(tok_off) : "memory");
    const unsigned l16 = lane * 16u;
    u32x4 pk[16];
#pragma unroll
    for (int nb = 0; nb < 4; ++nb)
#pragma unroll
      for (int qb = 0; qb < 2; ++qb)
#pragma unroll
        for (int s = 0; s < 2; ++s) {
          pk[(nb * 2 + qb) * 2 + s] = __builtin_bit_cast(u32x4, pack8n(acc[nb][qb], s));
          __builtin_amdgcn_raw_buffer_store_b128(pk[(nb * 2 + qb) * 2 + s], srdP, l16 + ((qb * 2 + (nb & 1)) * 2 + s) * 1024u, xg * 65536u + w * 16384u + (nb >> 1) * 8192u, 16);
        }
    asm volatile("s_nop 1" :: "v"(pk[0]), "v"(pk[1]), "v"(pk[2]), "v"(pk[3]), "v"(pk[4]), "v"(pk[5]), "v"(pk[6]), "v"(pk[7]),
                 "v"(pk[8]), "v"(pk[9]), "v"(pk[10]), "v"(pk[11]), "v"(pk[12]), "v"(pk[13]), "v"(pk[14]), "v"(pk[15]) : "memory");
    STAMP(6);
    asm volatile("s_waitcnt vmcnt(0)" : "+v"(toks) :: "memory");
    STAMP(7);
    const bool mine = (lane >> 1) == w;
    const unsigned fo = lane * 128u + xg * 4u;
    if (mine) asm volatile("buffer_store_dword %0, %1, %2, %3 offen sc1" :: "v"(toks), "v"(fo), "s"(srdH), "s"(flg_off) : "memory");
    const unsigned tau = __builtin_amdgcn_readlane(toks, xg);
#pragma unroll 1
    for (unsigned spin = 0; spin < (1u << 22); ++spin) {
      unsigned fl, tk;
      asm volatile("buffer_load_dword %0, %2, %3, %4 offen sc1\n\tbuffer_load_dword %1, %2, %3, %5 offen sc1\n\ts_waitcnt vmcnt(0)"
                   : "=&v"(fl), "=&v"(tk) : "v"(l8), "s"(srdH), "s"(flg_off + xg * 128u), "s"(tok_off) : "memory");
      if (mine && tk != toks) asm volatile("buffer_store_dword %0, %1, %2, %3 offen sc1" :: "v"(tk), "v"(fo), "s"(srdH), "s"(flg_off) : "memory");
      toks = tk;
      if (__builtin_amdgcn_ballot_w64(fl != tau) == 0ull) break;
      __builtin_amdgcn_s_sleep(isA ? SLEEP_A : SLEEP_B);
    }
    STAMP(15);
    unsigned tk2;
    asm volatile("buffer_load_dword %0, %1, %2, %3 offen sc1" : "=v"(tk2) : "v"(l8), "s"(srdH), "s"(tok_off) : "memory");
    u32x4 v[2][8];
#pragma unroll
    for (int x = 0; x < 8; ++x)
#pragma unroll
      for (int s = 0; s < 2; ++s) v[s][x] = __builtin_amdgcn_raw_buffer_load_b128(srdP, l16 + s * 1024u, x * 65536u + xg * 8192u + w * 2048u, 16);
    const unsigned q = 32u * (w >> 1) + r32, n0 = 64u * xg + 32u * (w & 1u) + 8u * hh;
    char* op = out + ((size_t)(b * 64u + q) * 1024u + (isA ? 0u : 512u) + n0) * 2u;
#pragma unroll
    for (int s = 0; s < 2; ++s) {
      u32x4 o;
#pragma unroll
      for (int j = 0; j < 4; ++j) {
        float lo = 0.f, hi = 0.f;
#pragma unroll
        for (int x = 0; x < 8; ++x) { lo += __uint_as_float(v[s][x][j] << 16); hi += __uint_as_float(v[s][x][j] & 0xffff0000u); }
        o[j] = cvtpk(lo, hi);
      }
      *(u32x4*)(op + s * 32) = o;
    }
    asm volatile("s_waitcnt vmcnt(0)" : "+v"(tk2) :: "memory");
    if (mine && tk2 != toks) asm volatile("buffer_store_dword %0, %1, %2, %3 offen sc1" :: "v"(tk2), "v"(fo), "s"(srdH), "s"(flg_off) : "memory");
  }
}

__global__ __launch_bounds__(256, 1)
void fused_attn(const char* __restrict__ Ax, const char* __restrict__ Ak, const char* __restrict__ Av, const char* __restrict__ Awq, const char* __restrict__ Awo,
                const char* __restrict__ Bx, const char* __restrict__ Bk, const char* __restrict__ Bv, const char* __restrict__ Bwq, const char* __restrict__ Bwo,
                char* __restrict__ part, char* __restrict__ out) {
  const char* code = (const char*)(__builtin_amdgcn_s_getpc() & ~127ull);
  if (blockIdx.x >= 128) body<true>(Ax, Ak, Av, Awq, Awo, part, Bk, Bv, out, code);
  else                   body<false>(Bx, Bk, Bv, Bwq, Bwo, part, Bk, Bv, out, code);
}

extern "C" void kernel_launch(void* const* d_in, const int* in_sizes, int n_in, void* d_out, int out_size, void* d_ws, size_t ws_size, hipStream_t stream) {
  (void)in_sizes; (void)n_in; (void)out_size; (void)ws_size;
  (void)hipFuncSetAttribute((const void*)fused_attn, hipFuncAttributeMaxDynamicSharedMemorySize, (int)LDS_BYTES);
  fused_attn<<<256, 256, LDS_BYTES, stream>>>((const char*)d_in[0], (const char*)d_in[1], (const char*)d_in[2], (const char*)d_in[3], (const char*)d_in[4],
                                              (const char*)d_in[5], (const char*)d_in[6], (const char*)d_in[7], (const char*)d_in[8], (const char*)d_in[9], (char*)d_ws, (char*)d_out);
}
```

```cpp
#include <hip/hip_runtime.h>
#include <hip/hip_bf16.h>
#include <utility>

typedef __bf16 bf16x8 __attribute__((ext_vector_type(8)));
typedef __bf16 bf16x4 __attribute__((ext_vector_type(4)));
typedef __bf16 bf16x2 __attribute__((ext_vector_type(2)));
typedef float f32x4 __attribute__((ext_vector_type(4)));
typedef float f32x2 __attribute__((ext_vector_type(2)));
typedef unsigned u32x4 __attribute__((ext_vector_type(4)));
#define LAS __attribute__((address_space(3)))
#define DI __device__ __forceinline__
typedef __amdgpu_buffer_rsrc_t srd_t;
template <int... Is> using iseq = std::integer_sequence<int, Is...>;
template <int N> using make_iseq = std::make_integer_sequence<int, N>;

#define KV_NT 1
#define W_NT 0
#define X_NT 0
#define SLEEP_A 8
#define SLEEP_B 2
#define PACE_A 0
#define PACE_ATT 0

constexpr unsigned SLOT = 8192, LDS_BYTES = 20 * SLOT;
constexpr unsigned HOFF = 32u << 20;
constexpr float SC = 0.12751743082f;
constexpr float THR = 8.0f;

#define NO_REDUCE_STAMPS
#define GAP() __builtin_amdgcn_sched_barrier(0)
#define STAMP(i) do {} while (0)
#define PINS(x) asm volatile("" : "+s"(x))
#define PINV(x) asm volatile("" : "+v"(x))
DI unsigned rfl(unsigned x) { return __builtin_amdgcn_readfirstlane(x); }
DI unsigned cvtpk(float lo, float hi) { f32x2 v = {lo, hi}; bf16x2 b = __builtin_convertvector(v, bf16x2); return __builtin_bit_cast(unsigned, b); }
DI u32x4 pack2(const f32x4& a, const f32x4& b) { return u32x4{cvtpk(a[0], a[1]), cvtpk(a[2], a[3]), cvtpk(b[0], b[1]), cvtpk(b[2], b[3])}; }
DI u32x4 natural8(u32x4 w) {
  auto s0 = __builtin_amdgcn_permlane32_swap(w[0], w[2], false, false);
  auto s1 = __builtin_amdgcn_permlane32_swap(w[1], w[3], false, false);
  auto t0 = __builtin_amdgcn_permlane16_swap(s0[0], s0[1], false, false);
  auto t1 = __builtin_amdgcn_permlane16_swap(s1[0], s1[1], false, false);
  return u32x4{t0[0], t1[0], t0[1], t1[1]};
}
#define MF "v_mfma_f32_16x16x32_bf16 "
template <bool Z> DI void mm_vvv(f32x4& c, const bf16x8& a, const bf16x8& b) {
  if constexpr (Z) asm volatile(MF "%0, %1, %2, 0" : "=&v"(c) : "v"(a), "v"(b));
  else             asm volatile(MF "%0, %1, %2, %0" : "+v"(c) : "v"(a), "v"(b));
}
template <bool Z> DI void mm_vva(f32x4& c, const bf16x8& a, const bf16x8& b) {
  if constexpr (Z) asm volatile(MF "%0, %1, %2, 0" : "=&v"(c) : "v"(a), "a"(b));
  else             asm volatile(MF "%0, %1, %2, %0" : "+v"(c) : "v"(a), "a"(b));
}
DI void mm_vca(f32x4& d, const bf16x8& a, const bf16x8& b, const f32x4& c) { asm volatile(MF "%0, %1, %2, %3" : "=&v"(d) : "v"(a), "a"(b), "v"(c)); }
DI void mm_avv(f32x4& c, const bf16x8& a, const bf16x8& b) { asm volatile(MF "%0, %1, %2, %0" : "+a"(c) : "v"(a), "v"(b)); }
DI void set_m0(unsigned v) { asm volatile("s_mov_b32 m0, %0" :: "s"(v) : "memory"); }
template <int NT, int P> DI void dma_p(unsigned voff, srd_t srd, unsigned soff) {
  if constexpr (NT) asm volatile("buffer_load_dwordx4 %0, %1, %2 offen offset:%3 nt lds" :: "v"(voff), "s"(srd), "s"(soff), "i"(1024 * P) : "memory");
  else              asm volatile("buffer_load_dwordx4 %0, %1, %2 offen offset:%3 lds" :: "v"(voff), "s"(srd), "s"(soff), "i"(1024 * P) : "memory");
}
struct Src { unsigned v[4]; srd_t srd; };
template <int NT> DI void dma_unit(const Src& s, unsigned soff, unsigned ldsaddr) {
  asm volatile("s_mov_b32 m0, %0\n\ts_nop 0" :: "s"(ldsaddr) : "memory");
  dma_p<NT, 0>(s.v[0], s.srd, soff); dma_p<NT, 1>(s.v[1], s.srd, soff); dma_p<NT, 2>(s.v[2], s.srd, soff); dma_p<NT, 3>(s.v[3], s.srd, soff);
}
template <int NT> DI void dma_elem(const Src& s, unsigned soff, unsigned ustep, unsigned ldsaddr) { dma_unit<NT>(s, soff, ldsaddr); dma_unit<NT>(s, soff + ustep, ldsaddr + 4096u); }
template <int N> DI void wait_vm() { asm volatile("s_waitcnt vmcnt(%0)" :: "i"(N) : "memory"); }
template <int N, int L> DI void wait_vm_lgkm() { asm volatile("" ::: "memory"); __builtin_amdgcn_s_waitcnt((N & 15) | ((N >> 4) << 14) | 0x70 | (L << 8)); asm volatile("" ::: "memory"); }
DI void wait_lgkm0() { wait_vm_lgkm<63, 0>(); }
DI void barrier() { asm volatile("s_barrier" ::: "memory"); }
DI bf16x8 lds128(unsigned a) { return *(const LAS bf16x8*)(size_t)(a); }
DI bf16x4 lds_tr1(unsigned a) { return __builtin_amdgcn_ds_read_tr16_b64_v4bf16((LAS bf16x4*)(size_t)(a)); }
DI bf16x8 cat(bf16x4 lo, bf16x4 hi) { return __builtin_shufflevector(lo, hi, 0, 1, 2, 3, 4, 5, 6, 7); }
DI bf16x8 lds_tr(unsigned a0, unsigned a1) { return cat(lds_tr1(a0), lds_tr1(a1)); }
DI srd_t mk_srd(const void* base, unsigned bytes) { return __builtin_amdgcn_make_buffer_rsrc((void*)base, (short)0, (int)bytes, 0x00020000); }
template <bool VX> DI void mk_src_rows(Src& s, const char* base, unsigned bytes, unsigned stride, unsigned lane) {
  s.srd = mk_srd(base, bytes);
  const unsigned row = (lane >> 2) & 3u, cb = lane >> 4, sl = lane & 3u;
#pragma unroll
  for (int p = 0; p < 4; ++p) s.v[p] = row * stride + cb * 64u + ((sl ^ (VX ? 2 * (p & 1) : (p & 2))) << 4) + (4u * stride - 1024u) * p;
}
template <bool VX> DI void mk_src_w(Src& s, const char* base, unsigned bytes, unsigned stride, unsigned lane) {
  s.srd = mk_srd(base, bytes);
  const unsigned sub = lane >> 5, cb = (lane >> 4) & 1u, row = (lane >> 2) & 3u, sl = lane & 3u;
#pragma unroll
  for (int p = 0; p < 4; ++p) s.v[p] = (4u * sub + row) * stride + cb * 64u + ((sl ^ (VX ? 2u * sub : 2u * (p & 1))) << 4) + (8u * stride - 1024u) * p;
}
DI unsigned ksoff(unsigned m, unsigned rk) { return ((m + rk) & 15u) * 8192u; }

struct PC {
  unsigned r01, r2;
  unsigned w01[2], w2[2];
  unsigned xb, xmask, xa;
  unsigned z, soff;
};
template <unsigned TS_, unsigned QS_, unsigned ZI_, unsigned ZM_, unsigned ZO_, unsigned US_, unsigned CS_> struct PK { static constexpr unsigned TS = TS_, QS = QS_, ZI = ZI_, ZM = ZM_, ZO = ZO_, US = US_, CS = CS_; };
template <int POS> DI unsigned ring_lds(const PC& c) { return POS == 2 ? c.r2 : c.r01 + POS * SLOT; }
template <int POS> DI unsigned ring_rd(const PC& c, int d) { return (POS == 2 ? c.w2[d & 1] : c.w01[d & 1] + POS * SLOT) + (d >> 1) * 256u; }
template <class K, int POS, int S, bool Z, bool RD, bool DM, int NT, bool XT, int M>
DI void proj_gap(f32x4 (&acc)[8][4], const bf16x8 (&cur)[4], bf16x8 (&nxt)[4], bf16x4 (&lo)[4], bf16x8 (&bf)[4], PC& c, unsigned soff, const Src& src, const Src& xs, unsigned xsoff) {
  constexpr int qb = M >> 2, db = M & 3;
  mm_vvv<Z>(acc[4 * S + db][qb], cur[db], bf[qb]);
  if constexpr (RD && M < 8) {
    constexpr int d = M >> 1; const unsigned a = S == 0 ? ring_rd<POS>(c, d) + 4096u : ring_rd<(POS + 1) % 3>(c, d);
    if constexpr (M & 1) nxt[d] = cat(lo[d], lds_tr1(a + K::TS)); else lo[d] = lds_tr1(a);
  }
  if constexpr (RD && S == 1 && db == 3) bf[qb] = lds128(c.xa + qb * K::QS);
  if constexpr (XT) {
    if constexpr (M < 8 && !(M & 1)) dma_p<X_NT, M / 2>(xs.v[M / 2], xs.srd, xsoff);
    if constexpr (M == 7) set_m0(ring_lds<POS>(c) + S * 4096u);
    if constexpr (M >= 8 && !(M & 1)) dma_p<NT, (M - 8) / 2>(src.v[(M - 8) / 2], src.srd, soff);
  } else if constexpr (DM && M >= 8 && !(M & 1)) dma_p<NT, (M - 8) / 2>(src.v[(M - 8) / 2], src.srd, soff);
  if constexpr (S == 0 && M == 9) { PINS(c.z); c.z = (c.z + K::ZI) & K::ZM; PINS(c.z); }
  if constexpr (S == 0 && M == 11) { c.xa = (c.z & c.xmask) | c.xb; PINV(c.xa); }
  if constexpr (S == 1 && DM && M == 9) { PINS(c.soff); c.soff = (c.soff + K::CS) & (16u * K::CS - 1u); PINS(c.soff); }
  GAP();
}
template <class K, int POS, int S, bool Z, bool RD, bool DM, int NT, int VM, bool BAR, bool XT, int... Ms>
DI void proj_unit(f32x4 (&acc)[8][4], const bf16x8 (&cur)[4], bf16x8 (&nxt)[4], bf16x8 (&bf)[4], PC& c, const Src& src, const Src& xs, unsigned xsoff, unsigned xdst, iseq<Ms...>) {
  wait_vm_lgkm<(RD ? VM : 63), (S == 0 ? 2 : 0)>();
  if constexpr (BAR) barrier();
  const unsigned soff = c.soff + S * K::US;
  if constexpr (XT) set_m0(xdst); else if constexpr (DM) set_m0(ring_lds<POS>(c) + S * 4096u);
  GAP();
  bf16x4 lo[4];
  (proj_gap<K, POS, S, Z, RD, DM, NT, XT, Ms>(acc, cur, nxt, lo, bf, c, soff, src, xs, xsoff), ...);
}
template <class K, int POS, bool Z, bool LAST, bool DM, int NT, int VM0, int VM1, bool BAR1 = false, bool XT0 = false, bool XT1 = false>
DI void proj_elem(f32x4 (&acc)[8][4], bf16x8 (&a0)[4], bf16x8 (&a1)[4], bf16x8 (&bf)[4], PC& c, const Src& src,
                  const Src* xs = nullptr, unsigned xso0 = 0, unsigned xd0 = 0, unsigned xso1 = 0, unsigned xd1 = 0) {
  proj_unit<K, POS, 0, Z, true, DM, NT, VM0, false, XT0>(acc, a0, a1, bf, c, src, xs ? *xs : src, xso0, xd0, make_iseq<16>{});
  proj_unit<K, POS, 1, Z, !LAST, DM, NT, VM1, BAR1, XT1>(acc, a1, a0, bf, c, src, xs ? *xs : src, xso1, xd1, make_iseq<16>{});
}
template <class K, int POS> DI void proj_start(bf16x8 (&a0)[4], bf16x8 (&bf)[4], PC& c, unsigned tb, unsigned xb, unsigned c0) {
  c.w01[0] = c.r01 + tb; c.w01[1] = c.w01[0] ^ 32u; c.w2[0] = c.r2 + tb; c.w2[1] = c.w2[0] ^ 32u;
  PINV(c.w01[0]); PINV(c.w01[1]); PINV(c.w2[0]); PINV(c.w2[1]);
  c.xb = xb; c.xmask = K::ZO; PINV(c.xmask);
  c.z = (c0 * K::ZI) & K::ZM;
  c.xa = (c.z & K::ZO) | xb; PINV(c.xa);
#pragma unroll
  for (int d = 0; d < 4; ++d) { const unsigned a = ring_rd<POS>(c, d); a0[d] = lds_tr(a, a + K::TS); }
  GAP();
#pragma unroll
  for (int q = 0; q < 4; ++q) bf[q] = lds128(c.xa + q * K::QS);
  GAP();
  c.soff = ((c0 + 3u) & 15u) * K::CS;
}
#define ACC_ALL(C, a) C(a[0][0]), C(a[0][1]), C(a[0][2]), C(a[0][3]), C(a[1][0]), C(a[1][1]), C(a[1][2]), C(a[1][3]), C(a[2][0]), C(a[2][1]), C(a[2][2]), C(a[2][3]), C(a[3][0]), C(a[3][1]), C(a[3][2]), C(a[3][3])
DI void acc_fence_v(f32x4 (&acc)[8][4]) {
  asm volatile("s_nop 7\n\ts_nop 3" : ACC_ALL("+v", acc) :: "memory");
  f32x4 (&hi)[4][4] = *(f32x4 (*)[4][4])&acc[4];
  asm volatile("" : ACC_ALL("+v", hi) :: "memory");
  GAP();
}
DI void acc_fence_a(f32x4 (&acc)[8][4]) {
  asm volatile("s_nop 7\n\ts_nop 3" : ACC_ALL("+a", acc) :: "memory");
  f32x4 (&hi)[4][4] = *(f32x4 (*)[4][4])&acc[4];
  asm volatile("" : ACC_ALL("+a", hi) :: "memory");
  GAP();
}

struct Stash { u32x4 p[64]; unsigned base[3]; };
template <int I> DI void stash_load1(Stash& st, unsigned vk, const Src& sK, const Src& sV, unsigned rk, unsigned wo_) {
  constexpr int m = I >> 2, isV = (I >> 1) & 1, pc_ = I & 1;
  const unsigned so = ksoff(m, rk) + wo_;
  if constexpr (I < 56) asm volatile("buffer_load_dwordx4 %0, %1, %2, %3 offen offset:%4 nt" : "=a"(st.p[I]) : "v"(isV ? sV.v[pc_] : vk), "s"(isV ? sV.srd : sK.srd), "s"(so), "i"(1024 * pc_) : "memory");
  else                  asm volatile("buffer_load_dwordx4 %0, %1, %2, %3 offen offset:%4 nt" : "=v"(st.p[I]) : "v"(isV ? sV.v[pc_] : vk), "s"(isV ? sV.srd : sK.srd), "s"(so), "i"(1024 * pc_) : "memory");
}
template <int... Is> DI void stash_load(Stash& st, unsigned vk, const Src& sK, const Src& sV, unsigned rk, unsigned wo_, iseq<Is...>) { (stash_load1<Is>(st, vk, sK, sV, rk, wo_), ...); }
constexpr int kv_pair(int m) { constexpr int t[10] = {0, 1, 2, 3, 8, 9, 4, 5, 6, 7}; return t[m % 10]; }
constexpr unsigned long long kv_tab() { unsigned long long v = 0; for (int i = 0; i < 16; ++i) v |= (unsigned long long)kv_pair(i) << (4 * i); return v; }
template <int I> DI void stash_write1(const Stash& st) {
  constexpr int m = I >> 2, isV = (I >> 1) & 1, pc_ = I & 1, off = kv_pair(m) * 16384 + isV * 8192 + pc_ * 1024;
  if constexpr (I < 56) asm volatile("ds_write_b128 %0, %1 offset:%2" :: "v"(st.base[off >> 16]), "a"(st.p[I]), "i"(off & 65535) : "memory");
  else                  asm volatile("ds_write_b128 %0, %1 offset:%2" :: "v"(st.base[off >> 16]), "v"(st.p[I]), "i"(off & 65535) : "memory");
}

struct AC {
  f32x4 o[8][4];
  bf16x8 q[4][4];
  bf16x8 kf[8], vf[8], pf[4];
  f32x4 negm[4];
  float t0[4], t1[4];
  float fac[4];
  bool pend;
};
typedef f32x4 ST[2][4];
template <int E> DI float sm_val(ST& S) { return S[(E >> 2) & 1][E >> 3][E & 3]; }
template <int E> DI void sm_exp1(ST& S) { float a; asm volatile("v_exp_f32_e32 %0, %1" : "=v"(a) : "v"(sm_val<E>(S))); S[(E >> 2) & 1][E >> 3][E & 3] = a; }
template <int QB, int P> DI void sm_cvt(ST& S, AC& c) {
  u32x4 w = __builtin_bit_cast(u32x4, c.pf[QB]);
  unsigned r;
  asm volatile("v_cvt_pk_bf16_f32 %0, %1, %2" : "=v"(r) : "v"(sm_val<8 * QB + 2 * P>(S)), "v"(sm_val<8 * QB + 2 * P + 1>(S)));
  w[P] = r;
  c.pf[QB] = __builtin_bit_cast(bf16x8, w);
}
template <int E> DI void sm_add2(ST& S, AC& c) {
  asm volatile("v_add_f32_e32 %0, %0, %2\n\tv_add_f32_e32 %1, %1, %3" : "+v"(c.t0[E >> 3]), "+v"(c.t1[E >> 3]) : "v"(sm_val<E>(S)), "v"(sm_val<E + 1>(S)));
}
template <int E, bool FIRST> DI void sm_max4(ST& S, float (&mx)[2]) {
  if constexpr (FIRST) asm volatile("v_max_f32_e32 %0, %2, %3\n\tv_max_f32_e32 %1, %4, %5" : "=&v"(mx[0]), "=v"(mx[1]) : "v"(sm_val<E>(S)), "v"(sm_val<E + 1>(S)), "v"(sm_val<E + 2>(S)), "v"(sm_val<E + 3>(S)));
  else asm volatile("v_max3_f32 %0, %0, %2, %3\n\tv_max3_f32 %1, %1, %4, %5" : "+v"(mx[0]), "+v"(mx[1]) : "v"(sm_val<E>(S)), "v"(sm_val<E + 1>(S)), "v"(sm_val<E + 2>(S)), "v"(sm_val<E + 3>(S)));
}
template <bool DM, int VM, int M> DI void mem_pre(unsigned ldsaddr) {
  if constexpr (M == 6) wait_lgkm0();
  if constexpr (DM && (M == 6 || M == 14)) set_m0(ldsaddr + (M == 14 ? 4096u : 0u));
  if constexpr (M == 16 && VM < 63) wait_vm<VM>();
}
template <bool DM, int NT, int M> DI void mem_post(const Src& src, unsigned soff, unsigned ustep) {
  if constexpr (DM && M >= 6 && M <= 20 && !(M & 1)) dma_p<NT, ((M - 6) / 2) & 3>(src.v[((M - 6) / 2) & 3], src.srd, soff + (M >= 14 ? ustep : 0u));
}
struct PA { unsigned rd0, rd1, soff, fill; };
template <bool QK, bool DM, int NT, int VM, int WR, int M, class F>
DI void gapA(AC& c, ST& S, ST& Sn, PA& pa, F& addr, const Src& src, unsigned ustep, const Stash& st, bf16x4& lo) {
  mem_pre<DM, VM, M>(pa.fill);
  if constexpr (QK) {
    constexpr int f = M >> 2, s = f >> 1, kb = f & 1, qb = M & 3;
    if constexpr (s == 0) mm_vca(Sn[kb][qb], c.kf[f], c.q[qb][0], c.negm[qb]); else mm_vva<false>(Sn[kb][qb], c.kf[f], c.q[qb][s]);
  } else if constexpr (DM) asm volatile("s_nop 0");
  mem_post<DM, NT, M>(src, pa.soff, ustep);
  if constexpr (WR >= 0 && M >= 8 && (M & 3) == 1) stash_write1<WR + (M - 8) / 4>(st);
  if constexpr (M == 2) addr(pa);
  if constexpr (QK && M == 12) asm volatile("" :: "v"(c.negm[0]), "v"(c.negm[1]), "v"(c.negm[2]), "v"(c.negm[3]));
  if constexpr (M >= 16) {
    constexpr int db = (M - 16) >> 1; const unsigned a = ((db & 1) ? pa.rd1 : pa.rd0) + (db >> 1) * 256u;
    if constexpr (M & 1) c.vf[db] = cat(lo, lds_tr1(a + 4096u)); else lo = lds_tr1(a);
  }
  if constexpr (M < 16) {
    sm_exp1<16 + M>(S);
    if constexpr (M & 1) sm_cvt<(M >> 3), ((M >> 1) & 3)>(S, c);
  } else {
    constexpr int i = (M - 16) >> 1;
    if constexpr (M & 1) sm_cvt<2 + (i >> 2), (i & 3)>(S, c); else sm_add2<2 * i>(S, c);
  }
  GAP();
}
template <bool QK, bool DM, int NT, int VM, int WR, class F, int... Ms>
DI void phaseA_(AC& c, ST& S, ST& Sn, F& addr, const Src& src, unsigned ustep, const Stash& st, iseq<Ms...>) {
  PA pa; bf16x4 lo;
  (gapA<QK, DM, NT, VM, WR, Ms>(c, S, Sn, pa, addr, src, ustep, st, lo), ...);
}
template <bool QK, bool DM, int NT, int VM, int WR = -1, class F>
DI void phaseA(AC& c, ST& S, ST& Sn, F addr, const Src& src, unsigned ustep, const Stash& st) {
  phaseA_<QK, DM, NT, VM, WR>(c, S, Sn, addr, src, ustep, st, make_iseq<32>{});
}
template <bool MAX> DI float xlane(float v) {
  auto a = __builtin_amdgcn_permlane32_swap(__float_as_uint(v), __float_as_uint(v), false, false);
  v = MAX ? fmaxf(__uint_as_float(a[0]), __uint_as_float(a[1])) : __uint_as_float(a[0]) + __uint_as_float(a[1]);
  auto b = __builtin_amdgcn_permlane16_swap(__float_as_uint(v), __float_as_uint(v), false, false);
  return MAX ? fmaxf(__uint_as_float(b[0]), __uint_as_float(b[1])) : __uint_as_float(b[0]) + __uint_as_float(b[1]);
}
DI float row_max(const f32x4& x, const f32x4& y) {
  return xlane<true>(fmaxf(fmaxf(fmaxf(x[0], x[1]), fmaxf(x[2], x[3])), fmaxf(fmaxf(y[0], y[1]), fmaxf(y[2], y[3]))));
}
template <bool SM, bool KR, bool DM, int NT, int VM, int WR, int M, class F>
DI void gapB(AC& c, ST& S, ST& Sn, PA& pa, F& addr, const Src& src, unsigned ustep, const Stash& st, float (&mx)[2]) {
  mem_pre<DM, (KR ? VM : 63), M>(pa.fill);
  { constexpr int db = (M >> 1) & 7, qb = 2 * (M >> 4) + (M & 1); mm_avv(c.o[db][qb], c.vf[db], c.pf[qb]); }
  mem_post<DM, NT, M>(src, pa.soff, ustep);
  if constexpr (WR >= 0 && M >= 8 && (M & 3) == 1) stash_write1<WR + (M - 8) / 4>(st);
  if constexpr (M == 0) addr(pa);
  if constexpr (M < 4) sm_add2<16 + 2 * M>(S, c);
  if constexpr (M >= 12 && M < 16) sm_add2<24 + 2 * (M - 12)>(S, c);
  if constexpr (KR && M >= 16 && !(M & 1)) { constexpr int f = (M - 16) >> 1; c.kf[f] = lds128(pa.rd0 + (f & 1) * 4096u + (f >> 1) * 256u); }
  if constexpr (SM) {
    if constexpr (M >= 4 && M < 12) { constexpr int i = M - 4; sm_max4<8 * (i & 3) + 4 * (i >> 2), (i == 0)>(Sn, mx); }
    if constexpr (M == 13) c.pend = __builtin_amdgcn_ballot_w64(!(fmaxf(mx[0], mx[1]) <= THR)) != 0ull;
    if constexpr (M == 15) {
      if (__builtin_expect(c.pend, 0)) {
#pragma unroll
        for (int q = 0; q < 4; ++q) {
          const float dl = fmaxf(row_max(Sn[0][q], Sn[1][q]), 0.f);
          c.fac[q] = __builtin_amdgcn_exp2f(-dl);
          Sn[0][q] -= dl; Sn[1][q] -= dl; c.negm[q] -= dl;
        }
      }
    }
    if constexpr (M >= 16) sm_exp1<M - 16>(Sn);
  }
  GAP();
}
template <bool SM, bool KR, bool DM, int NT, int VM, int WR, class F, int... Ms>
DI void phaseB_(AC& c, ST& S, ST& Sn, F& addr, const Src& src, unsigned ustep, const Stash& st, iseq<Ms...>) {
  float mx[2]; PA pa;
  (gapB<SM, KR, DM, NT, VM, WR, Ms>(c, S, Sn, pa, addr, src, ustep, st, mx), ...);
  if constexpr (SM) {
    if (__builtin_expect(c.pend, 0)) {
      asm volatile("s_nop 7\n\ts_nop 3" ::: "memory");
#pragma unroll
      for (int q = 0; q < 4; ++q) {
        const float f = c.fac[q];
        c.t0[q] *= f; c.t1[q] *= f;
#pragma unroll
        for (int db = 0; db < 8; ++db) {
          asm volatile("" : "+a"(c.o[db][q]));
          c.o[db][q] *= f;
          asm volatile("" : "+a"(c.o[db][q]));
        }
      }
      asm volatile("s_nop 1" ::: "memory");
    }
  }
}
template <bool SM, bool KR, bool DM, int NT, int VM, int WR = -1, class F>
DI void phaseB(AC& c, ST& S, ST& Sn, F addr, const Src& src, unsigned ustep, const Stash& st) {
  phaseB_<SM, KR, DM, NT, VM, WR>(c, S, Sn, addr, src, ustep, st, make_iseq<32>{});
}
DI void k_read(AC& ac, unsigned ka) {
#pragma unroll
  for (int f = 0; f < 8; ++f) ac.kf[f] = lds128(ka + (f & 1) * 4096u + (f >> 1) * 256u);
}
DI void qk0(AC& ac, ST& SA) {
#pragma unroll
  for (int kb = 0; kb < 2; ++kb)
#pragma unroll
    for (int q = 0; q < 4; ++q) mm_vva<true>(SA[kb][q], ac.kf[kb], ac.q[q][0]);
#pragma unroll
  for (int f = 2; f < 8; ++f)
#pragma unroll
    for (int q = 0; q < 4; ++q) mm_vva<false>(SA[f & 1][q], ac.kf[f], ac.q[q][f >> 1]);
}
template <int... Js> DI void exp_first(ST& SA, iseq<Js...>) { (sm_exp1<Js>(SA), ...); }
DI void sm_first(AC& ac, ST& SA) {
  asm volatile("s_nop 7\n\ts_nop 3" : "+v"(SA[0][0]), "+v"(SA[0][1]), "+v"(SA[0][2]), "+v"(SA[0][3]), "+v"(SA[1][0]), "+v"(SA[1][1]), "+v"(SA[1][2]), "+v"(SA[1][3]) :: "memory");
  GAP();
#pragma unroll
  for (int q = 0; q < 4; ++q) {
    const float m = row_max(SA[0][q], SA[1][q]);
    SA[0][q] -= m; SA[1][q] -= m;
    ac.negm[q] = f32x4{-m, -m, -m, -m};
    ac.t0[q] = 0.f; ac.t1[q] = 0.f; ac.fac[q] = 1.f;
  }
  asm volatile("" : "+v"(ac.negm[0]), "+v"(ac.negm[1]), "+v"(ac.negm[2]), "+v"(ac.negm[3]));
  exp_first(SA, make_iseq<16>{});
  GAP();
}
template <bool WR, int J> DI void q_conv1(AC& ac, f32x4 (&acc)[8][4], const Stash& st) {
  constexpr int s = J >> 2, qb = J & 3;
  if constexpr (WR) { stash_write1<(5 * J) / 2>(st); stash_write1<(5 * J) / 2 + 1>(st); if constexpr (J & 1) stash_write1<(5 * J) / 2 + 2>(st); }
  ac.q[qb][s] = __builtin_bit_cast(bf16x8, natural8(pack2(acc[2 * s][qb] * SC, acc[2 * s + 1][qb] * SC)));
  asm volatile("" : "+a"(ac.q[qb][s]));
  GAP();
}
template <bool WR, int... Js> DI void q_conv(AC& ac, f32x4 (&acc)[8][4], const Stash& st, iseq<Js...>) { (q_conv1<WR, Js>(ac, acc, st), ...); }

template <bool isA>
DI void body(const char* __restrict__ X, const char* __restrict__ K, const char* __restrict__ V, const char* __restrict__ Wq, const char* __restrict__ Wo,
             char* __restrict__ part, char* __restrict__ out) {
  extern __shared__ __attribute__((aligned(16))) char lds[];
  STAMP(0);
  const unsigned lane = threadIdx.x & 63u, w = rfl(threadIdx.x >> 6);
  const unsigned bid = blockIdx.x, xg = bid & 7u, rr = bid >> 3, b = rr & 15u, h = xg * 4u + w;
  const unsigned r16 = lane & 15u, g = lane >> 4;
  const char* xp  = X + (size_t)b * 65536u + w * 16384u;
  const char* wqp = Wq + h * 256u;
  const char* wop = Wo + (size_t)xg * 524288u + w * 256u;
  const size_t kvo = isA ? (size_t)(b * 8u + xg) * 131072u : (size_t)(b * 32u + h) * 131072u;
  const unsigned rk = isA ? b : b + 4u * w;
  typedef PK<512u, 4096u, 0x1100u, 0xF300u, 0xC300u, 128u, 32u * 8192u> KQ;
  typedef PK<512u, 4096u, 0x1100u, 0xF300u, 0xC300u, 4096u, 8192u> KT;
  typedef PK<2048u, 1024u, 0x1000u, 0xF000u, 0xF000u, 128u, 32u * 1024u> KO;
  asm volatile("" :: "v"(lds));
  PC pc;
  pc.r01 = 65536u + w * 16384u; pc.r2 = 131072u + w * 8192u;
  const unsigned cq = (b & 3u) * 4u, co = b;
  const srd_t srdH = mk_srd(part + HOFF, 4096u + 256u * 128u);
  const unsigned tok_off = rr * 128u, flg_off = 4096u + rr * 1024u;
  const unsigned l8 = (lane & 7u) * 4u;
  unsigned entry_flags;
  asm volatile("buffer_load_dword %0, %1, %2, %3 offen sc1" : "=v"(entry_flags) : "v"(l8), "s"(srdH), "s"(flg_off + xg * 128u) : "memory");
  Src sQ, sX, sK, sV, sO;
  mk_src_rows<false>(sK, K + kvo, 131072u, 256u, lane);
  mk_src_rows<true>(sV, V + kvo, 131072u, 256u, lane);
  mk_src_rows<false>(sX, xp, 16384u, 1024u, lane);
  mk_src_w<false>(sQ, wqp, 511u * 8192u + 256u, 8192u, lane);
  const unsigned p0 = b & 3u;
  Stash st;
  if constexpr (isA) stash_load(st, (w & 1u) ? sK.v[2] : sK.v[0], sK, sV, rk, w * 2048u, make_iseq<64>{});
  dma_unit<X_NT>(sX, p0 * 256u, p0 * 16384u + w * 4096u);
  dma_elem<W_NT>(sQ, cq * KQ::CS, KQ::US, pc.r2);
  dma_elem<W_NT>(sQ, (cq + 1u) * KQ::CS, KQ::US, pc.r01);
  mk_src_w<true>(sO, wop, 511u * 1024u + 256u, 1024u, lane);
  const unsigned rb = 1024u * (r16 >> 2) + 64u * (r16 & 3u) + 16u * (g ^ ((r16 >> 2) & 2u));
  const unsigned tb = 64u * ((lane >> 2) & 3u) + 32u * (g & 1u) + 8u * (lane & 3u);
  const unsigned tbV = tb + 1024u * g;
  const unsigned sb0 = w * 2048u + lane * 16u;
  st.base[0] = sb0; st.base[1] = sb0 + 65536u; st.base[2] = sb0 + 131072u;
  AC ac;
  {
    f32x4 acc[8][4];
    bf16x8 a0[4], a1[4], bf[4];
    asm volatile("s_waitcnt vmcnt(12)" : "+v"(entry_flags) :: "memory");
    if (w == 0) {
      const unsigned long long mk = __builtin_amdgcn_ballot_w64(((entry_flags >> (lane & 7u)) & 1u) != 0u);
      const unsigned tok = 0x5a5a5a00u | (~(unsigned)mk & 0xffu);
      if (lane == 0) asm volatile("buffer_store_dword %0, off, %1, %2 sc1" :: "v"(tok), "s"(srdH), "s"(tok_off + xg * 4u) : "memory");
    }
    barrier();
    STAMP(1);
    proj_start<KQ, 2>(a0, bf, pc, tbV, rb, cq);
    dma_elem<W_NT>(sQ, (cq + 2u) * KQ::CS, KQ::US, pc.r01 + SLOT);
    const unsigned p1 = (p0 + 1u) & 3u, p2 = (p0 + 2u) & 3u, p3 = (p0 + 3u) & 3u, xw = w * 4096u;
    proj_elem<KQ, 2, true,  false, true, W_NT, 16, 20, false, true, true>(acc, a0, a1, bf, pc, sQ, &sX, p1 * 256u, xw + p1 * 16384u, p2 * 256u, xw + p2 * 16384u);
    STAMP(10);
    proj_elem<KQ, 0, false, false, true, W_NT, 24, 28, false, true, false>(acc, a0, a1, bf, pc, sQ, &sX, p3 * 256u, xw + p3 * 16384u);
    STAMP(11);
    proj_elem<KQ, 1, false, false, true, W_NT, 28, 24>(acc, a0, a1, bf, pc, sQ);
    proj_elem<KQ, 2, false, false, true, W_NT, 20, 16, true>(acc, a0, a1, bf, pc, sQ);
    STAMP(8);
#pragma unroll 1
    for (unsigned e = 0; e < 3; ++e) {
      if constexpr (isA && PACE_A > 0) __builtin_amdgcn_s_sleep(PACE_A);
      proj_elem<KQ, 0, false, false, true, W_NT, 16, 16>(acc, a0, a1, bf, pc, sQ);
      if constexpr (isA && PACE_A > 0) __builtin_amdgcn_s_sleep(PACE_A);
      proj_elem<KQ, 1, false, false, true, W_NT, 16, 16>(acc, a0, a1, bf, pc, sQ);
      if constexpr (isA && PACE_A > 0) __builtin_amdgcn_s_sleep(PACE_A);
      proj_elem<KQ, 2, false, false, true, W_NT, 16, 16>(acc, a0, a1, bf, pc, sQ);
    }
    STAMP(9);
    if constexpr (isA) {
      proj_elem<KQ, 0, false, false, false, W_NT, 16, 12>(acc, a0, a1, bf, pc, sQ);
      proj_elem<KQ, 1, false, false, false, W_NT, 8, 4>(acc, a0, a1, bf, pc, sQ);
      proj_elem<KQ, 2, false, true, false, W_NT, 0, 0>(acc, a0, a1, bf, pc, sQ);
    } else {
      pc.soff = ksoff(0u, rk);
      proj_elem<KT, 0, false, false, true, KV_NT, 16, 16>(acc, a0, a1, bf, pc, sK);
      proj_elem<KT, 1, false, false, true, KV_NT, 16, 16>(acc, a0, a1, bf, pc, sK);
      pc.soff = ksoff(0u, rk);
      proj_elem<KT, 2, false, true, true, KV_NT, 16, 16>(acc, a0, a1, bf, pc, sV);
    }
    barrier();
    STAMP(2);
    if constexpr (!isA) dma_elem<KV_NT>(sK, ksoff(2u, rk), 4096u, w * 16384u);
    acc_fence_v(acc);
    q_conv<isA>(ac, acc, st, make_iseq<16>{});
  }
#pragma unroll
  for (int i = 0; i < 8; ++i)
#pragma unroll
    for (int q = 0; q < 4; ++q) { ac.o[i][q] = f32x4{}; asm volatile("" : "+a"(ac.o[i][q])); }
  ST SA, SB;
  if constexpr (isA) {
    const unsigned kb = rb, vb = SLOT + tbV;
    asm volatile("s_waitcnt lgkmcnt(0)" ::: "memory");
    barrier();
    k_read(ac, kb + kv_pair(0) * 16384u);
    wait_lgkm0();
    GAP();
    qk0(ac, SA);
    GAP();
    k_read(ac, kb + kv_pair(1) * 16384u);
    sm_first(ac, SA);
    STAMP(13);
    unsigned i4 = 0;
    auto av = [&](PA& p) { if constexpr (PACE_ATT > 0) __builtin_amdgcn_s_sleep(PACE_ATT);
                           PINS(i4); p.rd0 = vb + (((unsigned)(kv_tab() >> i4) & 15u) << 14); p.rd1 = p.rd0 ^ 32u; PINV(p.rd0); PINV(p.rd1); };
    auto ak = [&](PA& p) { if constexpr (PACE_ATT > 0) __builtin_amdgcn_s_sleep(PACE_ATT);
                           PINS(i4); p.rd0 = kb + (((unsigned)((kv_tab() >> 8) >> i4) & 15u) << 14); PINV(p.rd0); i4 += 4u; };
    auto an = [&](PA&) { i4 += 4u; };
    while (i4 < 24u) {
      phaseA<true, false, KV_NT, 63>(ac, SA, SB, av, sK, 0u, st);
      phaseB<true, true, false, KV_NT, 63>(ac, SA, SB, ak, sK, 0u, st);
      phaseA<true, false, KV_NT, 63>(ac, SB, SA, av, sK, 0u, st);
      phaseB<true, true, false, KV_NT, 63>(ac, SB, SA, ak, sK, 0u, st);
    }
    wait_lgkm0(); barrier();
    phaseA<true, false, KV_NT, 63, 40>(ac, SA, SB, av, sK, 0u, st);
    phaseB<true, true, false, KV_NT, 63, 46>(ac, SA, SB, ak, sK, 0u, st);
    phaseA<true, false, KV_NT, 63, 52>(ac, SB, SA, av, sK, 0u, st);
    phaseB<true, true, false, KV_NT, 63, 58>(ac, SB, SA, ak, sK, 0u, st);
    while (i4 < 56u) {
      if (i4 <= 40u) {
        wait_lgkm0(); barrier();
        if (i4 == 40u) {
          dma_elem<W_NT>(sO, co * KO::CS, KO::US, pc.r01);
          dma_elem<W_NT>(sO, ((co + 1u) & 15u) * KO::CS, KO::US, pc.r01 + SLOT);
        }
      }
      phaseA<true, false, KV_NT, 63>(ac, SA, SB, av, sK, 0u, st);
      phaseB<true, true, false, KV_NT, 63>(ac, SA, SB, ak, sK, 0u, st);
      phaseA<true, false, KV_NT, 63>(ac, SB, SA, av, sK, 0u, st);
      phaseB<true, true, false, KV_NT, 63>(ac, SB, SA, ak, sK, 0u, st);
    }
    STAMP(14);
    phaseA<true, false, KV_NT, 63>(ac, SA, SB, av, sK, 0u, st);
    phaseB<true, false, false, KV_NT, 63>(ac, SA, SB, an, sK, 0u, st);
    phaseA<false, false, KV_NT, 63>(ac, SB, SA, av, sK, 0u, st);
    phaseB<false, false, false, KV_NT, 63>(ac, SB, SA, an, sK, 0u, st);
  } else {
    const unsigned P0 = pc.r01, P1 = pc.r01 + SLOT, P2 = pc.r2, P3 = w * 16384u;
    unsigned v0 = tbV + P0, v2 = tbV + P2, x0 = v0 ^ 32u, x2 = v2 ^ 32u, k1 = rb + P1, k3 = rb + P3;
    PINV(v0); PINV(v2); PINV(x0); PINV(x2); PINV(k1); PINV(k3);
    wait_vm<24>();
    k_read(ac, k1 - SLOT);
    wait_lgkm0();
    GAP();
    dma_elem<KV_NT>(sV, ksoff(1u, rk), 4096u, P0);
    qk0(ac, SA);
    wait_vm<24>();
    GAP();
    k_read(ac, k1);
    sm_first(ac, SA);
    STAMP(13);
    unsigned so = ksoff(2u, rk), i = 0;
#define AV(RD, FILL) [&](PA& p) { PINS(so); p.soff = (so + 8192u) & 0x1ffffu; PINS(p.soff); p.fill = (FILL); p.rd0 = v##RD; p.rd1 = x##RD; }
#define AK(RD, FILL) [&](PA& p) { p.soff = so; so = (so + 8192u) & 0x1ffffu; PINS(so); p.fill = (FILL); p.rd0 = k##RD; }
#define TV(RD, FILL, SO) [&](PA& p) { p.soff = (SO); p.fill = (FILL); p.rd0 = v##RD; p.rd1 = x##RD; }
#define TK(RD, FILL, SO) [&](PA& p) { p.soff = (SO); p.fill = (FILL); p.rd0 = k##RD; }
    for (; i < 12; i += 2) {
      phaseA<true, true, KV_NT, 21>(ac, SA, SB, AV(2, P1), sK, 4096u, st);
      phaseB<true, true, true, KV_NT, 21>(ac, SA, SB, AK(3, P2), sV, 4096u, st);
      phaseA<true, true, KV_NT, 21>(ac, SB, SA, AV(0, P3), sK, 4096u, st);
      phaseB<true, true, true, KV_NT, 21>(ac, SB, SA, AK(1, P0), sV, 4096u, st);
    }
    STAMP(14);
    phaseA<true, true, KV_NT, 21>(ac, SA, SB, TV(2, P1, ksoff(15u, rk)), sK, 4096u, st);
    phaseB<true, true, true, KV_NT, 21>(ac, SA, SB, TK(3, P2, ksoff(14u, rk)), sV, 4096u, st);
    phaseA<true, true, KV_NT, 21>(ac, SB, SA, TV(0, P3, ksoff(15u, rk)), sV, 4096u, st);
    phaseB<true, true, true, W_NT, 21>(ac, SB, SA, TK(1, P0, co * KO::CS), sO, KO::US, st);
    phaseA<true, true, W_NT, 21>(ac, SA, SB, TV(2, P1, ((co + 1u) & 15u) * KO::CS), sO, KO::US, st);
    phaseB<true, false, true, W_NT, 63>(ac, SA, SB, TK(3, P2, ((co + 2u) & 15u) * KO::CS), sO, KO::US, st);
    unsigned v3 = tbV + P3, x3 = v3 ^ 32u; PINV(v3); PINV(x3);
    phaseA<false, false, W_NT, 24>(ac, SB, SA, TV(3, P3, 0u), sO, KO::US, st);
    phaseB<false, false, false, W_NT, 63>(ac, SB, SA, TK(3, P3, 0u), sO, KO::US, st);
#undef AV
#undef AK
#undef TV
#undef TK
  }
  acc_fence_a(ac.o);
  STAMP(3);
  if constexpr (isA) {
    barrier();
    dma_elem<W_NT>(sO, ((co + 2u) & 15u) * KO::CS, KO::US, pc.r2);
  }
  GAP();
  const unsigned l16 = lane * 16u;
#pragma unroll
  for (int qb = 0; qb < 4; ++qb) {
    const float inv = 1.0f / xlane<false>(ac.t0[qb] + ac.t1[qb]);
#pragma unroll
    for (int s = 0; s < 4; ++s)
      *(LAS u32x4*)(size_t)(w * 16384u + s * 4096u + qb * 1024u + l16) = pack2(ac.o[2 * s][qb] * inv, ac.o[2 * s + 1][qb] * inv);
  }
  wait_vm_lgkm<20, 0>();
  barrier();
  GAP();
  STAMP(4);
  {
    f32x4 acc[8][4];
    bf16x8 a0[4], a1[4], bf[4];
    proj_start<KO, 0>(a0, bf, pc, tb + 512u * g, l16, co);
    proj_elem<KO, 0, true, false, true, W_NT, 16, 16>(acc, a0, a1, bf, pc, sO);
#pragma unroll 1
    for (unsigned e = 0; e < 4; ++e) {
      proj_elem<KO, 1, false, false, true, W_NT, 16, 16>(acc, a0, a1, bf, pc, sO);
      proj_elem<KO, 2, false, false, true, W_NT, 16, 16>(acc, a0, a1, bf, pc, sO);
      proj_elem<KO, 0, false, false, true, W_NT, 16, 16>(acc, a0, a1, bf, pc, sO);
    }
    proj_elem<KO, 1, false, false, false, W_NT, 16, 12>(acc, a0, a1, bf, pc, sO);
    proj_elem<KO, 2, false, false, false, W_NT, 8, 4>(acc, a0, a1, bf, pc, sO);
    proj_elem<KO, 0, false, true, false, W_NT, 0, 0>(acc, a0, a1, bf, pc, sO);
    acc_fence_v(acc);
    STAMP(5);
    const srd_t srdP = mk_srd(part + (size_t)rr * 524288u, 524288u);
    unsigned toks;
    asm volatile("buffer_load_dword %0, %1, %2, %3 offen sc1" : "=v"(toks) : "v"(l8), "s"(srdH), "s"(tok_off) : "memory");
    u32x4 pk[16];
#pragma unroll
    for (int np = 0; np < 4; ++np)
#pragma unroll
      for (int qb = 0; qb < 4; ++qb) {
        pk[np * 4 + qb] = pack2(acc[2 * np][qb], acc[2 * np + 1][qb]);
        __builtin_amdgcn_raw_buffer_store_b128(pk[np * 4 + qb], srdP, l16 + (qb * 2 + (np & 1)) * 1024u, xg * 65536u + w * 16384u + (np >> 1) * 8192u, 16);
      }
    asm volatile("s_nop 1" :: "v"(pk[0]), "v"(pk[1]), "v"(pk[2]), "v"(pk[3]), "v"(pk[4]), "v"(pk[5]), "v"(pk[6]), "v"(pk[7]),
                 "v"(pk[8]), "v"(pk[9]), "v"(pk[10]), "v"(pk[11]), "v"(pk[12]), "v"(pk[13]), "v"(pk[14]), "v"(pk[15]) : "memory");
    STAMP(6);
    asm volatile("s_waitcnt vmcnt(0)" : "+v"(toks) :: "memory");
    STAMP(7);
    const bool mine = (lane >> 1) == w;
    const unsigned fo = lane * 128u + xg * 4u;
    if (mine) asm volatile("buffer_store_dword %0, %1, %2, %3 offen sc1" :: "v"(toks), "v"(fo), "s"(srdH), "s"(flg_off) : "memory");
    const unsigned tau = __builtin_amdgcn_readlane(toks, xg);
#pragma unroll 1
    for (unsigned spin = 0; spin < (1u << 22); ++spin) {
      unsigned fl, tk;
      asm volatile("buffer_load_dword %0, %2, %3, %4 offen sc1\n\tbuffer_load_dword %1, %2, %3, %5 offen sc1\n\ts_waitcnt vmcnt(0)"
                   : "=&v"(fl), "=&v"(tk) : "v"(l8), "s"(srdH), "s"(flg_off + xg * 128u), "s"(tok_off) : "memory");
      if (mine && tk != toks) asm volatile("buffer_store_dword %0, %1, %2, %3 offen sc1" :: "v"(tk), "v"(fo), "s"(srdH), "s"(flg_off) : "memory");
      toks = tk;
      if (__builtin_amdgcn_ballot_w64(fl != tau) == 0ull) break;
      __builtin_amdgcn_s_sleep(isA ? SLEEP_A : SLEEP_B);
    }
    STAMP(15);
    unsigned tk2;
    asm volatile("buffer_load_dword %0, %1, %2, %3 offen sc1" : "=v"(tk2) : "v"(l8), "s"(srdH), "s"(tok_off) : "memory");
    u32x4 v[2][8];
#pragma unroll
    for (int x = 0; x < 8; ++x)
#pragma unroll
      for (int s = 0; s < 2; ++s) v[s][x] = __builtin_amdgcn_raw_buffer_load_b128(srdP, l16 + s * 1024u, x * 65536u + xg * 8192u + w * 2048u, 16);
    char* op = out + ((size_t)(b * 64u + 16u * w + r16) * 1024u + (isA ? 0u : 512u) + 64u * xg + 8u * g) * 2u;
#pragma unroll
    for (int s = 0; s < 2; ++s) {
      u32x4 o;
#pragma unroll
      for (int j = 0; j < 4; ++j) {
        float lo = 0.f, hi = 0.f;
#pragma unroll
        for (int x = 0; x < 8; ++x) { lo += __uint_as_float(v[s][x][j] << 16); hi += __uint_as_float(v[s][x][j] & 0xffff0000u); }
        o[j] = cvtpk(lo, hi);
      }
      *(u32x4*)(op + s * 64) = natural8(o);
    }
    asm volatile("s_waitcnt vmcnt(0)" : "+v"(tk2) :: "memory");
    if (mine && tk2 != toks) asm volatile("buffer_store_dword %0, %1, %2, %3 offen sc1" :: "v"(tk2), "v"(fo), "s"(srdH), "s"(flg_off) : "memory");
  }
}

__global__ __launch_bounds__(256, 1)
void fused_attn(const char* __restrict__ Ax, const char* __restrict__ Ak, const char* __restrict__ Av, const char* __restrict__ Awq, const char* __restrict__ Awo,
                const char* __restrict__ Bx, const char* __restrict__ Bk, const char* __restrict__ Bv, const char* __restrict__ Bwq, const char* __restrict__ Bwo,
                char* __restrict__ part, char* __restrict__ out) {
  if (blockIdx.x >= 128) body<true>(Ax, Ak, Av, Awq, Awo, part, out);
  else                   body<false>(Bx, Bk, Bv, Bwq, Bwo, part, out);
}

extern "C" void kernel_launch(void* const* d_in, const int* in_sizes, int n_in, void* d_out, int out_size, void* d_ws, size_t ws_size, hipStream_t stream) {
  (void)in_sizes; (void)n_in; (void)out_size; (void)ws_size;
  (void)hipFuncSetAttribute((const void*)fused_attn, hipFuncAttributeMaxDynamicSharedMemorySize, (int)LDS_BYTES);
  fused_attn<<<256, 256, LDS_BYTES, stream>>>((const char*)d_in[0], (const char*)d_in[1], (const char*)d_in[2], (const char*)d_in[3], (const char*)d_in[4],
                                              (const char*)d_in[5], (const char*)d_in[6], (const char*)d_in[7], (const char*)d_in[8], (const char*)d_in[9], (char*)d_ws, (char*)d_out);
}
```

```cpp
#define PROBE_K 0
#define PROBE_WHO 0
#define PROBE_MUL 0ull
#include <hip/hip_runtime.h>
#include <hip/hip_bf16.h>
#include <utility>

typedef __bf16 bf16x8 __attribute__((ext_vector_type(8)));
typedef __bf16 bf16x4 __attribute__((ext_vector_type(4)));
typedef __bf16 bf16x2 __attribute__((ext_vector_type(2)));
typedef float f32x4 __attribute__((ext_vector_type(4)));
typedef float f32x2 __attribute__((ext_vector_type(2)));
typedef unsigned u32x4 __attribute__((ext_vector_type(4)));
#define LAS __attribute__((address_space(3)))
#define DI __device__ __forceinline__
typedef __amdgpu_buffer_rsrc_t srd_t;
template <int... Is> using iseq = std::integer_sequence<int, Is...>;
template <int N> using make_iseq = std::make_integer_sequence<int, N>;

#define KV_NT 1
#define W_NT 0
#define X_NT 0
#define SLEEP_A 8
#define SLEEP_B 2
#define PACE_A 0
#define PACE_ATT 0
#define TOUCH_Q 1
#define TOUCH_O 3
#define TO_SKIP 16u
#define TO_B 8u
#define WS_END 0

constexpr unsigned SLOT = 8192, LDS_BYTES = 20 * SLOT;
constexpr unsigned POFF = (unsigned)WS_END << 20, HOFF = POFF + (16u << 20);
constexpr float SC = 0.12751743082f;
constexpr float THR = 8.0f;

#define NO_REDUCE_STAMPS
#define GAP() __builtin_amdgcn_sched_barrier(0)
#define STAMP(i) do { if (i == 0 || i == PROBE_K) { GAP(); unsigned long long t_; asm volatile("s_memrealtime %0\n\ts_waitcnt lgkmcnt(0)" : "=s"(t_) :: "memory"); if (i == 0) pr0 = t_; prk = t_; GAP(); } } while (0)
#define PINS(x) asm volatile("" : "+s"(x))
#define PINV(x) asm volatile("" : "+v"(x))
DI unsigned rfl(unsigned x) { return __builtin_amdgcn_readfirstlane(x); }
DI unsigned cvtpk(float lo, float hi) { f32x2 v = {lo, hi}; bf16x2 b = __builtin_convertvector(v, bf16x2); return __builtin_bit_cast(unsigned, b); }
DI u32x4 pack2(const f32x4& a, const f32x4& b) { return u32x4{cvtpk(a[0], a[1]), cvtpk(a[2], a[3]), cvtpk(b[0], b[1]), cvtpk(b[2], b[3])}; }
DI u32x4 natural8(u32x4 w) {
  auto s0 = __builtin_amdgcn_permlane32_swap(w[0], w[2], false, false);
  auto s1 = __builtin_amdgcn_permlane32_swap(w[1], w[3], false, false);
  auto t0 = __builtin_amdgcn_permlane16_swap(s0[0], s0[1], false, false);
  auto t1 = __builtin_amdgcn_permlane16_swap(s1[0], s1[1], false, false);
  return u32x4{t0[0], t1[0], t0[1], t1[1]};
}
#define MF "v_mfma_f32_16x16x32_bf16 "
template <bool Z> DI void mm_vvv(f32x4& c, const bf16x8& a, const bf16x8& b) {
  if constexpr (Z) asm volatile(MF "%0, %1, %2, 0" : "=&v"(c) : "v"(a), "v"(b));
  else             asm volatile(MF "%0, %1, %2, %0" : "+v"(c) : "v"(a), "v"(b));
}
template <bool Z> DI void mm_vva(f32x4& c, const bf16x8& a, const bf16x8& b) {
  if constexpr (Z) asm volatile(MF "%0, %1, %2, 0" : "=&v"(c) : "v"(a), "a"(b));
  else             asm volatile(MF "%0, %1, %2, %0" : "+v"(c) : "v"(a), "a"(b));
}
DI void mm_vca(f32x4& d, const bf16x8& a, const bf16x8& b, const f32x4& c) { asm volatile(MF "%0, %1, %2, %3" : "=&v"(d) : "v"(a), "a"(b), "v"(c)); }
DI void mm_avv(f32x4& c, const bf16x8& a, const bf16x8& b) { asm volatile(MF "%0, %1, %2, %0" : "+a"(c) : "v"(a), "v"(b)); }
DI void set_m0(unsigned v) { asm volatile("s_mov_b32 m0, %0" :: "s"(v) : "memory"); }
template <int NT, int P> DI void dma_p(unsigned voff, srd_t srd, unsigned soff) {
  if constexpr (NT) asm volatile("buffer_load_dwordx4 %0, %1, %2 offen offset:%3 nt lds" :: "v"(voff), "s"(srd), "s"(soff), "i"(1024 * P) : "memory");
  else              asm volatile("buffer_load_dwordx4 %0, %1, %2 offen offset:%3 lds" :: "v"(voff), "s"(srd), "s"(soff), "i"(1024 * P) : "memory");
}
struct Src { unsigned v[4]; srd_t srd; };
template <int NT> DI void dma_unit(const Src& s, unsigned soff, unsigned ldsaddr) {
  asm volatile("s_mov_b32 m0, %0\n\ts_nop 0" :: "s"(ldsaddr) : "memory");
  dma_p<NT, 0>(s.v[0], s.srd, soff); dma_p<NT, 1>(s.v[1], s.srd, soff); dma_p<NT, 2>(s.v[2], s.srd, soff); dma_p<NT, 3>(s.v[3], s.srd, soff);
}
template <int NT> DI void dma_elem(const Src& s, unsigned soff, unsigned ustep, unsigned ldsaddr) { dma_unit<NT>(s, soff, ldsaddr); dma_unit<NT>(s, soff + ustep, ldsaddr + 4096u); }
template <int N> DI void wait_vm() { asm volatile("s_waitcnt vmcnt(%0)" :: "i"(N) : "memory"); }
template <int N, int L> DI void wait_vm_lgkm() { asm volatile("" ::: "memory"); __builtin_amdgcn_s_waitcnt((N & 15) | ((N >> 4) << 14) | 0x70 | (L << 8)); asm volatile("" ::: "memory"); }
DI void wait_lgkm0() { wait_vm_lgkm<63, 0>(); }
DI void barrier() { asm volatile("s_barrier" ::: "memory"); }
DI bf16x8 lds128(unsigned a) { return *(const LAS bf16x8*)(size_t)(a); }
DI bf16x4 lds_tr1(unsigned a) { return __builtin_amdgcn_ds_read_tr16_b64_v4bf16((LAS bf16x4*)(size_t)(a)); }
DI bf16x8 cat(bf16x4 lo, bf16x4 hi) { return __builtin_shufflevector(lo, hi, 0, 1, 2, 3, 4, 5, 6, 7); }
DI bf16x8 lds_tr(unsigned a0, unsigned a1) { return cat(lds_tr1(a0), lds_tr1(a1)); }
DI srd_t mk_srd(const void* base, unsigned bytes) { return __builtin_amdgcn_make_buffer_rsrc((void*)base, (short)0, (int)bytes, 0x00020000); }
template <bool VX> DI void mk_src_rows(Src& s, const char* base, unsigned bytes, unsigned stride, unsigned lane) {
  s.srd = mk_srd(base, bytes);
  const unsigned row = (lane >> 2) & 3u, cb = lane >> 4, sl = lane & 3u;
#pragma unroll
  for (int p = 0; p < 4; ++p) s.v[p] = row * stride + cb * 64u + ((sl ^ (VX ? 2 * (p & 1) : (p & 2))) << 4) + (4u * stride - 1024u) * p;
}
template <bool VX> DI void mk_src_w(Src& s, const char* base, unsigned bytes, unsigned stride, unsigned lane) {
  s.srd = mk_srd(base, bytes);
  const unsigned sub = lane >> 5, cb = (lane >> 4) & 1u, row = (lane >> 2) & 3u, sl = lane & 3u;
#pragma unroll
  for (int p = 0; p < 4; ++p) s.v[p] = (4u * sub + row) * stride + cb * 64u + ((sl ^ (VX ? 2u * sub : 2u * (p & 1))) << 4) + (8u * stride - 1024u) * p;
}
template <int NT> DI void touch(unsigned& d, unsigned voff, srd_t srd, unsigned soff) {
  if constexpr (NT) asm volatile("buffer_load_dword %0, %1, %2, %3 offen nt" : "+v"(d) : "v"(voff), "s"(srd), "s"(soff) : "memory");
  else              asm volatile("buffer_load_dword %0, %1, %2, %3 offen" : "+v"(d) : "v"(voff), "s"(srd), "s"(soff) : "memory");
}
DI unsigned ksoff(unsigned m, unsigned rk) { return ((m + rk) & 15u) * 8192u; }

struct PC {
  unsigned r01, r2;
  unsigned w01[2], w2[2];
  unsigned xb, xmask, xa;
  unsigned z, soff;
};
template <unsigned TS_, unsigned QS_, unsigned ZI_, unsigned ZM_, unsigned ZO_, unsigned US_, unsigned CS_, bool ROT_> struct PK {
  static constexpr unsigned TS = TS_, QS = QS_, ZI = ZI_, ZM = ZM_, ZO = ZO_, US = US_, CS = CS_; static constexpr bool ROT = ROT_; };
DI unsigned q_soff(unsigned z) { return ((z & 0xC300u) * 0x440u) & 0x3C0000u; }
template <int POS> DI unsigned ring_lds(const PC& c) { return POS == 2 ? c.r2 : c.r01 + POS * SLOT; }
template <int POS> DI unsigned ring_rd(const PC& c, int d) { return (POS == 2 ? c.w2[d & 1] : c.w01[d & 1] + POS * SLOT) + (d >> 1) * 256u; }
template <class K, int POS, int S, bool Z, bool RD, bool DM, int NT, bool XT, int M>
DI void proj_gap(f32x4 (&acc)[8][4], const bf16x8 (&cur)[4], bf16x8 (&nxt)[4], bf16x4 (&lo)[4], bf16x8 (&bf)[4], PC& c, unsigned soff, const Src& src, const Src& xs, unsigned xsoff) {
  constexpr int qb = M >> 2, db = M & 3;
  mm_vvv<Z>(acc[4 * S + db][qb], cur[db], bf[qb]);
  if constexpr (RD && M < 8) {
    constexpr int d = M >> 1; const unsigned a = S == 0 ? ring_rd<POS>(c, d) + 4096u : ring_rd<(POS + 1) % 3>(c, d);
    if constexpr (M & 1) nxt[d] = cat(lo[d], lds_tr1(a + K::TS)); else lo[d] = lds_tr1(a);
  }
  if constexpr (RD && S == 1 && db == 3) bf[qb] = lds128(c.xa + qb * K::QS);
  if constexpr (XT) {
    if constexpr (M < 8 && !(M & 1)) dma_p<X_NT, M / 2>(xs.v[M / 2], xs.srd, xsoff);
    if constexpr (M == 7) set_m0(ring_lds<POS>(c) + S * 4096u);
    if constexpr (M >= 8 && !(M & 1)) dma_p<NT, (M - 8) / 2>(src.v[(M - 8) / 2], src.srd, soff);
  } else if constexpr (DM && M >= 8 && !(M & 1)) dma_p<NT, (M - 8) / 2>(src.v[(M - 8) / 2], src.srd, soff);
  if constexpr (S == 0 && M == 9) { PINS(c.z); c.z = (c.z + K::ZI) & K::ZM; PINS(c.z); }
  if constexpr (S == 0 && M == 11) { c.xa = (c.z & c.xmask) | c.xb; PINV(c.xa); }
  if constexpr (S == 1 && DM && !K::ROT && M == 9) { PINS(c.soff); c.soff = (c.soff + K::CS) & (16u * K::CS - 1u); PINS(c.soff); }
  if constexpr (S == 1 && DM && K::ROT && M == 9) { PINS(c.z); c.soff = (c.z + 0x3100u) & 0xC300u; PINS(c.soff); }
  if constexpr (S == 1 && DM && K::ROT && M == 13) { PINS(c.soff); c.soff = (c.soff * 0x440u) & 0x3C0000u; PINS(c.soff); }
  GAP();
}
template <class K, int POS, int S, bool Z, bool RD, bool DM, int NT, int VM, bool BAR, bool XT, int... Ms>
DI void proj_unit(f32x4 (&acc)[8][4], const bf16x8 (&cur)[4], bf16x8 (&nxt)[4], bf16x8 (&bf)[4], PC& c, const Src& src, const Src& xs, unsigned xsoff, unsigned xdst, iseq<Ms...>) {
  wait_vm_lgkm<(RD ? VM : 63), (S == 0 ? 2 : 0)>();
  if constexpr (BAR) barrier();
  const unsigned soff = c.soff + S * K::US;
  if constexpr (XT) set_m0(xdst); else if constexpr (DM) set_m0(ring_lds<POS>(c) + S * 4096u);
  GAP();
  bf16x4 lo[4];
  (proj_gap<K, POS, S, Z, RD, DM, NT, XT, Ms>(acc, cur, nxt, lo, bf, c, soff, src, xs, xsoff), ...);
}
template <class K, int POS, bool Z, bool LAST, bool DM, int NT, int VM0, int VM1, bool BAR1 = false, bool XT0 = false, bool XT1 = false>
DI void proj_elem(f32x4 (&acc)[8][4], bf16x8 (&a0)[4], bf16x8 (&a1)[4], bf16x8 (&bf)[4], PC& c, const Src& src,
                  const Src* xs = nullptr, unsigned xso0 = 0, unsigned xd0 = 0, unsigned xso1 = 0, unsigned xd1 = 0) {
  proj_unit<K, POS, 0, Z, true, DM, NT, VM0, false, XT0>(acc, a0, a1, bf, c, src, xs ? *xs : src, xso0, xd0, make_iseq<16>{});
  proj_unit<K, POS, 1, Z, !LAST, DM, NT, VM1, BAR1, XT1>(acc, a1, a0, bf, c, src, xs ? *xs : src, xso1, xd1, make_iseq<16>{});
}
template <class K, int POS> DI void proj_start(bf16x8 (&a0)[4], bf16x8 (&bf)[4], PC& c, unsigned tb, unsigned xb, unsigned z0, unsigned s3) {
  c.w01[0] = c.r01 + tb; c.w01[1] = c.w01[0] ^ 32u; c.w2[0] = c.r2 + tb; c.w2[1] = c.w2[0] ^ 32u;
  PINV(c.w01[0]); PINV(c.w01[1]); PINV(c.w2[0]); PINV(c.w2[1]);
  c.xb = xb; c.xmask = K::ZO; PINV(c.xmask);
  c.z = z0;
  c.xa = (c.z & K::ZO) | xb; PINV(c.xa);
#pragma unroll
  for (int d = 0; d < 4; ++d) { const unsigned a = ring_rd<POS>(c, d); a0[d] = lds_tr(a, a + K::TS); }
  GAP();
#pragma unroll
  for (int q = 0; q < 4; ++q) bf[q] = lds128(c.xa + q * K::QS);
  GAP();
  c.soff = s3;
}
#define ACC_ALL(C, a) C(a[0][0]), C(a[0][1]), C(a[0][2]), C(a[0][3]), C(a[1][0]), C(a[1][1]), C(a[1][2]), C(a[1][3]), C(a[2][0]), C(a[2][1]), C(a[2][2]), C(a[2][3]), C(a[3][0]), C(a[3][1]), C(a[3][2]), C(a[3][3])
DI void acc_fence_v(f32x4 (&acc)[8][4]) {
  asm volatile("s_nop 7\n\ts_nop 3" : ACC_ALL("+v", acc) :: "memory");
  f32x4 (&hi)[4][4] = *(f32x4 (*)[4][4])&acc[4];
  asm volatile("" : ACC_ALL("+v", hi) :: "memory");
  GAP();
}
DI void acc_fence_a(f32x4 (&acc)[8][4]) {
  asm volatile("s_nop 7\n\ts_nop 3" : ACC_ALL("+a", acc) :: "memory");
  f32x4 (&hi)[4][4] = *(f32x4 (*)[4][4])&acc[4];
  asm volatile("" : ACC_ALL("+a", hi) :: "memory");
  GAP();
}

struct Stash { u32x4 p[64]; unsigned base[3]; };
template <int I> DI void stash_load1(Stash& st, unsigned vk, const Src& sK, const Src& sV, unsigned rk, unsigned wo_) {
  constexpr int m = I >> 2, isV = (I >> 1) & 1, pc_ = I & 1;
  const unsigned so = ksoff(m, rk) + wo_;
  if constexpr (I < 56) asm volatile("buffer_load_dwordx4 %0, %1, %2, %3 offen offset:%4 nt" : "=a"(st.p[I]) : "v"(isV ? sV.v[pc_] : vk), "s"(isV ? sV.srd : sK.srd), "s"(so), "i"(1024 * pc_) : "memory");
  else                  asm volatile("buffer_load_dwordx4 %0, %1, %2, %3 offen offset:%4 nt" : "=v"(st.p[I]) : "v"(isV ? sV.v[pc_] : vk), "s"(isV ? sV.srd : sK.srd), "s"(so), "i"(1024 * pc_) : "memory");
}
template <int... Is> DI void stash_load(Stash& st, unsigned vk, const Src& sK, const Src& sV, unsigned rk, unsigned wo_, iseq<Is...>) { (stash_load1<Is>(st, vk, sK, sV, rk, wo_), ...); }
constexpr int kv_pair(int m) { constexpr int t[10] = {0, 1, 2, 3, 8, 9, 4, 5, 6, 7}; return t[m % 10]; }
constexpr unsigned long long kv_tab() { unsigned long long v = 0; for (int i = 0; i < 16; ++i) v |= (unsigned long long)kv_pair(i) << (4 * i); return v; }
template <int I> DI void stash_write1(const Stash& st) {
  constexpr int m = I >> 2, isV = (I >> 1) & 1, pc_ = I & 1, off = kv_pair(m) * 16384 + isV * 8192 + pc_ * 1024;
  if constexpr (I < 56) asm volatile("ds_write_b128 %0, %1 offset:%2" :: "v"(st.base[off >> 16]), "a"(st.p[I]), "i"(off & 65535) : "memory");
  else                  asm volatile("ds_write_b128 %0, %1 offset:%2" :: "v"(st.base[off >> 16]), "v"(st.p[I]), "i"(off & 65535) : "memory");
}

struct AC {
  f32x4 o[8][4];
  bf16x8 q[4][4];
  bf16x8 kf[8], vf[8], pf[4];
  f32x4 negm[4];
  float t0[4], t1[4];
  float fac[4];
  bool pend;
};
typedef f32x4 ST[2][4];
template <int E> DI float sm_val(ST& S) { return S[(E >> 2) & 1][E >> 3][E & 3]; }
template <int E> DI void sm_exp1(ST& S) { float a; asm volatile("v_exp_f32_e32 %0, %1" : "=v"(a) : "v"(sm_val<E>(S))); S[(E >> 2) & 1][E >> 3][E & 3] = a; }
template <int QB, int P> DI void sm_cvt(ST& S, AC& c) {
  u32x4 w = __builtin_bit_cast(u32x4, c.pf[QB]);
  unsigned r;
  asm volatile("v_cvt_pk_bf16_f32 %0, %1, %2" : "=v"(r) : "v"(sm_val<8 * QB + 2 * P>(S)), "v"(sm_val<8 * QB + 2 * P + 1>(S)));
  w[P] = r;
  c.pf[QB] = __builtin_bit_cast(bf16x8, w);
}
template <int E> DI void sm_add2(ST& S, AC& c) {
  asm volatile("v_add_f32_e32 %0, %0, %2\n\tv_add_f32_e32 %1, %1, %3" : "+v"(c.t0[E >> 3]), "+v"(c.t1[E >> 3]) : "v"(sm_val<E>(S)), "v"(sm_val<E + 1>(S)));
}
template <int E, bool FIRST> DI void sm_max4(ST& S, float (&mx)[2]) {
  if constexpr (FIRST) asm volatile("v_max_f32_e32 %0, %2, %3\n\tv_max_f32_e32 %1, %4, %5" : "=&v"(mx[0]), "=v"(mx[1]) : "v"(sm_val<E>(S)), "v"(sm_val<E + 1>(S)), "v"(sm_val<E + 2>(S)), "v"(sm_val<E + 3>(S)));
  else asm volatile("v_max3_f32 %0, %0, %2, %3\n\tv_max3_f32 %1, %1, %4, %5" : "+v"(mx[0]), "+v"(mx[1]) : "v"(sm_val<E>(S)), "v"(sm_val<E + 1>(S)), "v"(sm_val<E + 2>(S)), "v"(sm_val<E + 3>(S)));
}
template <bool DM, int VM, int M> DI void mem_pre(unsigned ldsaddr) {
  if constexpr (M == 6) wait_lgkm0();
  if constexpr (DM && (M == 6 || M == 14)) set_m0(ldsaddr + (M == 14 ? 4096u : 0u));
  if constexpr (M == 16 && VM < 63) wait_vm<VM>();
}
template <bool DM, int NT, int M> DI void mem_post(const Src& src, unsigned soff, unsigned ustep) {
  if constexpr (DM && M >= 6 && M <= 20 && !(M & 1)) dma_p<NT, ((M - 6) / 2) & 3>(src.v[((M - 6) / 2) & 3], src.srd, soff + (M >= 14 ? ustep : 0u));
}
struct PA { unsigned rd0, rd1, soff, fill; };
template <bool QK, bool DM, int NT, int VM, int WR, int M, class F>
DI void gapA(AC& c, ST& S, ST& Sn, PA& pa, F& addr, const Src& src, unsigned ustep, const Stash& st, bf16x4& lo) {
  mem_pre<DM, VM, M>(pa.fill);
  if constexpr (QK) {
    constexpr int f = M >> 2, s = f >> 1, kb = f & 1, qb = M & 3;
    if constexpr (s == 0) mm_vca(Sn[kb][qb], c.kf[f], c.q[qb][0], c.negm[qb]); else mm_vva<false>(Sn[kb][qb], c.kf[f], c.q[qb][s]);
  } else if constexpr (DM) asm volatile("s_nop 0");
  mem_post<DM, NT, M>(src, pa.soff, ustep);
  if constexpr (WR >= 0 && M >= 8 && (M & 3) == 1) stash_write1<WR + (M - 8) / 4>(st);
  if constexpr (M == 2) addr(pa);
  if constexpr (QK && M == 12) asm volatile("" :: "v"(c.negm[0]), "v"(c.negm[1]), "v"(c.negm[2]), "v"(c.negm[3]));
  if constexpr (M >= 16) {
    constexpr int db = (M - 16) >> 1; const unsigned a = ((db & 1) ? pa.rd1 : pa.rd0) + (db >> 1) * 256u;
    if constexpr (M & 1) c.vf[db] = cat(lo, lds_tr1(a + 4096u)); else lo = lds_tr1(a);
  }
  if constexpr (M < 16) {
    sm_exp1<16 + M>(S);
    if constexpr (M & 1) sm_cvt<(M >> 3), ((M >> 1) & 3)>(S, c);
  } else {
    constexpr int i = (M - 16) >> 1;
    if constexpr (M & 1) sm_cvt<2 + (i >> 2), (i & 3)>(S, c); else sm_add2<2 * i>(S, c);
  }
  GAP();
}
template <bool QK, bool DM, int NT, int VM, int WR, class F, int... Ms>
DI void phaseA_(AC& c, ST& S, ST& Sn, F& addr, const Src& src, unsigned ustep, const Stash& st, iseq<Ms...>) {
  PA pa; bf16x4 lo;
  (gapA<QK, DM, NT, VM, WR, Ms>(c, S, Sn, pa, addr, src, ustep, st, lo), ...);
}
template <bool QK, bool DM, int NT, int VM, int WR = -1, class F>
DI void phaseA(AC& c, ST& S, ST& Sn, F addr, const Src& src, unsigned ustep, const Stash& st) {
  phaseA_<QK, DM, NT, VM, WR>(c, S, Sn, addr, src, ustep, st, make_iseq<32>{});
}
template <bool MAX> DI float xlane(float v) {
  auto a = __builtin_amdgcn_permlane32_swap(__float_as_uint(v), __float_as_uint(v), false, false);
  v = MAX ? fmaxf(__uint_as_float(a[0]), __uint_as_float(a[1])) : __uint_as_float(a[0]) + __uint_as_float(a[1]);
  auto b = __builtin_amdgcn_permlane16_swap(__float_as_uint(v), __float_as_uint(v), false, false);
  return MAX ? fmaxf(__uint_as_float(b[0]), __uint_as_float(b[1])) : __uint_as_float(b[0]) + __uint_as_float(b[1]);
}
DI float row_max(const f32x4& x, const f32x4& y) {
  return xlane<true>(fmaxf(fmaxf(fmaxf(x[0], x[1]), fmaxf(x[2], x[3])), fmaxf(fmaxf(y[0], y[1]), fmaxf(y[2], y[3]))));
}
template <bool SM, bool KR, bool DM, int NT, int VM, int WR, int M, class F>
DI void gapB(AC& c, ST& S, ST& Sn, PA& pa, F& addr, const Src& src, unsigned ustep, const Stash& st, float (&mx)[2]) {
  mem_pre<DM, (KR ? VM : 63), M>(pa.fill);
  { constexpr int db = (M >> 1) & 7, qb = 2 * (M >> 4) + (M & 1); mm_avv(c.o[db][qb], c.vf[db], c.pf[qb]); }
  mem_post<DM, NT, M>(src, pa.soff, ustep);
  if constexpr (WR >= 0 && M >= 8 && (M & 3) == 1) stash_write1<WR + (M - 8) / 4>(st);
  if constexpr (M == 0) addr(pa);
  if constexpr (M < 4) sm_add2<16 + 2 * M>(S, c);
  if constexpr (M >= 12 && M < 16) sm_add2<24 + 2 * (M - 12)>(S, c);
  if constexpr (KR && M >= 16 && !(M & 1)) { constexpr int f = (M - 16) >> 1; c.kf[f] = lds128(pa.rd0 + (f & 1) * 4096u + (f >> 1) * 256u); }
  if constexpr (SM) {
    if constexpr (M >= 4 && M < 12) { constexpr int i = M - 4; sm_max4<8 * (i & 3) + 4 * (i >> 2), (i == 0)>(Sn, mx); }
    if constexpr (M == 13) c.pend = __builtin_amdgcn_ballot_w64(!(fmaxf(mx[0], mx[1]) <= THR)) != 0ull;
    if constexpr (M == 15) {
      if (__builtin_expect(c.pend, 0)) {
#pragma unroll
        for (int q = 0; q < 4; ++q) {
          const float dl = fmaxf(row_max(Sn[0][q], Sn[1][q]), 0.f);
          c.fac[q] = __builtin_amdgcn_exp2f(-dl);
          Sn[0][q] -= dl; Sn[1][q] -= dl; c.negm[q] -= dl;
        }
      }
    }
    if constexpr (M >= 16) sm_exp1<M - 16>(Sn);
  }
  GAP();
}
template <bool SM, bool KR, bool DM, int NT, int VM, int WR, class F, int... Ms>
DI void phaseB_(AC& c, ST& S, ST& Sn, F& addr, const Src& src, unsigned ustep, const Stash& st, iseq<Ms...>) {
  float mx[2]; PA pa;
  (gapB<SM, KR, DM, NT, VM, WR, Ms>(c, S, Sn, pa, addr, src, ustep, st, mx), ...);
  if constexpr (SM) {
    if (__builtin_expect(c.pend, 0)) {
      asm volatile("s_nop 7\n\ts_nop 3" ::: "memory");
#pragma unroll
      for (int q = 0; q < 4; ++q) {
        const float f = c.fac[q];
        c.t0[q] *= f; c.t1[q] *= f;
#pragma unroll
        for (int db = 0; db < 8; ++db) {
          asm volatile("" : "+a"(c.o[db][q]));
          c.o[db][q] *= f;
          asm volatile("" : "+a"(c.o[db][q]));
        }
      }
      asm volatile("s_nop 1" ::: "memory");
    }
  }
}
template <bool SM, bool KR, bool DM, int NT, int VM, int WR = -1, class F>
DI void phaseB(AC& c, ST& S, ST& Sn, F addr, const Src& src, unsigned ustep, const Stash& st) {
  phaseB_<SM, KR, DM, NT, VM, WR>(c, S, Sn, addr, src, ustep, st, make_iseq<32>{});
}
DI void k_read(AC& ac, unsigned ka) {
#pragma unroll
  for (int f = 0; f < 8; ++f) ac.kf[f] = lds128(ka + (f & 1) * 4096u + (f >> 1) * 256u);
}
DI void qk0(AC& ac, ST& SA) {
#pragma unroll
  for (int kb = 0; kb < 2; ++kb)
#pragma unroll
    for (int q = 0; q < 4; ++q) mm_vva<true>(SA[kb][q], ac.kf[kb], ac.q[q][0]);
#pragma unroll
  for (int f = 2; f < 8; ++f)
#pragma unroll
    for (int q = 0; q < 4; ++q) mm_vva<false>(SA[f & 1][q], ac.kf[f], ac.q[q][f >> 1]);
}
template <int... Js> DI void exp_first(ST& SA, iseq<Js...>) { (sm_exp1<Js>(SA), ...); }
DI void sm_first(AC& ac, ST& SA) {
  asm volatile("s_nop 7\n\ts_nop 3" : "+v"(SA[0][0]), "+v"(SA[0][1]), "+v"(SA[0][2]), "+v"(SA[0][3]), "+v"(SA[1][0]), "+v"(SA[1][1]), "+v"(SA[1][2]), "+v"(SA[1][3]) :: "memory");
  GAP();
#pragma unroll
  for (int q = 0; q < 4; ++q) {
    const float m = row_max(SA[0][q], SA[1][q]);
    SA[0][q] -= m; SA[1][q] -= m;
    ac.negm[q] = f32x4{-m, -m, -m, -m};
    ac.t0[q] = 0.f; ac.t1[q] = 0.f; ac.fac[q] = 1.f;
  }
  asm volatile("" : "+v"(ac.negm[0]), "+v"(ac.negm[1]), "+v"(ac.negm[2]), "+v"(ac.negm[3]));
  exp_first(SA, make_iseq<16>{});
  GAP();
}
template <bool WR, int J> DI void q_conv1(AC& ac, f32x4 (&acc)[8][4], const Stash& st) {
  constexpr int s = J >> 2, qb = J & 3;
  if constexpr (WR) { stash_write1<(5 * J) / 2>(st); stash_write1<(5 * J) / 2 + 1>(st); if constexpr (J & 1) stash_write1<(5 * J) / 2 + 2>(st); }
  ac.q[qb][s] = __builtin_bit_cast(bf16x8, natural8(pack2(acc[2 * s][qb] * SC, acc[2 * s + 1][qb] * SC)));
  asm volatile("" : "+a"(ac.q[qb][s]));
  GAP();
}
template <bool WR, int... Js> DI void q_conv(AC& ac, f32x4 (&acc)[8][4], const Stash& st, iseq<Js...>) { (q_conv1<WR, Js>(ac, acc, st), ...); }

template <bool isA>
DI void body(const char* __restrict__ X, const char* __restrict__ K, const char* __restrict__ V, const char* __restrict__ Wq, const char* __restrict__ Wo,
             const char* __restrict__ Wo2, char* __restrict__ part, char* __restrict__ out) {
  extern __shared__ __attribute__((aligned(16))) char lds[];
  unsigned long long pr0 = 0, prk = 0;
  STAMP(0);
  const unsigned lane = threadIdx.x & 63u, w = rfl(threadIdx.x >> 6);
  const unsigned bid = blockIdx.x, xg = bid & 7u, rr = bid >> 3, b = rr & 15u, h = xg * 4u + w;
  const unsigned r16 = lane & 15u, g = lane >> 4;
  const char* xp  = X + (size_t)b * 65536u + w * 16384u;
  const char* wqp = Wq + h * 256u;
  const char* wop = Wo + (size_t)xg * 524288u + w * 256u;
  const size_t kvo = isA ? (size_t)(b * 8u + xg) * 131072u : (size_t)(b * 32u + h) * 131072u;
  const unsigned rk = isA ? b : b + 4u * w;
  typedef PK<512u, 4096u, 0x1300u, 0xF300u, 0xC300u, 128u, 32u * 8192u, true> KQ;
  typedef PK<512u, 4096u, 0x1300u, 0xF300u, 0xC300u, 4096u, 8192u, false> KT;
  typedef PK<2048u, 1024u, 0x1000u, 0xF000u, 0xF000u, 128u, 32u * 1024u, false> KO;
  asm volatile("" :: "v"(lds));
  PC pc;
  pc.r01 = 65536u + w * 16384u; pc.r2 = 131072u + w * 8192u;
  const unsigned zq = ((b & 3u) << 14) | ((b >> 2) << 8), co = b;
  const srd_t srdH = mk_srd(part + HOFF, 4096u + 256u * 128u);
  const unsigned tok_off = rr * 128u, flg_off = 4096u + rr * 1024u;
  const unsigned l8 = (lane & 7u) * 4u;
  unsigned entry_flags;
  asm volatile("buffer_load_dword %0, %1, %2, %3 offen sc1" : "=v"(entry_flags) : "v"(l8), "s"(srdH), "s"(flg_off + xg * 128u) : "memory");
  Src sQ, sX, sK, sV, sO;
  mk_src_w<false>(sQ, wqp, 511u * 8192u + 256u, 8192u, lane);
  mk_src_rows<false>(sX, xp, 16384u, 1024u, lane);
  const unsigned p0 = b & 3u, p1 = (p0 + 1u) & 3u, p2 = (p0 + 2u) & 3u, p3 = (p0 + 3u) & 3u, xw = w * 4096u;
  dma_elem<TOUCH_Q>(sQ, q_soff(zq), KQ::US, pc.r2);
  dma_unit<X_NT>(sX, p0 * 256u, xw + p0 * 16384u);
  dma_elem<W_NT>(sQ, q_soff(zq + 0x1300u), KQ::US, pc.r01);
  mk_src_rows<false>(sK, K + kvo, 131072u, 256u, lane);
  mk_src_rows<true>(sV, V + kvo, 131072u, 256u, lane);
  mk_src_w<true>(sO, wop, 511u * 1024u + 256u, 1024u, lane);
  Stash st;
  if constexpr (isA) {
    dma_unit<X_NT>(sX, p1 * 256u, xw + p1 * 16384u);
    dma_unit<X_NT>(sX, p2 * 256u, xw + p2 * 16384u);
    dma_unit<X_NT>(sX, p3 * 256u, xw + p3 * 16384u);
    dma_elem<W_NT>(sQ, q_soff(zq + 0x2200u), KQ::US, pc.r01 + SLOT);
    stash_load(st, (w & 1u) ? sK.v[2] : sK.v[0], sK, sV, rk, w * 2048u, make_iseq<64>{});
  }
  const unsigned rb = 1024u * (r16 >> 2) + 64u * (r16 & 3u) + 16u * (g ^ ((r16 >> 2) & 2u));
  const unsigned tb = 64u * ((lane >> 2) & 3u) + 32u * (g & 1u) + 8u * (lane & 3u);
  const unsigned tbV = tb + 1024u * g;
  const unsigned sb0 = w * 2048u + lane * 16u;
  st.base[0] = sb0; st.base[1] = sb0 + 65536u; st.base[2] = sb0 + 131072u;
  AC ac;
  {
    f32x4 acc[8][4];
    bf16x8 a0[4], a1[4], bf[4];
    asm volatile("s_waitcnt vmcnt(%1)" : "+v"(entry_flags) : "i"(isA ? 63 : 8) : "memory");
    if (w == 0) {
      const unsigned long long mk = __builtin_amdgcn_ballot_w64(((entry_flags >> (lane & 7u)) & 1u) != 0u);
      const unsigned tok = 0x5a5a5a00u | (~(unsigned)mk & 0xffu);
      if (lane == 0) asm volatile("buffer_store_dword %0, off, %1, %2 sc1" :: "v"(tok), "s"(srdH), "s"(tok_off + xg * 4u) : "memory");
    }
    barrier();
    STAMP(1);
    proj_start<KQ, 2>(a0, bf, pc, tbV, rb, zq, q_soff(zq + 0x3100u));
    if constexpr (isA) {
      proj_elem<KQ, 2, true,  false, true, W_NT, 63, 63>(acc, a0, a1, bf, pc, sQ);
      STAMP(10);
      proj_elem<KQ, 0, false, false, true, W_NT, 63, 63>(acc, a0, a1, bf, pc, sQ);
      STAMP(11);
      proj_elem<KQ, 1, false, false, true, W_NT, 63, 16>(acc, a0, a1, bf, pc, sQ);
      proj_elem<KQ, 2, false, false, true, W_NT, 16, 16>(acc, a0, a1, bf, pc, sQ);
    } else {
      dma_elem<W_NT>(sQ, q_soff(zq + 0x2200u), KQ::US, pc.r01 + SLOT);
      proj_elem<KQ, 2, true,  false, true, W_NT, 16, 20, false, true, true>(acc, a0, a1, bf, pc, sQ, &sX, p1 * 256u, xw + p1 * 16384u, p2 * 256u, xw + p2 * 16384u);
      STAMP(10);
      proj_elem<KQ, 0, false, false, true, W_NT, 24, 28, false, true, false>(acc, a0, a1, bf, pc, sQ, &sX, p3 * 256u, xw + p3 * 16384u);
      STAMP(11);
      proj_elem<KQ, 1, false, false, true, W_NT, 28, 24>(acc, a0, a1, bf, pc, sQ);
      proj_elem<KQ, 2, false, false, true, W_NT, 20, 16, true>(acc, a0, a1, bf, pc, sQ);
    }
    STAMP(8);
#pragma unroll 1
    for (unsigned e = 0; e < 3; ++e) {
      if constexpr (isA && PACE_A > 0) __builtin_amdgcn_s_sleep(PACE_A);
      proj_elem<KQ, 0, false, false, true, W_NT, 16, 16>(acc, a0, a1, bf, pc, sQ);
      if constexpr (isA && PACE_A > 0) __builtin_amdgcn_s_sleep(PACE_A);
      proj_elem<KQ, 1, false, false, true, W_NT, 16, 16>(acc, a0, a1, bf, pc, sQ);
      if constexpr (isA && PACE_A > 0) __builtin_amdgcn_s_sleep(PACE_A);
      proj_elem<KQ, 2, false, false, true, W_NT, 16, 16>(acc, a0, a1, bf, pc, sQ);
    }
    STAMP(9);
    if constexpr (isA) {
      proj_elem<KQ, 0, false, false, false, W_NT, 16, 12>(acc, a0, a1, bf, pc, sQ);
      proj_elem<KQ, 1, false, false, false, W_NT, 8, 4>(acc, a0, a1, bf, pc, sQ);
      proj_elem<KQ, 2, false, true, false, W_NT, 0, 0>(acc, a0, a1, bf, pc, sQ);
    } else {
      pc.soff = ksoff(0u, rk);
      proj_elem<KT, 0, false, false, true, KV_NT, 16, 16>(acc, a0, a1, bf, pc, sK);
      proj_elem<KT, 1, false, false, true, KV_NT, 16, 16>(acc, a0, a1, bf, pc, sK);
      pc.soff = ksoff(0u, rk);
      proj_elem<KT, 2, false, true, true, KV_NT, 16, 16>(acc, a0, a1, bf, pc, sV);
    }
    barrier();
    STAMP(2);
    if constexpr (!isA) dma_elem<KV_NT>(sK, ksoff(2u, rk), 4096u, w * 16384u);
    acc_fence_v(acc);
    q_conv<isA>(ac, acc, st, make_iseq<16>{});
  }
#pragma unroll
  for (int i = 0; i < 8; ++i)
#pragma unroll
    for (int q = 0; q < 4; ++q) { ac.o[i][q] = f32x4{}; asm volatile("" : "+a"(ac.o[i][q])); }
  ST SA, SB;
  if constexpr (isA) {
    const unsigned kb = rb, vb = SLOT + tbV;
    asm volatile("s_waitcnt lgkmcnt(0)" ::: "memory");
    barrier();
    k_read(ac, kb + kv_pair(0) * 16384u);
    wait_lgkm0();
    GAP();
    qk0(ac, SA);
    GAP();
    k_read(ac, kb + kv_pair(1) * 16384u);
    sm_first(ac, SA);
    STAMP(13);
    unsigned i4 = 0;
    auto av = [&](PA& p) { if constexpr (PACE_ATT > 0) __builtin_amdgcn_s_sleep(PACE_ATT);
                           PINS(i4); p.rd0 = vb + (((unsigned)(kv_tab() >> i4) & 15u) << 14); p.rd1 = p.rd0 ^ 32u; PINV(p.rd0); PINV(p.rd1); };
    auto ak = [&](PA& p) { if constexpr (PACE_ATT > 0) __builtin_amdgcn_s_sleep(PACE_ATT);
                           PINS(i4); p.rd0 = kb + (((unsigned)((kv_tab() >> 8) >> i4) & 15u) << 14); PINV(p.rd0); i4 += 4u; };
    auto an = [&](PA&) { i4 += 4u; };
    unsigned dum = lane, to = (lane >> 3) * 1024u + (lane & 4u) * 32u + (lane & 3u) * 4u;
    PINV(to);
    const char* ta = wop + b * KO::CS;
    const char* tb2 = Wo2 + (size_t)xg * 524288u + w * 256u + b * KO::CS - 32768;
    auto at = [&](PA& p) __attribute__((always_inline)) {
      PINS(i4); p.rd0 = vb + (((unsigned)(kv_tab() >> i4) & 15u) << 14); p.rd1 = p.rd0 ^ 32u; PINV(p.rd0); PINV(p.rd1);
      if constexpr (TOUCH_O) {
        const char* tp;
        asm volatile("s_cmp_lt_u32 %1, 16\n\ts_cselect_b64 %0, %2, %3" : "=s"(tp) : "s"(i4), "s"(ta), "s"(tb2) : "scc");
        if constexpr (TOUCH_O & 1) asm volatile("global_load_dword %0, %1, %2 nt" : "+v"(dum) : "v"(to), "s"(tp) : "memory");
        if constexpr (TOUCH_O & 2) asm volatile("global_load_dword %0, %1, %2" : "+v"(dum) : "v"(to), "s"(tp) : "memory");
        to += 8192u; PINV(to);
      }
    };
    while (i4 < 24u) {
      phaseA<true, false, KV_NT, 63>(ac, SA, SB, at, sK, 0u, st);
      phaseB<true, true, false, KV_NT, 63>(ac, SA, SB, ak, sK, 0u, st);
      phaseA<true, false, KV_NT, 63>(ac, SB, SA, at, sK, 0u, st);
      phaseB<true, true, false, KV_NT, 63>(ac, SB, SA, ak, sK, 0u, st);
    }
    wait_lgkm0(); barrier();
    phaseA<true, false, KV_NT, 63, 40>(ac, SA, SB, at, sK, 0u, st);
    phaseB<true, true, false, KV_NT, 63, 46>(ac, SA, SB, ak, sK, 0u, st);
    phaseA<true, false, KV_NT, 63, 52>(ac, SB, SA, at, sK, 0u, st);
    phaseB<true, true, false, KV_NT, 63, 58>(ac, SB, SA, ak, sK, 0u, st);
    while (i4 < 56u) {
      if (i4 <= 40u) {
        wait_lgkm0(); barrier();
        if (i4 == 40u) {
          asm volatile("s_waitcnt vmcnt(0)" : "+v"(dum) :: "memory");
          dma_elem<W_NT>(sO, co * KO::CS, KO::US, pc.r01);
          dma_elem<W_NT>(sO, ((co + 1u) & 15u) * KO::CS, KO::US, pc.r01 + SLOT);
        }
      }
      phaseA<true, false, KV_NT, 63>(ac, SA, SB, av, sK, 0u, st);
      phaseB<true, true, false, KV_NT, 63>(ac, SA, SB, ak, sK, 0u, st);
      phaseA<true, false, KV_NT, 63>(ac, SB, SA, av, sK, 0u, st);
      phaseB<true, true, false, KV_NT, 63>(ac, SB, SA, ak, sK, 0u, st);
    }
    STAMP(14);
    phaseA<true, false, KV_NT, 63>(ac, SA, SB, av, sK, 0u, st);
    phaseB<true, false, false, KV_NT, 63>(ac, SA, SB, an, sK, 0u, st);
    phaseA<false, false, KV_NT, 63>(ac, SB, SA, av, sK, 0u, st);
    phaseB<false, false, false, KV_NT, 63>(ac, SB, SA, an, sK, 0u, st);
  } else {
    const unsigned P0 = pc.r01, P1 = pc.r01 + SLOT, P2 = pc.r2, P3 = w * 16384u;
    unsigned v0 = tbV + P0, v2 = tbV + P2, x0 = v0 ^ 32u, x2 = v2 ^ 32u, k1 = rb + P1, k3 = rb + P3;
    PINV(v0); PINV(v2); PINV(x0); PINV(x2); PINV(k1); PINV(k3);
    wait_vm<24>();
    k_read(ac, k1 - SLOT);
    wait_lgkm0();
    GAP();
    dma_elem<KV_NT>(sV, ksoff(1u, rk), 4096u, P0);
    qk0(ac, SA);
    wait_vm<24>();
    GAP();
    k_read(ac, k1);
    sm_first(ac, SA);
    STAMP(13);
    unsigned so = ksoff(2u, rk), i = 0;
#define AV(RD, FILL) [&](PA& p) { PINS(so); p.soff = (so + 8192u) & 0x1ffffu; PINS(p.soff); p.fill = (FILL); p.rd0 = v##RD; p.rd1 = x##RD; }
#define AK(RD, FILL) [&](PA& p) { p.soff = so; so = (so + 8192u) & 0x1ffffu; PINS(so); p.fill = (FILL); p.rd0 = k##RD; }
#define TV(RD, FILL, SO) [&](PA& p) { p.soff = (SO); p.fill = (FILL); p.rd0 = v##RD; p.rd1 = x##RD; }
#define TK(RD, FILL, SO) [&](PA& p) { p.soff = (SO); p.fill = (FILL); p.rd0 = k##RD; }
    for (; i < 12; i += 2) {
      phaseA<true, true, KV_NT, 21>(ac, SA, SB, AV(2, P1), sK, 4096u, st);
      phaseB<true, true, true, KV_NT, 21>(ac, SA, SB, AK(3, P2), sV, 4096u, st);
      phaseA<true, true, KV_NT, 21>(ac, SB, SA, AV(0, P3), sK, 4096u, st);
      phaseB<true, true, true, KV_NT, 21>(ac, SB, SA, AK(1, P0), sV, 4096u, st);
    }
    STAMP(14);
    phaseA<true, true, KV_NT, 21>(ac, SA, SB, TV(2, P1, ksoff(15u, rk)), sK, 4096u, st);
    phaseB<true, true, true, KV_NT, 21>(ac, SA, SB, TK(3, P2, ksoff(14u, rk)), sV, 4096u, st);
    phaseA<true, true, KV_NT, 21>(ac, SB, SA, TV(0, P3, ksoff(15u, rk)), sV, 4096u, st);
    phaseB<true, true, true, W_NT, 21>(ac, SB, SA, TK(1, P0, co * KO::CS), sO, KO::US, st);
    phaseA<true, true, W_NT, 21>(ac, SA, SB, TV(2, P1, ((co + 1u) & 15u) * KO::CS), sO, KO::US, st);
    phaseB<true, false, true, W_NT, 63>(ac, SA, SB, TK(3, P2, ((co + 2u) & 15u) * KO::CS), sO, KO::US, st);
    unsigned v3 = tbV + P3, x3 = v3 ^ 32u; PINV(v3); PINV(x3);
    phaseA<false, false, W_NT, 24>(ac, SB, SA, TV(3, P3, 0u), sO, KO::US, st);
    phaseB<false, false, false, W_NT, 63>(ac, SB, SA, TK(3, P3, 0u), sO, KO::US, st);
#undef AV
#undef AK
#undef TV
#undef TK
  }
  acc_fence_a(ac.o);
  STAMP(3);
  if constexpr (isA) {
    barrier();
    dma_elem<W_NT>(sO, ((co + 2u) & 15u) * KO::CS, KO::US, pc.r2);
  }
  GAP();
  const unsigned l16 = lane * 16u;
#pragma unroll
  for (int qb = 0; qb < 4; ++qb) {
    const float inv = 1.0f / xlane<false>(ac.t0[qb] + ac.t1[qb]);
#pragma unroll
    for (int s = 0; s < 4; ++s)
      *(LAS u32x4*)(size_t)(w * 16384u + s * 4096u + qb * 1024u + l16) = pack2(ac.o[2 * s][qb] * inv, ac.o[2 * s + 1][qb] * inv);
  }
  wait_vm_lgkm<20, 0>();
  barrier();
  GAP();
  STAMP(4);
  {
    f32x4 acc[8][4];
    bf16x8 a0[4], a1[4], bf[4];
    proj_start<KO, 0>(a0, bf, pc, tb + 512u * g, l16, co << 12, ((co + 3u) & 15u) * KO::CS);
    proj_elem<KO, 0, true, false, true, W_NT, 16, 16>(acc, a0, a1, bf, pc, sO);
#pragma unroll 1
    for (unsigned e = 0; e < 4; ++e) {
      proj_elem<KO, 1, false, false, true, W_NT, 16, 16>(acc, a0, a1, bf, pc, sO);
      proj_elem<KO, 2, false, false, true, W_NT, 16, 16>(acc, a0, a1, bf, pc, sO);
      proj_elem<KO, 0, false, false, true, W_NT, 16, 16>(acc, a0, a1, bf, pc, sO);
    }
    proj_elem<KO, 1, false, false, false, W_NT, 16, 12>(acc, a0, a1, bf, pc, sO);
    proj_elem<KO, 2, false, false, false, W_NT, 8, 4>(acc, a0, a1, bf, pc, sO);
    proj_elem<KO, 0, false, true, false, W_NT, 0, 0>(acc, a0, a1, bf, pc, sO);
    acc_fence_v(acc);
    STAMP(5);
    const srd_t srdP = mk_srd(part + POFF + (size_t)rr * 524288u, 524288u);
    unsigned toks;
    asm volatile("buffer_load_dword %0, %1, %2, %3 offen sc1" : "=v"(toks) : "v"(l8), "s"(srdH), "s"(tok_off) : "memory");
    u32x4 pk[16];
#pragma unroll
    for (int np = 0; np < 4; ++np)
#pragma unroll
      for (int qb = 0; qb < 4; ++qb) {
        pk[np * 4 + qb] = pack2(acc[2 * np][qb], acc[2 * np + 1][qb]);
        __builtin_amdgcn_raw_buffer_store_b128(pk[np * 4 + qb], srdP, l16 + (qb * 2 + (np & 1)) * 1024u, xg * 65536u + w * 16384u + (np >> 1) * 8192u, 16);
      }
    asm volatile("s_nop 1" :: "v"(pk[0]), "v"(pk[1]), "v"(pk[2]), "v"(pk[3]), "v"(pk[4]), "v"(pk[5]), "v"(pk[6]), "v"(pk[7]),
                 "v"(pk[8]), "v"(pk[9]), "v"(pk[10]), "v"(pk[11]), "v"(pk[12]), "v"(pk[13]), "v"(pk[14]), "v"(pk[15]) : "memory");
    STAMP(6);
    asm volatile("s_waitcnt vmcnt(0)" : "+v"(toks) :: "memory");
    STAMP(7);
    const bool mine = (lane >> 1) == w;
    const unsigned fo = lane * 128u + xg * 4u;
    if (mine) asm volatile("buffer_store_dword %0, %1, %2, %3 offen sc1" :: "v"(toks), "v"(fo), "s"(srdH), "s"(flg_off) : "memory");
    const unsigned tau = __builtin_amdgcn_readlane(toks, xg);
#pragma unroll 1
    for (unsigned spin = 0; spin < (1u << 22); ++spin) {
      unsigned fl, tk;
      asm volatile("buffer_load_dword %0, %2, %3, %4 offen sc1\n\tbuffer_load_dword %1, %2, %3, %5 offen sc1\n\ts_waitcnt vmcnt(0)"
                   : "=&v"(fl), "=&v"(tk) : "v"(l8), "s"(srdH), "s"(flg_off + xg * 128u), "s"(tok_off) : "memory");
      if (mine && tk != toks) asm volatile("buffer_store_dword %0, %1, %2, %3 offen sc1" :: "v"(tk), "v"(fo), "s"(srdH), "s"(flg_off) : "memory");
      toks = tk;
      if (__builtin_amdgcn_ballot_w64(fl != tau) == 0ull) break;
      __builtin_amdgcn_s_sleep(isA ? SLEEP_A : SLEEP_B);
    }
    STAMP(15);
    unsigned tk2;
    asm volatile("buffer_load_dword %0, %1, %2, %3 offen sc1" : "=v"(tk2) : "v"(l8), "s"(srdH), "s"(tok_off) : "memory");
    u32x4 v[2][8];
#pragma unroll
    for (int x = 0; x < 8; ++x)
#pragma unroll
      for (int s = 0; s < 2; ++s) v[s][x] = __builtin_amdgcn_raw_buffer_load_b128(srdP, l16 + s * 1024u, x * 65536u + xg * 8192u + w * 2048u, 16);
    char* op = out + ((size_t)(b * 64u + 16u * w + r16) * 1024u + (isA ? 0u : 512u) + 64u * xg + 8u * g) * 2u;
#pragma unroll
    for (int s = 0; s < 2; ++s) {
      u32x4 o;
#pragma unroll
      for (int j = 0; j < 4; ++j) {
        float lo = 0.f, hi = 0.f;
#pragma unroll
        for (int x = 0; x < 8; ++x) { lo += __uint_as_float(v[s][x][j] << 16); hi += __uint_as_float(v[s][x][j] & 0xffff0000u); }
        o[j] = cvtpk(lo, hi);
      }
      *(u32x4*)(op + s * 64) = natural8(o);
    }
    asm volatile("s_waitcnt vmcnt(0)" : "+v"(tk2) :: "memory");
    if (mine && tk2 != toks) asm volatile("buffer_store_dword %0, %1, %2, %3 offen sc1" :: "v"(tk2), "v"(fo), "s"(srdH), "s"(flg_off) : "memory");
  }
  asm volatile("s_waitcnt vmcnt(0)" ::: "memory");
  STAMP(12);
  if (PROBE_WHO == 3 || (PROBE_WHO == 0 && bid == 0) || (PROBE_WHO == 1 && !isA) || (PROBE_WHO == 2 && isA) || (PROBE_WHO == 4 && bid == 128)) {
    const unsigned long long target = pr0 + 10000ull + PROBE_MUL * (prk - pr0);
    for (int i = 0; i < 1000000; ++i) { unsigned long long t_; asm volatile("s_memrealtime %0\n\ts_waitcnt lgkmcnt(0)" : "=s"(t_) :: "memory"); if (t_ >= target) break; }
  }
}

__global__ __launch_bounds__(256, 1)
void fused_attn(const char* __restrict__ Ax, const char* __restrict__ Ak, const char* __restrict__ Av, const char* __restrict__ Awq, const char* __restrict__ Awo,
                const char* __restrict__ Bx, const char* __restrict__ Bk, const char* __restrict__ Bv, const char* __restrict__ Bwq, const char* __restrict__ Bwo,
                char* __restrict__ part, char* __restrict__ out) {
  if (blockIdx.x >= 128) body<true>(Ax, Ak, Av, Awq, Awo, Bwo, part, out);
  else                   body<false>(Bx, Bk, Bv, Bwq, Bwo, Bwo, part, out);
}

extern "C" void kernel_launch(void* const* d_in, const int* in_sizes, int n_in, void* d_out, int out_size, void* d_ws, size_t ws_size, hipStream_t stream) {
  (void)in_sizes; (void)n_in; (void)out_size; (void)ws_size;
  (void)hipFuncSetAttribute((const void*)fused_attn, hipFuncAttributeMaxDynamicSharedMemorySize, (int)LDS_BYTES);
  fused_attn<<<256, 256, LDS_BYTES, stream>>>((const char*)d_in[0], (const char*)d_in[1], (const char*)d_in[2], (const char*)d_in[3], (const char*)d_in[4],
                                              (const char*)d_in[5], (const char*)d_in[6], (const char*)d_in[7], (const char*)d_in[8], (const char*)d_in[9], (char*)d_ws, (char*)d_out);
}
```

```cpp
#include <hip/hip_runtime.h>
#include <hip/hip_bf16.h>
#include <utility>

typedef __bf16 bf16x8 __attribute__((ext_vector_type(8)));
typedef __bf16 bf16x4 __attribute__((ext_vector_type(4)));
typedef __bf16 bf16x2 __attribute__((ext_vector_type(2)));
typedef float f32x4 __attribute__((ext_vector_type(4)));
typedef float f32x2 __attribute__((ext_vector_type(2)));
typedef float f32x16 __attribute__((ext_vector_type(16)));
typedef unsigned u32x4 __attribute__((ext_vector_type(4)));
#define LAS __attribute__((address_space(3)))
#define DI __device__ __forceinline__
typedef __amdgpu_buffer_rsrc_t srd_t;
template <int... Is> using iseq = std::integer_sequence<int, Is...>;
template <int N> using make_iseq = std::make_integer_sequence<int, N>;

#define KV_NT 1
#define W_NT 0
#define X_NT 0
#define SLEEP_A 8
#define SLEEP_B 0
#define PACE_A 0
#define PACE_ATT 0
#define TOUCH_Q 1
#define TOUCH_O 3
#define TO_SKIP 16u
#define TO_B 8u
#define WARM_CODE 0
#define ALT 0
#define ORD 2
#define EARLY_O 0
#define WS_END 0

constexpr unsigned SLOT = 8192, LDS_BYTES = 20 * SLOT;
constexpr unsigned POFF = (unsigned)WS_END << 20, HOFF = POFF + (16u << 20);
constexpr float SC = 0.12751743082f;
constexpr float LMAX = 1048576.0f;

#define NO_REDUCE_STAMPS
#define GAP() __builtin_amdgcn_sched_barrier(0)
#define STAMP(i) do {} while (0)
#define CYC(i) do {} while (0)
#define PINS(x) asm volatile("" : "+s"(x))
#define PINV(x) asm volatile("" : "+v"(x))
DI unsigned rfl(unsigned x) { return __builtin_amdgcn_readfirstlane(x); }
DI unsigned cvtpk(float lo, float hi) { f32x2 v = {lo, hi}; bf16x2 b = __builtin_convertvector(v, bf16x2); return __builtin_bit_cast(unsigned, b); }
DI u32x4 pack2(const f32x4& a, const f32x4& b) { return u32x4{cvtpk(a[0], a[1]), cvtpk(a[2], a[3]), cvtpk(b[0], b[1]), cvtpk(b[2], b[3])}; }
DI u32x4 natural8(u32x4 w) {
  auto s0 = __builtin_amdgcn_permlane32_swap(w[0], w[2], false, false);
  auto s1 = __builtin_amdgcn_permlane32_swap(w[1], w[3], false, false);
  auto t0 = __builtin_amdgcn_permlane16_swap(s0[0], s0[1], false, false);
  auto t1 = __builtin_amdgcn_permlane16_swap(s1[0], s1[1], false, false);
  return u32x4{t0[0], t1[0], t0[1], t1[1]};
}
#define MF "v_mfma_f32_16x16x32_bf16 "
template <bool Z> DI void mm_vvv(f32x4& c, const bf16x8& a, const bf16x8& b) {
  if constexpr (Z) asm volatile(MF "%0, %1, %2, 0" : "=&v"(c) : "v"(a), "v"(b));
  else             asm volatile(MF "%0, %1, %2, %0" : "+v"(c) : "v"(a), "v"(b));
}
template <bool Z> DI void mm_vva(f32x4& c, const bf16x8& a, const bf16x8& b) {
  if constexpr (Z) asm volatile(MF "%0, %1, %2, 0" : "=&v"(c) : "v"(a), "a"(b));
  else             asm volatile(MF "%0, %1, %2, %0" : "+v"(c) : "v"(a), "a"(b));
}
DI void mm_vca(f32x4& d, const bf16x8& a, const bf16x8& b, const f32x4& c) { asm volatile(MF "%0, %1, %2, %3" : "=&v"(d) : "v"(a), "a"(b), "v"(c)); }
DI void mm_avv(f32x4& c, const bf16x8& a, const bf16x8& b) { asm volatile(MF "%0, %1, %2, %0" : "+a"(c) : "v"(a), "v"(b)); }
DI void set_m0(unsigned v) { asm volatile("s_mov_b32 m0, %0" :: "s"(v) : "memory"); }
template <int NT, int P> DI void dma_p(unsigned voff, srd_t srd, unsigned soff) {
  if constexpr (NT) asm volatile("buffer_load_dwordx4 %0, %1, %2 offen offset:%3 nt lds" :: "v"(voff), "s"(srd), "s"(soff), "i"(1024 * P) : "memory");
  else              asm volatile("buffer_load_dwordx4 %0, %1, %2 offen offset:%3 lds" :: "v"(voff), "s"(srd), "s"(soff), "i"(1024 * P) : "memory");
}
struct Src { unsigned v[4]; srd_t srd; };
template <int NT> DI void dma_unit(const Src& s, unsigned soff, unsigned ldsaddr) {
  asm volatile("s_mov_b32 m0, %0\n\ts_nop 0" :: "s"(ldsaddr) : "memory");
  dma_p<NT, 0>(s.v[0], s.srd, soff); dma_p<NT, 1>(s.v[1], s.srd, soff); dma_p<NT, 2>(s.v[2], s.srd, soff); dma_p<NT, 3>(s.v[3], s.srd, soff);
}
template <int NT> DI void dma_elem(const Src& s, unsigned soff, unsigned ustep, unsigned ldsaddr) { dma_unit<NT>(s, soff, ldsaddr); dma_unit<NT>(s, soff + ustep, ldsaddr + 4096u); }
template <int N> DI void wait_vm() { asm volatile("s_waitcnt vmcnt(%0)" :: "i"(N) : "memory"); }
template <int N, int L> DI void wait_vm_lgkm() { asm volatile("" ::: "memory"); __builtin_amdgcn_s_waitcnt((N & 15) | ((N >> 4) << 14) | 0x70 | (L << 8)); asm volatile("" ::: "memory"); }
DI void wait_lgkm0() { wait_vm_lgkm<63, 0>(); }
DI void barrier() { asm volatile("s_barrier" ::: "memory"); }
DI bf16x8 lds128(unsigned a) { return *(const LAS bf16x8*)(size_t)(a); }
DI bf16x4 lds_tr1(unsigned a) { return __builtin_amdgcn_ds_read_tr16_b64_v4bf16((LAS bf16x4*)(size_t)(a)); }
DI bf16x8 cat(bf16x4 lo, bf16x4 hi) { return __builtin_shufflevector(lo, hi, 0, 1, 2, 3, 4, 5, 6, 7); }
DI bf16x8 lds_tr(unsigned a0, unsigned a1) { return cat(lds_tr1(a0), lds_tr1(a1)); }
DI srd_t mk_srd(const void* base, unsigned bytes) { return __builtin_amdgcn_make_buffer_rsrc((void*)base, (short)0, (int)bytes, 0x00020000); }
template <bool VX> DI void mk_src_rows(Src& s, const char* base, unsigned bytes, unsigned stride, unsigned lane) {
  s.srd = mk_srd(base, bytes);
  const unsigned row = (lane >> 2) & 3u, cb = lane >> 4, sl = lane & 3u;
#pragma unroll
  for (int p = 0; p < 4; ++p) s.v[p] = row * stride + cb * 64u + ((sl ^ (VX ? 2 * (p & 1) : (p & 2))) << 4) + (4u * stride - 1024u) * p;
}
template <bool VX> DI void mk_src_w(Src& s, const char* base, unsigned bytes, unsigned stride, unsigned lane) {
  s.srd = mk_srd(base, bytes);
  const unsigned sub = lane >> 5, cb = (lane >> 4) & 1u, row = (lane >> 2) & 3u, sl = lane & 3u;
#pragma unroll
  for (int p = 0; p < 4; ++p) s.v[p] = (4u * sub + row) * stride + cb * 64u + ((sl ^ (VX ? 2u * sub : 2u * (p & 1))) << 4) + (8u * stride - 1024u) * p;
}
template <int NT> DI void touch(unsigned& d, unsigned voff, srd_t srd, unsigned soff) {
  if constexpr (NT) asm volatile("buffer_load_dword %0, %1, %2, %3 offen nt" : "+v"(d) : "v"(voff), "s"(srd), "s"(soff) : "memory");
  else              asm volatile("buffer_load_dword %0, %1, %2, %3 offen" : "+v"(d) : "v"(voff), "s"(srd), "s"(soff) : "memory");
}
DI unsigned ksoff(unsigned m, unsigned rk) { return ((m + rk) & 15u) * 8192u; }

struct PC {
  unsigned r01, r2;
  unsigned w01[2], w2[2];
  unsigned xb, xmask, xa;
  unsigned z, soff;
};
template <unsigned TS_, unsigned QS_, unsigned ZI_, unsigned ZM_, unsigned ZO_, unsigned US_, unsigned CS_, bool ROT_> struct PK {
  static constexpr unsigned TS = TS_, QS = QS_, ZI = ZI_, ZM = ZM_, ZO = ZO_, US = US_, CS = CS_; static constexpr bool ROT = ROT_; };
DI unsigned q_soff(unsigned z) { return ((z & 0xC300u) * 0x440u) & 0x3C0000u; }
template <int POS> DI unsigned ring_lds(const PC& c) { return POS == 2 ? c.r2 : c.r01 + POS * SLOT; }
template <int POS> DI unsigned ring_rd(const PC& c, int d) { return (POS == 2 ? c.w2[d & 1] : c.w01[d & 1] + POS * SLOT) + (d >> 1) * 256u; }
struct NoX { static constexpr bool ACT = true; template <int G> DI void run() const {} };
template <class K, int POS, int S, bool Z, bool RD, bool DM, int NT, bool XT, int M, class X>
DI void proj_gap(f32x4 (&acc)[8][4], const bf16x8 (&cur)[4], bf16x8 (&nxt)[4], bf16x4 (&lo)[4], bf16x8 (&bf)[4], PC& c, unsigned soff, const Src& src, const Src& xs, unsigned xsoff, const X& ex) {
  constexpr int qb = M >> 2, db = M & 3;
  mm_vvv<Z>(acc[4 * S + db][qb], cur[db], bf[qb]);
  if constexpr (RD && M < 8) {
    constexpr int d = M >> 1; const unsigned a = S == 0 ? ring_rd<POS>(c, d) + 4096u : ring_rd<(POS + 1) % 3>(c, d);
    if constexpr (M & 1) nxt[d] = cat(lo[d], lds_tr1(a + K::TS)); else lo[d] = lds_tr1(a);
  }
  if constexpr (X::ACT && RD && S == 1 && db == 3) bf[qb] = lds128(c.xa + qb * K::QS);
  if constexpr (XT) {
    if constexpr (M < 8 && !(M & 1)) dma_p<X_NT, M / 2>(xs.v[M / 2], xs.srd, xsoff);
    if constexpr (M == 7) set_m0(ring_lds<POS>(c) + S * 4096u);
    if constexpr (M >= 8 && !(M & 1)) dma_p<NT, (M - 8) / 2>(src.v[(M - 8) / 2], src.srd, soff);
  } else if constexpr (DM && M >= 8 && !(M & 1)) dma_p<NT, (M - 8) / 2>(src.v[(M - 8) / 2], src.srd, soff);
  if constexpr (X::ACT && S == 0 && M == 9) { PINS(c.z); c.z = (c.z + K::ZI) & K::ZM; PINS(c.z); }
  if constexpr (X::ACT && S == 0 && M == 11) { c.xa = (c.z & c.xmask) | c.xb; PINV(c.xa); }
  if constexpr (S == 1 && DM && !K::ROT && M == 9) { PINS(c.soff); c.soff = (c.soff + K::CS) & (16u * K::CS - 1u); PINS(c.soff); }
  if constexpr (S == 1 && DM && K::ROT && M == 9) { PINS(c.z); c.soff = (c.z + 0x3100u) & 0xC300u; PINS(c.soff); }
  if constexpr (S == 1 && DM && K::ROT && M == 13) { PINS(c.soff); c.soff = (c.soff * 0x440u) & 0x3C0000u; PINS(c.soff); }
  ex.template run<16 * S + M>();
  GAP();
}
template <class K, int POS, int S, bool Z, bool RD, bool DM, int NT, int VM, bool BAR, bool XT, class X, int... Ms>
DI void proj_unit(f32x4 (&acc)[8][4], const bf16x8 (&cur)[4], bf16x8 (&nxt)[4], bf16x8 (&bf)[4], PC& c, const Src& src, const Src& xs, unsigned xsoff, unsigned xdst, const X& ex, iseq<Ms...>) {
  wait_vm_lgkm<(RD ? VM : 63), (S == 0 ? 2 : 0)>();
  if constexpr (BAR) barrier();
  const unsigned soff = c.soff + S * K::US;
  if constexpr (XT) set_m0(xdst); else if constexpr (DM) set_m0(ring_lds<POS>(c) + S * 4096u);
  GAP();
  bf16x4 lo[4];
  (proj_gap<K, POS, S, Z, RD, DM, NT, XT, Ms>(acc, cur, nxt, lo, bf, c, soff, src, xs, xsoff, ex), ...);
}
template <class K, int POS, bool Z, bool LAST, bool DM, int NT, int VM0, int VM1, bool BAR1 = false, bool XT0 = false, bool XT1 = false, class X = NoX>
DI void proj_elem(f32x4 (&acc)[8][4], bf16x8 (&a0)[4], bf16x8 (&a1)[4], bf16x8 (&bf)[4], PC& c, const Src& src, const X& ex = X{},
                  const Src* xs = nullptr, unsigned xso0 = 0, unsigned xd0 = 0, unsigned xso1 = 0, unsigned xd1 = 0) {
  proj_unit<K, POS, 0, Z, true, DM, NT, VM0, false, XT0>(acc, a0, a1, bf, c, src, xs ? *xs : src, xso0, xd0, ex, make_iseq<16>{});
  proj_unit<K, POS, 1, Z, !LAST, DM, NT, VM1, BAR1, XT1>(acc, a1, a0, bf, c, src, xs ? *xs : src, xso1, xd1, ex, make_iseq<16>{});
}
template <class K> DI void proj_setup(PC& c, unsigned tb, unsigned xb, unsigned z0, unsigned s3) {
  c.w01[0] = c.r01 + tb; c.w01[1] = c.w01[0] ^ 32u; c.w2[0] = c.r2 + tb; c.w2[1] = c.w2[0] ^ 32u;
  PINV(c.w01[0]); PINV(c.w01[1]); PINV(c.w2[0]); PINV(c.w2[1]);
  c.xb = xb; c.xmask = K::ZO; PINV(c.xmask);
  c.z = z0; c.soff = s3;
}
template <class K, int POS> DI void proj_start(bf16x8 (&a0)[4], bf16x8 (&bf)[4], PC& c, unsigned tb, unsigned xb, unsigned z0, unsigned s3) {
  proj_setup<K>(c, tb, xb, z0, s3);
  c.xa = (c.z & K::ZO) | xb; PINV(c.xa);
#pragma unroll
  for (int d = 0; d < 4; ++d) { const unsigned a = ring_rd<POS>(c, d); a0[d] = lds_tr(a, a + K::TS); }
  GAP();
#pragma unroll
  for (int q = 0; q < 4; ++q) bf[q] = lds128(c.xa + q * K::QS);
  GAP();
}
#define ACC_ALL(C, a) C(a[0][0]), C(a[0][1]), C(a[0][2]), C(a[0][3]), C(a[1][0]), C(a[1][1]), C(a[1][2]), C(a[1][3]), C(a[2][0]), C(a[2][1]), C(a[2][2]), C(a[2][3]), C(a[3][0]), C(a[3][1]), C(a[3][2]), C(a[3][3])
DI void acc_fence_v(f32x4 (&acc)[8][4]) {
  asm volatile("s_nop 7\n\ts_nop 3" : ACC_ALL("+v", acc) :: "memory");
  f32x4 (&hi)[4][4] = *(f32x4 (*)[4][4])&acc[4];
  asm volatile("" : ACC_ALL("+v", hi) :: "memory");
  GAP();
}
DI void acc_fence_a(f32x4 (&acc)[8][4]) {
  asm volatile("s_nop 7\n\ts_nop 3" : ACC_ALL("+a", acc) :: "memory");
  f32x4 (&hi)[4][4] = *(f32x4 (*)[4][4])&acc[4];
  asm volatile("" : ACC_ALL("+a", hi) :: "memory");
  GAP();
}

struct Stash { u32x4 p[64]; unsigned base[3]; };
template <int I> DI void stash_load1(Stash& st, unsigned vk, const Src& sK, const Src& sV, unsigned rk, unsigned wo_) {
  constexpr int m = I >> 2, isV = (I >> 1) & 1, pc_ = I & 1;
  const unsigned so = ksoff(m, rk) + wo_;
  if constexpr (I < 56) asm volatile("buffer_load_dwordx4 %0, %1, %2, %3 offen offset:%4 nt" : "=a"(st.p[I]) : "v"(isV ? sV.v[pc_] : vk), "s"(isV ? sV.srd : sK.srd), "s"(so), "i"(1024 * pc_) : "memory");
  else                  asm volatile("buffer_load_dwordx4 %0, %1, %2, %3 offen offset:%4 nt" : "=v"(st.p[I]) : "v"(isV ? sV.v[pc_] : vk), "s"(isV ? sV.srd : sK.srd), "s"(so), "i"(1024 * pc_) : "memory");
}
template <int... Is> DI void stash_load(Stash& st, unsigned vk, const Src& sK, const Src& sV, unsigned rk, unsigned wo_, iseq<Is...>) { (stash_load1<Is>(st, vk, sK, sV, rk, wo_), ...); }
constexpr int kv_pair(int m) { constexpr int t[10] = {0, 1, 2, 3, 8, 9, 4, 5, 6, 7}; return t[m % 10]; }
constexpr unsigned long long kv_tab() { unsigned long long v = 0; for (int i = 0; i < 16; ++i) v |= (unsigned long long)kv_pair(i) << (4 * i); return v; }
template <int I> DI void stash_write1(const Stash& st) {
  constexpr int m = I >> 2, isV = (I >> 1) & 1, pc_ = I & 1, off = kv_pair(m) * 16384 + isV * 8192 + pc_ * 1024;
  if constexpr (I < 56) asm volatile("ds_write_b128 %0, %1 offset:%2" :: "v"(st.base[off >> 16]), "a"(st.p[I]), "i"(off & 65535) : "memory");
  else                  asm volatile("ds_write_b128 %0, %1 offset:%2" :: "v"(st.base[off >> 16]), "v"(st.p[I]), "i"(off & 65535) : "memory");
}
template <int... Is> DI void stash_late(const Stash& st, iseq<Is...>) { (stash_write1<40 + Is>(st), ...); }

struct AC {
  f32x4 o[8][4];
  bf16x8 q[4][4];
  bf16x8 kf[8], vf[8], pf[4];
  f32x4 negm[4];
  f32x4 l[4];
  bf16x8 ones;
};
typedef f32x4 ST[2][4];
constexpr int NEB = 22;
template <int E> DI float sm_val(ST& S) { return S[(E >> 2) & 1][E >> 3][E & 3]; }
template <int E> DI void sm_exp1(ST& S) { float a; asm volatile("v_exp_f32_e32 %0, %1" : "=v"(a) : "v"(sm_val<E>(S))); S[(E >> 2) & 1][E >> 3][E & 3] = a; }
template <int QB, int P> DI void sm_cvt(ST& S, AC& c) {
  u32x4 w = __builtin_bit_cast(u32x4, c.pf[QB]);
  unsigned r;
  asm volatile("v_cvt_pk_bf16_f32 %0, %1, %2" : "=v"(r) : "v"(sm_val<8 * QB + 2 * P>(S)), "v"(sm_val<8 * QB + 2 * P + 1>(S)));
  w[P] = r;
  c.pf[QB] = __builtin_bit_cast(bf16x8, w);
}
template <bool DM, int VM, int R, int M> DI void mem_pre(unsigned ldsaddr) {
  if constexpr (M == 6) wait_lgkm0();
  if constexpr (DM && (M == 6 || M == 14)) set_m0(ldsaddr + (M == 14 ? 4096u : 0u));
  if constexpr (M == R && VM < 63) wait_vm<VM>();
}
template <bool DM, int NT, int M> DI void mem_post(const Src& src, unsigned soff, unsigned ustep) {
  if constexpr (DM && M >= 6 && M <= 20 && !(M & 1)) dma_p<NT, ((M - 6) / 2) & 3>(src.v[((M - 6) / 2) & 3], src.srd, soff + (M >= 14 ? ustep : 0u));
}
template <int WR, int M> DI void mem_wr(const Stash& st) {
  if constexpr (WR >= 0 && M >= 8 && M < 16)
    asm volatile("ds_write_b128 %0, %1 offset:%2" :: "v"(st.base[0]), "a"(st.p[WR + M - 8]), "i"((M - 8) * 1024) : "memory");
}
struct PA { unsigned rd0, rd1, soff, fill; };
template <bool QK, bool DM, int NT, int VM, int WR, int M, class F>
DI void gapA(AC& c, ST& S, ST& Sn, PA& pa, F& addr, const Src& src, unsigned ustep, const Stash& st, bf16x4& lo) {
  constexpr int R = QK ? 16 : 0;
  if constexpr ((DM || !QK) && M == 0) addr(pa);
  if constexpr (QK) mem_pre<DM, VM, 16, M>(pa.fill);
  if constexpr (QK) {
    constexpr int f = M >> 2, s = f >> 1, kb = f & 1, qb = M & 3;
    if constexpr (s == 0) mm_vca(Sn[kb][qb], c.kf[f], c.q[qb][0], c.negm[qb]); else mm_vva<false>(Sn[kb][qb], c.kf[f], c.q[qb][s]);
  }
  mem_post<DM, NT, M>(src, pa.soff, ustep);
  mem_wr<WR, M>(st);
  if constexpr (QK && !DM && M == 10) addr(pa);
  if constexpr (QK && M == 12) asm volatile("" :: "v"(c.negm[0]), "v"(c.negm[1]), "v"(c.negm[2]), "v"(c.negm[3]));
  if constexpr (M >= R && M < R + 16) {
    constexpr int db = (M - R) >> 1; const unsigned a = ((db & 1) ? pa.rd1 : pa.rd0) + (db >> 1) * 256u;
    if constexpr (M & 1) c.vf[db] = cat(lo, lds_tr1(a + 4096u)); else lo = lds_tr1(a);
  }
  if constexpr (NEB + M < 32) sm_exp1<NEB + M>(S);
  if constexpr (M >= 16 && (M & 1)) sm_cvt<2 + ((M - 16) >> 3), ((M - 16) >> 1) & 3>(S, c);
  GAP();
}
template <bool QK, bool DM, int NT, int VM, int WR, class F, int... Ms>
DI void phaseA_(AC& c, ST& S, ST& Sn, F& addr, const Src& src, unsigned ustep, const Stash& st, iseq<Ms...>) {
  PA pa; bf16x4 lo;
  (gapA<QK, DM, NT, VM, WR, Ms>(c, S, Sn, pa, addr, src, ustep, st, lo), ...);
}
template <bool QK, bool DM, int NT, int VM, int WR = -1, class F>
DI void phaseA(AC& c, ST& S, ST& Sn, F addr, const Src& src, unsigned ustep, const Stash& st) {
  phaseA_<QK, DM, NT, VM, WR>(c, S, Sn, addr, src, ustep, st, make_iseq<32>{});
}
template <bool MAX> DI float xlane(float v) {
  auto a = __builtin_amdgcn_permlane32_swap(__float_as_uint(v), __float_as_uint(v), false, false);
  v = MAX ? fmaxf(__uint_as_float(a[0]), __uint_as_float(a[1])) : __uint_as_float(a[0]) + __uint_as_float(a[1]);
  auto b = __builtin_amdgcn_permlane16_swap(__float_as_uint(v), __float_as_uint(v), false, false);
  return MAX ? fmaxf(__uint_as_float(b[0]), __uint_as_float(b[1])) : __uint_as_float(b[0]) + __uint_as_float(b[1]);
}
DI float row_max(const f32x4& x, const f32x4& y) {
  return xlane<true>(fmaxf(fmaxf(fmaxf(x[0], x[1]), fmaxf(x[2], x[3])), fmaxf(fmaxf(y[0], y[1]), fmaxf(y[2], y[3]))));
}
template <bool SM, bool KR, bool DM, int NT, int VM, int WR, int M, class F>
DI void gapB(AC& c, ST& Sn, PA& pa, F& addr, const Src& src, unsigned ustep, const Stash& st) {
  mem_pre<DM, (KR ? VM : 63), 18, M>(pa.fill);
  {
    constexpr int h = M / 18, r = M % 18, qb = 2 * h + (r & 1);
    if constexpr (r < 2) mm_vvv<false>(c.l[qb], c.ones, c.pf[qb]); else mm_avv(c.o[(r - 2) >> 1][qb], c.vf[(r - 2) >> 1], c.pf[qb]);
  }
  mem_post<DM, NT, M>(src, pa.soff, ustep);
  mem_wr<WR, M>(st);
  if constexpr (M == 0) addr(pa);
  if constexpr (KR && M >= 18 && M < 34 && !(M & 1)) { constexpr int f = (M - 18) >> 1; c.kf[f] = lds128(pa.rd0 + (f & 1) * 4096u + (f >> 1) * 256u); }
  if constexpr (SM) {
    if constexpr (M >= 4 && M < 18) sm_exp1<M - 4>(Sn);
    if constexpr (M >= 19 && M < 35 && (M & 1)) sm_exp1<14 + (M - 19) / 2>(Sn);
    if constexpr (M >= 20 && !(M & 1)) sm_cvt<((M - 20) >> 3), ((M - 20) >> 1) & 3>(Sn, c);
  }
  GAP();
}
template <bool SM, bool KR, bool DM, int NT, int VM, int WR, class F, int... Ms>
DI void phaseB_(AC& c, ST& Sn, F& addr, const Src& src, unsigned ustep, const Stash& st, iseq<Ms...>) {
  PA pa;
  (gapB<SM, KR, DM, NT, VM, WR, Ms>(c, Sn, pa, addr, src, ustep, st), ...);
}
template <bool SM, bool KR, bool DM, int NT, int VM, int WR = -1, class F>
DI void phaseB(AC& c, ST& Sn, F addr, const Src& src, unsigned ustep, const Stash& st) {
  phaseB_<SM, KR, DM, NT, VM, WR>(c, Sn, addr, src, ustep, st, make_iseq<36>{});
}
DI void k_read(AC& ac, unsigned ka) {
#pragma unroll
  for (int f = 0; f < 8; ++f) ac.kf[f] = lds128(ka + (f & 1) * 4096u + (f >> 1) * 256u);
}
template <bool DM, int M> DI void qk0_gap(AC& ac, ST& SA, const Src& src, unsigned soff, unsigned fill) {
  constexpr int f = M >> 2, qb = M & 3;
  if constexpr (DM && (M == 0 || M == 16)) set_m0(fill + M * 256u);
  mm_vva<(f < 2)>(SA[f & 1][qb], ac.kf[f], ac.q[qb][f >> 1]);
  if constexpr (DM && qb == 0) dma_p<KV_NT, f & 3>(src.v[f & 3], src.srd, soff + (f >> 2) * 4096u);
  GAP();
}
template <bool DM, int... Ms> DI void qk0(AC& ac, ST& SA, const Src& src, unsigned soff, unsigned fill, iseq<Ms...>) { (qk0_gap<DM, Ms>(ac, SA, src, soff, fill), ...); }
template <int... Js> DI void exp_first(ST& SA, iseq<Js...>) { (sm_exp1<Js>(SA), ...); }
template <int... Js> DI void cvt_first(ST& SA, AC& c, iseq<Js...>) { (sm_cvt<(Js >> 2), (Js & 3)>(SA, c), ...); }
DI void sm_first(AC& ac, ST& SA) {
  asm volatile("s_nop 7\n\ts_nop 3" : "+v"(SA[0][0]), "+v"(SA[0][1]), "+v"(SA[0][2]), "+v"(SA[0][3]), "+v"(SA[1][0]), "+v"(SA[1][1]), "+v"(SA[1][2]), "+v"(SA[1][3]) :: "memory");
  GAP();
#pragma unroll
  for (int q = 0; q < 4; ++q) {
    const float m = row_max(SA[0][q], SA[1][q]);
    SA[0][q] -= m; SA[1][q] -= m;
    ac.negm[q] = f32x4{-m, -m, -m, -m};
    ac.l[q] = f32x4{};
    ac.pf[q] = bf16x8{};
  }
  ac.ones = __builtin_bit_cast(bf16x8, u32x4{0x3f803f80u, 0x3f803f80u, 0x3f803f80u, 0x3f803f80u});
  asm volatile("" : "+v"(ac.negm[0]), "+v"(ac.negm[1]), "+v"(ac.negm[2]), "+v"(ac.negm[3]), "+v"(ac.l[0]), "+v"(ac.l[1]), "+v"(ac.l[2]), "+v"(ac.l[3]), "+v"(ac.ones));
  exp_first(SA, make_iseq<NEB>{});
  cvt_first(SA, ac, make_iseq<8>{});
  GAP();
}
template <int CH, int Q, int J> DI void o_word(const AC& c, const float (&inv)[4], bf16x8 (&nb)[4]) {
  u32x4 w = __builtin_bit_cast(u32x4, nb[Q]);
  unsigned r; float t;
  asm volatile("v_accvgpr_read_b32 %0, %2\n\tv_accvgpr_read_b32 %1, %3\n\tv_mul_f32_e32 %0, %0, %4\n\tv_mul_f32_e32 %1, %1, %4\n\tv_cvt_pk_bf16_f32 %0, %0, %1"
               : "=&v"(r), "=&v"(t) : "a"(c.o[2 * CH + (J >> 1)][Q][2 * (J & 1)]), "a"(c.o[2 * CH + (J >> 1)][Q][2 * (J & 1) + 1]), "v"(inv[Q]));
  w[J] = r;
  nb[Q] = __builtin_bit_cast(bf16x8, w);
}
template <int CH, int Q> DI void o_put(const bf16x8 (&nb)[4], unsigned ow) { asm volatile("ds_write_b128 %0, %1 offset:%2" :: "v"(ow), "v"(nb[Q]), "i"(CH * 4096 + Q * 1024) : "memory"); }
template <int E> struct CvX {
  static constexpr bool ACT = E == 3;
  const AC& c; const float (&inv)[4]; const bf16x8 (&cur)[4]; bf16x8 (&nb)[4]; unsigned ow;
  template <int G> DI void run() const {
    if constexpr (E < 3 && (G & 1)) o_word<E + 1, (G >> 3), (G >> 1) & 3>(c, inv, nb);
    if constexpr (E < 3 && ((G & 15) == 8 || (G & 15) == 10)) o_put<E, 2 * (G >> 4) + ((G >> 1) & 1)>(cur, ow);
    if constexpr (E == 2 && (G & 15) == 12) o_put<3, (G >> 4)>(nb, ow);
    if constexpr (E == 3 && (G == 8 || G == 10)) o_put<3, 2 + ((G >> 1) & 1)>(cur, ow);
  }
};
template <class K, int VM, int M> DI void gapL(AC& c, float (&inv)[4], bf16x8 (&nb)[4], bf16x8 (&a0)[4], bf16x4& lo, const PC& pc) {
  if constexpr (M == 6) wait_vm_lgkm<VM, 0>();
  if constexpr (M < 4) mm_vvv<false>(c.l[M], c.ones, c.pf[M]); else mm_avv(c.o[(M - 4) >> 2][M & 3], c.vf[(M - 4) >> 2], c.pf[M & 3]);
  if constexpr (M >= 6 && M < 14) {
    constexpr int d = (M - 6) >> 1; const unsigned a = ring_rd<0>(pc, d);
    if constexpr (M & 1) a0[d] = cat(lo, lds_tr1(a + K::TS)); else lo = lds_tr1(a);
  }
  if constexpr (M >= 4 && M < 12 && !(M & 1)) asm volatile("v_rcp_f32_e32 %0, %1" : "=v"(inv[(M - 4) >> 1]) : "v"(c.l[(M - 4) >> 1][0]));
  if constexpr (M >= 16 && M < 32) o_word<0, ((M - 16) >> 2), (M & 3)>(c, inv, nb);
  GAP();
}
template <class K, int VM, int... Ms> DI void phaseL(AC& c, float (&inv)[4], bf16x8 (&nb)[4], bf16x8 (&a0)[4], const PC& pc, iseq<Ms...>) {
  bf16x4 lo;
#pragma unroll
  for (int q = 0; q < 4; ++q) nb[q] = bf16x8{};
  (gapL<K, VM, Ms>(c, inv, nb, a0, lo, pc), ...);
}
struct Pub { u32x4 pk[16]; srd_t srd; unsigned v[2], so; };
template <int F, int H> DI void pub_half(const f32x4 (&acc)[8][4], Pub& p) {
  const f32x4& a = acc[4 * (F >> 3) + 2 * (F & 1) + H][(F >> 1) & 3];
  unsigned r0, r1;
  asm volatile("v_cvt_pk_bf16_f32 %0, %2, %3\n\tv_cvt_pk_bf16_f32 %1, %4, %5" : "=&v"(r0), "=v"(r1) : "v"(a[0]), "v"(a[1]), "v"(a[2]), "v"(a[3]));
  p.pk[F][2 * H] = r0; p.pk[F][2 * H + 1] = r1;
  if constexpr (H) asm volatile("buffer_store_dwordx4 %0, %1, %2, %3 offen offset:%4 sc1" :: "v"(p.pk[F]), "v"(p.v[(F >> 2) & 1]), "s"(p.srd), "s"(p.so + (F >> 3) * 8192u), "i"((F & 3) * 1024) : "memory");
}
struct PubX {
  static constexpr bool ACT = false;
  const f32x4 (&acc)[8][4]; Pub& p;
  template <int G> DI void run() const { if constexpr (G >= 16) pub_half<((G - 16) >> 1), (G & 1)>(acc, p); }
};
template <int... Is> DI void pub_rest(const f32x4 (&acc)[8][4], Pub& p, iseq<Is...>) { (pub_half<8 + (Is >> 1), (Is & 1)>(acc, p), ...); }
DI void redo_head(const AC& ac, const char* __restrict__ Kp, const char* __restrict__ Vp, unsigned lane, unsigned ow) {
  const unsigned r16 = lane & 15u, g = lane >> 4;
#pragma unroll 1
  for (unsigned qb = 0; qb < 4; ++qb) {
    bf16x8 qq[4];
#pragma unroll
    for (int s = 0; s < 4; ++s) qq[s] = qb == 0 ? ac.q[0][s] : qb == 1 ? ac.q[1][s] : qb == 2 ? ac.q[2][s] : ac.q[3][s];
    f32x4 o[8];
#pragma unroll
    for (int d = 0; d < 8; ++d) o[d] = f32x4{};
    float m = -__builtin_inff(), l = 0.f;
#pragma unroll 1
    for (unsigned t = 0; t < 16; ++t) {
      f32x4 s0 = {}, s1 = {};
      const char* kp = Kp + (t * 32u + r16) * 256u + g * 16u;
#pragma unroll
      for (int s = 0; s < 4; ++s) {
        s0 = __builtin_amdgcn_mfma_f32_16x16x32_bf16(*(const bf16x8*)(kp + s * 64), qq[s], s0, 0, 0, 0);
        s1 = __builtin_amdgcn_mfma_f32_16x16x32_bf16(*(const bf16x8*)(kp + s * 64 + 4096), qq[s], s1, 0, 0, 0);
      }
      const float mn = fmaxf(m, row_max(s0, s1)), al = __builtin_amdgcn_exp2f(m - mn);
      m = mn;
#pragma unroll
      for (int j = 0; j < 4; ++j) { s0[j] = __builtin_amdgcn_exp2f(s0[j] - mn); s1[j] = __builtin_amdgcn_exp2f(s1[j] - mn); }
      l = l * al + ((s0[0] + s0[1]) + (s0[2] + s0[3])) + ((s1[0] + s1[1]) + (s1[2] + s1[3]));
      const bf16x8 pf = __builtin_bit_cast(bf16x8, pack2(s0, s1));
      const __bf16* vp = (const __bf16*)(Vp + (t * 32u + g * 4u) * 256u) + r16;
#pragma unroll
      for (int d = 0; d < 8; ++d) {
        bf16x8 vf;
#pragma unroll
        for (int j = 0; j < 8; ++j) vf[j] = vp[((j & 3) + (j >> 2) * 16) * 128 + d * 16];
        o[d] = __builtin_amdgcn_mfma_f32_16x16x32_bf16(vf, pf, o[d] * al, 0, 0, 0);
      }
    }
    const float inv = 1.0f / xlane<false>(l);
#pragma unroll
    for (int s = 0; s < 4; ++s) *(LAS u32x4*)(size_t)(ow + s * 4096u + qb * 1024u) = pack2(o[2 * s] * inv, o[2 * s + 1] * inv);
  }
}
template <bool isA, int J> DI void q_conv1(AC& ac, f32x4 (&acc)[8][4], Stash& st, const Src& sK, const Src& sV, unsigned rk, unsigned p3) {
  constexpr int s = J >> 2, qb = J & 3;
  if constexpr (isA) { stash_write1<(5 * J) / 2>(st); stash_write1<(5 * J) / 2 + 1>(st); if constexpr (J & 1) stash_write1<(5 * J) / 2 + 2>(st); }
  else {
    constexpr int p = 2 * (J & 1);
    const Src& src = s < 2 ? sK : sV;
    const unsigned so = ksoff(s == 0 ? 2u : s == 2 ? 14u : 15u, rk) + (J & 2) * 2048u;
    if constexpr (s == 0 || s == 3) {
      if constexpr (p == 0) asm volatile("s_mov_b32 m0, %0\n\ts_nop 0" :: "s"(p3 + (s == 3 ? SLOT : 0u) + (J & 2) * 2048u) : "memory");
      dma_p<KV_NT, p>(src.v[p], src.srd, so); dma_p<KV_NT, p + 1>(src.v[p + 1], src.srd, so);
    } else {
      asm volatile("buffer_load_dwordx4 %0, %1, %2, %3 offen offset:%4 nt" : "=a"(st.p[2 * J - 8]) : "v"(src.v[p]), "s"(src.srd), "s"(so), "i"(1024 * p) : "memory");
      asm volatile("buffer_load_dwordx4 %0, %1, %2, %3 offen offset:%4 nt" : "=a"(st.p[2 * J - 7]) : "v"(src.v[p + 1]), "s"(src.srd), "s"(so), "i"(1024 * p + 1024) : "memory");
    }
  }
  ac.q[qb][s] = __builtin_bit_cast(bf16x8, natural8(pack2(acc[2 * s][qb] * SC, acc[2 * s + 1][qb] * SC)));
  asm volatile("" : "+a"(ac.q[qb][s]));
  GAP();
}
template <bool isA, int... Js> DI void q_conv(AC& ac, f32x4 (&acc)[8][4], Stash& st, const Src& sK, const Src& sV, unsigned rk, unsigned p3, iseq<Js...>) { (q_conv1<isA, Js>(ac, acc, st, sK, sV, rk, p3), ...); }
DI void zero_o(AC& ac) {
#pragma unroll
  for (int i = 0; i < 8; ++i)
#pragma unroll
    for (int q = 0; q < 4; ++q) { ac.o[i][q] = f32x4{}; asm volatile("" : "+a"(ac.o[i][q])); }
}

DI unsigned vbid() {
  const unsigned i = blockIdx.x;
  return i;
}
template <bool isA>
DI void body(const char* __restrict__ X, const char* __restrict__ K, const char* __restrict__ V, const char* __restrict__ Wq, const char* __restrict__ Wo,
             const char* __restrict__ Wo2, const char* code, char* __restrict__ part, char* __restrict__ out) {
  extern __shared__ __attribute__((aligned(16))) char lds[];
  STAMP(0);
  unsigned tid = threadIdx.x;
  PINV(tid);
  const unsigned lane = tid & 63u, w = rfl(tid >> 6);
  const unsigned bid = vbid(), xg = bid & 7u, rr = bid >> 3, h = xg * 4u + w;
  const unsigned b = rr & 15u;
  const unsigned r16 = lane & 15u, g = lane >> 4;
  const char* xp  = X + (size_t)b * 65536u + w * 16384u;
  const char* wqp = Wq + h * 256u;
  const char* wop = Wo + (size_t)xg * 524288u + w * 256u;
  const size_t kvo = isA ? (size_t)(b * 8u + xg) * 131072u : (size_t)(b * 32u + h) * 131072u;
  const unsigned rk = isA ? b : b + 4u * w;
  typedef PK<512u, 4096u, 0x1300u, 0xF300u, 0xC300u, 128u, 32u * 8192u, true> KQ;
  typedef PK<512u, 4096u, 0x1300u, 0xF300u, 0xC300u, 4096u, 8192u, false> KT;
  typedef PK<2048u, 1024u, 0x1000u, 0xF000u, 0xF000u, 128u, 32u * 1024u, false> KO;
  asm volatile("" :: "v"(lds));
  PC pc;
  pc.r01 = 65536u + w * 16384u; pc.r2 = 131072u + w * 8192u;
  const unsigned zq = ((b & 3u) << 14) | ((b >> 2) << 8), co = 4u * w;
  const srd_t srdH = mk_srd(part + HOFF, 4096u + 256u * 128u);
  const unsigned tok_off = rr * 128u, flg_off = 4096u + rr * 1024u;
  const unsigned l8 = (lane & 7u) * 4u;
  unsigned entry_flags;
  asm volatile("buffer_load_dword %0, %1, %2, %3 offen sc1" : "=v"(entry_flags) : "v"(l8), "s"(srdH), "s"(flg_off + xg * 128u) : "memory");
  if constexpr (WARM_CODE && isA) { if (w == 3) asm volatile("global_load_dword %0, %1, %2" : "+v"(entry_flags) : "v"((b * 64u + lane) * 128u), "s"(code) : "memory"); }
  Src sQ, sX, sK, sV, sO;
  mk_src_w<false>(sQ, wqp, 511u * 8192u + 256u, 8192u, lane);
  mk_src_rows<false>(sX, xp, 16384u, 1024u, lane);
  const unsigned p0 = b & 3u, p1 = (p0 + 1u) & 3u, p2 = (p0 + 2u) & 3u, p3 = (p0 + 3u) & 3u, xw = w * 4096u;
  Stash st;
  unsigned dum0 = lane;
  if constexpr (isA && ORD == 0) {
    mk_src_rows<false>(sK, K + kvo, 131072u, 256u, lane);
    mk_src_rows<true>(sV, V + kvo, 131072u, 256u, lane);
    const unsigned to = (lane >> 1) * 1024u + (lane & 1u) * 128u + b * KO::CS;
    const char* wb = Wo2 + (size_t)xg * 524288u + w * 256u;
    if constexpr (EARLY_O == 1) asm volatile("global_load_dword %0, %1, %2\n\tglobal_load_dword %0, %1, %3" : "=&v"(dum0) : "v"(to), "s"(wop), "s"(wb) : "memory");
    stash_load(st, (w & 1u) ? sK.v[2] : sK.v[0], sK, sV, rk, w * 2048u, make_iseq<64>{});
    if constexpr (EARLY_O == 2) asm volatile("global_load_dword %0, %1, %2\n\tglobal_load_dword %0, %1, %3" : "=&v"(dum0) : "v"(to), "s"(wop), "s"(wb) : "memory");
  }
  dma_elem<TOUCH_Q>(sQ, q_soff(zq), KQ::US, pc.r2);
  if constexpr (!(isA && ORD == 2)) dma_unit<X_NT>(sX, p0 * 256u, xw + p0 * 16384u);
  dma_elem<W_NT>(sQ, q_soff(zq + 0x1300u), KQ::US, pc.r01);
  if constexpr (!(isA && ORD == 0)) {
    mk_src_rows<false>(sK, K + kvo, 131072u, 256u, lane);
    mk_src_rows<true>(sV, V + kvo, 131072u, 256u, lane);
  }
  mk_src_w<true>(sO, wop, 511u * 1024u + 256u, 1024u, lane);
  if constexpr (isA) {
    if constexpr (ORD == 2) {
      stash_load(st, (w & 1u) ? sK.v[2] : sK.v[0], sK, sV, rk, w * 2048u, make_iseq<64>{});
      dma_unit<X_NT>(sX, p0 * 256u, xw + p0 * 16384u);
    }
    dma_elem<W_NT>(sQ, q_soff(zq + 0x2200u), KQ::US, pc.r01 + SLOT);
    dma_unit<X_NT>(sX, p1 * 256u, xw + p1 * 16384u);
    dma_unit<X_NT>(sX, p2 * 256u, xw + p2 * 16384u);
    dma_unit<X_NT>(sX, p3 * 256u, xw + p3 * 16384u);
    if constexpr (ORD == 1) stash_load(st, (w & 1u) ? sK.v[2] : sK.v[0], sK, sV, rk, w * 2048u, make_iseq<64>{});
  }
  const unsigned rb = 1024u * (r16 >> 2) + 64u * (r16 & 3u) + 16u * (g ^ ((r16 >> 2) & 2u));
  const unsigned tb = 64u * ((lane >> 2) & 3u) + 32u * (g & 1u) + 8u * (lane & 3u);
  const unsigned tbV = tb + 1024u * g;
  const unsigned sb0 = w * 2048u + lane * 16u;
  st.base[0] = sb0; st.base[1] = sb0 + 65536u; st.base[2] = sb0 + 131072u;
  AC ac;
  if constexpr (!isA) zero_o(ac);
  {
    f32x4 acc[8][4];
    bf16x8 a0[4], a1[4], bf[4];
    asm volatile("s_waitcnt vmcnt(%2)" : "+v"(entry_flags), "+v"(dum0) : "i"(isA ? (ORD == 1 ? 63 : ORD == 0 ? 28 : 20) : 8) : "memory");
    if (w == 0) {
      const unsigned long long mk = __builtin_amdgcn_ballot_w64(((entry_flags >> (lane & 7u)) & 1u) != 0u);
      const unsigned tok = 0x5a5a5a00u | (~(unsigned)mk & 0xffu);
      if (lane == 0) asm volatile("buffer_store_dword %0, off, %1, %2 sc1" :: "v"(tok), "s"(srdH), "s"(tok_off + xg * 4u) : "memory");
    }
    barrier();
    STAMP(1);
    proj_start<KQ, 2>(a0, bf, pc, tbV, rb, zq, q_soff(zq + 0x3100u));
    if constexpr (isA) {
      constexpr int V1 = ORD == 0 ? 28 : 63, V2 = ORD == 1 ? 63 : 28;
      proj_elem<KQ, 2, true,  false, true, W_NT, 63, V1>(acc, a0, a1, bf, pc, sQ);
      STAMP(10);
      proj_elem<KQ, 0, false, false, true, W_NT, V1, V2>(acc, a0, a1, bf, pc, sQ);
      STAMP(11);
      proj_elem<KQ, 1, false, false, true, W_NT, V2, 16>(acc, a0, a1, bf, pc, sQ);
      proj_elem<KQ, 2, false, false, true, W_NT, 16, 16, ORD != 1>(acc, a0, a1, bf, pc, sQ);
    } else {
      dma_elem<W_NT>(sQ, q_soff(zq + 0x2200u), KQ::US, pc.r01 + SLOT);
      proj_elem<KQ, 2, true,  false, true, W_NT, 16, 20, false, true, true>(acc, a0, a1, bf, pc, sQ, NoX{}, &sX, p1 * 256u, xw + p1 * 16384u, p2 * 256u, xw + p2 * 16384u);
      STAMP(10);
      proj_elem<KQ, 0, false, false, true, W_NT, 24, 28, false, true, false>(acc, a0, a1, bf, pc, sQ, NoX{}, &sX, p3 * 256u, xw + p3 * 16384u);
      STAMP(11);
      proj_elem<KQ, 1, false, false, true, W_NT, 28, 24>(acc, a0, a1, bf, pc, sQ);
      proj_elem<KQ, 2, false, false, true, W_NT, 20, 16, true>(acc, a0, a1, bf, pc, sQ);
    }
    STAMP(8);
#pragma unroll 1
    for (unsigned e = 0; e < 3; ++e) {
      if constexpr (isA && PACE_A > 0) __builtin_amdgcn_s_sleep(PACE_A);
      proj_elem<KQ, 0, false, false, true, W_NT, 16, 16>(acc, a0, a1, bf, pc, sQ);
      if constexpr (isA && PACE_A > 0) __builtin_amdgcn_s_sleep(PACE_A);
      proj_elem<KQ, 1, false, false, true, W_NT, 16, 16>(acc, a0, a1, bf, pc, sQ);
      if constexpr (isA && PACE_A > 0) __builtin_amdgcn_s_sleep(PACE_A);
      proj_elem<KQ, 2, false, false, true, W_NT, 16, 16>(acc, a0, a1, bf, pc, sQ);
    }
    STAMP(9);
    if constexpr (isA) {
      proj_elem<KQ, 0, false, false, false, W_NT, 16, 12>(acc, a0, a1, bf, pc, sQ);
      proj_elem<KQ, 1, false, false, false, W_NT, 8, 4>(acc, a0, a1, bf, pc, sQ);
      proj_elem<KQ, 2, false, true, false, W_NT, 0, 0>(acc, a0, a1, bf, pc, sQ);
    } else {
      pc.soff = ksoff(0u, rk);
      proj_elem<KT, 0, false, false, true, KV_NT, 16, 16>(acc, a0, a1, bf, pc, sK);
      proj_elem<KT, 1, false, false, true, KV_NT, 16, 16>(acc, a0, a1, bf, pc, sK);
      pc.soff = ksoff(0u, rk);
      proj_elem<KT, 2, false, true, true, KV_NT, 16, 16>(acc, a0, a1, bf, pc, sV);
    }
    barrier();
    STAMP(2);
    acc_fence_v(acc);
    q_conv<isA>(ac, acc, st, sK, sV, rk, w * 16384u, make_iseq<16>{});
  }
  ST SA, SB;
  if constexpr (isA) {
    zero_o(ac);
    const unsigned kb = rb, vb = SLOT + tbV;
    asm volatile("s_waitcnt lgkmcnt(0)" ::: "memory");
    barrier();
    k_read(ac, kb + kv_pair(0) * 16384u);
    wait_lgkm0();
    GAP();
    qk0<false>(ac, SA, sK, 0u, 0u, make_iseq<32>{});
    k_read(ac, kb + kv_pair(1) * 16384u);
    sm_first(ac, SA);
    STAMP(13);
    unsigned i4 = 0;
    auto av = [&](PA& p) { if constexpr (PACE_ATT > 0) __builtin_amdgcn_s_sleep(PACE_ATT);
                           PINS(i4); p.rd0 = vb + (((unsigned)(kv_tab() >> i4) & 15u) << 14); p.rd1 = p.rd0 ^ 32u; PINV(p.rd0); PINV(p.rd1); };
    auto ak = [&](PA& p) { if constexpr (PACE_ATT > 0) __builtin_amdgcn_s_sleep(PACE_ATT);
                           PINS(i4); p.rd0 = kb + (((unsigned)((kv_tab() >> 8) >> i4) & 15u) << 14); PINV(p.rd0); i4 += 4u; };
    auto an = [&](PA&) { i4 += 4u; };
    unsigned dum = lane;
    const char* ta = wop + b * KO::CS;
    const char* tb2 = Wo2 + (size_t)xg * 524288u + w * 256u + b * KO::CS - 32768;
    while (i4 < 56u) {
      if (TOUCH_O && i4 < 32u) {
        const char* tp;
        const unsigned to = (lane >> 2) * 1024u + (lane & 2u) * 64u + (lane & 1u) * 4u + i4 * 2048u;
        asm volatile("s_cmp_lt_u32 %1, 16\n\ts_cselect_b64 %0, %2, %3" : "=s"(tp) : "s"(i4), "s"(ta), "s"(tb2) : "scc");
        asm volatile("global_load_dword %0, %1, %2 nt\n\tglobal_load_dword %0, %1, %2" : "+v"(dum) : "v"(to), "s"(tp) : "memory");
      }
      if (i4 >= 24u) {
        if (i4 <= 40u) { wait_lgkm0(); barrier(); }
        if (i4 == 24u) stash_late(st, make_iseq<24>{});
        if (i4 >= 40u) dma_elem<W_NT>(sO, (co + ((i4 >> 4) & 1u)) * KO::CS, KO::US, pc.r01 + ((i4 >> 4) & 1u) * SLOT);
      }
      phaseA<true, false, KV_NT, 63>(ac, SA, SB, av, sK, 0u, st);
      phaseB<true, true, false, KV_NT, 63>(ac, SB, ak, sK, 0u, st);
      phaseA<true, false, KV_NT, 63>(ac, SB, SA, av, sK, 0u, st);
      phaseB<true, true, false, KV_NT, 63>(ac, SA, ak, sK, 0u, st);
    }
    STAMP(14);
    asm volatile("s_waitcnt vmcnt(16)" : "+v"(dum) :: "memory");
    phaseA<true, false, KV_NT, 63>(ac, SA, SB, av, sK, 0u, st);
    phaseB<true, false, false, KV_NT, 63>(ac, SB, an, sK, 0u, st);
    phaseA<false, false, KV_NT, 63>(ac, SB, SA, av, sK, 0u, st);
  } else {
    const unsigned P0 = pc.r01, P1 = pc.r01 + SLOT, P2 = pc.r2, P3 = w * 16384u;
    unsigned v0 = tbV + P0, v2 = tbV + P2, x0 = v0 ^ 32u, x2 = v2 ^ 32u, k1 = rb + P1, k3 = rb + P3;
    PINV(v0); PINV(v2); PINV(x0); PINV(x2); PINV(k1); PINV(k3);
    wait_vm<48>();
    k_read(ac, k1 - SLOT);
    wait_lgkm0();
    GAP();
    qk0<true>(ac, SA, sV, ksoff(1u, rk), P0, make_iseq<32>{});
    wait_vm<48>();
    GAP();
    k_read(ac, k1);
    sm_first(ac, SA);
    STAMP(13);
    unsigned so = ksoff(2u, rk), i = 0;
#define AV(RD, FILL) [&](PA& p) { PINS(so); p.soff = (so + 8192u) & 0x1ffffu; PINS(p.soff); p.fill = (FILL); p.rd0 = v##RD; p.rd1 = x##RD; }
#define AK(RD, FILL) [&](PA& p) { p.soff = so; so = (so + 8192u) & 0x1ffffu; PINS(so); p.fill = (FILL); p.rd0 = k##RD; }
#define TV(RD, FILL, SO) [&](PA& p) { p.soff = (SO); p.fill = (FILL); p.rd0 = v##RD; p.rd1 = x##RD; }
#define TK(RD, FILL, SO) [&](PA& p) { p.soff = (SO); p.fill = (FILL); p.rd0 = k##RD; }
    for (; i < 12; i += 2) {
      phaseA<true, true, KV_NT, 21>(ac, SA, SB, AV(2, P1), sK, 4096u, st);
      phaseB<true, true, true, KV_NT, 22>(ac, SB, AK(3, P2), sV, 4096u, st);
      phaseA<true, true, KV_NT, 21>(ac, SB, SA, AV(0, P3), sK, 4096u, st);
      phaseB<true, true, true, KV_NT, 22>(ac, SA, AK(1, P0), sV, 4096u, st);
    }
    STAMP(14);
    st.base[0] = P1 + lane * 16u;
    phaseA<true, false, KV_NT, 16, 0>(ac, SA, SB, TV(2, P1, 0u), sK, 0u, st);
    st.base[0] = P2 + lane * 16u;
    phaseB<true, true, false, KV_NT, 8, 8>(ac, SB, TK(3, P2, 0u), sV, 0u, st);
    phaseA<true, false, KV_NT, 0>(ac, SB, SA, TV(0, P3, 0u), sV, 0u, st);
    phaseB<true, true, true, W_NT, 63>(ac, SA, TK(1, P0, co * KO::CS), sO, KO::US, st);
    phaseA<true, true, W_NT, 63>(ac, SA, SB, TV(2, P1, (co + 1u) * KO::CS), sO, KO::US, st);
    phaseB<true, false, true, W_NT, 63>(ac, SB, TK(3, P2, (co + 2u) * KO::CS), sO, KO::US, st);
    unsigned v4 = tbV + P3 + SLOT, x4 = v4 ^ 32u; PINV(v4); PINV(x4);
    phaseA<false, false, W_NT, 63>(ac, SB, SA, TV(4, P3, 0u), sO, KO::US, st);
#undef AV
#undef AK
#undef TV
#undef TK
  }
  {
    const unsigned l16 = lane * 16u;
    unsigned ow = w * 16384u + l16;
    PINV(ow);
    f32x4 acc[8][4];
    bf16x8 a0[4], a1[4], b0[4], bf[4];
    float inv[4];
    proj_setup<KO>(pc, tb + 512u * g, l16, (co + 3u) << 12, (co + 3u) * KO::CS);
    GAP();
    phaseL<KO, (isA ? 12 : 20)>(ac, inv, b0, a0, pc, make_iseq<36>{});
    STAMP(3);
    if constexpr (isA) {
      barrier();
      dma_elem<W_NT>(sO, (co + 2u) * KO::CS, KO::US, pc.r2);
    }
    const bool ok = ac.l[0][0] <= LMAX && ac.l[1][0] <= LMAX && ac.l[2][0] <= LMAX && ac.l[3][0] <= LMAX;
    if (__builtin_expect(__builtin_amdgcn_ballot_w64(!ok) != 0ull, 0)) {
      acc_fence_a(ac.o);
      redo_head(ac, K + kvo, V + kvo, lane, ow);
#pragma unroll
      for (int q = 0; q < 4; ++q) {
        inv[q] = 1.0f;
        b0[q] = lds128(ow + q * 1024u);
#pragma unroll
        for (int s = 1; s < 4; ++s) {
          const u32x4 t = __builtin_bit_cast(u32x4, lds128(ow + s * 4096u + q * 1024u));
          ac.o[2 * s][q] = f32x4{__uint_as_float(t[0] << 16), __uint_as_float(t[0] & 0xffff0000u), __uint_as_float(t[1] << 16), __uint_as_float(t[1] & 0xffff0000u)};
          ac.o[2 * s + 1][q] = f32x4{__uint_as_float(t[2] << 16), __uint_as_float(t[2] & 0xffff0000u), __uint_as_float(t[3] << 16), __uint_as_float(t[3] & 0xffff0000u)};
        }
      }
      wait_lgkm0();
      acc_fence_a(ac.o);
    }
#pragma unroll
    for (int q = 0; q < 4; ++q) bf[q] = bf16x8{};
    GAP();
    proj_elem<KO, 0, true, false, true, W_NT, 16, 16>(acc, a0, a1, b0, pc, sO, CvX<0>{ac, inv, b0, bf, ow});
    CYC(5);
    proj_elem<KO, 1, false, false, true, W_NT, 16, 16>(acc, a0, a1, bf, pc, sO, CvX<1>{ac, inv, bf, b0, ow});
    CYC(6);
    proj_elem<KO, 2, false, false, true, W_NT, 16, 16>(acc, a0, a1, b0, pc, sO, CvX<2>{ac, inv, b0, bf, ow});
    CYC(7);
    proj_elem<KO, 0, false, false, true, W_NT, 16, 16, true>(acc, a0, a1, bf, pc, sO, CvX<3>{ac, inv, bf, b0, ow});
    STAMP(4);
#pragma unroll 1
    for (unsigned e = 0; e < 3; ++e) {
      proj_elem<KO, 1, false, false, true, W_NT, 16, 16>(acc, a0, a1, bf, pc, sO);
      proj_elem<KO, 2, false, false, true, W_NT, 16, 16>(acc, a0, a1, bf, pc, sO);
      proj_elem<KO, 0, false, false, true, W_NT, 16, 16>(acc, a0, a1, bf, pc, sO);
    }
    unsigned toks;
    asm volatile("buffer_load_dword %0, %1, %2, %3 offen sc1" : "=v"(toks) : "v"(l8), "s"(srdH), "s"(tok_off) : "memory");
    Pub pb;
    pb.srd = mk_srd(part + POFF + (size_t)rr * 524288u, 524288u);
    pb.v[0] = l16; pb.v[1] = l16 + 4096u; pb.so = xg * 65536u + w * 16384u;
    PINV(pb.v[1]);
#pragma unroll
    for (int i = 0; i < 16; ++i) pb.pk[i] = u32x4{};
    proj_elem<KO, 1, false, false, false, W_NT, 17, 13>(acc, a0, a1, bf, pc, sO);
    proj_elem<KO, 2, false, false, false, W_NT, 9, 5>(acc, a0, a1, bf, pc, sO);
    proj_elem<KO, 0, false, true, false, W_NT, 0, 0>(acc, a0, a1, bf, pc, sO, PubX{acc, pb});
    STAMP(5);
    pub_rest(acc, pb, make_iseq<16>{});
    unsigned fl;
    asm volatile("buffer_load_dword %0, %1, %2, %3 offen sc1" : "=v"(fl) : "v"(l8), "s"(srdH), "s"(flg_off + xg * 128u) : "memory");
    PINV(toks);
    STAMP(6);
    const unsigned fo = lane * 128u + xg * 4u, tau = __builtin_amdgcn_readlane(toks, xg);
    unsigned oo = ((b * 64u + 16u * w + r16) * 1024u + (isA ? 0u : 512u) + 64u * xg + 8u * g) * 2u;
    PINV(oo);
    wait_vm<9>();
    if (lane == 2u * w) asm volatile("buffer_store_dword %0, %1, %2, %3 offen sc1" :: "v"(toks), "v"(fo), "s"(srdH), "s"(flg_off) : "memory");
    wait_vm<2>();
    if (lane == 2u * w + 1u) asm volatile("buffer_store_dword %0, %1, %2, %3 offen sc1" :: "v"(toks), "v"(fo), "s"(srdH), "s"(flg_off) : "memory");
    asm volatile("s_waitcnt vmcnt(2)" : "+v"(fl) : "v"(pb.pk[0]), "v"(pb.pk[1]), "v"(pb.pk[2]), "v"(pb.pk[3]), "v"(pb.pk[4]), "v"(pb.pk[5]), "v"(pb.pk[6]), "v"(pb.pk[7]),
                 "v"(pb.pk[8]), "v"(pb.pk[9]), "v"(pb.pk[10]), "v"(pb.pk[11]), "v"(pb.pk[12]), "v"(pb.pk[13]), "v"(pb.pk[14]), "v"(pb.pk[15]) : "memory");
    STAMP(7);
    f32x16 f[8];
    unsigned got = 0xffffff00u, spin = 1u << 22, fresh, so;
    asm volatile(
        ".Ltop_%=:\n\tv_cmp_eq_u32_e32 vcc, %[tau], %[fl]\n\ts_andn2_b32 %[fr], vcc_lo, %[got]\n\ts_cbranch_scc0 .Lpoll_%=\n\ts_or_b32 %[got], %[got], %[fr]\n\t"
        "s_bitcmp1_b32 %[fr], 0\n\ts_cbranch_scc0 .La0_%=\n\ts_add_u32 %[so], %[ro], 0\n\t"
        "buffer_load_dwordx4 v[136:139], %[l16], %[sp], %[so] offen sc1\n\tbuffer_load_dwordx4 v[140:143], %[l16], %[sp], %[so] offen offset:1024 sc1\n.La0_%=:\n\t"
        "s_bitcmp1_b32 %[fr], 1\n\ts_cbranch_scc0 .La1_%=\n\ts_add_u32 %[so], %[ro], 65536\n\t"
        "buffer_load_dwordx4 v[152:155], %[l16], %[sp], %[so] offen sc1\n\tbuffer_load_dwordx4 v[156:159], %[l16], %[sp], %[so] offen offset:1024 sc1\n.La1_%=:\n\t"
        "s_bitcmp1_b32 %[fr], 2\n\ts_cbranch_scc0 .La2_%=\n\ts_add_u32 %[so], %[ro], 131072\n\t"
        "buffer_load_dwordx4 v[168:171], %[l16], %[sp], %[so] offen sc1\n\tbuffer_load_dwordx4 v[172:175], %[l16], %[sp], %[so] offen offset:1024 sc1\n.La2_%=:\n\t"
        "s_bitcmp1_b32 %[fr], 3\n\ts_cbranch_scc0 .La3_%=\n\ts_add_u32 %[so], %[ro], 196608\n\t"
        "buffer_load_dwordx4 v[184:187], %[l16], %[sp], %[so] offen sc1\n\tbuffer_load_dwordx4 v[188:191], %[l16], %[sp], %[so] offen offset:1024 sc1\n.La3_%=:\n\t"
        "s_bitcmp1_b32 %[fr], 4\n\ts_cbranch_scc0 .La4_%=\n\ts_add_u32 %[so], %[ro], 262144\n\t"
        "buffer_load_dwordx4 v[200:203], %[l16], %[sp], %[so] offen sc1\n\tbuffer_load_dwordx4 v[204:207], %[l16], %[sp], %[so] offen offset:1024 sc1\n.La4_%=:\n\t"
        "s_bitcmp1_b32 %[fr], 5\n\ts_cbranch_scc0 .La5_%=\n\ts_add_u32 %[so], %[ro], 327680\n\t"
        "buffer_load_dwordx4 v[216:219], %[l16], %[sp], %[so] offen sc1\n\tbuffer_load_dwordx4 v[220:223], %[l16], %[sp], %[so] offen offset:1024 sc1\n.La5_%=:\n\t"
        "s_bitcmp1_b32 %[fr], 6\n\ts_cbranch_scc0 .La6_%=\n\ts_add_u32 %[so], %[ro], 393216\n\t"
        "buffer_load_dwordx4 v[232:235], %[l16], %[sp], %[so] offen sc1\n\tbuffer_load_dwordx4 v[236:239], %[l16], %[sp], %[so] offen offset:1024 sc1\n.La6_%=:\n\t"
        "s_bitcmp1_b32 %[fr], 7\n\ts_cbranch_scc0 .La7_%=\n\ts_add_u32 %[so], %[ro], 458752\n\t"
        "buffer_load_dwordx4 v[248:251], %[l16], %[sp], %[so] offen sc1\n\tbuffer_load_dwordx4 v[252:255], %[l16], %[sp], %[so] offen offset:1024 sc1\n.La7_%=:\n\t"
        ".Lpoll_%=:\n\ts_cmp_eq_u32 %[got], -1\n\ts_cselect_b32 %[so], %[tok], %[flg]\n\tbuffer_load_dword %[fl], %[l8], %[sh], %[so] offen sc1\n\t"
        "s_cmp_eq_u32 %[fr], 0\n\ts_cbranch_scc1 .Lwait_%=\n\ts_waitcnt vmcnt(1)\n\t"
        "s_bitcmp1_b32 %[fr], 0\n\ts_cbranch_scc0 .Lb0_%=\n\t"
        "v_lshlrev_b32_e32 v128, 16, v136\n\tv_and_b32_e32 v129, %[hi], v136\n\tv_lshlrev_b32_e32 v130, 16, v137\n\tv_and_b32_e32 v131, %[hi], v137\n\t"
        "v_lshlrev_b32_e32 v132, 16, v138\n\tv_and_b32_e32 v133, %[hi], v138\n\tv_lshlrev_b32_e32 v134, 16, v139\n\tv_and_b32_e32 v135, %[hi], v139\n\t"
        "v_lshlrev_b32_e32 v136, 16, v140\n\tv_and_b32_e32 v137, %[hi], v140\n\tv_lshlrev_b32_e32 v138, 16, v141\n\tv_and_b32_e32 v139, %[hi], v141\n\t"
        "v_lshlrev_b32_e32 v140, 16, v142\n\tv_and_b32_e32 v141, %[hi], v142\n\tv_lshlrev_b32_e32 v142, 16, v143\n\tv_and_b32_e32 v143, %[hi], v143\n\t"
        ".Lb0_%=:\n\t"
        "s_bitcmp1_b32 %[fr], 1\n\ts_cbranch_scc0 .Lb1_%=\n\t"
        "v_lshlrev_b32_e32 v144, 16, v152\n\tv_and_b32_e32 v145, %[hi], v152\n\tv_lshlrev_b32_e32 v146, 16, v153\n\tv_and_b32_e32 v147, %[hi], v153\n\t"
        "v_lshlrev_b32_e32 v148, 16, v154\n\tv_and_b32_e32 v149, %[hi], v154\n\tv_lshlrev_b32_e32 v150, 16, v155\n\tv_and_b32_e32 v151, %[hi], v155\n\t"
        "v_lshlrev_b32_e32 v152, 16, v156\n\tv_and_b32_e32 v153, %[hi], v156\n\tv_lshlrev_b32_e32 v154, 16, v157\n\tv_and_b32_e32 v155, %[hi], v157\n\t"
        "v_lshlrev_b32_e32 v156, 16, v158\n\tv_and_b32_e32 v157, %[hi], v158\n\tv_lshlrev_b32_e32 v158, 16, v159\n\tv_and_b32_e32 v159, %[hi], v159\n\t"
        ".Lb1_%=:\n\t"
        "s_bitcmp1_b32 %[fr], 2\n\ts_cbranch_scc0 .Lb2_%=\n\t"
        "v_lshlrev_b32_e32 v160, 16, v168\n\tv_and_b32_e32 v161, %[hi], v168\n\tv_lshlrev_b32_e32 v162, 16, v169\n\tv_and_b32_e32 v163, %[hi], v169\n\t"
        "v_lshlrev_b32_e32 v164, 16, v170\n\tv_and_b32_e32 v165, %[hi], v170\n\tv_lshlrev_b32_e32 v166, 16, v171\n\tv_and_b32_e32 v167, %[hi], v171\n\t"
        "v_lshlrev_b32_e32 v168, 16, v172\n\tv_and_b32_e32 v169, %[hi], v172\n\tv_lshlrev_b32_e32 v170, 16, v173\n\tv_and_b32_e32 v171, %[hi], v173\n\t"
        "v_lshlrev_b32_e32 v172, 16, v174\n\tv_and_b32_e32 v173, %[hi], v174\n\tv_lshlrev_b32_e32 v174, 16, v175\n\tv_and_b32_e32 v175, %[hi], v175\n\t"
        ".Lb2_%=:\n\t"
        "s_bitcmp1_b32 %[fr], 3\n\ts_cbranch_scc0 .Lb3_%=\n\t"
        "v_lshlrev_b32_e32 v176, 16, v184\n\tv_and_b32_e32 v177, %[hi], v184\n\tv_lshlrev_b32_e32 v178, 16, v185\n\tv_and_b32_e32 v179, %[hi], v185\n\t"
        "v_lshlrev_b32_e32 v180, 16, v186\n\tv_and_b32_e32 v181, %[hi], v186\n\tv_lshlrev_b32_e32 v182, 16, v187\n\tv_and_b32_e32 v183, %[hi], v187\n\t"
        "v_lshlrev_b32_e32 v184, 16, v188\n\tv_and_b32_e32 v185, %[hi], v188\n\tv_lshlrev_b32_e32 v186, 16, v189\n\tv_and_b32_e32 v187, %[hi], v189\n\t"
        "v_lshlrev_b32_e32 v188, 16, v190\n\tv_and_b32_e32 v189, %[hi], v190\n\tv_lshlrev_b32_e32 v190, 16, v191\n\tv_and_b32_e32 v191, %[hi], v191\n\t"
        ".Lb3_%=:\n\t"
        "s_bitcmp1_b32 %[fr], 4\n\ts_cbranch_scc0 .Lb4_%=\n\t"
        "v_lshlrev_b32_e32 v192, 16, v200\n\tv_and_b32_e32 v193, %[hi], v200\n\tv_lshlrev_b32_e32 v194, 16, v201\n\tv_and_b32_e32 v195, %[hi], v201\n\t"
        "v_lshlrev_b32_e32 v196, 16, v202\n\tv_and_b32_e32 v197, %[hi], v202\n\tv_lshlrev_b32_e32 v198, 16, v203\n\tv_and_b32_e32 v199, %[hi], v203\n\t"
        "v_lshlrev_b32_e32 v200, 16, v204\n\tv_and_b32_e32 v201, %[hi], v204\n\tv_lshlrev_b32_e32 v202, 16, v205\n\tv_and_b32_e32 v203, %[hi], v205\n\t"
        "v_lshlrev_b32_e32 v204, 16, v206\n\tv_and_b32_e32 v205, %[hi], v206\n\tv_lshlrev_b32_e32 v206, 16, v207\n\tv_and_b32_e32 v207, %[hi], v207\n\t"
        ".Lb4_%=:\n\t"
        "s_bitcmp1_b32 %[fr], 5\n\ts_cbranch_scc0 .Lb5_%=\n\t"
        "v_lshlrev_b32_e32 v208, 16, v216\n\tv_and_b32_e32 v209, %[hi], v216\n\tv_lshlrev_b32_e32 v210, 16, v217\n\tv_and_b32_e32 v211, %[hi], v217\n\t"
        "v_lshlrev_b32_e32 v212, 16, v218\n\tv_and_b32_e32 v213, %[hi], v218\n\tv_lshlrev_b32_e32 v214, 16, v219\n\tv_and_b32_e32 v215, %[hi], v219\n\t"
        "v_lshlrev_b32_e32 v216, 16, v220\n\tv_and_b32_e32 v217, %[hi], v220\n\tv_lshlrev_b32_e32 v218, 16, v221\n\tv_and_b32_e32 v219, %[hi], v221\n\t"
        "v_lshlrev_b32_e32 v220, 16, v222\n\tv_and_b32_e32 v221, %[hi], v222\n\tv_lshlrev_b32_e32 v222, 16, v223\n\tv_and_b32_e32 v223, %[hi], v223\n\t"
        ".Lb5_%=:\n\t"
        "s_bitcmp1_b32 %[fr], 6\n\ts_cbranch_scc0 .Lb6_%=\n\t"
        "v_lshlrev_b32_e32 v224, 16, v232\n\tv_and_b32_e32 v225, %[hi], v232\n\tv_lshlrev_b32_e32 v226, 16, v233\n\tv_and_b32_e32 v227, %[hi], v233\n\t"
        "v_lshlrev_b32_e32 v228, 16, v234\n\tv_and_b32_e32 v229, %[hi], v234\n\tv_lshlrev_b32_e32 v230, 16, v235\n\tv_and_b32_e32 v231, %[hi], v235\n\t"
        "v_lshlrev_b32_e32 v232, 16, v236\n\tv_and_b32_e32 v233, %[hi], v236\n\tv_lshlrev_b32_e32 v234, 16, v237\n\tv_and_b32_e32 v235, %[hi], v237\n\t"
        "v_lshlrev_b32_e32 v236, 16, v238\n\tv_and_b32_e32 v237, %[hi], v238\n\tv_lshlrev_b32_e32 v238, 16, v239\n\tv_and_b32_e32 v239, %[hi], v239\n\t"
        ".Lb6_%=:\n\t"
        "s_bitcmp1_b32 %[fr], 7\n\ts_cbranch_scc0 .Lb7_%=\n\t"
        "v_lshlrev_b32_e32 v240, 16, v248\n\tv_and_b32_e32 v241, %[hi], v248\n\tv_lshlrev_b32_e32 v242, 16, v249\n\tv_and_b32_e32 v243, %[hi], v249\n\t"
        "v_lshlrev_b32_e32 v244, 16, v250\n\tv_and_b32_e32 v245, %[hi], v250\n\tv_lshlrev_b32_e32 v246, 16, v251\n\tv_and_b32_e32 v247, %[hi], v251\n\t"
        "v_lshlrev_b32_e32 v248, 16, v252\n\tv_and_b32_e32 v249, %[hi], v252\n\tv_lshlrev_b32_e32 v250, 16, v253\n\tv_and_b32_e32 v251, %[hi], v253\n\t"
        "v_lshlrev_b32_e32 v252, 16, v254\n\tv_and_b32_e32 v253, %[hi], v254\n\tv_lshlrev_b32_e32 v254, 16, v255\n\tv_and_b32_e32 v255, %[hi], v255\n\t"
        ".Lb7_%=:\n\t"
        "s_cmp_eq_u32 %[got], -1\n\ts_cbranch_scc1 .Ldone_%=\n"
        ".Lwait_%=:\n\ts_sleep %[slp]\n\ts_waitcnt vmcnt(0)\n\ts_sub_u32 %[spin], %[spin], 1\n\ts_cbranch_scc0 .Ltop_%=\n"
        ".Ldone_%=:"
        : "=&{v[128:143]}"(f[0]), "=&{v[144:159]}"(f[1]), "=&{v[160:175]}"(f[2]), "=&{v[176:191]}"(f[3]), "=&{v[192:207]}"(f[4]), "=&{v[208:223]}"(f[5]), "=&{v[224:239]}"(f[6]), "=&{v[240:255]}"(f[7]),
          [fl] "+v"(fl), [got] "+s"(got), [spin] "+s"(spin), [fr] "=&s"(fresh), [so] "=&s"(so)
        : [tau] "s"(tau), [l16] "v"(l16), [l8] "v"(l8), [sp] "s"(pb.srd), [sh] "s"(srdH), [ro] "s"(xg * 8192u + w * 2048u), [tok] "s"(tok_off), [flg] "s"(flg_off + xg * 128u),
          [hi] "s"(0xffff0000u), [slp] "i"(isA ? SLEEP_A : SLEEP_B)
        : "vcc", "scc", "memory");
    STAMP(15);
    const f32x16 t = ((f[0] + f[1]) + (f[2] + f[3])) + ((f[4] + f[5]) + (f[6] + f[7]));
#pragma unroll
    for (int s = 0; s < 2; ++s)
      *(u32x4*)(out + oo + s * 64) = natural8(u32x4{cvtpk(t[8 * s], t[8 * s + 1]), cvtpk(t[8 * s + 2], t[8 * s + 3]), cvtpk(t[8 * s + 4], t[8 * s + 5]), cvtpk(t[8 * s + 6], t[8 * s + 7])});
    asm volatile("s_waitcnt vmcnt(2)" : "+v"(fl) :: "memory");
    if ((lane >> 1) == w && fl != toks) asm volatile("buffer_store_dword %0, %1, %2, %3 offen sc1" :: "v"(fl), "v"(fo), "s"(srdH), "s"(flg_off) : "memory");
  }
}

__global__ __launch_bounds__(256, 1)
void fused_attn(const char* __restrict__ Ax, const char* __restrict__ Ak, const char* __restrict__ Av, const char* __restrict__ Awq, const char* __restrict__ Awo,
                const char* __restrict__ Bx, const char* __restrict__ Bk, const char* __restrict__ Bv, const char* __restrict__ Bwq, const char* __restrict__ Bwo,
                char* __restrict__ part, char* __restrict__ out) {
  const char* code = (const char*)(__builtin_amdgcn_s_getpc() & ~127ull);
  if (vbid() >= 128) { body<true>(Ax, Ak, Av, Awq, Awo, Bwo, code, part, out); __builtin_amdgcn_endpgm(); }
  body<false>(Bx, Bk, Bv, Bwq, Bwo, Bwo, code, part, out);
}

extern "C" void kernel_launch(void* const* d_in, const int* in_sizes, int n_in, void* d_out, int out_size, void* d_ws, size_t ws_size, hipStream_t stream) {
  (void)in_sizes; (void)n_in; (void)out_size; (void)ws_size;
  (void)hipFuncSetAttribute((const void*)fused_attn, hipFuncAttributeMaxDynamicSharedMemorySize, (int)LDS_BYTES);
  fused_attn<<<256, 256, LDS_BYTES, stream>>>((const char*)d_in[0], (const char*)d_in[1], (const char*)d_in[2], (const char*)d_in[3], (const char*)d_in[4],
                                              (const char*)d_in[5], (const char*)d_in[6], (const char*)d_in[7], (const char*)d_in[8], (const char*)d_in[9], (char*)d_ws, (char*)d_out);
}
```

```cpp
#include <hip/hip_runtime.h>
#include <hip/hip_bf16.h>
#include <utility>

typedef __bf16 bf16x8 __attribute__((ext_vector_type(8)));
typedef __bf16 bf16x4 __attribute__((ext_vector_type(4)));
typedef __bf16 bf16x2 __attribute__((ext_vector_type(2)));
typedef float f32x4 __attribute__((ext_vector_type(4)));
typedef float f32x2 __attribute__((ext_vector_type(2)));
typedef float f32x16 __attribute__((ext_vector_type(16)));
typedef unsigned u32x4 __attribute__((ext_vector_type(4)));
#define LAS __attribute__((address_space(3)))
#define DI __device__ __forceinline__
typedef __amdgpu_buffer_rsrc_t srd_t;
template <int... Is> using iseq = std::integer_sequence<int, Is...>;
template <int N> using make_iseq = std::make_integer_sequence<int, N>;

#define KV_NT 1
#define W_NT 0
#define X_NT 0
#define SLEEP_A 8
#define SLEEP_B 0
#define PACE_A 0
#define PACE_ATT 0
#define TOUCH_Q 1
#define TOUCH_O 3
#define TO_SKIP 16u
#define TO_B 8u
#define WARM_CODE 32
#define ALT 0
#define ORD 2
#define EARLY_O 0
#define TOUCH_B 1
#define PUB_SLEEP 0
#define KVPOL "nt"
#define WS_END 0

constexpr unsigned SLOT = 8192, LDS_BYTES = 20 * SLOT;
constexpr unsigned POFF = (unsigned)WS_END << 20, HOFF = POFF + (16u << 20);
constexpr float SC = 0.12751743082f;
constexpr float LMAX = 1048576.0f;

#define NO_REDUCE_STAMPS
#define GAP() __builtin_amdgcn_sched_barrier(0)
#define STAMP(i) do {} while (0)
#define CYC(i) do {} while (0)
#define PINS(x) asm volatile("" : "+s"(x))
#define PINV(x) asm volatile("" : "+v"(x))
DI unsigned rfl(unsigned x) { return __builtin_amdgcn_readfirstlane(x); }
DI unsigned cvtpk(float lo, float hi) { f32x2 v = {lo, hi}; bf16x2 b = __builtin_convertvector(v, bf16x2); return __builtin_bit_cast(unsigned, b); }
DI u32x4 pack2(const f32x4& a, const f32x4& b) { return u32x4{cvtpk(a[0], a[1]), cvtpk(a[2], a[3]), cvtpk(b[0], b[1]), cvtpk(b[2], b[3])}; }
DI u32x4 natural8(u32x4 w) {
  auto s0 = __builtin_amdgcn_permlane32_swap(w[0], w[2], false, false);
  auto s1 = __builtin_amdgcn_permlane32_swap(w[1], w[3], false, false);
  auto t0 = __builtin_amdgcn_permlane16_swap(s0[0], s0[1], false, false);
  auto t1 = __builtin_amdgcn_permlane16_swap(s1[0], s1[1], false, false);
  return u32x4{t0[0], t1[0], t0[1], t1[1]};
}
#define MF "v_mfma_f32_16x16x32_bf16 "
template <bool Z> DI void mm_vvv(f32x4& c, const bf16x8& a, const bf16x8& b) {
  if constexpr (Z) asm volatile(MF "%0, %1, %2, 0" : "=&v"(c) : "v"(a), "v"(b));
  else             asm volatile(MF "%0, %1, %2, %0" : "+v"(c) : "v"(a), "v"(b));
}
template <bool Z> DI void mm_vva(f32x4& c, const bf16x8& a, const bf16x8& b) {
  if constexpr (Z) asm volatile(MF "%0, %1, %2, 0" : "=&v"(c) : "v"(a), "a"(b));
  else             asm volatile(MF "%0, %1, %2, %0" : "+v"(c) : "v"(a), "a"(b));
}
DI void mm_vca(f32x4& d, const bf16x8& a, const bf16x8& b, const f32x4& c) { asm volatile(MF "%0, %1, %2, %3" : "=&v"(d) : "v"(a), "a"(b), "v"(c)); }
DI void mm_avv(f32x4& c, const bf16x8& a, const bf16x8& b) { asm volatile(MF "%0, %1, %2, %0" : "+a"(c) : "v"(a), "v"(b)); }
DI void set_m0(unsigned v) { asm volatile("s_mov_b32 m0, %0" :: "s"(v) : "memory"); }
template <int NT, int P> DI void dma_p(unsigned voff, srd_t srd, unsigned soff) {
  if constexpr (NT) asm volatile("buffer_load_dwordx4 %0, %1, %2 offen offset:%3 " KVPOL " lds" :: "v"(voff), "s"(srd), "s"(soff), "i"(1024 * P) : "memory");
  else              asm volatile("buffer_load_dwordx4 %0, %1, %2 offen offset:%3 lds" :: "v"(voff), "s"(srd), "s"(soff), "i"(1024 * P) : "memory");
}
struct Src { unsigned v[4]; srd_t srd; };
template <int NT> DI void dma_unit(const Src& s, unsigned soff, unsigned ldsaddr) {
  asm volatile("s_mov_b32 m0, %0\n\ts_nop 0" :: "s"(ldsaddr) : "memory");
  dma_p<NT, 0>(s.v[0], s.srd, soff); dma_p<NT, 1>(s.v[1], s.srd, soff); dma_p<NT, 2>(s.v[2], s.srd, soff); dma_p<NT, 3>(s.v[3], s.srd, soff);
}
template <int NT> DI void dma_elem(const Src& s, unsigned soff, unsigned ustep, unsigned ldsaddr) { dma_unit<NT>(s, soff, ldsaddr); dma_unit<NT>(s, soff + ustep, ldsaddr + 4096u); }
template <int N> DI void wait_vm() { asm volatile("s_waitcnt vmcnt(%0)" :: "i"(N) : "memory"); }
template <int N, int L> DI void wait_vm_lgkm() { asm volatile("" ::: "memory"); __builtin_amdgcn_s_waitcnt((N & 15) | ((N >> 4) << 14) | 0x70 | (L << 8)); asm volatile("" ::: "memory"); }
DI void wait_lgkm0() { wait_vm_lgkm<63, 0>(); }
DI void barrier() { asm volatile("s_barrier" ::: "memory"); }
DI bf16x8 lds128(unsigned a) { return *(const LAS bf16x8*)(size_t)(a); }
DI bf16x4 lds_tr1(unsigned a) { return __builtin_amdgcn_ds_read_tr16_b64_v4bf16((LAS bf16x4*)(size_t)(a)); }
DI bf16x8 cat(bf16x4 lo, bf16x4 hi) { return __builtin_shufflevector(lo, hi, 0, 1, 2, 3, 4, 5, 6, 7); }
DI bf16x8 lds_tr(unsigned a0, unsigned a1) { return cat(lds_tr1(a0), lds_tr1(a1)); }
DI srd_t mk_srd(const void* base, unsigned bytes) { return __builtin_amdgcn_make_buffer_rsrc((void*)base, (short)0, (int)bytes, 0x00020000); }
template <bool VX> DI void mk_src_rows(Src& s, const char* base, unsigned bytes, unsigned stride, unsigned lane) {
  s.srd = mk_srd(base, bytes);
  const unsigned row = (lane >> 2) & 3u, cb = lane >> 4, sl = lane & 3u;
#pragma unroll
  for (int p = 0; p < 4; ++p) s.v[p] = row * stride + cb * 64u + ((sl ^ (VX ? 2 * (p & 1) : (p & 2))) << 4) + (4u * stride - 1024u) * p;
}
template <bool VX> DI void mk_src_w(Src& s, const char* base, unsigned bytes, unsigned stride, unsigned lane) {
  s.srd = mk_srd(base, bytes);
  const unsigned sub = lane >> 5, cb = (lane >> 4) & 1u, row = (lane >> 2) & 3u, sl = lane & 3u;
#pragma unroll
  for (int p = 0; p < 4; ++p) s.v[p] = (4u * sub + row) * stride + cb * 64u + ((sl ^ (VX ? 2u * sub : 2u * (p & 1))) << 4) + (8u * stride - 1024u) * p;
}
template <int NT> DI void touch(unsigned& d, unsigned voff, srd_t srd, unsigned soff) {
  if constexpr (NT) asm volatile("buffer_load_dword %0, %1, %2, %3 offen nt" : "+v"(d) : "v"(voff), "s"(srd), "s"(soff) : "memory");
  else              asm volatile("buffer_load_dword %0, %1, %2, %3 offen" : "+v"(d) : "v"(voff), "s"(srd), "s"(soff) : "memory");
}
DI unsigned ksoff(unsigned m, unsigned rk) { return ((m + rk) & 15u) * 8192u; }

struct PC {
  unsigned r01, r2;
  unsigned w01[2], w2[2];
  unsigned xb, xmask, xa;
  unsigned z, soff;
};
template <unsigned TS_, unsigned QS_, unsigned ZI_, unsigned ZM_, unsigned ZO_, unsigned US_, unsigned CS_, bool ROT_> struct PK {
  static constexpr unsigned TS = TS_, QS = QS_, ZI = ZI_, ZM = ZM_, ZO = ZO_, US = US_, CS = CS_; static constexpr bool ROT = ROT_; };
DI unsigned q_soff(unsigned z) { return ((z & 0xC300u) * 0x440u) & 0x3C0000u; }
template <int POS> DI unsigned ring_lds(const PC& c) { return POS == 2 ? c.r2 : c.r01 + POS * SLOT; }
template <int POS> DI unsigned ring_rd(const PC& c, int d) { return (POS == 2 ? c.w2[d & 1] : c.w01[d & 1] + POS * SLOT) + (d >> 1) * 256u; }
struct NoX { static constexpr bool ACT = true; template <int G> DI void run() const {} };
template <class K, int POS, int S, bool Z, bool RD, bool DM, int NT, bool XT, int M, class X>
DI void proj_gap(f32x4 (&acc)[8][4], const bf16x8 (&cur)[4], bf16x8 (&nxt)[4], bf16x4 (&lo)[4], bf16x8 (&bf)[4], PC& c, unsigned soff, const Src& src, const Src& xs, unsigned xsoff, const X& ex) {
  constexpr int qb = M >> 2, db = M & 3;
  mm_vvv<Z>(acc[4 * S + db][qb], cur[db], bf[qb]);
  if constexpr (RD && M < 8) {
    constexpr int d = M >> 1; const unsigned a = S == 0 ? ring_rd<POS>(c, d) + 4096u : ring_rd<(POS + 1) % 3>(c, d);
    if constexpr (M & 1) nxt[d] = cat(lo[d], lds_tr1(a + K::TS)); else lo[d] = lds_tr1(a);
  }
  if constexpr (X::ACT && RD && S == 1 && db == 3) bf[qb] = lds128(c.xa + qb * K::QS);
  if constexpr (XT) {
    if constexpr (M < 8 && !(M & 1)) dma_p<X_NT, M / 2>(xs.v[M / 2], xs.srd, xsoff);
    if constexpr (M == 7) set_m0(ring_lds<POS>(c) + S * 4096u);
    if constexpr (M >= 8 && !(M & 1)) dma_p<NT, (M - 8) / 2>(src.v[(M - 8) / 2], src.srd, soff);
  } else if constexpr (DM && M >= 8 && !(M & 1)) dma_p<NT, (M - 8) / 2>(src.v[(M - 8) / 2], src.srd, soff);
  if constexpr (X::ACT && S == 0 && M == 9) { PINS(c.z); c.z = (c.z + K::ZI) & K::ZM; PINS(c.z); }
  if constexpr (X::ACT && S == 0 && M == 11) { c.xa = (c.z & c.xmask) | c.xb; PINV(c.xa); }
  if constexpr (S == 1 && DM && !K::ROT && M == 9) { PINS(c.soff); c.soff = (c.soff + K::CS) & (16u * K::CS - 1u); PINS(c.soff); }
  if constexpr (S == 1 && DM && K::ROT && M == 9) { PINS(c.z); c.soff = (c.z + 0x3100u) & 0xC300u; PINS(c.soff); }
  if constexpr (S == 1 && DM && K::ROT && M == 13) { PINS(c.soff); c.soff = (c.soff * 0x440u) & 0x3C0000u; PINS(c.soff); }
  ex.template run<16 * S + M>();
  GAP();
}
template <class K, int POS, int S, bool Z, bool RD, bool DM, int NT, int VM, bool BAR, bool XT, class X, int... Ms>
DI void proj_unit(f32x4 (&acc)[8][4], const bf16x8 (&cur)[4], bf16x8 (&nxt)[4], bf16x8 (&bf)[4], PC& c, const Src& src, const Src& xs, unsigned xsoff, unsigned xdst, const X& ex, iseq<Ms...>) {
  wait_vm_lgkm<(RD ? VM : 63), (S == 0 ? 2 : 0)>();
  if constexpr (BAR) barrier();
  const unsigned soff = c.soff + S * K::US;
  if constexpr (XT) set_m0(xdst); else if constexpr (DM) set_m0(ring_lds<POS>(c) + S * 4096u);
  GAP();
  bf16x4 lo[4];
  (proj_gap<K, POS, S, Z, RD, DM, NT, XT, Ms>(acc, cur, nxt, lo, bf, c, soff, src, xs, xsoff, ex), ...);
}
template <class K, int POS, bool Z, bool LAST, bool DM, int NT, int VM0, int VM1, bool BAR1 = false, bool XT0 = false, bool XT1 = false, class X = NoX>
DI void proj_elem(f32x4 (&acc)[8][4], bf16x8 (&a0)[4], bf16x8 (&a1)[4], bf16x8 (&bf)[4], PC& c, const Src& src, const X& ex = X{},
                  const Src* xs = nullptr, unsigned xso0 = 0, unsigned xd0 = 0, unsigned xso1 = 0, unsigned xd1 = 0) {
  proj_unit<K, POS, 0, Z, true, DM, NT, VM0, false, XT0>(acc, a0, a1, bf, c, src, xs ? *xs : src, xso0, xd0, ex, make_iseq<16>{});
  proj_unit<K, POS, 1, Z, !LAST, DM, NT, VM1, BAR1, XT1>(acc, a1, a0, bf, c, src, xs ? *xs : src, xso1, xd1, ex, make_iseq<16>{});
}
template <class K> DI void proj_setup(PC& c, unsigned tb, unsigned xb, unsigned z0, unsigned s3) {
  c.w01[0] = c.r01 + tb; c.w01[1] = c.w01[0] ^ 32u; c.w2[0] = c.r2 + tb; c.w2[1] = c.w2[0] ^ 32u;
  PINV(c.w01[0]); PINV(c.w01[1]); PINV(c.w2[0]); PINV(c.w2[1]);
  c.xb = xb; c.xmask = K::ZO; PINV(c.xmask);
  c.z = z0; c.soff = s3;
}
template <class K, int POS> DI void proj_start(bf16x8 (&a0)[4], bf16x8 (&bf)[4], PC& c, unsigned tb, unsigned xb, unsigned z0, unsigned s3) {
  proj_setup<K>(c, tb, xb, z0, s3);
  c.xa = (c.z & K::ZO) | xb; PINV(c.xa);
#pragma unroll
  for (int d = 0; d < 4; ++d) { const unsigned a = ring_rd<POS>(c, d); a0[d] = lds_tr(a, a + K::TS); }
  GAP();
#pragma unroll
  for (int q = 0; q < 4; ++q) bf[q] = lds128(c.xa + q * K::QS);
  GAP();
}
#define ACC_ALL(C, a) C(a[0][0]), C(a[0][1]), C(a[0][2]), C(a[0][3]), C(a[1][0]), C(a[1][1]), C(a[1][2]), C(a[1][3]), C(a[2][0]), C(a[2][1]), C(a[2][2]), C(a[2][3]), C(a[3][0]), C(a[3][1]), C(a[3][2]), C(a[3][3])
DI void acc_fence_v(f32x4 (&acc)[8][4]) {
  asm volatile("s_nop 7\n\ts_nop 3" : ACC_ALL("+v", acc) :: "memory");
  f32x4 (&hi)[4][4] = *(f32x4 (*)[4][4])&acc[4];
  asm volatile("" : ACC_ALL("+v", hi) :: "memory");
  GAP();
}
DI void acc_fence_a(f32x4 (&acc)[8][4]) {
  asm volatile("s_nop 7\n\ts_nop 3" : ACC_ALL("+a", acc) :: "memory");
  f32x4 (&hi)[4][4] = *(f32x4 (*)[4][4])&acc[4];
  asm volatile("" : ACC_ALL("+a", hi) :: "memory");
  GAP();
}

struct Stash { u32x4 p[64]; unsigned base[3]; };
template <int I> DI void stash_load1(Stash& st, unsigned vk, const Src& sK, const Src& sV, unsigned rk, unsigned wo_) {
  constexpr int m = I >> 2, isV = (I >> 1) & 1, pc_ = I & 1;
  const unsigned so = ksoff(m, rk) + wo_;
  if constexpr (I < 56) asm volatile("buffer_load_dwordx4 %0, %1, %2, %3 offen offset:%4 " KVPOL : "=a"(st.p[I]) : "v"(isV ? sV.v[pc_] : vk), "s"(isV ? sV.srd : sK.srd), "s"(so), "i"(1024 * pc_) : "memory");
  else                  asm volatile("buffer_load_dwordx4 %0, %1, %2, %3 offen offset:%4 " KVPOL : "=v"(st.p[I]) : "v"(isV ? sV.v[pc_] : vk), "s"(isV ? sV.srd : sK.srd), "s"(so), "i"(1024 * pc_) : "memory");
}
template <int... Is> DI void stash_load(Stash& st, unsigned vk, const Src& sK, const Src& sV, unsigned rk, unsigned wo_, iseq<Is...>) { (stash_load1<Is>(st, vk, sK, sV, rk, wo_), ...); }
constexpr int kv_pair(int m) { constexpr int t[10] = {0, 1, 2, 3, 8, 9, 4, 5, 6, 7}; return t[m % 10]; }
constexpr unsigned long long kv_tab() { unsigned long long v = 0; for (int i = 0; i < 16; ++i) v |= (unsigned long long)kv_pair(i) << (4 * i); return v; }
template <int I> DI void stash_write1(const Stash& st) {
  constexpr int m = I >> 2, isV = (I >> 1) & 1, pc_ = I & 1, off = kv_pair(m) * 16384 + isV * 8192 + pc_ * 1024;
  if constexpr (I < 56) asm volatile("ds_write_b128 %0, %1 offset:%2" :: "v"(st.base[off >> 16]), "a"(st.p[I]), "i"(off & 65535) : "memory");
  else                  asm volatile("ds_write_b128 %0, %1 offset:%2" :: "v"(st.base[off >> 16]), "v"(st.p[I]), "i"(off & 65535) : "memory");
}
template <int... Is> DI void stash_late(const Stash& st, iseq<Is...>) { (stash_write1<40 + Is>(st), ...); }

struct AC {
  f32x4 o[8][4];
  bf16x8 q[4][4];
  bf16x8 kf[8], vf[8], pf[4];
  f32x4 negm[4];
  f32x4 l[4];
  bf16x8 ones;
};
typedef f32x4 ST[2][4];
constexpr int NEB = 22;
template <int E> DI float sm_val(ST& S) { return S[(E >> 2) & 1][E >> 3][E & 3]; }
template <int E> DI void sm_exp1(ST& S) { float a; asm volatile("v_exp_f32_e32 %0, %1" : "=v"(a) : "v"(sm_val<E>(S))); S[(E >> 2) & 1][E >> 3][E & 3] = a; }
template <int QB, int P> DI void sm_cvt(ST& S, AC& c) {
  u32x4 w = __builtin_bit_cast(u32x4, c.pf[QB]);
  unsigned r;
  asm volatile("v_cvt_pk_bf16_f32 %0, %1, %2" : "=v"(r) : "v"(sm_val<8 * QB + 2 * P>(S)), "v"(sm_val<8 * QB + 2 * P + 1>(S)));
  w[P] = r;
  c.pf[QB] = __builtin_bit_cast(bf16x8, w);
}
template <bool DM, int VM, int R, int M> DI void mem_pre(unsigned ldsaddr) {
  if constexpr (M == 6) wait_lgkm0();
  if constexpr (DM && (M == 6 || M == 14)) set_m0(ldsaddr + (M == 14 ? 4096u : 0u));
  if constexpr (M == R && VM < 63) wait_vm<VM>();
}
template <bool DM, int NT, int M> DI void mem_post(const Src& src, unsigned soff, unsigned ustep) {
  if constexpr (DM && M >= 6 && M <= 20 && !(M & 1)) dma_p<NT, ((M - 6) / 2) & 3>(src.v[((M - 6) / 2) & 3], src.srd, soff + (M >= 14 ? ustep : 0u));
}
template <int WR, int M> DI void mem_wr(const Stash& st) {
  if constexpr (WR >= 0 && M >= 8 && M < 16)
    asm volatile("ds_write_b128 %0, %1 offset:%2" :: "v"(st.base[0]), "a"(st.p[WR + M - 8]), "i"((M - 8) * 1024) : "memory");
}
struct PA { unsigned rd0, rd1, soff, fill; };
template <bool QK, bool DM, int NT, int VM, int WR, int M, class F>
DI void gapA(AC& c, ST& S, ST& Sn, PA& pa, F& addr, const Src& src, unsigned ustep, const Stash& st, bf16x4& lo) {
  constexpr int R = QK ? 16 : 0;
  if constexpr ((DM || !QK) && M == 0) addr(pa);
  if constexpr (QK) mem_pre<DM, VM, 16, M>(pa.fill); else if constexpr (M == 0 && VM < 63) wait_vm<VM>();
  if constexpr (QK) {
    constexpr int f = M >> 2, s = f >> 1, kb = f & 1, qb = M & 3;
    if constexpr (s == 0) mm_vca(Sn[kb][qb], c.kf[f], c.q[qb][0], c.negm[qb]); else mm_vva<false>(Sn[kb][qb], c.kf[f], c.q[qb][s]);
  }
  mem_post<DM, NT, M>(src, pa.soff, ustep);
  mem_wr<WR, M>(st);
  if constexpr (QK && !DM && M == 10) addr(pa);
  if constexpr (QK && M == 12) asm volatile("" :: "v"(c.negm[0]), "v"(c.negm[1]), "v"(c.negm[2]), "v"(c.negm[3]));
  if constexpr (M >= R && M < R + 16) {
    constexpr int db = (M - R) >> 1; const unsigned a = ((db & 1) ? pa.rd1 : pa.rd0) + (db >> 1) * 256u;
    if constexpr (M & 1) c.vf[db] = cat(lo, lds_tr1(a + 4096u)); else lo = lds_tr1(a);
  }
  if constexpr (NEB + M < 32) sm_exp1<NEB + M>(S);
  if constexpr (M >= 16 && (M & 1)) sm_cvt<2 + ((M - 16) >> 3), ((M - 16) >> 1) & 3>(S, c);
  GAP();
}
template <bool QK, bool DM, int NT, int VM, int WR, class F, int... Ms>
DI void phaseA_(AC& c, ST& S, ST& Sn, F& addr, const Src& src, unsigned ustep, const Stash& st, iseq<Ms...>) {
  PA pa; bf16x4 lo;
  (gapA<QK, DM, NT, VM, WR, Ms>(c, S, Sn, pa, addr, src, ustep, st, lo), ...);
}
template <bool QK, bool DM, int NT, int VM, int WR = -1, class F>
DI void phaseA(AC& c, ST& S, ST& Sn, F addr, const Src& src, unsigned ustep, const Stash& st) {
  phaseA_<QK, DM, NT, VM, WR>(c, S, Sn, addr, src, ustep, st, make_iseq<32>{});
}
template <bool MAX> DI float xlane(float v) {
  auto a = __builtin_amdgcn_permlane32_swap(__float_as_uint(v), __float_as_uint(v), false, false);
  v = MAX ? fmaxf(__uint_as_float(a[0]), __uint_as_float(a[1])) : __uint_as_float(a[0]) + __uint_as_float(a[1]);
  auto b = __builtin_amdgcn_permlane16_swap(__float_as_uint(v), __float_as_uint(v), false, false);
  return MAX ? fmaxf(__uint_as_float(b[0]), __uint_as_float(b[1])) : __uint_as_float(b[0]) + __uint_as_float(b[1]);
}
DI float row_max(const f32x4& x, const f32x4& y) {
  return xlane<true>(fmaxf(fmaxf(fmaxf(x[0], x[1]), fmaxf(x[2], x[3])), fmaxf(fmaxf(y[0], y[1]), fmaxf(y[2], y[3]))));
}
template <bool SM, bool KR, bool DM, int NT, int VM, int WR, int M, class F>
DI void gapB(AC& c, ST& Sn, PA& pa, F& addr, const Src& src, unsigned ustep, const Stash& st) {
  mem_pre<DM, (KR ? VM : 63), 18, M>(pa.fill);
  {
    constexpr int h = M / 18, r = M % 18, qb = 2 * h + (r & 1);
    if constexpr (r < 2) mm_vvv<false>(c.l[qb], c.ones, c.pf[qb]); else mm_avv(c.o[(r - 2) >> 1][qb], c.vf[(r - 2) >> 1], c.pf[qb]);
  }
  mem_post<DM, NT, M>(src, pa.soff, ustep);
  mem_wr<WR, M>(st);
  if constexpr (M == 0) addr(pa);
  if constexpr (KR && M >= 18 && M < 34 && !(M & 1)) { constexpr int f = (M - 18) >> 1; c.kf[f] = lds128(pa.rd0 + (f & 1) * 4096u + (f >> 1) * 256u); }
  if constexpr (SM) {
    if constexpr (M >= 4 && M < 18) sm_exp1<M - 4>(Sn);
    if constexpr (M >= 19 && M < 35 && (M & 1)) sm_exp1<14 + (M - 19) / 2>(Sn);
    if constexpr (M >= 20 && !(M & 1)) sm_cvt<((M - 20) >> 3), ((M - 20) >> 1) & 3>(Sn, c);
  }
  GAP();
}
template <bool SM, bool KR, bool DM, int NT, int VM, int WR, class F, int... Ms>
DI void phaseB_(AC& c, ST& Sn, F& addr, const Src& src, unsigned ustep, const Stash& st, iseq<Ms...>) {
  PA pa;
  (gapB<SM, KR, DM, NT, VM, WR, Ms>(c, Sn, pa, addr, src, ustep, st), ...);
}
template <bool SM, bool KR, bool DM, int NT, int VM, int WR = -1, class F>
DI void phaseB(AC& c, ST& Sn, F addr, const Src& src, unsigned ustep, const Stash& st) {
  phaseB_<SM, KR, DM, NT, VM, WR>(c, Sn, addr, src, ustep, st, make_iseq<36>{});
}
DI void k_read(AC& ac, unsigned ka) {
#pragma unroll
  for (int f = 0; f < 8; ++f) ac.kf[f] = lds128(ka + (f & 1) * 4096u + (f >> 1) * 256u);
}
template <bool DM, int M> DI void qk0_gap(AC& ac, ST& SA, const Src& src, unsigned soff, unsigned fill) {
  constexpr int f = M >> 2, qb = M & 3;
  if constexpr (DM && (M == 0 || M == 16)) set_m0(fill + M * 256u);
  mm_vva<(f < 2)>(SA[f & 1][qb], ac.kf[f], ac.q[qb][f >> 1]);
  if constexpr (DM && qb == 0) dma_p<KV_NT, f & 3>(src.v[f & 3], src.srd, soff + (f >> 2) * 4096u);
  GAP();
}
template <bool DM, int... Ms> DI void qk0(AC& ac, ST& SA, const Src& src, unsigned soff, unsigned fill, iseq<Ms...>) { (qk0_gap<DM, Ms>(ac, SA, src, soff, fill), ...); }
template <int... Js> DI void exp_first(ST& SA, iseq<Js...>) { (sm_exp1<Js>(SA), ...); }
template <int... Js> DI void cvt_first(ST& SA, AC& c, iseq<Js...>) { (sm_cvt<(Js >> 2), (Js & 3)>(SA, c), ...); }
DI void sm_first(AC& ac, ST& SA) {
  asm volatile("s_nop 7\n\ts_nop 3" : "+v"(SA[0][0]), "+v"(SA[0][1]), "+v"(SA[0][2]), "+v"(SA[0][3]), "+v"(SA[1][0]), "+v"(SA[1][1]), "+v"(SA[1][2]), "+v"(SA[1][3]) :: "memory");
  GAP();
#pragma unroll
  for (int q = 0; q < 4; ++q) {
    const float m = row_max(SA[0][q], SA[1][q]);
    SA[0][q] -= m; SA[1][q] -= m;
    ac.negm[q] = f32x4{-m, -m, -m, -m};
    ac.l[q] = f32x4{};
    ac.pf[q] = bf16x8{};
  }
  ac.ones = __builtin_bit_cast(bf16x8, u32x4{0x3f803f80u, 0x3f803f80u, 0x3f803f80u, 0x3f803f80u});
  asm volatile("" : "+v"(ac.negm[0]), "+v"(ac.negm[1]), "+v"(ac.negm[2]), "+v"(ac.negm[3]), "+v"(ac.l[0]), "+v"(ac.l[1]), "+v"(ac.l[2]), "+v"(ac.l[3]), "+v"(ac.ones));
  exp_first(SA, make_iseq<NEB>{});
  cvt_first(SA, ac, make_iseq<8>{});
  GAP();
}
template <int CH, int Q, int J> DI void o_word(const AC& c, const float (&inv)[4], bf16x8 (&nb)[4]) {
  u32x4 w = __builtin_bit_cast(u32x4, nb[Q]);
  unsigned r; float t;
  asm volatile("v_accvgpr_read_b32 %0, %2\n\tv_accvgpr_read_b32 %1, %3\n\tv_mul_f32_e32 %0, %0, %4\n\tv_mul_f32_e32 %1, %1, %4\n\tv_cvt_pk_bf16_f32 %0, %0, %1"
               : "=&v"(r), "=&v"(t) : "a"(c.o[2 * CH + (J >> 1)][Q][2 * (J & 1)]), "a"(c.o[2 * CH + (J >> 1)][Q][2 * (J & 1) + 1]), "v"(inv[Q]));
  w[J] = r;
  nb[Q] = __builtin_bit_cast(bf16x8, w);
}
template <int CH, int Q> DI void o_put(const bf16x8 (&nb)[4], unsigned ow) { asm volatile("ds_write_b128 %0, %1 offset:%2" :: "v"(ow), "v"(nb[Q]), "i"(CH * 4096 + Q * 1024) : "memory"); }
template <int E> struct CvX {
  static constexpr bool ACT = E == 3;
  const AC& c; const float (&inv)[4]; const bf16x8 (&cur)[4]; bf16x8 (&nb)[4]; unsigned ow;
  template <int G> DI void run() const {
    if constexpr (E < 3 && (G & 1)) o_word<E + 1, (G >> 3), (G >> 1) & 3>(c, inv, nb);
    if constexpr (E < 3 && ((G & 15) == 8 || (G & 15) == 10)) o_put<E, 2 * (G >> 4) + ((G >> 1) & 1)>(cur, ow);
    if constexpr (E == 2 && (G & 15) == 12) o_put<3, (G >> 4)>(nb, ow);
    if constexpr (E == 3 && (G == 8 || G == 10)) o_put<3, 2 + ((G >> 1) & 1)>(cur, ow);
  }
};
template <class K, int VM, int M> DI void gapL(AC& c, float (&inv)[4], bf16x8 (&nb)[4], bf16x8 (&a0)[4], bf16x4& lo, const PC& pc) {
  if constexpr (M == 6) wait_vm_lgkm<VM, 0>();
  if constexpr (M < 4) mm_vvv<false>(c.l[M], c.ones, c.pf[M]); else mm_avv(c.o[(M - 4) >> 2][M & 3], c.vf[(M - 4) >> 2], c.pf[M & 3]);
  if constexpr (M >= 6 && M < 14) {
    constexpr int d = (M - 6) >> 1; const unsigned a = ring_rd<0>(pc, d);
    if constexpr (M & 1) a0[d] = cat(lo, lds_tr1(a + K::TS)); else lo = lds_tr1(a);
  }
  if constexpr (M >= 4 && M < 12 && !(M & 1)) asm volatile("v_rcp_f32_e32 %0, %1" : "=v"(inv[(M - 4) >> 1]) : "v"(c.l[(M - 4) >> 1][0]));
  if constexpr (M >= 16 && M < 32) o_word<0, ((M - 16) >> 2), (M & 3)>(c, inv, nb);
  GAP();
}
template <class K, int VM, int... Ms> DI void phaseL(AC& c, float (&inv)[4], bf16x8 (&nb)[4], bf16x8 (&a0)[4], const PC& pc, iseq<Ms...>) {
  bf16x4 lo;
#pragma unroll
  for (int q = 0; q < 4; ++q) nb[q] = bf16x8{};
  (gapL<K, VM, Ms>(c, inv, nb, a0, lo, pc), ...);
}
struct Pub { u32x4 pk[16]; srd_t srd; unsigned v[2], so; };
template <int F, int H> DI void pub_half(const f32x4 (&acc)[8][4], Pub& p) {
  const f32x4& a = acc[4 * (F >> 3) + 2 * (F & 1) + H][(F >> 1) & 3];
  unsigned r0, r1;
  asm volatile("v_cvt_pk_bf16_f32 %0, %2, %3\n\tv_cvt_pk_bf16_f32 %1, %4, %5" : "=&v"(r0), "=v"(r1) : "v"(a[0]), "v"(a[1]), "v"(a[2]), "v"(a[3]));
  p.pk[F][2 * H] = r0; p.pk[F][2 * H + 1] = r1;
  if constexpr (H) asm volatile("buffer_store_dwordx4 %0, %1, %2, %3 offen offset:%4 sc1" :: "v"(p.pk[F]), "v"(p.v[(F >> 2) & 1]), "s"(p.srd), "s"(p.so + (F >> 3) * 8192u), "i"((F & 3) * 1024) : "memory");
}
struct PubX {
  static constexpr bool ACT = false;
  const f32x4 (&acc)[8][4]; Pub& p;
  template <int G> DI void run() const { if constexpr (G >= 16) pub_half<((G - 16) >> 1), (G & 1)>(acc, p); }
};
template <int... Is> DI void pub_rest(const f32x4 (&acc)[8][4], Pub& p, iseq<Is...>) { (pub_half<8 + (Is >> 1), (Is & 1)>(acc, p), ...); }
template <int... Is> DI void pub_slow(const f32x4 (&acc)[8][4], Pub& p, iseq<Is...>) { ((pub_half<Is, 0>(acc, p), pub_half<Is, 1>(acc, p), __builtin_amdgcn_s_sleep(PUB_SLEEP)), ...); }
DI void redo_head(const AC& ac, const char* __restrict__ Kp, const char* __restrict__ Vp, unsigned lane, unsigned ow) {
  const unsigned r16 = lane & 15u, g = lane >> 4;
#pragma unroll 1
  for (unsigned qb = 0; qb < 4; ++qb) {
    bf16x8 qq[4];
#pragma unroll
    for (int s = 0; s < 4; ++s) qq[s] = qb == 0 ? ac.q[0][s] : qb == 1 ? ac.q[1][s] : qb == 2 ? ac.q[2][s] : ac.q[3][s];
    f32x4 o[8];
#pragma unroll
    for (int d = 0; d < 8; ++d) o[d] = f32x4{};
    float m = -__builtin_inff(), l = 0.f;
#pragma unroll 1
    for (unsigned t = 0; t < 16; ++t) {
      f32x4 s0 = {}, s1 = {};
      const char* kp = Kp + (t * 32u + r16) * 256u + g * 16u;
#pragma unroll
      for (int s = 0; s < 4; ++s) {
        s0 = __builtin_amdgcn_mfma_f32_16x16x32_bf16(*(const bf16x8*)(kp + s * 64), qq[s], s0, 0, 0, 0);
        s1 = __builtin_amdgcn_mfma_f32_16x16x32_bf16(*(const bf16x8*)(kp + s * 64 + 4096), qq[s], s1, 0, 0, 0);
      }
      const float mn = fmaxf(m, row_max(s0, s1)), al = __builtin_amdgcn_exp2f(m - mn);
      m = mn;
#pragma unroll
      for (int j = 0; j < 4; ++j) { s0[j] = __builtin_amdgcn_exp2f(s0[j] - mn); s1[j] = __builtin_amdgcn_exp2f(s1[j] - mn); }
      l = l * al + ((s0[0] + s0[1]) + (s0[2] + s0[3])) + ((s1[0] + s1[1]) + (s1[2] + s1[3]));
      const bf16x8 pf = __builtin_bit_cast(bf16x8, pack2(s0, s1));
      const __bf16* vp = (const __bf16*)(Vp + (t * 32u + g * 4u) * 256u) + r16;
#pragma unroll
      for (int d = 0; d < 8; ++d) {
        bf16x8 vf;
#pragma unroll
        for (int j = 0; j < 8; ++j) vf[j] = vp[((j & 3) + (j >> 2) * 16) * 128 + d * 16];
        o[d] = __builtin_amdgcn_mfma_f32_16x16x32_bf16(vf, pf, o[d] * al, 0, 0, 0);
      }
    }
    const float inv = 1.0f / xlane<false>(l);
#pragma unroll
    for (int s = 0; s < 4; ++s) *(LAS u32x4*)(size_t)(ow + s * 4096u + qb * 1024u) = pack2(o[2 * s] * inv, o[2 * s + 1] * inv);
  }
}
template <bool isA, int J> DI void q_conv1(AC& ac, f32x4 (&acc)[8][4], Stash& st, const Src& sK, const Src& sV, unsigned rk, unsigned p3) {
  constexpr int s = J >> 2, qb = J & 3;
  if constexpr (isA) { stash_write1<(5 * J) / 2>(st); stash_write1<(5 * J) / 2 + 1>(st); if constexpr (J & 1) stash_write1<(5 * J) / 2 + 2>(st); }
  else {
    constexpr int p = 2 * (J & 1);
    const Src& src = s < 2 ? sK : sV;
    const unsigned so = ksoff(s == 0 ? 2u : s == 3 ? 14u : 13u, rk) + (J & 2) * 2048u;
    if constexpr (s == 0 || s == 3) {
      if constexpr (p == 0) asm volatile("s_mov_b32 m0, %0\n\ts_nop 0" :: "s"(p3 + (s == 3 ? SLOT : 0u) + (J & 2) * 2048u) : "memory");
      dma_p<KV_NT, p>(src.v[p], src.srd, so); dma_p<KV_NT, p + 1>(src.v[p + 1], src.srd, so);
    } else {
      asm volatile("buffer_load_dwordx4 %0, %1, %2, %3 offen offset:%4 " KVPOL : "=a"(st.p[2 * J - 8]) : "v"(src.v[p]), "s"(src.srd), "s"(so), "i"(1024 * p) : "memory");
      asm volatile("buffer_load_dwordx4 %0, %1, %2, %3 offen offset:%4 " KVPOL : "=a"(st.p[2 * J - 7]) : "v"(src.v[p + 1]), "s"(src.srd), "s"(so), "i"(1024 * p + 1024) : "memory");
    }
  }
  ac.q[qb][s] = __builtin_bit_cast(bf16x8, natural8(pack2(acc[2 * s][qb] * SC, acc[2 * s + 1][qb] * SC)));
  asm volatile("" : "+a"(ac.q[qb][s]));
  GAP();
}
template <bool isA, int... Js> DI void q_conv(AC& ac, f32x4 (&acc)[8][4], Stash& st, const Src& sK, const Src& sV, unsigned rk, unsigned p3, iseq<Js...>) { (q_conv1<isA, Js>(ac, acc, st, sK, sV, rk, p3), ...); }
DI void zero_o(AC& ac) {
#pragma unroll
  for (int i = 0; i < 8; ++i)
#pragma unroll
    for (int q = 0; q < 4; ++q) { ac.o[i][q] = f32x4{}; asm volatile("" : "+a"(ac.o[i][q])); }
}

DI unsigned vbid() {
  const unsigned i = blockIdx.x;
  return i;
}
template <bool isA>
DI void body(const char* __restrict__ X, const char* __restrict__ K, const char* __restrict__ V, const char* __restrict__ Wq, const char* __restrict__ Wo,
             const char* __restrict__ Wo2, srd_t code, char* __restrict__ part, char* __restrict__ out) {
  extern __shared__ __attribute__((aligned(16))) char lds[];
  STAMP(0);
  unsigned tid = threadIdx.x;
  PINV(tid);
  const unsigned lane = tid & 63u, w = rfl(tid >> 6);
  const unsigned bid = vbid(), xg = bid & 7u, rr = bid >> 3, h = xg * 4u + w;
  const unsigned b = rr & 15u;
  const unsigned r16 = lane & 15u, g = lane >> 4;
  const char* xp  = X + (size_t)b * 65536u + w * 16384u;
  const char* wqp = Wq + h * 256u;
  const char* wop = Wo + (size_t)xg * 524288u + w * 256u;
  const size_t kvo = isA ? (size_t)(b * 8u + xg) * 131072u : (size_t)(b * 32u + h) * 131072u;
  const unsigned rk = isA ? b : b + 4u * w;
  typedef PK<512u, 4096u, 0x1300u, 0xF300u, 0xC300u, 128u, 32u * 8192u, true> KQ;
  typedef PK<512u, 4096u, 0x1300u, 0xF300u, 0xC300u, 4096u, 8192u, false> KT;
  typedef PK<2048u, 1024u, 0x1000u, 0xF000u, 0xF000u, 128u, 32u * 1024u, false> KO;
  asm volatile("" :: "v"(lds));
  PC pc;
  pc.r01 = 65536u + w * 16384u; pc.r2 = 131072u + w * 8192u;
  const unsigned zq = ((b & 3u) << 14) | ((b >> 2) << 8), co = 4u * w;
  const srd_t srdH = mk_srd(part + HOFF, 4096u + 256u * 128u);
  const unsigned tok_off = rr * 128u, flg_off = 4096u + rr * 1024u;
  const unsigned l8 = (lane & 7u) * 4u;
  unsigned entry_flags;
  asm volatile("buffer_load_dword %0, %1, %2, %3 offen sc1" : "=v"(entry_flags) : "v"(l8), "s"(srdH), "s"(flg_off + xg * 128u) : "memory");
  if constexpr (WARM_CODE && isA) { if (w == 3 && lane < WARM_CODE) asm volatile("buffer_load_dword %0, %1, %2, 0 offen" : "+v"(entry_flags) : "v"((b * WARM_CODE + lane) * 128u), "s"(code) : "memory"); }
  Src sQ, sX, sK, sV, sO;
  mk_src_w<false>(sQ, wqp, 511u * 8192u + 256u, 8192u, lane);
  mk_src_rows<false>(sX, xp, 16384u, 1024u, lane);
  const unsigned p0 = b & 3u, p1 = (p0 + 1u) & 3u, p2 = (p0 + 2u) & 3u, p3 = (p0 + 3u) & 3u, xw = w * 4096u;
  Stash st;
  unsigned dum0 = lane;
  if constexpr (isA && ORD == 0) {
    mk_src_rows<false>(sK, K + kvo, 131072u, 256u, lane);
    mk_src_rows<true>(sV, V + kvo, 131072u, 256u, lane);
    const unsigned to = (lane >> 1) * 1024u + (lane & 1u) * 128u + b * KO::CS;
    const char* wb = Wo2 + (size_t)xg * 524288u + w * 256u;
    if constexpr (EARLY_O == 1) asm volatile("global_load_dword %0, %1, %2\n\tglobal_load_dword %0, %1, %3" : "=&v"(dum0) : "v"(to), "s"(wop), "s"(wb) : "memory");
    stash_load(st, (w & 1u) ? sK.v[2] : sK.v[0], sK, sV, rk, w * 2048u, make_iseq<64>{});
    if constexpr (EARLY_O == 2) asm volatile("global_load_dword %0, %1, %2\n\tglobal_load_dword %0, %1, %3" : "=&v"(dum0) : "v"(to), "s"(wop), "s"(wb) : "memory");
  }
  dma_elem<TOUCH_Q>(sQ, q_soff(zq), KQ::US, pc.r2);
  if constexpr (!(isA && ORD == 2)) dma_unit<X_NT>(sX, p0 * 256u, xw + p0 * 16384u);
  dma_elem<W_NT>(sQ, q_soff(zq + 0x1300u), KQ::US, pc.r01);
  if constexpr (!(isA && ORD == 0)) {
    mk_src_rows<false>(sK, K + kvo, 131072u, 256u, lane);
    mk_src_rows<true>(sV, V + kvo, 131072u, 256u, lane);
  }
  mk_src_w<true>(sO, wop, 511u * 1024u + 256u, 1024u, lane);
  if constexpr (isA) {
    if constexpr (ORD == 2) {
      stash_load(st, (w & 1u) ? sK.v[2] : sK.v[0], sK, sV, rk, w * 2048u, make_iseq<64>{});
      dma_unit<X_NT>(sX, p0 * 256u, xw + p0 * 16384u);
    }
    dma_elem<W_NT>(sQ, q_soff(zq + 0x2200u), KQ::US, pc.r01 + SLOT);
    dma_unit<X_NT>(sX, p1 * 256u, xw + p1 * 16384u);
    dma_unit<X_NT>(sX, p2 * 256u, xw + p2 * 16384u);
    dma_unit<X_NT>(sX, p3 * 256u, xw + p3 * 16384u);
    if constexpr (ORD == 1) stash_load(st, (w & 1u) ? sK.v[2] : sK.v[0], sK, sV, rk, w * 2048u, make_iseq<64>{});
  }
  const unsigned rb = 1024u * (r16 >> 2) + 64u * (r16 & 3u) + 16u * (g ^ ((r16 >> 2) & 2u));
  const unsigned tb = 64u * ((lane >> 2) & 3u) + 32u * (g & 1u) + 8u * (lane & 3u);
  const unsigned tbV = tb + 1024u * g;
  const unsigned sb0 = w * 2048u + lane * 16u;
  st.base[0] = sb0; st.base[1] = sb0 + 65536u; st.base[2] = sb0 + 131072u;
  AC ac;
  if constexpr (!isA) zero_o(ac);
  {
    f32x4 acc[8][4];
    bf16x8 a0[4], a1[4], bf[4];
    asm volatile("s_waitcnt vmcnt(%2)" : "+v"(entry_flags), "+v"(dum0) : "i"(isA ? (ORD == 1 ? 63 : ORD == 0 ? 28 : 20) : 8) : "memory");
    if (w == 0) {
      const unsigned long long mk = __builtin_amdgcn_ballot_w64(((entry_flags >> (lane & 7u)) & 1u) != 0u);
      const unsigned tok = 0x5a5a5a00u | (~(unsigned)mk & 0xffu);
      if (lane == 0) asm volatile("buffer_store_dword %0, off, %1, %2 sc1" :: "v"(tok), "s"(srdH), "s"(tok_off + xg * 4u) : "memory");
    }
    barrier();
    STAMP(1);
    proj_start<KQ, 2>(a0, bf, pc, tbV, rb, zq, q_soff(zq + 0x3100u));
    if constexpr (isA) {
      constexpr int V1 = ORD == 0 ? 28 : 63, V2 = ORD == 1 ? 63 : 28;
      proj_elem<KQ, 2, true,  false, true, W_NT, 63, V1>(acc, a0, a1, bf, pc, sQ);
      STAMP(10);
      proj_elem<KQ, 0, false, false, true, W_NT, V1, V2>(acc, a0, a1, bf, pc, sQ);
      STAMP(11);
      proj_elem<KQ, 1, false, false, true, W_NT, V2, 16>(acc, a0, a1, bf, pc, sQ);
      proj_elem<KQ, 2, false, false, true, W_NT, 16, 16, ORD != 1>(acc, a0, a1, bf, pc, sQ);
    } else {
      dma_elem<W_NT>(sQ, q_soff(zq + 0x2200u), KQ::US, pc.r01 + SLOT);
      proj_elem<KQ, 2, true,  false, true, W_NT, 16, 20, false, true, true>(acc, a0, a1, bf, pc, sQ, NoX{}, &sX, p1 * 256u, xw + p1 * 16384u, p2 * 256u, xw + p2 * 16384u);
      STAMP(10);
      proj_elem<KQ, 0, false, false, true, W_NT, 24, 28, false, true, false>(acc, a0, a1, bf, pc, sQ, NoX{}, &sX, p3 * 256u, xw + p3 * 16384u);
      STAMP(11);
      proj_elem<KQ, 1, false, false, true, W_NT, 28, 24>(acc, a0, a1, bf, pc, sQ);
      proj_elem<KQ, 2, false, false, true, W_NT, 20, 16, true>(acc, a0, a1, bf, pc, sQ);
    }
    STAMP(8);
#pragma unroll 1
    for (unsigned e = 0; e < 3; ++e) {
      if constexpr (isA && PACE_A > 0) __builtin_amdgcn_s_sleep(PACE_A);
      proj_elem<KQ, 0, false, false, true, W_NT, 16, 16>(acc, a0, a1, bf, pc, sQ);
      if constexpr (isA && PACE_A > 0) __builtin_amdgcn_s_sleep(PACE_A);
      proj_elem<KQ, 1, false, false, true, W_NT, 16, 16>(acc, a0, a1, bf, pc, sQ);
      if constexpr (isA && PACE_A > 0) __builtin_amdgcn_s_sleep(PACE_A);
      proj_elem<KQ, 2, false, false, true, W_NT, 16, 16>(acc, a0, a1, bf, pc, sQ);
    }
    STAMP(9);
    if constexpr (isA) {
      proj_elem<KQ, 0, false, false, false, W_NT, 16, 12>(acc, a0, a1, bf, pc, sQ);
      proj_elem<KQ, 1, false, false, false, W_NT, 8, 4>(acc, a0, a1, bf, pc, sQ);
      proj_elem<KQ, 2, false, true, false, W_NT, 0, 0>(acc, a0, a1, bf, pc, sQ);
    } else {
      pc.soff = ksoff(0u, rk);
      proj_elem<KT, 0, false, false, true, KV_NT, 16, 16>(acc, a0, a1, bf, pc, sK);
      proj_elem<KT, 1, false, false, true, KV_NT, 16, 16>(acc, a0, a1, bf, pc, sK);
      pc.soff = ksoff(0u, rk);
      proj_elem<KT, 2, false, true, true, KV_NT, 16, 16>(acc, a0, a1, bf, pc, sV);
    }
    barrier();
    STAMP(2);
    acc_fence_v(acc);
    q_conv<isA>(ac, acc, st, sK, sV, rk, w * 16384u, make_iseq<16>{});
  }
  ST SA, SB;
  if constexpr (isA) {
    zero_o(ac);
    const unsigned kb = rb, vb = SLOT + tbV;
    asm volatile("s_waitcnt lgkmcnt(0)" ::: "memory");
    barrier();
    k_read(ac, kb + kv_pair(0) * 16384u);
    wait_lgkm0();
    GAP();
    qk0<false>(ac, SA, sK, 0u, 0u, make_iseq<32>{});
    k_read(ac, kb + kv_pair(1) * 16384u);
    sm_first(ac, SA);
    STAMP(13);
    unsigned i4 = 0;
    auto av = [&](PA& p) { if constexpr (PACE_ATT > 0) __builtin_amdgcn_s_sleep(PACE_ATT);
                           PINS(i4); p.rd0 = vb + (((unsigned)(kv_tab() >> i4) & 15u) << 14); p.rd1 = p.rd0 ^ 32u; PINV(p.rd0); PINV(p.rd1); };
    auto ak = [&](PA& p) { if constexpr (PACE_ATT > 0) __builtin_amdgcn_s_sleep(PACE_ATT);
                           PINS(i4); p.rd0 = kb + (((unsigned)((kv_tab() >> 8) >> i4) & 15u) << 14); PINV(p.rd0); i4 += 4u; };
    auto an = [&](PA&) { i4 += 4u; };
    unsigned dum = lane, to = (lane >> 3) * 1024u + (lane & 4u) * 32u + (lane & 3u) * 4u;
    PINV(to);
    const char* ta = wop + b * KO::CS;
    const char* tb2 = Wo2 + (size_t)xg * 524288u + w * 256u + b * KO::CS;
    auto at = [&](PA& p) __attribute__((always_inline)) {
      PINS(i4); p.rd0 = vb + (((unsigned)(kv_tab() >> i4) & 15u) << 14); p.rd1 = p.rd0 ^ 32u; PINV(p.rd0); PINV(p.rd1);
      if constexpr (TOUCH_O) {
        const char* tp;
        if constexpr (TOUCH_B == 1) asm volatile("s_bitcmp0_b32 %1, 4\n\ts_cselect_b64 %0, %2, %3" : "=s"(tp) : "s"(i4), "s"(ta), "s"(tb2) : "scc");
        else if constexpr (TOUCH_B == 2) asm volatile("s_cmp_lt_u32 %1, 40\n\ts_cselect_b64 %0, %2, %3" : "=s"(tp) : "s"(i4), "s"(ta), "s"(tb2) : "scc");
        else tp = ta;
        asm volatile("global_load_dword %0, %1, %2 nt\n\tglobal_load_dword %0, %1, %2" : "+v"(dum) : "v"(to), "s"(tp) : "memory");
        to = (to + 8192u) & 32767u; PINV(to);
      }
    };
    while (i4 < 56u) {
      if (i4 >= 24u) {
        if (i4 <= 40u) { wait_lgkm0(); barrier(); }
        if (i4 == 24u) stash_late(st, make_iseq<24>{});
        if (i4 >= 40u) dma_elem<W_NT>(sO, (co + ((i4 >> 4) & 1u)) * KO::CS, KO::US, pc.r01 + ((i4 >> 4) & 1u) * SLOT);
      }
      phaseA<true, false, KV_NT, 63>(ac, SA, SB, at, sK, 0u, st);
      phaseB<true, true, false, KV_NT, 63>(ac, SB, ak, sK, 0u, st);
      phaseA<true, false, KV_NT, 63>(ac, SB, SA, at, sK, 0u, st);
      phaseB<true, true, false, KV_NT, 63>(ac, SA, ak, sK, 0u, st);
    }
    STAMP(14);
    if constexpr (TOUCH_B != 3) asm volatile("s_waitcnt vmcnt(0)" : "+v"(dum) :: "memory");
#define TB3 if constexpr (TOUCH_B == 3) { asm volatile("global_load_dword %0, %1, %2 nt\n\tglobal_load_dword %0, %1, %2" : "+v"(dum) : "v"(to), "s"(tb2) : "memory"); to = (to + 8192u) & 32767u; PINV(to); }
    TB3
    phaseA<true, false, KV_NT, 63>(ac, SA, SB, av, sK, 0u, st);
    TB3
    phaseB<true, false, false, KV_NT, 63>(ac, SB, an, sK, 0u, st);
    TB3
    phaseA<false, false, KV_NT, 63>(ac, SB, SA, av, sK, 0u, st);
    TB3
    dum0 = dum;
  } else {
    const unsigned P0 = pc.r01, P1 = pc.r01 + SLOT, P2 = pc.r2, P3 = w * 16384u;
    unsigned v0 = tbV + P0, v2 = tbV + P2, x0 = v0 ^ 32u, x2 = v2 ^ 32u, k1 = rb + P1, k3 = rb + P3;
    PINV(v0); PINV(v2); PINV(x0); PINV(x2); PINV(k1); PINV(k3);
    wait_vm<48>();
    k_read(ac, k1 - SLOT);
    wait_lgkm0();
    GAP();
    qk0<true>(ac, SA, sV, ksoff(1u, rk), P0, make_iseq<32>{});
    wait_vm<48>();
    GAP();
    k_read(ac, k1);
    sm_first(ac, SA);
    STAMP(13);
    unsigned so = ksoff(2u, rk), i = 0;
#define AV(RD, FILL) [&](PA& p) { PINS(so); p.soff = (so + 8192u) & 0x1ffffu; PINS(p.soff); p.fill = (FILL); p.rd0 = v##RD; p.rd1 = x##RD; }
#define AK(RD, FILL) [&](PA& p) { p.soff = so; so = (so + 8192u) & 0x1ffffu; PINS(so); p.fill = (FILL); p.rd0 = k##RD; }
#define TV(RD, FILL, SO) [&](PA& p) { p.soff = (SO); p.fill = (FILL); p.rd0 = v##RD; p.rd1 = x##RD; }
#define TK(RD, FILL, SO) [&](PA& p) { p.soff = (SO); p.fill = (FILL); p.rd0 = k##RD; }
    for (; i < 10; i += 2) {
      phaseA<true, true, KV_NT, 21>(ac, SA, SB, AV(2, P1), sK, 4096u, st);
      phaseB<true, true, true, KV_NT, 22>(ac, SB, AK(3, P2), sV, 4096u, st);
      phaseA<true, true, KV_NT, 21>(ac, SB, SA, AV(0, P3), sK, 4096u, st);
      phaseB<true, true, true, KV_NT, 22>(ac, SA, AK(1, P0), sV, 4096u, st);
    }
    st.base[0] = P1 + lane * 16u;
    phaseA<true, false, KV_NT, 16, 0>(ac, SA, SB, TV(2, P1, 0u), sK, 0u, st);
    phaseB<true, true, true, KV_NT, 14>(ac, SB, TK(3, P2, ksoff(12u, rk)), sV, 4096u, st);
    phaseA<true, true, KV_NT, 13>(ac, SB, SA, TV(0, P3, ksoff(14u, rk)), sK, 4096u, st);
    st.base[0] = P0 + lane * 16u;
    phaseB<true, true, false, KV_NT, 63, 8>(ac, SA, TK(1, P0, 0u), sV, 0u, st);
    STAMP(14);
    phaseA<true, true, KV_NT, 13>(ac, SA, SB, TV(2, P1, ksoff(15u, rk)), sK, 4096u, st);
    phaseB<true, true, false, KV_NT, 8>(ac, SB, TK(3, P2, 0u), sV, 0u, st);
    phaseA<true, true, KV_NT, 63>(ac, SB, SA, TV(0, P3, ksoff(15u, rk)), sV, 4096u, st);
    phaseB<true, true, true, W_NT, 14>(ac, SA, TK(1, P0, co * KO::CS), sO, KO::US, st);
    unsigned v4 = tbV + P3 + SLOT, x4 = v4 ^ 32u; PINV(v4); PINV(x4);
    phaseA<true, true, W_NT, 63>(ac, SA, SB, TV(4, P1, (co + 1u) * KO::CS), sO, KO::US, st);
    phaseB<true, false, true, W_NT, 63>(ac, SB, TK(3, P2, (co + 2u) * KO::CS), sO, KO::US, st);
    unsigned v3 = tbV + P3, x3 = v3 ^ 32u; PINV(v3); PINV(x3);
    phaseA<false, false, W_NT, 24>(ac, SB, SA, TV(3, P3, 0u), sO, KO::US, st);
#undef AV
#undef AK
#undef TV
#undef TK
  }
  {
    const unsigned l16 = lane * 16u;
    unsigned ow = w * 16384u + l16;
    PINV(ow);
    f32x4 acc[8][4];
    bf16x8 a0[4], a1[4], b0[4], bf[4];
    float inv[4];
    proj_setup<KO>(pc, tb + 512u * g, l16, (co + 3u) << 12, (co + 3u) * KO::CS);
    GAP();
    phaseL<KO, (isA ? 12 : 20)>(ac, inv, b0, a0, pc, make_iseq<36>{});
    STAMP(3);
    if constexpr (isA) {
      barrier();
      dma_elem<W_NT>(sO, (co + 2u) * KO::CS, KO::US, pc.r2);
    }
    const bool ok = ac.l[0][0] <= LMAX && ac.l[1][0] <= LMAX && ac.l[2][0] <= LMAX && ac.l[3][0] <= LMAX;
    if (__builtin_expect(__builtin_amdgcn_ballot_w64(!ok) != 0ull, 0)) {
      acc_fence_a(ac.o);
      redo_head(ac, K + kvo, V + kvo, lane, ow);
#pragma unroll
      for (int q = 0; q < 4; ++q) {
        inv[q] = 1.0f;
        b0[q] = lds128(ow + q * 1024u);
#pragma unroll
        for (int s = 1; s < 4; ++s) {
          const u32x4 t = __builtin_bit_cast(u32x4, lds128(ow + s * 4096u + q * 1024u));
          ac.o[2 * s][q] = f32x4{__uint_as_float(t[0] << 16), __uint_as_float(t[0] & 0xffff0000u), __uint_as_float(t[1] << 16), __uint_as_float(t[1] & 0xffff0000u)};
          ac.o[2 * s + 1][q] = f32x4{__uint_as_float(t[2] << 16), __uint_as_float(t[2] & 0xffff0000u), __uint_as_float(t[3] << 16), __uint_as_float(t[3] & 0xffff0000u)};
        }
      }
      wait_lgkm0();
      acc_fence_a(ac.o);
    }
#pragma unroll
    for (int q = 0; q < 4; ++q) bf[q] = bf16x8{};
    GAP();
    proj_elem<KO, 0, true, false, true, W_NT, 16, 16>(acc, a0, a1, b0, pc, sO, CvX<0>{ac, inv, b0, bf, ow});
    proj_elem<KO, 1, false, false, true, W_NT, 16, 16>(acc, a0, a1, bf, pc, sO, CvX<1>{ac, inv, bf, b0, ow});
    proj_elem<KO, 2, false, false, true, W_NT, 16, 16>(acc, a0, a1, b0, pc, sO, CvX<2>{ac, inv, b0, bf, ow});
    if constexpr (isA && TOUCH_B == 3) PINV(dum0);
    proj_elem<KO, 0, false, false, true, W_NT, 16, 16, true>(acc, a0, a1, bf, pc, sO, CvX<3>{ac, inv, bf, b0, ow});
    STAMP(4);
#pragma unroll 1
    for (unsigned e = 0; e < 3; ++e) {
      proj_elem<KO, 1, false, false, true, W_NT, 16, 16>(acc, a0, a1, bf, pc, sO);
      proj_elem<KO, 2, false, false, true, W_NT, 16, 16>(acc, a0, a1, bf, pc, sO);
      proj_elem<KO, 0, false, false, true, W_NT, 16, 16>(acc, a0, a1, bf, pc, sO);
    }
    CYC(6);
    unsigned toks;
    asm volatile("buffer_load_dword %0, %1, %2, %3 offen sc1" : "=v"(toks) : "v"(l8), "s"(srdH), "s"(tok_off) : "memory");
    Pub pb;
    pb.srd = mk_srd(part + POFF + (size_t)rr * 524288u, 524288u);
    pb.v[0] = l16; pb.v[1] = l16 + 4096u; pb.so = xg * 65536u + w * 16384u;
    PINV(pb.v[1]);
#pragma unroll
    for (int i = 0; i < 16; ++i) pb.pk[i] = u32x4{};
    proj_elem<KO, 1, false, false, false, W_NT, 17, 13>(acc, a0, a1, bf, pc, sO);
    proj_elem<KO, 2, false, false, false, W_NT, 9, 5>(acc, a0, a1, bf, pc, sO);
    CYC(7);
    if constexpr (isA && PUB_SLEEP > 0) {
      proj_elem<KO, 0, false, true, false, W_NT, 0, 0>(acc, a0, a1, bf, pc, sO);
      STAMP(5);
      pub_slow(acc, pb, make_iseq<16>{});
    } else {
      proj_elem<KO, 0, false, true, false, W_NT, 0, 0>(acc, a0, a1, bf, pc, sO, PubX{acc, pb});
      STAMP(5);
      pub_rest(acc, pb, make_iseq<16>{});
    }
    unsigned fl;
    asm volatile("buffer_load_dword %0, %1, %2, %3 offen sc1" : "=v"(fl) : "v"(l8), "s"(srdH), "s"(flg_off + xg * 128u) : "memory");
    PINV(toks);
    STAMP(6);
    const unsigned fo = lane * 128u + xg * 4u, tau = __builtin_amdgcn_readlane(toks, xg);
    unsigned oo = ((b * 64u + 16u * w + r16) * 1024u + (isA ? 0u : 512u) + 64u * xg + 8u * g) * 2u;
    PINV(oo);
    wait_vm<9>();
    if (lane == 2u * w) asm volatile("buffer_store_dword %0, %1, %2, %3 offen sc1" :: "v"(toks), "v"(fo), "s"(srdH), "s"(flg_off) : "memory");
    wait_vm<2>();
    if (lane == 2u * w + 1u) asm volatile("buffer_store_dword %0, %1, %2, %3 offen sc1" :: "v"(toks), "v"(fo), "s"(srdH), "s"(flg_off) : "memory");
    asm volatile("s_waitcnt vmcnt(2)" : "+v"(fl) : "v"(pb.pk[0]), "v"(pb.pk[1]), "v"(pb.pk[2]), "v"(pb.pk[3]), "v"(pb.pk[4]), "v"(pb.pk[5]), "v"(pb.pk[6]), "v"(pb.pk[7]),
                 "v"(pb.pk[8]), "v"(pb.pk[9]), "v"(pb.pk[10]), "v"(pb.pk[11]), "v"(pb.pk[12]), "v"(pb.pk[13]), "v"(pb.pk[14]), "v"(pb.pk[15]) : "memory");
    STAMP(7);
    f32x16 f[8];
    unsigned got = 0xffffff00u, spin = 1u << 22, fresh, so;
    asm volatile(
        ".Ltop_%=:\n\tv_cmp_eq_u32_e32 vcc, %[tau], %[fl]\n\ts_andn2_b32 %[fr], vcc_lo, %[got]\n\ts_cbranch_scc0 .Lpoll_%=\n\ts_or_b32 %[got], %[got], %[fr]\n\t"
        "s_bitcmp1_b32 %[fr], 0\n\ts_cbranch_scc0 .La0_%=\n\ts_add_u32 %[so], %[ro], 0\n\t"
        "buffer_load_dwordx4 v[136:139], %[l16], %[sp], %[so] offen sc1\n\tbuffer_load_dwordx4 v[140:143], %[l16], %[sp], %[so] offen offset:1024 sc1\n.La0_%=:\n\t"
        "s_bitcmp1_b32 %[fr], 1\n\ts_cbranch_scc0 .La1_%=\n\ts_add_u32 %[so], %[ro], 65536\n\t"
        "buffer_load_dwordx4 v[152:155], %[l16], %[sp], %[so] offen sc1\n\tbuffer_load_dwordx4 v[156:159], %[l16], %[sp], %[so] offen offset:1024 sc1\n.La1_%=:\n\t"
        "s_bitcmp1_b32 %[fr], 2\n\ts_cbranch_scc0 .La2_%=\n\ts_add_u32 %[so], %[ro], 131072\n\t"
        "buffer_load_dwordx4 v[168:171], %[l16], %[sp], %[so] offen sc1\n\tbuffer_load_dwordx4 v[172:175], %[l16], %[sp], %[so] offen offset:1024 sc1\n.La2_%=:\n\t"
        "s_bitcmp1_b32 %[fr], 3\n\ts_cbranch_scc0 .La3_%=\n\ts_add_u32 %[so], %[ro], 196608\n\t"
        "buffer_load_dwordx4 v[184:187], %[l16], %[sp], %[so] offen sc1\n\tbuffer_load_dwordx4 v[188:191], %[l16], %[sp], %[so] offen offset:1024 sc1\n.La3_%=:\n\t"
        "s_bitcmp1_b32 %[fr], 4\n\ts_cbranch_scc0 .La4_%=\n\ts_add_u32 %[so], %[ro], 262144\n\t"
        "buffer_load_dwordx4 v[200:203], %[l16], %[sp], %[so] offen sc1\n\tbuffer_load_dwordx4 v[204:207], %[l16], %[sp], %[so] offen offset:1024 sc1\n.La4_%=:\n\t"
        "s_bitcmp1_b32 %[fr], 5\n\ts_cbranch_scc0 .La5_%=\n\ts_add_u32 %[so], %[ro], 327680\n\t"
        "buffer_load_dwordx4 v[216:219], %[l16], %[sp], %[so] offen sc1\n\tbuffer_load_dwordx4 v[220:223], %[l16], %[sp], %[so] offen offset:1024 sc1\n.La5_%=:\n\t"
        "s_bitcmp1_b32 %[fr], 6\n\ts_cbranch_scc0 .La6_%=\n\ts_add_u32 %[so], %[ro], 393216\n\t"
        "buffer_load_dwordx4 v[232:235], %[l16], %[sp], %[so] offen sc1\n\tbuffer_load_dwordx4 v[236:239], %[l16], %[sp], %[so] offen offset:1024 sc1\n.La6_%=:\n\t"
        "s_bitcmp1_b32 %[fr], 7\n\ts_cbranch_scc0 .La7_%=\n\ts_add_u32 %[so], %[ro], 458752\n\t"
        "buffer_load_dwordx4 v[248:251], %[l16], %[sp], %[so] offen sc1\n\tbuffer_load_dwordx4 v[252:255], %[l16], %[sp], %[so] offen offset:1024 sc1\n.La7_%=:\n\t"
        ".Lpoll_%=:\n\ts_cmp_eq_u32 %[got], -1\n\ts_cselect_b32 %[so], %[tok], %[flg]\n\tbuffer_load_dword %[fl], %[l8], %[sh], %[so] offen sc1\n\t"
        "s_cmp_eq_u32 %[fr], 0\n\ts_cbranch_scc1 .Lwait_%=\n\ts_waitcnt vmcnt(1)\n\t"
        "s_bitcmp1_b32 %[fr], 0\n\ts_cbranch_scc0 .Lb0_%=\n\t"
        "v_lshlrev_b32_e32 v128, 16, v136\n\tv_and_b32_e32 v129, %[hi], v136\n\tv_lshlrev_b32_e32 v130, 16, v137\n\tv_and_b32_e32 v131, %[hi], v137\n\t"
        "v_lshlrev_b32_e32 v132, 16, v138\n\tv_and_b32_e32 v133, %[hi], v138\n\tv_lshlrev_b32_e32 v134, 16, v139\n\tv_and_b32_e32 v135, %[hi], v139\n\t"
        "v_lshlrev_b32_e32 v136, 16, v140\n\tv_and_b32_e32 v137, %[hi], v140\n\tv_lshlrev_b32_e32 v138, 16, v141\n\tv_and_b32_e32 v139, %[hi], v141\n\t"
        "v_lshlrev_b32_e32 v140, 16, v142\n\tv_and_b32_e32 v141, %[hi], v142\n\tv_lshlrev_b32_e32 v142, 16, v143\n\tv_and_b32_e32 v143, %[hi], v143\n\t"
        ".Lb0_%=:\n\t"
        "s_bitcmp1_b32 %[fr], 1\n\ts_cbranch_scc0 .Lb1_%=\n\t"
        "v_lshlrev_b32_e32 v144, 16, v152\n\tv_and_b32_e32 v145, %[hi], v152\n\tv_lshlrev_b32_e32 v146, 16, v153\n\tv_and_b32_e32 v147, %[hi], v153\n\t"
        "v_lshlrev_b32_e32 v148, 16, v154\n\tv_and_b32_e32 v149, %[hi], v154\n\tv_lshlrev_b32_e32 v150, 16, v155\n\tv_and_b32_e32 v151, %[hi], v155\n\t"
        "v_lshlrev_b32_e32 v152, 16, v156\n\tv_and_b32_e32 v153, %[hi], v156\n\tv_lshlrev_b32_e32 v154, 16, v157\n\tv_and_b32_e32 v155, %[hi], v157\n\t"
        "v_lshlrev_b32_e32 v156, 16, v158\n\tv_and_b32_e32 v157, %[hi], v158\n\tv_lshlrev_b32_e32 v158, 16, v159\n\tv_and_b32_e32 v159, %[hi], v159\n\t"
        ".Lb1_%=:\n\t"
        "s_bitcmp1_b32 %[fr], 2\n\ts_cbranch_scc0 .Lb2_%=\n\t"
        "v_lshlrev_b32_e32 v160, 16, v168\n\tv_and_b32_e32 v161, %[hi], v168\n\tv_lshlrev_b32_e32 v162, 16, v169\n\tv_and_b32_e32 v163, %[hi], v169\n\t"
        "v_lshlrev_b32_e32 v164, 16, v170\n\tv_and_b32_e32 v165, %[hi], v170\n\tv_lshlrev_b32_e32 v166, 16, v171\n\tv_and_b32_e32 v167, %[hi], v171\n\t"
        "v_lshlrev_b32_e32 v168, 16, v172\n\tv_and_b32_e32 v169, %[hi], v172\n\tv_lshlrev_b32_e32 v170, 16, v173\n\tv_and_b32_e32 v171, %[hi], v173\n\t"
        "v_lshlrev_b32_e32 v172, 16, v174\n\tv_and_b32_e32 v173, %[hi], v174\n\tv_lshlrev_b32_e32 v174, 16, v175\n\tv_and_b32_e32 v175, %[hi], v175\n\t"
        ".Lb2_%=:\n\t"
        "s_bitcmp1_b32 %[fr], 3\n\ts_cbranch_scc0 .Lb3_%=\n\t"
        "v_lshlrev_b32_e32 v176, 16, v184\n\tv_and_b32_e32 v177, %[hi], v184\n\tv_lshlrev_b32_e32 v178, 16, v185\n\tv_and_b32_e32 v179, %[hi], v185\n\t"
        "v_lshlrev_b32_e32 v180, 16, v186\n\tv_and_b32_e32 v181, %[hi], v186\n\tv_lshlrev_b32_e32 v182, 16, v187\n\tv_and_b32_e32 v183, %[hi], v187\n\t"
        "v_lshlrev_b32_e32 v184, 16, v188\n\tv_and_b32_e32 v185, %[hi], v188\n\tv_lshlrev_b32_e32 v186, 16, v189\n\tv_and_b32_e32 v187, %[hi], v189\n\t"
        "v_lshlrev_b32_e32 v188, 16, v190\n\tv_and_b32_e32 v189, %[hi], v190\n\tv_lshlrev_b32_e32 v190, 16, v191\n\tv_and_b32_e32 v191, %[hi], v191\n\t"
        ".Lb3_%=:\n\t"
        "s_bitcmp1_b32 %[fr], 4\n\ts_cbranch_scc0 .Lb4_%=\n\t"
        "v_lshlrev_b32_e32 v192, 16, v200\n\tv_and_b32_e32 v193, %[hi], v200\n\tv_lshlrev_b32_e32 v194, 16, v201\n\tv_and_b32_e32 v195, %[hi], v201\n\t"
        "v_lshlrev_b32_e32 v196, 16, v202\n\tv_and_b32_e32 v197, %[hi], v202\n\tv_lshlrev_b32_e32 v198, 16, v203\n\tv_and_b32_e32 v199, %[hi], v203\n\t"
        "v_lshlrev_b32_e32 v200, 16, v204\n\tv_and_b32_e32 v201, %[hi], v204\n\tv_lshlrev_b32_e32 v202, 16, v205\n\tv_and_b32_e32 v203, %[hi], v205\n\t"
        "v_lshlrev_b32_e32 v204, 16, v206\n\tv_and_b32_e32 v205, %[hi], v206\n\tv_lshlrev_b32_e32 v206, 16, v207\n\tv_and_b32_e32 v207, %[hi], v207\n\t"
        ".Lb4_%=:\n\t"
        "s_bitcmp1_b32 %[fr], 5\n\ts_cbranch_scc0 .Lb5_%=\n\t"
        "v_lshlrev_b32_e32 v208, 16, v216\n\tv_and_b32_e32 v209, %[hi], v216\n\tv_lshlrev_b32_e32 v210, 16, v217\n\tv_and_b32_e32 v211, %[hi], v217\n\t"
        "v_lshlrev_b32_e32 v212, 16, v218\n\tv_and_b32_e32 v213, %[hi], v218\n\tv_lshlrev_b32_e32 v214, 16, v219\n\tv_and_b32_e32 v215, %[hi], v219\n\t"
        "v_lshlrev_b32_e32 v216, 16, v220\n\tv_and_b32_e32 v217, %[hi], v220\n\tv_lshlrev_b32_e32 v218, 16, v221\n\tv_and_b32_e32 v219, %[hi], v221\n\t"
        "v_lshlrev_b32_e32 v220, 16, v222\n\tv_and_b32_e32 v221, %[hi], v222\n\tv_lshlrev_b32_e32 v222, 16, v223\n\tv_and_b32_e32 v223, %[hi], v223\n\t"
        ".Lb5_%=:\n\t"
        "s_bitcmp1_b32 %[fr], 6\n\ts_cbranch_scc0 .Lb6_%=\n\t"
        "v_lshlrev_b32_e32 v224, 16, v232\n\tv_and_b32_e32 v225, %[hi], v232\n\tv_lshlrev_b32_e32 v226, 16, v233\n\tv_and_b32_e32 v227, %[hi], v233\n\t"
        "v_lshlrev_b32_e32 v228, 16, v234\n\tv_and_b32_e32 v229, %[hi], v234\n\tv_lshlrev_b32_e32 v230, 16, v235\n\tv_and_b32_e32 v231, %[hi], v235\n\t"
        "v_lshlrev_b32_e32 v232, 16, v236\n\tv_and_b32_e32 v233, %[hi], v236\n\tv_lshlrev_b32_e32 v234, 16, v237\n\tv_and_b32_e32 v235, %[hi], v237\n\t"
        "v_lshlrev_b32_e32 v236, 16, v238\n\tv_and_b32_e32 v237, %[hi], v238\n\tv_lshlrev_b32_e32 v238, 16, v239\n\tv_and_b32_e32 v239, %[hi], v239\n\t"
        ".Lb6_%=:\n\t"
        "s_bitcmp1_b32 %[fr], 7\n\ts_cbranch_scc0 .Lb7_%=\n\t"
        "v_lshlrev_b32_e32 v240, 16, v248\n\tv_and_b32_e32 v241, %[hi], v248\n\tv_lshlrev_b32_e32 v242, 16, v249\n\tv_and_b32_e32 v243, %[hi], v249\n\t"
        "v_lshlrev_b32_e32 v244, 16, v250\n\tv_and_b32_e32 v245, %[hi], v250\n\tv_lshlrev_b32_e32 v246, 16, v251\n\tv_and_b32_e32 v247, %[hi], v251\n\t"
        "v_lshlrev_b32_e32 v248, 16, v252\n\tv_and_b32_e32 v249, %[hi], v252\n\tv_lshlrev_b32_e32 v250, 16, v253\n\tv_and_b32_e32 v251, %[hi], v253\n\t"
        "v_lshlrev_b32_e32 v252, 16, v254\n\tv_and_b32_e32 v253, %[hi], v254\n\tv_lshlrev_b32_e32 v254, 16, v255\n\tv_and_b32_e32 v255, %[hi], v255\n\t"
        ".Lb7_%=:\n\t"
        "s_cmp_eq_u32 %[got], -1\n\ts_cbranch_scc1 .Ldone_%=\n"
        ".Lwait_%=:\n\ts_sleep %[slp]\n\ts_waitcnt vmcnt(0)\n\ts_sub_u32 %[spin], %[spin], 1\n\ts_cbranch_scc0 .Ltop_%=\n"
        ".Ldone_%=:"
        : "=&{v[128:143]}"(f[0]), "=&{v[144:159]}"(f[1]), "=&{v[160:175]}"(f[2]), "=&{v[176:191]}"(f[3]), "=&{v[192:207]}"(f[4]), "=&{v[208:223]}"(f[5]), "=&{v[224:239]}"(f[6]), "=&{v[240:255]}"(f[7]),
          [fl] "+v"(fl), [got] "+s"(got), [spin] "+s"(spin), [fr] "=&s"(fresh), [so] "=&s"(so)
        : [tau] "s"(tau), [l16] "v"(l16), [l8] "v"(l8), [sp] "s"(pb.srd), [sh] "s"(srdH), [ro] "s"(xg * 8192u + w * 2048u), [tok] "s"(tok_off), [flg] "s"(flg_off + xg * 128u),
          [hi] "s"(0xffff0000u), [slp] "i"(isA ? SLEEP_A : SLEEP_B)
        : "vcc", "scc", "memory");
    STAMP(15);
    const f32x16 t = ((f[0] + f[1]) + (f[2] + f[3])) + ((f[4] + f[5]) + (f[6] + f[7]));
#pragma unroll
    for (int s = 0; s < 2; ++s)
      *(u32x4*)(out + oo + s * 64) = natural8(u32x4{cvtpk(t[8 * s], t[8 * s + 1]), cvtpk(t[8 * s + 2], t[8 * s + 3]), cvtpk(t[8 * s + 4], t[8 * s + 5]), cvtpk(t[8 * s + 6], t[8 * s + 7])});
    asm volatile("s_waitcnt vmcnt(2)" : "+v"(fl) :: "memory");
    if ((lane >> 1) == w && fl != toks) asm volatile("buffer_store_dword %0, %1, %2, %3 offen sc1" :: "v"(fl), "v"(fo), "s"(srdH), "s"(flg_off) : "memory");
  }
}

__global__ __launch_bounds__(256, 1)
void fused_attn(const char* __restrict__ Ax, const char* __restrict__ Ak, const char* __restrict__ Av, const char* __restrict__ Awq, const char* __restrict__ Awo,
                const char* __restrict__ Bx, const char* __restrict__ Bk, const char* __restrict__ Bv, const char* __restrict__ Bwq, const char* __restrict__ Bwo,
                char* __restrict__ part, char* __restrict__ out) {
  const char* pc; unsigned left;
  asm volatile("s_getpc_b64 %0\n.Lpc_%=:\n\ts_mov_b32 %1, .Lhot_end-.Lpc_%=" : "=s"(pc), "=s"(left));
  const srd_t code = mk_srd(pc, left);
  if (vbid() >= 128) { body<true>(Ax, Ak, Av, Awq, Awo, Bwo, code, part, out); asm volatile(".Lhot_end:"); __builtin_amdgcn_endpgm(); }
  body<false>(Bx, Bk, Bv, Bwq, Bwo, Bwo, code, part, out);
}

extern "C" void kernel_launch(void* const* d_in, const int* in_sizes, int n_in, void* d_out, int out_size, void* d_ws, size_t ws_size, hipStream_t stream) {
  (void)in_sizes; (void)n_in; (void)out_size; (void)ws_size;
  (void)hipFuncSetAttribute((const void*)fused_attn, hipFuncAttributeMaxDynamicSharedMemorySize, (int)LDS_BYTES);
  fused_attn<<<256, 256, LDS_BYTES, stream>>>((const char*)d_in[0], (const char*)d_in[1], (const char*)d_in[2], (const char*)d_in[3], (const char*)d_in[4],
                                              (const char*)d_in[5], (const char*)d_in[6], (const char*)d_in[7], (const char*)d_in[8], (const char*)d_in[9], (char*)d_ws, (char*)d_out);
}
```

```cpp
#include <hip/hip_runtime.h>
#include <hip/hip_bf16.h>
#include <utility>

typedef __bf16 bf16x8 __attribute__((ext_vector_type(8)));
typedef __bf16 bf16x4 __attribute__((ext_vector_type(4)));
typedef __bf16 bf16x2 __attribute__((ext_vector_type(2)));
typedef float f32x4 __attribute__((ext_vector_type(4)));
typedef float f32x2 __attribute__((ext_vector_type(2)));
typedef float f32x16 __attribute__((ext_vector_type(16)));
typedef unsigned u32x4 __attribute__((ext_vector_type(4)));
#define LAS __attribute__((address_space(3)))
#define DI __device__ __forceinline__
typedef __amdgpu_buffer_rsrc_t srd_t;
template <int... Is> using iseq = std::integer_sequence<int, Is...>;
template <int N> using make_iseq = std::make_integer_sequence<int, N>;

#define KV_NT 1
#define W_NT 0
#define X_NT 0
#define SLEEP_A 8
#define SLEEP_B 0
#define PACE_A 0
#define PACE_ATT 0
#define TOUCH_Q 1
#define TOUCH_O 3
#define TO_SKIP 16u
#define TO_B 8u
#define WARM_CODE 32
#define ALT 0
#define ORD 2
#define EARLY_O 0
#define TOUCH_B 4
#define PUB_SLEEP 0
#define KVPOL "nt"
#define CVV 0
#define DELAY_A 0
#define WS_END 0

constexpr unsigned SLOT = 8192, LDS_BYTES = 20 * SLOT;
constexpr unsigned POFF = (unsigned)WS_END << 20, HOFF = POFF + (16u << 20);
constexpr float SC = 0.12751743082f;
constexpr float LMAX = 1048576.0f;

#define NO_REDUCE_STAMPS
#define GAP() __builtin_amdgcn_sched_barrier(0)
#define STAMP(i) do {} while (0)
#define CYC(i) do {} while (0)
#define PINS(x) asm volatile("" : "+s"(x))
#define PINV(x) asm volatile("" : "+v"(x))
DI unsigned rfl(unsigned x) { return __builtin_amdgcn_readfirstlane(x); }
DI unsigned cvtpk(float lo, float hi) { f32x2 v = {lo, hi}; bf16x2 b = __builtin_convertvector(v, bf16x2); return __builtin_bit_cast(unsigned, b); }
DI u32x4 pack2(const f32x4& a, const f32x4& b) { return u32x4{cvtpk(a[0], a[1]), cvtpk(a[2], a[3]), cvtpk(b[0], b[1]), cvtpk(b[2], b[3])}; }
DI u32x4 natural8(u32x4 w) {
  auto s0 = __builtin_amdgcn_permlane32_swap(w[0], w[2], false, false);
  auto s1 = __builtin_amdgcn_permlane32_swap(w[1], w[3], false, false);
  auto t0 = __builtin_amdgcn_permlane16_swap(s0[0], s0[1], false, false);
  auto t1 = __builtin_amdgcn_permlane16_swap(s1[0], s1[1], false, false);
  return u32x4{t0[0], t1[0], t0[1], t1[1]};
}
#define MF "v_mfma_f32_16x16x32_bf16 "
template <bool Z> DI void mm_vvv(f32x4& c, const bf16x8& a, const bf16x8& b) {
  if constexpr (Z) asm volatile(MF "%0, %1, %2, 0" : "=&v"(c) : "v"(a), "v"(b));
  else             asm volatile(MF "%0, %1, %2, %0" : "+v"(c) : "v"(a), "v"(b));
}
template <bool Z> DI void mm_vva(f32x4& c, const bf16x8& a, const bf16x8& b) {
  if constexpr (Z) asm volatile(MF "%0, %1, %2, 0" : "=&v"(c) : "v"(a), "a"(b));
  else             asm volatile(MF "%0, %1, %2, %0" : "+v"(c) : "v"(a), "a"(b));
}
DI void mm_vca(f32x4& d, const bf16x8& a, const bf16x8& b, const f32x4& c) { asm volatile(MF "%0, %1, %2, %3" : "=&v"(d) : "v"(a), "a"(b), "v"(c)); }
DI void mm_avv(f32x4& c, const bf16x8& a, const bf16x8& b) { asm volatile(MF "%0, %1, %2, %0" : "+a"(c) : "v"(a), "v"(b)); }
DI void set_m0(unsigned v) { asm volatile("s_mov_b32 m0, %0" :: "s"(v) : "memory"); }
template <int NT, int P> DI void dma_p(unsigned voff, srd_t srd, unsigned soff) {
  if constexpr (NT) asm volatile("buffer_load_dwordx4 %0, %1, %2 offen offset:%3 " KVPOL " lds" :: "v"(voff), "s"(srd), "s"(soff), "i"(1024 * P) : "memory");
  else              asm volatile("buffer_load_dwordx4 %0, %1, %2 offen offset:%3 lds" :: "v"(voff), "s"(srd), "s"(soff), "i"(1024 * P) : "memory");
}
struct Src { unsigned v[4]; srd_t srd; };
template <int NT> DI void dma_unit(const Src& s, unsigned soff, unsigned ldsaddr) {
  asm volatile("s_mov_b32 m0, %0\n\ts_nop 0" :: "s"(ldsaddr) : "memory");
  dma_p<NT, 0>(s.v[0], s.srd, soff); dma_p<NT, 1>(s.v[1], s.srd, soff); dma_p<NT, 2>(s.v[2], s.srd, soff); dma_p<NT, 3>(s.v[3], s.srd, soff);
}
template <int NT> DI void dma_elem(const Src& s, unsigned soff, unsigned ustep, unsigned ldsaddr) { dma_unit<NT>(s, soff, ldsaddr); dma_unit<NT>(s, soff + ustep, ldsaddr + 4096u); }
template <int N> DI void wait_vm() { asm volatile("s_waitcnt vmcnt(%0)" :: "i"(N) : "memory"); }
template <int N, int L> DI void wait_vm_lgkm() { asm volatile("" ::: "memory"); __builtin_amdgcn_s_waitcnt((N & 15) | ((N >> 4) << 14) | 0x70 | (L << 8)); asm volatile("" ::: "memory"); }
DI void wait_lgkm0() { wait_vm_lgkm<63, 0>(); }
DI void barrier() { asm volatile("s_barrier" ::: "memory"); }
DI bf16x8 lds128(unsigned a) { return *(const LAS bf16x8*)(size_t)(a); }
DI bf16x4 lds_tr1(unsigned a) { return __builtin_amdgcn_ds_read_tr16_b64_v4bf16((LAS bf16x4*)(size_t)(a)); }
DI bf16x8 cat(bf16x4 lo, bf16x4 hi) { return __builtin_shufflevector(lo, hi, 0, 1, 2, 3, 4, 5, 6, 7); }
DI bf16x8 lds_tr(unsigned a0, unsigned a1) { return cat(lds_tr1(a0), lds_tr1(a1)); }
DI srd_t mk_srd(const void* base, unsigned bytes) { return __builtin_amdgcn_make_buffer_rsrc((void*)base, (short)0, (int)bytes, 0x00020000); }
template <bool VX> DI void mk_src_rows(Src& s, const char* base, unsigned bytes, unsigned stride, unsigned lane) {
  s.srd = mk_srd(base, bytes);
  const unsigned row = (lane >> 2) & 3u, cb = lane >> 4, sl = lane & 3u;
#pragma unroll
  for (int p = 0; p < 4; ++p) s.v[p] = row * stride + cb * 64u + ((sl ^ (VX ? 2 * (p & 1) : (p & 2))) << 4) + (4u * stride - 1024u) * p;
}
template <bool VX> DI void mk_src_w(Src& s, const char* base, unsigned bytes, unsigned stride, unsigned lane) {
  s.srd = mk_srd(base, bytes);
  const unsigned sub = lane >> 5, cb = (lane >> 4) & 1u, row = (lane >> 2) & 3u, sl = lane & 3u;
#pragma unroll
  for (int p = 0; p < 4; ++p) s.v[p] = (4u * sub + row) * stride + cb * 64u + ((sl ^ (VX ? 2u * sub : 2u * (p & 1))) << 4) + (8u * stride - 1024u) * p;
}
template <int NT> DI void touch(unsigned& d, unsigned voff, srd_t srd, unsigned soff) {
  if constexpr (NT) asm volatile("buffer_load_dword %0, %1, %2, %3 offen nt" : "+v"(d) : "v"(voff), "s"(srd), "s"(soff) : "memory");
  else              asm volatile("buffer_load_dword %0, %1, %2, %3 offen" : "+v"(d) : "v"(voff), "s"(srd), "s"(soff) : "memory");
}
DI unsigned ksoff(unsigned m, unsigned rk) { return ((m + rk) & 15u) * 8192u; }

struct PC {
  unsigned r01, r2;
  unsigned w01[2], w2[2];
  unsigned xb, xmask, xa;
  unsigned z, soff;
};
template <unsigned TS_, unsigned QS_, unsigned ZI_, unsigned ZM_, unsigned ZO_, unsigned US_, unsigned CS_, bool ROT_> struct PK {
  static constexpr unsigned TS = TS_, QS = QS_, ZI = ZI_, ZM = ZM_, ZO = ZO_, US = US_, CS = CS_; static constexpr bool ROT = ROT_; };
DI unsigned q_soff(unsigned z) { return ((z & 0xC300u) * 0x440u) & 0x3C0000u; }
template <int POS> DI unsigned ring_lds(const PC& c) { return POS == 2 ? c.r2 : c.r01 + POS * SLOT; }
template <int POS> DI unsigned ring_rd(const PC& c, int d) { return (POS == 2 ? c.w2[d & 1] : c.w01[d & 1] + POS * SLOT) + (d >> 1) * 256u; }
struct NoX { static constexpr bool ACT = true; template <int G> DI void run() const {} };
template <class K, int POS, int S, bool Z, bool RD, bool DM, int NT, bool XT, int M, class X>
DI void proj_gap(f32x4 (&acc)[8][4], const bf16x8 (&cur)[4], bf16x8 (&nxt)[4], bf16x4 (&lo)[4], bf16x8 (&bf)[4], PC& c, unsigned soff, const Src& src, const Src& xs, unsigned xsoff, const X& ex) {
  constexpr int qb = M >> 2, db = M & 3;
  mm_vvv<Z>(acc[4 * S + db][qb], cur[db], bf[qb]);
  if constexpr (RD && M < 8) {
    constexpr int d = M >> 1; const unsigned a = S == 0 ? ring_rd<POS>(c, d) + 4096u : ring_rd<(POS + 1) % 3>(c, d);
    if constexpr (M & 1) nxt[d] = cat(lo[d], lds_tr1(a + K::TS)); else lo[d] = lds_tr1(a);
  }
  if constexpr (X::ACT && RD && S == 1 && db == 3) bf[qb] = lds128(c.xa + qb * K::QS);
  if constexpr (XT) {
    if constexpr (M < 8 && !(M & 1)) dma_p<X_NT, M / 2>(xs.v[M / 2], xs.srd, xsoff);
    if constexpr (M == 7) set_m0(ring_lds<POS>(c) + S * 4096u);
    if constexpr (M >= 8 && !(M & 1)) dma_p<NT, (M - 8) / 2>(src.v[(M - 8) / 2], src.srd, soff);
  } else if constexpr (DM && M >= 8 && !(M & 1)) dma_p<NT, (M - 8) / 2>(src.v[(M - 8) / 2], src.srd, soff);
  if constexpr (X::ACT && S == 0 && M == 9) { PINS(c.z); c.z = (c.z + K::ZI) & K::ZM; PINS(c.z); }
  if constexpr (X::ACT && S == 0 && M == 11) { c.xa = (c.z & c.xmask) | c.xb; PINV(c.xa); }
  if constexpr (S == 1 && DM && !K::ROT && M == 9) { PINS(c.soff); c.soff = (c.soff + K::CS) & (16u * K::CS - 1u); PINS(c.soff); }
  if constexpr (S == 1 && DM && K::ROT && M == 9) { PINS(c.z); c.soff = (c.z + 0x3100u) & 0xC300u; PINS(c.soff); }
  if constexpr (S == 1 && DM && K::ROT && M == 13) { PINS(c.soff); c.soff = (c.soff * 0x440u) & 0x3C0000u; PINS(c.soff); }
  ex.template run<16 * S + M>();
  GAP();
}
template <class K, int POS, int S, bool Z, bool RD, bool DM, int NT, int VM, bool BAR, bool XT, class X, int... Ms>
DI void proj_unit(f32x4 (&acc)[8][4], const bf16x8 (&cur)[4], bf16x8 (&nxt)[4], bf16x8 (&bf)[4], PC& c, const Src& src, const Src& xs, unsigned xsoff, unsigned xdst, const X& ex, iseq<Ms...>) {
  wait_vm_lgkm<(RD ? VM : 63), (S == 0 ? 2 : 0)>();
  if constexpr (BAR) barrier();
  const unsigned soff = c.soff + S * K::US;
  if constexpr (XT) set_m0(xdst); else if constexpr (DM) set_m0(ring_lds<POS>(c) + S * 4096u);
  GAP();
  bf16x4 lo[4];
  (proj_gap<K, POS, S, Z, RD, DM, NT, XT, Ms>(acc, cur, nxt, lo, bf, c, soff, src, xs, xsoff, ex), ...);
}
template <class K, int POS, bool Z, bool LAST, bool DM, int NT, int VM0, int VM1, bool BAR1 = false, bool XT0 = false, bool XT1 = false, class X = NoX>
DI void proj_elem(f32x4 (&acc)[8][4], bf16x8 (&a0)[4], bf16x8 (&a1)[4], bf16x8 (&bf)[4], PC& c, const Src& src, const X& ex = X{},
                  const Src* xs = nullptr, unsigned xso0 = 0, unsigned xd0 = 0, unsigned xso1 = 0, unsigned xd1 = 0) {
  proj_unit<K, POS, 0, Z, true, DM, NT, VM0, false, XT0>(acc, a0, a1, bf, c, src, xs ? *xs : src, xso0, xd0, ex, make_iseq<16>{});
  proj_unit<K, POS, 1, Z, !LAST, DM, NT, VM1, BAR1, XT1>(acc, a1, a0, bf, c, src, xs ? *xs : src, xso1, xd1, ex, make_iseq<16>{});
}
template <class K> DI void proj_setup(PC& c, unsigned tb, unsigned xb, unsigned z0, unsigned s3) {
  c.w01[0] = c.r01 + tb; c.w01[1] = c.w01[0] ^ 32u; c.w2[0] = c.r2 + tb; c.w2[1] = c.w2[0] ^ 32u;
  PINV(c.w01[0]); PINV(c.w01[1]); PINV(c.w2[0]); PINV(c.w2[1]);
  c.xb = xb; c.xmask = K::ZO; PINV(c.xmask);
  c.z = z0; c.soff = s3;
}
template <class K, int POS> DI void proj_start(bf16x8 (&a0)[4], bf16x8 (&bf)[4], PC& c, unsigned tb, unsigned xb, unsigned z0, unsigned s3) {
  proj_setup<K>(c, tb, xb, z0, s3);
  c.xa = (c.z & K::ZO) | xb; PINV(c.xa);
#pragma unroll
  for (int d = 0; d < 4; ++d) { const unsigned a = ring_rd<POS>(c, d); a0[d] = lds_tr(a, a + K::TS); }
  GAP();
#pragma unroll
  for (int q = 0; q < 4; ++q) bf[q] = lds128(c.xa + q * K::QS);
  GAP();
}
#define ACC_ALL(C, a) C(a[0][0]), C(a[0][1]), C(a[0][2]), C(a[0][3]), C(a[1][0]), C(a[1][1]), C(a[1][2]), C(a[1][3]), C(a[2][0]), C(a[2][1]), C(a[2][2]), C(a[2][3]), C(a[3][0]), C(a[3][1]), C(a[3][2]), C(a[3][3])
DI void acc_fence_v(f32x4 (&acc)[8][4]) {
  asm volatile("s_nop 7\n\ts_nop 3" : ACC_ALL("+v", acc) :: "memory");
  f32x4 (&hi)[4][4] = *(f32x4 (*)[4][4])&acc[4];
  asm volatile("" : ACC_ALL("+v", hi) :: "memory");
  GAP();
}
DI void acc_fence_a(f32x4 (&acc)[8][4]) {
  asm volatile("s_nop 7\n\ts_nop 3" : ACC_ALL("+a", acc) :: "memory");
  f32x4 (&hi)[4][4] = *(f32x4 (*)[4][4])&acc[4];
  asm volatile("" : ACC_ALL("+a", hi) :: "memory");
  GAP();
}

struct Stash { u32x4 p[64]; unsigned base[3]; };
template <int I> DI void stash_load1(Stash& st, unsigned vk, const Src& sK, const Src& sV, unsigned rk, unsigned wo_) {
  constexpr int m = I >> 2, isV = (I >> 1) & 1, pc_ = I & 1;
  const unsigned so = ksoff(m, rk) + wo_;
  if constexpr (I < 56) asm volatile("buffer_load_dwordx4 %0, %1, %2, %3 offen offset:%4 " KVPOL : "=a"(st.p[I]) : "v"(isV ? sV.v[pc_] : vk), "s"(isV ? sV.srd : sK.srd), "s"(so), "i"(1024 * pc_) : "memory");
  else                  asm volatile("buffer_load_dwordx4 %0, %1, %2, %3 offen offset:%4 " KVPOL : "=v"(st.p[I]) : "v"(isV ? sV.v[pc_] : vk), "s"(isV ? sV.srd : sK.srd), "s"(so), "i"(1024 * pc_) : "memory");
}
template <int... Is> DI void stash_load(Stash& st, unsigned vk, const Src& sK, const Src& sV, unsigned rk, unsigned wo_, iseq<Is...>) { (stash_load1<Is>(st, vk, sK, sV, rk, wo_), ...); }
constexpr int kv_pair(int m) { constexpr int t[10] = {0, 1, 2, 3, 8, 9, 4, 5, 6, 7}; return t[m % 10]; }
constexpr unsigned long long kv_tab() { unsigned long long v = 0; for (int i = 0; i < 16; ++i) v |= (unsigned long long)kv_pair(i) << (4 * i); return v; }
template <int I> DI void stash_write1(const Stash& st) {
  constexpr int m = I >> 2, isV = (I >> 1) & 1, pc_ = I & 1, off = kv_pair(m) * 16384 + isV * 8192 + pc_ * 1024;
  if constexpr (I < 56) asm volatile("ds_write_b128 %0, %1 offset:%2" :: "v"(st.base[off >> 16]), "a"(st.p[I]), "i"(off & 65535) : "memory");
  else                  asm volatile("ds_write_b128 %0, %1 offset:%2" :: "v"(st.base[off >> 16]), "v"(st.p[I]), "i"(off & 65535) : "memory");
}
template <int... Is> DI void stash_late(const Stash& st, iseq<Is...>) { (stash_write1<40 + Is>(st), ...); }

struct AC {
  f32x4 o[8][4];
  bf16x8 q[4][4];
  bf16x8 kf[8], vf[8], pf[4];
  f32x4 negm[4];
  f32x4 l[4];
  bf16x8 ones;
};
typedef f32x4 ST[2][4];
constexpr int NEB = 22;
template <int E> DI float sm_val(ST& S) { return S[(E >> 2) & 1][E >> 3][E & 3]; }
template <int E> DI void sm_exp1(ST& S) { float a; asm volatile("v_exp_f32_e32 %0, %1" : "=v"(a) : "v"(sm_val<E>(S))); S[(E >> 2) & 1][E >> 3][E & 3] = a; }
template <int QB, int P> DI void sm_cvt(ST& S, AC& c) {
  u32x4 w = __builtin_bit_cast(u32x4, c.pf[QB]);
  unsigned r;
  asm volatile("v_cvt_pk_bf16_f32 %0, %1, %2" : "=v"(r) : "v"(sm_val<8 * QB + 2 * P>(S)), "v"(sm_val<8 * QB + 2 * P + 1>(S)));
  w[P] = r;
  c.pf[QB] = __builtin_bit_cast(bf16x8, w);
}
template <bool DM, int VM, int R, int M> DI void mem_pre(unsigned ldsaddr) {
  if constexpr (M == 6) wait_lgkm0();
  if constexpr (DM && (M == 6 || M == 14)) set_m0(ldsaddr + (M == 14 ? 4096u : 0u));
  if constexpr (M == R && VM < 63) wait_vm<VM>();
}
template <bool DM, int NT, int M> DI void mem_post(const Src& src, unsigned soff, unsigned ustep) {
  if constexpr (DM && M >= 6 && M <= 20 && !(M & 1)) dma_p<NT, ((M - 6) / 2) & 3>(src.v[((M - 6) / 2) & 3], src.srd, soff + (M >= 14 ? ustep : 0u));
}
template <int WR, int M> DI void mem_wr(const Stash& st) {
  if constexpr (WR >= 0 && M >= 8 && M < 16)
    asm volatile("ds_write_b128 %0, %1 offset:%2" :: "v"(st.base[0]), "a"(st.p[WR + M - 8]), "i"((M - 8) * 1024) : "memory");
}
struct PA { unsigned rd0, rd1, soff, fill; };
template <bool QK, bool DM, int NT, int VM, int WR, int M, class F>
DI void gapA(AC& c, ST& S, ST& Sn, PA& pa, F& addr, const Src& src, unsigned ustep, const Stash& st, bf16x4& lo) {
  constexpr int R = QK ? 16 : 0;
  if constexpr ((DM || !QK) && M == 0) addr(pa);
  if constexpr (QK) mem_pre<DM, VM, 16, M>(pa.fill); else if constexpr (M == 0 && VM < 63) wait_vm<VM>();
  if constexpr (QK) {
    constexpr int f = M >> 2, s = f >> 1, kb = f & 1, qb = M & 3;
    if constexpr (s == 0) mm_vca(Sn[kb][qb], c.kf[f], c.q[qb][0], c.negm[qb]); else mm_vva<false>(Sn[kb][qb], c.kf[f], c.q[qb][s]);
  }
  mem_post<DM, NT, M>(src, pa.soff, ustep);
  mem_wr<WR, M>(st);
  if constexpr (QK && !DM && M == 10) addr(pa);
  if constexpr (QK && M == 12) asm volatile("" :: "v"(c.negm[0]), "v"(c.negm[1]), "v"(c.negm[2]), "v"(c.negm[3]));
  if constexpr (M >= R && M < R + 16) {
    constexpr int db = (M - R) >> 1; const unsigned a = ((db & 1) ? pa.rd1 : pa.rd0) + (db >> 1) * 256u;
    if constexpr (M & 1) c.vf[db] = cat(lo, lds_tr1(a + 4096u)); else lo = lds_tr1(a);
  }
  if constexpr (NEB + M < 32) sm_exp1<NEB + M>(S);
  if constexpr (M >= 16 && (M & 1)) sm_cvt<2 + ((M - 16) >> 3), ((M - 16) >> 1) & 3>(S, c);
  GAP();
}
template <bool QK, bool DM, int NT, int VM, int WR, class F, int... Ms>
DI void phaseA_(AC& c, ST& S, ST& Sn, F& addr, const Src& src, unsigned ustep, const Stash& st, iseq<Ms...>) {
  PA pa; bf16x4 lo;
  (gapA<QK, DM, NT, VM, WR, Ms>(c, S, Sn, pa, addr, src, ustep, st, lo), ...);
}
template <bool QK, bool DM, int NT, int VM, int WR = -1, class F>
DI void phaseA(AC& c, ST& S, ST& Sn, F addr, const Src& src, unsigned ustep, const Stash& st) {
  phaseA_<QK, DM, NT, VM, WR>(c, S, Sn, addr, src, ustep, st, make_iseq<32>{});
}
template <bool MAX> DI float xlane(float v) {
  auto a = __builtin_amdgcn_permlane32_swap(__float_as_uint(v), __float_as_uint(v), false, false);
  v = MAX ? fmaxf(__uint_as_float(a[0]), __uint_as_float(a[1])) : __uint_as_float(a[0]) + __uint_as_float(a[1]);
  auto b = __builtin_amdgcn_permlane16_swap(__float_as_uint(v), __float_as_uint(v), false, false);
  return MAX ? fmaxf(__uint_as_float(b[0]), __uint_as_float(b[1])) : __uint_as_float(b[0]) + __uint_as_float(b[1]);
}
DI float row_max(const f32x4& x, const f32x4& y) {
  return xlane<true>(fmaxf(fmaxf(fmaxf(x[0], x[1]), fmaxf(x[2], x[3])), fmaxf(fmaxf(y[0], y[1]), fmaxf(y[2], y[3]))));
}
template <bool SM, bool KR, bool DM, int NT, int VM, int WR, int M, class F>
DI void gapB(AC& c, ST& Sn, PA& pa, F& addr, const Src& src, unsigned ustep, const Stash& st) {
  mem_pre<DM, (KR ? VM : 63), 18, M>(pa.fill);
  {
    constexpr int h = M / 18, r = M % 18, qb = 2 * h + (r & 1);
    if constexpr (r < 2) mm_vvv<false>(c.l[qb], c.ones, c.pf[qb]); else mm_avv(c.o[(r - 2) >> 1][qb], c.vf[(r - 2) >> 1], c.pf[qb]);
  }
  mem_post<DM, NT, M>(src, pa.soff, ustep);
  mem_wr<WR, M>(st);
  if constexpr (M == 0) addr(pa);
  if constexpr (KR && M >= 18 && M < 34 && !(M & 1)) { constexpr int f = (M - 18) >> 1; c.kf[f] = lds128(pa.rd0 + (f & 1) * 4096u + (f >> 1) * 256u); }
  if constexpr (SM) {
    if constexpr (M >= 4 && M < 18) sm_exp1<M - 4>(Sn);
    if constexpr (M >= 19 && M < 35 && (M & 1)) sm_exp1<14 + (M - 19) / 2>(Sn);
    if constexpr (M >= 20 && !(M & 1)) sm_cvt<((M - 20) >> 3), ((M - 20) >> 1) & 3>(Sn, c);
  }
  GAP();
}
template <bool SM, bool KR, bool DM, int NT, int VM, int WR, class F, int... Ms>
DI void phaseB_(AC& c, ST& Sn, F& addr, const Src& src, unsigned ustep, const Stash& st, iseq<Ms...>) {
  PA pa;
  (gapB<SM, KR, DM, NT, VM, WR, Ms>(c, Sn, pa, addr, src, ustep, st), ...);
}
template <bool SM, bool KR, bool DM, int NT, int VM, int WR = -1, class F>
DI void phaseB(AC& c, ST& Sn, F addr, const Src& src, unsigned ustep, const Stash& st) {
  phaseB_<SM, KR, DM, NT, VM, WR>(c, Sn, addr, src, ustep, st, make_iseq<36>{});
}
DI void k_read(AC& ac, unsigned ka) {
#pragma unroll
  for (int f = 0; f < 8; ++f) ac.kf[f] = lds128(ka + (f & 1) * 4096u + (f >> 1) * 256u);
}
template <bool DM, int M> DI void qk0_gap(AC& ac, ST& SA, const Src& src, unsigned soff, unsigned fill) {
  constexpr int f = M >> 2, qb = M & 3;
  if constexpr (DM && (M == 0 || M == 16)) set_m0(fill + M * 256u);
  mm_vva<(f < 2)>(SA[f & 1][qb], ac.kf[f], ac.q[qb][f >> 1]);
  if constexpr (DM && qb == 0) dma_p<KV_NT, f & 3>(src.v[f & 3], src.srd, soff + (f >> 2) * 4096u);
  GAP();
}
template <bool DM, int... Ms> DI void qk0(AC& ac, ST& SA, const Src& src, unsigned soff, unsigned fill, iseq<Ms...>) { (qk0_gap<DM, Ms>(ac, SA, src, soff, fill), ...); }
template <int... Js> DI void exp_first(ST& SA, iseq<Js...>) { (sm_exp1<Js>(SA), ...); }
template <int... Js> DI void cvt_first(ST& SA, AC& c, iseq<Js...>) { (sm_cvt<(Js >> 2), (Js & 3)>(SA, c), ...); }
DI void sm_first(AC& ac, ST& SA) {
  asm volatile("s_nop 7\n\ts_nop 3" : "+v"(SA[0][0]), "+v"(SA[0][1]), "+v"(SA[0][2]), "+v"(SA[0][3]), "+v"(SA[1][0]), "+v"(SA[1][1]), "+v"(SA[1][2]), "+v"(SA[1][3]) :: "memory");
  GAP();
#pragma unroll
  for (int q = 0; q < 4; ++q) {
    const float m = row_max(SA[0][q], SA[1][q]);
    SA[0][q] -= m; SA[1][q] -= m;
    ac.negm[q] = f32x4{-m, -m, -m, -m};
    ac.l[q] = f32x4{};
    ac.pf[q] = bf16x8{};
  }
  ac.ones = __builtin_bit_cast(bf16x8, u32x4{0x3f803f80u, 0x3f803f80u, 0x3f803f80u, 0x3f803f80u});
  asm volatile("" : "+v"(ac.negm[0]), "+v"(ac.negm[1]), "+v"(ac.negm[2]), "+v"(ac.negm[3]), "+v"(ac.l[0]), "+v"(ac.l[1]), "+v"(ac.l[2]), "+v"(ac.l[3]), "+v"(ac.ones));
  exp_first(SA, make_iseq<NEB>{});
  cvt_first(SA, ac, make_iseq<8>{});
  GAP();
}
template <int CH, int Q, int J> DI void o_word(const AC& c, const float (&inv)[4], bf16x8 (&nb)[4]) {
  u32x4 w = __builtin_bit_cast(u32x4, nb[Q]);
  unsigned r; float t;
  asm volatile("v_accvgpr_read_b32 %0, %2\n\tv_accvgpr_read_b32 %1, %3\n\tv_mul_f32_e32 %0, %0, %4\n\tv_mul_f32_e32 %1, %1, %4\n\tv_cvt_pk_bf16_f32 %0, %0, %1"
               : "=&v"(r), "=&v"(t) : "a"(c.o[2 * CH + (J >> 1)][Q][2 * (J & 1)]), "a"(c.o[2 * CH + (J >> 1)][Q][2 * (J & 1) + 1]), "v"(inv[Q]));
  w[J] = r;
  nb[Q] = __builtin_bit_cast(bf16x8, w);
}
template <int CH, int Q> DI void o_put(const bf16x8 (&nb)[4], unsigned ow) { asm volatile("ds_write_b128 %0, %1 offset:%2" :: "v"(ow), "v"(nb[Q]), "i"(CH * 4096 + Q * 1024) : "memory"); }
template <int E> struct CvX {
  static constexpr bool ACT = E == 3;
  const AC& c; const float (&inv)[4]; const bf16x8 (&cur)[4]; bf16x8 (&nb)[4]; unsigned ow;
  template <int G> DI void run() const {
    if constexpr (E < 3 && (G & 1)) o_word<E + 1, (G >> 3), (G >> 1) & 3>(c, inv, nb);
    if constexpr (E < 3 && ((G & 15) == 8 || (G & 15) == 10)) o_put<E, 2 * (G >> 4) + ((G >> 1) & 1)>(cur, ow);
    if constexpr (E == 2 && (G & 15) == 12) o_put<3, (G >> 4)>(nb, ow);
    if constexpr (E == 3 && (G == 8 || G == 10)) o_put<3, 2 + ((G >> 1) & 1)>(cur, ow);
  }
};
template <class K, int VM, int M> DI void gapL(AC& c, float (&inv)[4], bf16x8 (&nb)[4], bf16x8 (&a0)[4], bf16x4& lo, const PC& pc) {
  if constexpr (M == 6) wait_vm_lgkm<VM, 0>();
  if constexpr (M < 4) mm_vvv<false>(c.l[M], c.ones, c.pf[M]); else mm_avv(c.o[(M - 4) >> 2][M & 3], c.vf[(M - 4) >> 2], c.pf[M & 3]);
  if constexpr (M >= 6 && M < 14) {
    constexpr int d = (M - 6) >> 1; const unsigned a = ring_rd<0>(pc, d);
    if constexpr (M & 1) a0[d] = cat(lo, lds_tr1(a + K::TS)); else lo = lds_tr1(a);
  }
  if constexpr (M >= 4 && M < 12 && !(M & 1)) asm volatile("v_rcp_f32_e32 %0, %1" : "=v"(inv[(M - 4) >> 1]) : "v"(c.l[(M - 4) >> 1][0]));
  if constexpr (M >= 16 && M < 32) o_word<0, ((M - 16) >> 2), (M & 3)>(c, inv, nb);
  GAP();
}
template <class K, int VM, int... Ms> DI void phaseL(AC& c, float (&inv)[4], bf16x8 (&nb)[4], bf16x8 (&a0)[4], const PC& pc, iseq<Ms...>) {
  bf16x4 lo;
#pragma unroll
  for (int q = 0; q < 4; ++q) nb[q] = bf16x8{};
  (gapL<K, VM, Ms>(c, inv, nb, a0, lo, pc), ...);
}
struct Pub { u32x4 pk[16]; srd_t srd; unsigned v[2], so; };
template <int F, int H> DI void pub_half(const f32x4 (&acc)[8][4], Pub& p) {
  const f32x4& a = acc[4 * (F >> 3) + 2 * (F & 1) + H][(F >> 1) & 3];
  unsigned r0, r1;
  asm volatile("v_cvt_pk_bf16_f32 %0, %2, %3\n\tv_cvt_pk_bf16_f32 %1, %4, %5" : "=&v"(r0), "=v"(r1) : "v"(a[0]), "v"(a[1]), "v"(a[2]), "v"(a[3]));
  p.pk[F][2 * H] = r0; p.pk[F][2 * H + 1] = r1;
  if constexpr (H) asm volatile("buffer_store_dwordx4 %0, %1, %2, %3 offen offset:%4 sc1" :: "v"(p.pk[F]), "v"(p.v[(F >> 2) & 1]), "s"(p.srd), "s"(p.so + (F >> 3) * 8192u), "i"((F & 3) * 1024) : "memory");
}
struct PubX {
  static constexpr bool ACT = false;
  const f32x4 (&acc)[8][4]; Pub& p;
  template <int G> DI void run() const { if constexpr (G >= 16) pub_half<((G - 16) >> 1), (G & 1)>(acc, p); }
};
template <int... Is> DI void pub_rest(const f32x4 (&acc)[8][4], Pub& p, iseq<Is...>) { (pub_half<8 + (Is >> 1), (Is & 1)>(acc, p), ...); }
template <int... Is> DI void pub_slow(const f32x4 (&acc)[8][4], Pub& p, iseq<Is...>) { ((pub_half<Is, 0>(acc, p), pub_half<Is, 1>(acc, p), __builtin_amdgcn_s_sleep(PUB_SLEEP)), ...); }
DI void redo_head(const AC& ac, const char* __restrict__ Kp, const char* __restrict__ Vp, unsigned lane, unsigned ow) {
  const unsigned r16 = lane & 15u, g = lane >> 4;
#pragma unroll 1
  for (unsigned qb = 0; qb < 4; ++qb) {
    bf16x8 qq[4];
#pragma unroll
    for (int s = 0; s < 4; ++s) qq[s] = qb == 0 ? ac.q[0][s] : qb == 1 ? ac.q[1][s] : qb == 2 ? ac.q[2][s] : ac.q[3][s];
    f32x4 o[8];
#pragma unroll
    for (int d = 0; d < 8; ++d) o[d] = f32x4{};
    float m = -__builtin_inff(), l = 0.f;
#pragma unroll 1
    for (unsigned t = 0; t < 16; ++t) {
      f32x4 s0 = {}, s1 = {};
      const char* kp = Kp + (t * 32u + r16) * 256u + g * 16u;
#pragma unroll
      for (int s = 0; s < 4; ++s) {
        s0 = __builtin_amdgcn_mfma_f32_16x16x32_bf16(*(const bf16x8*)(kp + s * 64), qq[s], s0, 0, 0, 0);
        s1 = __builtin_amdgcn_mfma_f32_16x16x32_bf16(*(const bf16x8*)(kp + s * 64 + 4096), qq[s], s1, 0, 0, 0);
      }
      const float mn = fmaxf(m, row_max(s0, s1)), al = __builtin_amdgcn_exp2f(m - mn);
      m = mn;
#pragma unroll
      for (int j = 0; j < 4; ++j) { s0[j] = __builtin_amdgcn_exp2f(s0[j] - mn); s1[j] = __builtin_amdgcn_exp2f(s1[j] - mn); }
      l = l * al + ((s0[0] + s0[1]) + (s0[2] + s0[3])) + ((s1[0] + s1[1]) + (s1[2] + s1[3]));
      const bf16x8 pf = __builtin_bit_cast(bf16x8, pack2(s0, s1));
      const __bf16* vp = (const __bf16*)(Vp + (t * 32u + g * 4u) * 256u) + r16;
#pragma unroll
      for (int d = 0; d < 8; ++d) {
        bf16x8 vf;
#pragma unroll
        for (int j = 0; j < 8; ++j) vf[j] = vp[((j & 3) + (j >> 2) * 16) * 128 + d * 16];
        o[d] = __builtin_amdgcn_mfma_f32_16x16x32_bf16(vf, pf, o[d] * al, 0, 0, 0);
      }
    }
    const float inv = 1.0f / xlane<false>(l);
#pragma unroll
    for (int s = 0; s < 4; ++s) *(LAS u32x4*)(size_t)(ow + s * 4096u + qb * 1024u) = pack2(o[2 * s] * inv, o[2 * s + 1] * inv);
  }
}
template <bool isA, int J> DI void q_conv1(AC& ac, f32x4 (&acc)[8][4], Stash& st, const Src& sK, const Src& sV, unsigned rk, unsigned p3) {
  constexpr int s = J >> 2, qb = J & 3;
  if constexpr (isA) { stash_write1<(5 * J) / 2>(st); stash_write1<(5 * J) / 2 + 1>(st); if constexpr (J & 1) stash_write1<(5 * J) / 2 + 2>(st); }
  else {
    constexpr int p = 2 * (J & 1);
    const Src& src = s < 2 ? sK : sV;
    const unsigned so = ksoff(s == 0 ? 2u : s == 3 ? 14u : 13u, rk) + (J & 2) * 2048u;
    if constexpr (s == 0 || s == 3) {
      if constexpr (p == 0) asm volatile("s_mov_b32 m0, %0\n\ts_nop 0" :: "s"(p3 + (s == 3 ? SLOT : 0u) + (J & 2) * 2048u) : "memory");
      dma_p<KV_NT, p>(src.v[p], src.srd, so); dma_p<KV_NT, p + 1>(src.v[p + 1], src.srd, so);
    } else {
      asm volatile("buffer_load_dwordx4 %0, %1, %2, %3 offen offset:%4 " KVPOL : "=a"(st.p[2 * J - 8]) : "v"(src.v[p]), "s"(src.srd), "s"(so), "i"(1024 * p) : "memory");
      asm volatile("buffer_load_dwordx4 %0, %1, %2, %3 offen offset:%4 " KVPOL : "=a"(st.p[2 * J - 7]) : "v"(src.v[p + 1]), "s"(src.srd), "s"(so), "i"(1024 * p + 1024) : "memory");
    }
  }
  ac.q[qb][s] = __builtin_bit_cast(bf16x8, natural8(pack2(acc[2 * s][qb] * SC, acc[2 * s + 1][qb] * SC)));
  asm volatile("" : "+a"(ac.q[qb][s]));
  GAP();
}
template <bool isA, int... Js> DI void q_conv(AC& ac, f32x4 (&acc)[8][4], Stash& st, const Src& sK, const Src& sV, unsigned rk, unsigned p3, iseq<Js...>) { (q_conv1<isA, Js>(ac, acc, st, sK, sV, rk, p3), ...); }
DI void zero_o(AC& ac) {
#pragma unroll
  for (int i = 0; i < 8; ++i)
#pragma unroll
    for (int q = 0; q < 4; ++q) { ac.o[i][q] = f32x4{}; asm volatile("" : "+a"(ac.o[i][q])); }
}

DI unsigned vbid() {
  const unsigned i = blockIdx.x;
  return i;
}
template <bool isA>
DI void body(const char* __restrict__ X, const char* __restrict__ K, const char* __restrict__ V, const char* __restrict__ Wq, const char* __restrict__ Wo,
             const char* __restrict__ Wo2, srd_t code, char* __restrict__ part, char* __restrict__ out) {
  extern __shared__ __attribute__((aligned(16))) char lds[];
  STAMP(0);
  unsigned tid = threadIdx.x;
  PINV(tid);
  const unsigned lane = tid & 63u, w = rfl(tid >> 6);
  const unsigned bid = vbid(), xg = bid & 7u, rr = bid >> 3, h = xg * 4u + w;
  const unsigned b = rr & 15u;
  const unsigned r16 = lane & 15u, g = lane >> 4;
  const char* xp  = X + (size_t)b * 65536u + w * 16384u;
  const char* wqp = Wq + h * 256u;
  const char* wop = Wo + (size_t)xg * 524288u + w * 256u;
  const size_t kvo = isA ? (size_t)(b * 8u + xg) * 131072u : (size_t)(b * 32u + h) * 131072u;
  const unsigned rk = isA ? b : b + 4u * w;
  typedef PK<512u, 4096u, 0x1300u, 0xF300u, 0xC300u, 128u, 32u * 8192u, true> KQ;
  typedef PK<512u, 4096u, 0x1300u, 0xF300u, 0xC300u, 4096u, 8192u, false> KT;
  typedef PK<2048u, 1024u, 0x1000u, 0xF000u, 0xF000u, 128u, 32u * 1024u, false> KO;
  asm volatile("" :: "v"(lds));
  PC pc;
  pc.r01 = 65536u + w * 16384u; pc.r2 = 131072u + w * 8192u;
  const unsigned zq = ((b & 3u) << 14) | ((b >> 2) << 8), co = 4u * w;
  const srd_t srdH = mk_srd(part + HOFF, 4096u + 256u * 128u);
  const unsigned tok_off = rr * 128u, flg_off = 4096u + rr * 1024u;
  const unsigned l8 = (lane & 7u) * 4u;
  unsigned entry_flags;
  asm volatile("buffer_load_dword %0, %1, %2, %3 offen sc1" : "=v"(entry_flags) : "v"(l8), "s"(srdH), "s"(flg_off + xg * 128u) : "memory");
  if constexpr (WARM_CODE && isA) { if (w == 3 && lane < WARM_CODE) asm volatile("buffer_load_dword %0, %1, %2, 0 offen" : "+v"(entry_flags) : "v"((b * WARM_CODE + lane) * 128u), "s"(code) : "memory"); }
  Src sQ, sX, sK, sV, sO;
  mk_src_w<false>(sQ, wqp, 511u * 8192u + 256u, 8192u, lane);
  mk_src_rows<false>(sX, xp, 16384u, 1024u, lane);
  const unsigned p0 = b & 3u, p1 = (p0 + 1u) & 3u, p2 = (p0 + 2u) & 3u, p3 = (p0 + 3u) & 3u, xw = w * 4096u;
  Stash st;
  unsigned dum0 = lane;
  if constexpr (isA && ORD == 0) {
    mk_src_rows<false>(sK, K + kvo, 131072u, 256u, lane);
    mk_src_rows<true>(sV, V + kvo, 131072u, 256u, lane);
    const unsigned to = (lane >> 1) * 1024u + (lane & 1u) * 128u + b * KO::CS;
    const char* wb = Wo2 + (size_t)xg * 524288u + w * 256u;
    if constexpr (EARLY_O == 1) asm volatile("global_load_dword %0, %1, %2\n\tglobal_load_dword %0, %1, %3" : "=&v"(dum0) : "v"(to), "s"(wop), "s"(wb) : "memory");
    stash_load(st, (w & 1u) ? sK.v[2] : sK.v[0], sK, sV, rk, w * 2048u, make_iseq<64>{});
    if constexpr (EARLY_O == 2) asm volatile("global_load_dword %0, %1, %2\n\tglobal_load_dword %0, %1, %3" : "=&v"(dum0) : "v"(to), "s"(wop), "s"(wb) : "memory");
  }
  dma_elem<TOUCH_Q>(sQ, q_soff(zq), KQ::US, pc.r2);
  if constexpr (!(isA && ORD == 2)) dma_unit<X_NT>(sX, p0 * 256u, xw + p0 * 16384u);
  dma_elem<W_NT>(sQ, q_soff(zq + 0x1300u), KQ::US, pc.r01);
  if constexpr (!(isA && ORD == 0)) {
    mk_src_rows<false>(sK, K + kvo, 131072u, 256u, lane);
    mk_src_rows<true>(sV, V + kvo, 131072u, 256u, lane);
  }
  mk_src_w<true>(sO, wop, 511u * 1024u + 256u, 1024u, lane);
  if constexpr (isA) {
    if constexpr (ORD == 2) {
      if constexpr (DELAY_A > 0) __builtin_amdgcn_s_sleep(DELAY_A);
      stash_load(st, (w & 1u) ? sK.v[2] : sK.v[0], sK, sV, rk, w * 2048u, make_iseq<64>{});
      dma_unit<X_NT>(sX, p0 * 256u, xw + p0 * 16384u);
    }
    dma_elem<W_NT>(sQ, q_soff(zq + 0x2200u), KQ::US, pc.r01 + SLOT);
    dma_unit<X_NT>(sX, p1 * 256u, xw + p1 * 16384u);
    dma_unit<X_NT>(sX, p2 * 256u, xw + p2 * 16384u);
    dma_unit<X_NT>(sX, p3 * 256u, xw + p3 * 16384u);
    if constexpr (ORD == 1) stash_load(st, (w & 1u) ? sK.v[2] : sK.v[0], sK, sV, rk, w * 2048u, make_iseq<64>{});
  }
  const unsigned rb = 1024u * (r16 >> 2) + 64u * (r16 & 3u) + 16u * (g ^ ((r16 >> 2) & 2u));
  const unsigned tb = 64u * ((lane >> 2) & 3u) + 32u * (g & 1u) + 8u * (lane & 3u);
  const unsigned tbV = tb + 1024u * g;
  const unsigned sb0 = w * 2048u + lane * 16u;
  st.base[0] = sb0; st.base[1] = sb0 + 65536u; st.base[2] = sb0 + 131072u;
  AC ac;
  if constexpr (!isA) zero_o(ac);
  {
    f32x4 acc[8][4];
    bf16x8 a0[4], a1[4], bf[4];
    asm volatile("s_waitcnt vmcnt(%2)" : "+v"(entry_flags), "+v"(dum0) : "i"(isA ? (ORD == 1 ? 63 : ORD == 0 ? 28 : 20) : 8) : "memory");
    if (w == 0) {
      const unsigned long long mk = __builtin_amdgcn_ballot_w64(((entry_flags >> (lane & 7u)) & 1u) != 0u);
      const unsigned tok = 0x5a5a5a00u | (~(unsigned)mk & 0xffu);
      if (lane == 0) asm volatile("buffer_store_dword %0, off, %1, %2 sc1" :: "v"(tok), "s"(srdH), "s"(tok_off + xg * 4u) : "memory");
    }
    barrier();
    STAMP(1);
    proj_start<KQ, 2>(a0, bf, pc, tbV, rb, zq, q_soff(zq + 0x3100u));
    if constexpr (isA) {
      constexpr int V1 = ORD == 0 ? 28 : 63, V2 = ORD == 1 ? 63 : 28;
      proj_elem<KQ, 2, true,  false, true, W_NT, 63, V1>(acc, a0, a1, bf, pc, sQ);
      STAMP(10);
      proj_elem<KQ, 0, false, false, true, W_NT, V1, V2>(acc, a0, a1, bf, pc, sQ);
      STAMP(11);
      proj_elem<KQ, 1, false, false, true, W_NT, V2, 16>(acc, a0, a1, bf, pc, sQ);
      proj_elem<KQ, 2, false, false, true, W_NT, 16, 16, ORD != 1>(acc, a0, a1, bf, pc, sQ);
    } else {
      dma_elem<W_NT>(sQ, q_soff(zq + 0x2200u), KQ::US, pc.r01 + SLOT);
      proj_elem<KQ, 2, true,  false, true, W_NT, 16, 20, false, true, true>(acc, a0, a1, bf, pc, sQ, NoX{}, &sX, p1 * 256u, xw + p1 * 16384u, p2 * 256u, xw + p2 * 16384u);
      STAMP(10);
      proj_elem<KQ, 0, false, false, true, W_NT, 24, 28, false, true, false>(acc, a0, a1, bf, pc, sQ, NoX{}, &sX, p3 * 256u, xw + p3 * 16384u);
      STAMP(11);
      proj_elem<KQ, 1, false, false, true, W_NT, 28, 24>(acc, a0, a1, bf, pc, sQ);
      proj_elem<KQ, 2, false, false, true, W_NT, 20, 16, true>(acc, a0, a1, bf, pc, sQ);
    }
    STAMP(8);
#pragma unroll 1
    for (unsigned e = 0; e < 3; ++e) {
      if constexpr (isA && PACE_A > 0) __builtin_amdgcn_s_sleep(PACE_A);
      proj_elem<KQ, 0, false, false, true, W_NT, 16, 16>(acc, a0, a1, bf, pc, sQ);
      if constexpr (isA && PACE_A > 0) __builtin_amdgcn_s_sleep(PACE_A);
      proj_elem<KQ, 1, false, false, true, W_NT, 16, 16>(acc, a0, a1, bf, pc, sQ);
      if constexpr (isA && PACE_A > 0) __builtin_amdgcn_s_sleep(PACE_A);
      proj_elem<KQ, 2, false, false, true, W_NT, 16, 16>(acc, a0, a1, bf, pc, sQ);
    }
    STAMP(9);
    if constexpr (isA) {
      proj_elem<KQ, 0, false, false, false, W_NT, 16, 12>(acc, a0, a1, bf, pc, sQ);
      proj_elem<KQ, 1, false, false, false, W_NT, 8, 4>(acc, a0, a1, bf, pc, sQ);
      proj_elem<KQ, 2, false, true, false, W_NT, 0, 0>(acc, a0, a1, bf, pc, sQ);
    } else {
      pc.soff = ksoff(0u, rk);
      proj_elem<KT, 0, false, false, true, KV_NT, 16, 16>(acc, a0, a1, bf, pc, sK);
      proj_elem<KT, 1, false, false, true, KV_NT, 16, 16>(acc, a0, a1, bf, pc, sK);
      pc.soff = ksoff(0u, rk);
      proj_elem<KT, 2, false, true, true, KV_NT, 16, 16>(acc, a0, a1, bf, pc, sV);
    }
    barrier();
    STAMP(2);
    acc_fence_v(acc);
    q_conv<isA>(ac, acc, st, sK, sV, rk, w * 16384u, make_iseq<16>{});
  }
  ST SA, SB;
  if constexpr (isA) {
    zero_o(ac);
    const unsigned kb = rb, vb = SLOT + tbV;
    unsigned dum = lane, to = TOUCH_B == 4 ? (lane >> 4) * 1024u + (lane & 8u) * 16u + (lane & 7u) * 4u : (lane >> 3) * 1024u + (lane & 4u) * 32u + (lane & 3u) * 4u;
    PINV(to);
    const char* ta = wop + b * KO::CS;
    const char* tb2 = Wo2 + (size_t)xg * 524288u + w * 256u + b * KO::CS;
    if constexpr (TOUCH_B == 4) asm volatile("global_load_dword %0, %1, %2 nt\n\tglobal_load_dword %0, %1, %2\n\tv_add_u32_e32 %1, 0x1000, %1" : "+v"(dum), "+v"(to) : "s"(ta) : "memory");
    asm volatile("s_waitcnt lgkmcnt(0)" ::: "memory");
    barrier();
    k_read(ac, kb + kv_pair(0) * 16384u);
    wait_lgkm0();
    GAP();
    qk0<false>(ac, SA, sK, 0u, 0u, make_iseq<32>{});
    k_read(ac, kb + kv_pair(1) * 16384u);
    if constexpr (TOUCH_B == 4) asm volatile("global_load_dword %0, %1, %2 nt\n\tglobal_load_dword %0, %1, %2\n\tv_add_u32_e32 %1, 0x1000, %1" : "+v"(dum), "+v"(to) : "s"(ta) : "memory");
    sm_first(ac, SA);
    STAMP(13);
    unsigned i4 = 0;
    auto av = [&](PA& p) { if constexpr (PACE_ATT > 0) __builtin_amdgcn_s_sleep(PACE_ATT);
                           PINS(i4); p.rd0 = vb + (((unsigned)(kv_tab() >> i4) & 15u) << 14); p.rd1 = p.rd0 ^ 32u; PINV(p.rd0); PINV(p.rd1); };
    auto ak = [&](PA& p) { if constexpr (PACE_ATT > 0) __builtin_amdgcn_s_sleep(PACE_ATT);
                           PINS(i4); p.rd0 = kb + (((unsigned)((kv_tab() >> 8) >> i4) & 15u) << 14); PINV(p.rd0); i4 += 4u; };
    auto an = [&](PA&) { i4 += 4u; };
    auto at = [&](PA& p) __attribute__((always_inline)) {
      PINS(i4); p.rd0 = vb + (((unsigned)(kv_tab() >> i4) & 15u) << 14); p.rd1 = p.rd0 ^ 32u; PINV(p.rd0); PINV(p.rd1);
      if constexpr (TOUCH_O) {
        const char* tp;
        if constexpr (TOUCH_B == 4) {
          asm volatile("s_cmp_lt_u32 %1, 24\n\ts_cselect_b64 %0, %2, %3" : "=s"(tp) : "s"(i4), "s"(ta), "s"(tb2 - 32768) : "scc");
          asm volatile("global_load_dword %0, %1, %2 nt\n\tglobal_load_dword %0, %1, %2\n\tv_add_u32_e32 %1, 0x1000, %1" : "+v"(dum), "+v"(to) : "s"(tp) : "memory");
        } else {
          asm volatile("s_bitcmp0_b32 %1, 4\n\ts_cselect_b64 %0, %2, %3" : "=s"(tp) : "s"(i4), "s"(ta), "s"(tb2) : "scc");
          asm volatile("global_load_dword %0, %1, %2 nt\n\tglobal_load_dword %0, %1, %2" : "+v"(dum) : "v"(to), "s"(tp) : "memory");
          to = (to + 8192u) & 32767u; PINV(to);
        }
      }
    };
    while (i4 < 56u) {
      if (i4 >= 24u) {
        if (i4 <= 40u) { wait_lgkm0(); barrier(); }
        if (i4 == 24u) stash_late(st, make_iseq<24>{});
        if (i4 >= 40u) dma_elem<W_NT>(sO, (co + ((i4 >> 4) & 1u)) * KO::CS, KO::US, pc.r01 + ((i4 >> 4) & 1u) * SLOT);
      }
      phaseA<true, false, KV_NT, 63>(ac, SA, SB, at, sK, 0u, st);
      phaseB<true, true, false, KV_NT, 63>(ac, SB, ak, sK, 0u, st);
      phaseA<true, false, KV_NT, 63>(ac, SB, SA, at, sK, 0u, st);
      phaseB<true, true, false, KV_NT, 63>(ac, SA, ak, sK, 0u, st);
    }
    STAMP(14);
    if constexpr (TOUCH_B < 3) asm volatile("s_waitcnt vmcnt(0)" : "+v"(dum) :: "memory");
#define TB3 if constexpr (TOUCH_B == 3) { asm volatile("global_load_dword %0, %1, %2 nt\n\tglobal_load_dword %0, %1, %2" : "+v"(dum) : "v"(to), "s"(tb2) : "memory"); to = (to + 8192u) & 32767u; PINV(to); }
    TB3
    phaseA<true, false, KV_NT, 63>(ac, SA, SB, av, sK, 0u, st);
    TB3
    phaseB<true, false, false, KV_NT, 63>(ac, SB, an, sK, 0u, st);
    TB3
    phaseA<false, false, KV_NT, 63>(ac, SB, SA, av, sK, 0u, st);
    TB3
    dum0 = dum;
  } else {
    const unsigned P0 = pc.r01, P1 = pc.r01 + SLOT, P2 = pc.r2, P3 = w * 16384u;
    unsigned v0 = tbV + P0, v2 = tbV + P2, x0 = v0 ^ 32u, x2 = v2 ^ 32u, k1 = rb + P1, k3 = rb + P3;
    PINV(v0); PINV(v2); PINV(x0); PINV(x2); PINV(k1); PINV(k3);
    wait_vm<48>();
    k_read(ac, k1 - SLOT);
    wait_lgkm0();
    GAP();
    qk0<true>(ac, SA, sV, ksoff(1u, rk), P0, make_iseq<32>{});
    wait_vm<48>();
    GAP();
    k_read(ac, k1);
    sm_first(ac, SA);
    STAMP(13);
    unsigned so = ksoff(2u, rk), i = 0;
#define AV(RD, FILL) [&](PA& p) { PINS(so); p.soff = (so + 8192u) & 0x1ffffu; PINS(p.soff); p.fill = (FILL); p.rd0 = v##RD; p.rd1 = x##RD; }
#define AK(RD, FILL) [&](PA& p) { p.soff = so; so = (so + 8192u) & 0x1ffffu; PINS(so); p.fill = (FILL); p.rd0 = k##RD; }
#define TV(RD, FILL, SO) [&](PA& p) { p.soff = (SO); p.fill = (FILL); p.rd0 = v##RD; p.rd1 = x##RD; }
#define TK(RD, FILL, SO) [&](PA& p) { p.soff = (SO); p.fill = (FILL); p.rd0 = k##RD; }
    for (; i < 10; i += 2) {
      phaseA<true, true, KV_NT, 21>(ac, SA, SB, AV(2, P1), sK, 4096u, st);
      phaseB<true, true, true, KV_NT, 22>(ac, SB, AK(3, P2), sV, 4096u, st);
      phaseA<true, true, KV_NT, 21>(ac, SB, SA, AV(0, P3), sK, 4096u, st);
      phaseB<true, true, true, KV_NT, 22>(ac, SA, AK(1, P0), sV, 4096u, st);
    }
    st.base[0] = P1 + lane * 16u;
    phaseA<true, false, KV_NT, 16, 0>(ac, SA, SB, TV(2, P1, 0u), sK, 0u, st);
    phaseB<true, true, true, KV_NT, 14>(ac, SB, TK(3, P2, ksoff(12u, rk)), sV, 4096u, st);
    phaseA<true, true, KV_NT, 13>(ac, SB, SA, TV(0, P3, ksoff(14u, rk)), sK, 4096u, st);
    st.base[0] = P0 + lane * 16u;
    phaseB<true, true, false, KV_NT, 63, 8>(ac, SA, TK(1, P0, 0u), sV, 0u, st);
    STAMP(14);
    phaseA<true, true, KV_NT, 13>(ac, SA, SB, TV(2, P1, ksoff(15u, rk)), sK, 4096u, st);
    phaseB<true, true, false, KV_NT, 8>(ac, SB, TK(3, P2, 0u), sV, 0u, st);
    phaseA<true, true, KV_NT, 63>(ac, SB, SA, TV(0, P3, ksoff(15u, rk)), sV, 4096u, st);
    phaseB<true, true, true, W_NT, 14>(ac, SA, TK(1, P0, co * KO::CS), sO, KO::US, st);
    unsigned v4 = tbV + P3 + SLOT, x4 = v4 ^ 32u; PINV(v4); PINV(x4);
    phaseA<true, true, W_NT, 63>(ac, SA, SB, TV(4, P1, (co + 1u) * KO::CS), sO, KO::US, st);
    phaseB<true, false, true, W_NT, 63>(ac, SB, TK(3, P2, (co + 2u) * KO::CS), sO, KO::US, st);
    unsigned v3 = tbV + P3, x3 = v3 ^ 32u; PINV(v3); PINV(x3);
    phaseA<false, false, W_NT, 24>(ac, SB, SA, TV(3, P3, 0u), sO, KO::US, st);
#undef AV
#undef AK
#undef TV
#undef TK
  }
  {
    const unsigned l16 = lane * 16u;
    unsigned ow = w * 16384u + l16;
    PINV(ow);
    f32x4 acc[8][4];
    bf16x8 a0[4], a1[4], b0[4], bf[4];
    float inv[4];
    proj_setup<KO>(pc, tb + 512u * g, l16, (co + 3u) << 12, (co + 3u) * KO::CS);
    GAP();
    phaseL<KO, (isA ? 12 : 20)>(ac, inv, b0, a0, pc, make_iseq<36>{});
    STAMP(3);
    if constexpr (isA) {
      barrier();
      dma_elem<W_NT>(sO, (co + 2u) * KO::CS, KO::US, pc.r2);
    }
    const bool ok = ac.l[0][0] <= LMAX && ac.l[1][0] <= LMAX && ac.l[2][0] <= LMAX && ac.l[3][0] <= LMAX;
    if (__builtin_expect(__builtin_amdgcn_ballot_w64(!ok) != 0ull, 0)) {
      acc_fence_a(ac.o);
      redo_head(ac, K + kvo, V + kvo, lane, ow);
#pragma unroll
      for (int q = 0; q < 4; ++q) {
        inv[q] = 1.0f;
        b0[q] = lds128(ow + q * 1024u);
#pragma unroll
        for (int s = 1; s < 4; ++s) {
          const u32x4 t = __builtin_bit_cast(u32x4, lds128(ow + s * 4096u + q * 1024u));
          ac.o[2 * s][q] = f32x4{__uint_as_float(t[0] << 16), __uint_as_float(t[0] & 0xffff0000u), __uint_as_float(t[1] << 16), __uint_as_float(t[1] & 0xffff0000u)};
          ac.o[2 * s + 1][q] = f32x4{__uint_as_float(t[2] << 16), __uint_as_float(t[2] & 0xffff0000u), __uint_as_float(t[3] << 16), __uint_as_float(t[3] & 0xffff0000u)};
        }
      }
      wait_lgkm0();
      acc_fence_a(ac.o);
    }
#pragma unroll
    for (int q = 0; q < 4; ++q) bf[q] = bf16x8{};
    GAP();
    proj_elem<KO, 0, true, false, true, W_NT, 16, 16>(acc, a0, a1, b0, pc, sO, CvX<0>{ac, inv, b0, bf, ow});
    proj_elem<KO, 1, false, false, true, W_NT, 16, 16>(acc, a0, a1, bf, pc, sO, CvX<1>{ac, inv, bf, b0, ow});
    proj_elem<KO, 2, false, false, true, W_NT, 16, 16>(acc, a0, a1, b0, pc, sO, CvX<2>{ac, inv, b0, bf, ow});
    if constexpr (isA && TOUCH_B >= 3) PINV(dum0);
    proj_elem<KO, 0, false, false, true, W_NT, 16, 16, true>(acc, a0, a1, bf, pc, sO, CvX<3>{ac, inv, bf, b0, ow});
    STAMP(4);
#pragma unroll 1
    for (unsigned e = 0; e < 3; ++e) {
      proj_elem<KO, 1, false, false, true, W_NT, 16, 16>(acc, a0, a1, bf, pc, sO);
      proj_elem<KO, 2, false, false, true, W_NT, 16, 16>(acc, a0, a1, bf, pc, sO);
      proj_elem<KO, 0, false, false, true, W_NT, 16, 16>(acc, a0, a1, bf, pc, sO);
    }
    CYC(6);
    unsigned toks;
    asm volatile("buffer_load_dword %0, %1, %2, %3 offen sc1" : "=v"(toks) : "v"(l8), "s"(srdH), "s"(tok_off) : "memory");
    Pub pb;
    pb.srd = mk_srd(part + POFF + (size_t)rr * 524288u, 524288u);
    pb.v[0] = l16; pb.v[1] = l16 + 4096u; pb.so = xg * 65536u + w * 16384u;
    PINV(pb.v[1]);
#pragma unroll
    for (int i = 0; i < 16; ++i) pb.pk[i] = u32x4{};
    proj_elem<KO, 1, false, false, false, W_NT, 17, 13>(acc, a0, a1, bf, pc, sO);
    proj_elem<KO, 2, false, false, false, W_NT, 9, 5>(acc, a0, a1, bf, pc, sO);
    CYC(7);
    if constexpr (isA && PUB_SLEEP > 0) {
      proj_elem<KO, 0, false, true, false, W_NT, 0, 0>(acc, a0, a1, bf, pc, sO);
      STAMP(5);
      pub_slow(acc, pb, make_iseq<16>{});
    } else {
      proj_elem<KO, 0, false, true, false, W_NT, 0, 0>(acc, a0, a1, bf, pc, sO, PubX{acc, pb});
      STAMP(5);
      pub_rest(acc, pb, make_iseq<16>{});
    }
    unsigned fl;
    asm volatile("buffer_load_dword %0, %1, %2, %3 offen sc1" : "=v"(fl) : "v"(l8), "s"(srdH), "s"(flg_off + xg * 128u) : "memory");
    PINV(toks);
    STAMP(6);
    const unsigned fo = lane * 128u + xg * 4u, tau = __builtin_amdgcn_readlane(toks, xg);
    unsigned oo = ((b * 64u + 16u * w + r16) * 1024u + (isA ? 0u : 512u) + 64u * xg + 8u * g) * 2u;
    PINV(oo);
    wait_vm<9>();
    if (lane == 2u * w) asm volatile("buffer_store_dword %0, %1, %2, %3 offen sc1" :: "v"(toks), "v"(fo), "s"(srdH), "s"(flg_off) : "memory");
    wait_vm<2>();
    if (lane == 2u * w + 1u) asm volatile("buffer_store_dword %0, %1, %2, %3 offen sc1" :: "v"(toks), "v"(fo), "s"(srdH), "s"(flg_off) : "memory");
    asm volatile("s_waitcnt vmcnt(2)" : "+v"(fl) : "v"(pb.pk[0]), "v"(pb.pk[1]), "v"(pb.pk[2]), "v"(pb.pk[3]), "v"(pb.pk[4]), "v"(pb.pk[5]), "v"(pb.pk[6]), "v"(pb.pk[7]),
                 "v"(pb.pk[8]), "v"(pb.pk[9]), "v"(pb.pk[10]), "v"(pb.pk[11]), "v"(pb.pk[12]), "v"(pb.pk[13]), "v"(pb.pk[14]), "v"(pb.pk[15]) : "memory");
    STAMP(7);
    f32x16 f[8];
    unsigned got = 0xffffff00u, spin = 1u << 22, fresh, so;
    asm volatile(
        ".Ltop_%=:\n\tv_cmp_eq_u32_e32 vcc, %[tau], %[fl]\n\ts_andn2_b32 %[fr], vcc_lo, %[got]\n\ts_cbranch_scc0 .Lpoll_%=\n\ts_or_b32 %[got], %[got], %[fr]\n\t"
        "s_bitcmp1_b32 %[fr], 0\n\ts_cbranch_scc0 .La0_%=\n\ts_add_u32 %[so], %[ro], 0\n\t"
        "buffer_load_dwordx4 v[136:139], %[l16], %[sp], %[so] offen sc1\n\tbuffer_load_dwordx4 v[140:143], %[l16], %[sp], %[so] offen offset:1024 sc1\n.La0_%=:\n\t"
        "s_bitcmp1_b32 %[fr], 1\n\ts_cbranch_scc0 .La1_%=\n\ts_add_u32 %[so], %[ro], 65536\n\t"
        "buffer_load_dwordx4 v[152:155], %[l16], %[sp], %[so] offen sc1\n\tbuffer_load_dwordx4 v[156:159], %[l16], %[sp], %[so] offen offset:1024 sc1\n.La1_%=:\n\t"
        "s_bitcmp1_b32 %[fr], 2\n\ts_cbranch_scc0 .La2_%=\n\ts_add_u32 %[so], %[ro], 131072\n\t"
        "buffer_load_dwordx4 v[168:171], %[l16], %[sp], %[so] offen sc1\n\tbuffer_load_dwordx4 v[172:175], %[l16], %[sp], %[so] offen offset:1024 sc1\n.La2_%=:\n\t"
        "s_bitcmp1_b32 %[fr], 3\n\ts_cbranch_scc0 .La3_%=\n\ts_add_u32 %[so], %[ro], 196608\n\t"
        "buffer_load_dwordx4 v[184:187], %[l16], %[sp], %[so] offen sc1\n\tbuffer_load_dwordx4 v[188:191], %[l16], %[sp], %[so] offen offset:1024 sc1\n.La3_%=:\n\t"
        "s_bitcmp1_b32 %[fr], 4\n\ts_cbranch_scc0 .La4_%=\n\ts_add_u32 %[so], %[ro], 262144\n\t"
        "buffer_load_dwordx4 v[200:203], %[l16], %[sp], %[so] offen sc1\n\tbuffer_load_dwordx4 v[204:207], %[l16], %[sp], %[so] offen offset:1024 sc1\n.La4_%=:\n\t"
        "s_bitcmp1_b32 %[fr], 5\n\ts_cbranch_scc0 .La5_%=\n\ts_add_u32 %[so], %[ro], 327680\n\t"
        "buffer_load_dwordx4 v[216:219], %[l16], %[sp], %[so] offen sc1\n\tbuffer_load_dwordx4 v[220:223], %[l16], %[sp], %[so] offen offset:1024 sc1\n.La5_%=:\n\t"
        "s_bitcmp1_b32 %[fr], 6\n\ts_cbranch_scc0 .La6_%=\n\ts_add_u32 %[so], %[ro], 393216\n\t"
        "buffer_load_dwordx4 v[232:235], %[l16], %[sp], %[so] offen sc1\n\tbuffer_load_dwordx4 v[236:239], %[l16], %[sp], %[so] offen offset:1024 sc1\n.La6_%=:\n\t"
        "s_bitcmp1_b32 %[fr], 7\n\ts_cbranch_scc0 .La7_%=\n\ts_add_u32 %[so], %[ro], 458752\n\t"
        "buffer_load_dwordx4 v[248:251], %[l16], %[sp], %[so] offen sc1\n\tbuffer_load_dwordx4 v[252:255], %[l16], %[sp], %[so] offen offset:1024 sc1\n.La7_%=:\n\t"
        ".Lpoll_%=:\n\ts_cmp_eq_u32 %[got], -1\n\ts_cselect_b32 %[so], %[tok], %[flg]\n\tbuffer_load_dword %[fl], %[l8], %[sh], %[so] offen sc1\n\t"
        "s_cmp_eq_u32 %[fr], 0\n\ts_cbranch_scc1 .Lwait_%=\n\ts_waitcnt vmcnt(1)\n\t"
        "s_bitcmp1_b32 %[fr], 0\n\ts_cbranch_scc0 .Lb0_%=\n\t"
        "v_lshlrev_b32_e32 v128, 16, v136\n\tv_and_b32_e32 v129, %[hi], v136\n\tv_lshlrev_b32_e32 v130, 16, v137\n\tv_and_b32_e32 v131, %[hi], v137\n\t"
        "v_lshlrev_b32_e32 v132, 16, v138\n\tv_and_b32_e32 v133, %[hi], v138\n\tv_lshlrev_b32_e32 v134, 16, v139\n\tv_and_b32_e32 v135, %[hi], v139\n\t"
        "v_lshlrev_b32_e32 v136, 16, v140\n\tv_and_b32_e32 v137, %[hi], v140\n\tv_lshlrev_b32_e32 v138, 16, v141\n\tv_and_b32_e32 v139, %[hi], v141\n\t"
        "v_lshlrev_b32_e32 v140, 16, v142\n\tv_and_b32_e32 v141, %[hi], v142\n\tv_lshlrev_b32_e32 v142, 16, v143\n\tv_and_b32_e32 v143, %[hi], v143\n\t"
        ".Lb0_%=:\n\t"
        "s_bitcmp1_b32 %[fr], 1\n\ts_cbranch_scc0 .Lb1_%=\n\t"
        "v_lshlrev_b32_e32 v144, 16, v152\n\tv_and_b32_e32 v145, %[hi], v152\n\tv_lshlrev_b32_e32 v146, 16, v153\n\tv_and_b32_e32 v147, %[hi], v153\n\t"
        "v_lshlrev_b32_e32 v148, 16, v154\n\tv_and_b32_e32 v149, %[hi], v154\n\tv_lshlrev_b32_e32 v150, 16, v155\n\tv_and_b32_e32 v151, %[hi], v155\n\t"
        "v_lshlrev_b32_e32 v152, 16, v156\n\tv_and_b32_e32 v153, %[hi], v156\n\tv_lshlrev_b32_e32 v154, 16, v157\n\tv_and_b32_e32 v155, %[hi], v157\n\t"
        "v_lshlrev_b32_e32 v156, 16, v158\n\tv_and_b32_e32 v157, %[hi], v158\n\tv_lshlrev_b32_e32 v158, 16, v159\n\tv_and_b32_e32 v159, %[hi], v159\n\t"
        ".Lb1_%=:\n\t"
        "s_bitcmp1_b32 %[fr], 2\n\ts_cbranch_scc0 .Lb2_%=\n\t"
        "v_lshlrev_b32_e32 v160, 16, v168\n\tv_and_b32_e32 v161, %[hi], v168\n\tv_lshlrev_b32_e32 v162, 16, v169\n\tv_and_b32_e32 v163, %[hi], v169\n\t"
        "v_lshlrev_b32_e32 v164, 16, v170\n\tv_and_b32_e32 v165, %[hi], v170\n\tv_lshlrev_b32_e32 v166, 16, v171\n\tv_and_b32_e32 v167, %[hi], v171\n\t"
        "v_lshlrev_b32_e32 v168, 16, v172\n\tv_and_b32_e32 v169, %[hi], v172\n\tv_lshlrev_b32_e32 v170, 16, v173\n\tv_and_b32_e32 v171, %[hi], v173\n\t"
        "v_lshlrev_b32_e32 v172, 16, v174\n\tv_and_b32_e32 v173, %[hi], v174\n\tv_lshlrev_b32_e32 v174, 16, v175\n\tv_and_b32_e32 v175, %[hi], v175\n\t"
        ".Lb2_%=:\n\t"
        "s_bitcmp1_b32 %[fr], 3\n\ts_cbranch_scc0 .Lb3_%=\n\t"
        "v_lshlrev_b32_e32 v176, 16, v184\n\tv_and_b32_e32 v177, %[hi], v184\n\tv_lshlrev_b32_e32 v178, 16, v185\n\tv_and_b32_e32 v179, %[hi], v185\n\t"
        "v_lshlrev_b32_e32 v180, 16, v186\n\tv_and_b32_e32 v181, %[hi], v186\n\tv_lshlrev_b32_e32 v182, 16, v187\n\tv_and_b32_e32 v183, %[hi], v187\n\t"
        "v_lshlrev_b32_e32 v184, 16, v188\n\tv_and_b32_e32 v185, %[hi], v188\n\tv_lshlrev_b32_e32 v186, 16, v189\n\tv_and_b32_e32 v187, %[hi], v189\n\t"
        "v_lshlrev_b32_e32 v188, 16, v190\n\tv_and_b32_e32 v189, %[hi], v190\n\tv_lshlrev_b32_e32 v190, 16, v191\n\tv_and_b32_e32 v191, %[hi], v191\n\t"
        ".Lb3_%=:\n\t"
        "s_bitcmp1_b32 %[fr], 4\n\ts_cbranch_scc0 .Lb4_%=\n\t"
        "v_lshlrev_b32_e32 v192, 16, v200\n\tv_and_b32_e32 v193, %[hi], v200\n\tv_lshlrev_b32_e32 v194, 16, v201\n\tv_and_b32_e32 v195, %[hi], v201\n\t"
        "v_lshlrev_b32_e32 v196, 16, v202\n\tv_and_b32_e32 v197, %[hi], v202\n\tv_lshlrev_b32_e32 v198, 16, v203\n\tv_and_b32_e32 v199, %[hi], v203\n\t"
        "v_lshlrev_b32_e32 v200, 16, v204\n\tv_and_b32_e32 v201, %[hi], v204\n\tv_lshlrev_b32_e32 v202, 16, v205\n\tv_and_b32_e32 v203, %[hi], v205\n\t"
        "v_lshlrev_b32_e32 v204, 16, v206\n\tv_and_b32_e32 v205, %[hi], v206\n\tv_lshlrev_b32_e32 v206, 16, v207\n\tv_and_b32_e32 v207, %[hi], v207\n\t"
        ".Lb4_%=:\n\t"
        "s_bitcmp1_b32 %[fr], 5\n\ts_cbranch_scc0 .Lb5_%=\n\t"
        "v_lshlrev_b32_e32 v208, 16, v216\n\tv_and_b32_e32 v209, %[hi], v216\n\tv_lshlrev_b32_e32 v210, 16, v217\n\tv_and_b32_e32 v211, %[hi], v217\n\t"
        "v_lshlrev_b32_e32 v212, 16, v218\n\tv_and_b32_e32 v213, %[hi], v218\n\tv_lshlrev_b32_e32 v214, 16, v219\n\tv_and_b32_e32 v215, %[hi], v219\n\t"
        "v_lshlrev_b32_e32 v216, 16, v220\n\tv_and_b32_e32 v217, %[hi], v220\n\tv_lshlrev_b32_e32 v218, 16, v221\n\tv_and_b32_e32 v219, %[hi], v221\n\t"
        "v_lshlrev_b32_e32 v220, 16, v222\n\tv_and_b32_e32 v221, %[hi], v222\n\tv_lshlrev_b32_e32 v222, 16, v223\n\tv_and_b32_e32 v223, %[hi], v223\n\t"
        ".Lb5_%=:\n\t"
        "s_bitcmp1_b32 %[fr], 6\n\ts_cbranch_scc0 .Lb6_%=\n\t"
        "v_lshlrev_b32_e32 v224, 16, v232\n\tv_and_b32_e32 v225, %[hi], v232\n\tv_lshlrev_b32_e32 v226, 16, v233\n\tv_and_b32_e32 v227, %[hi], v233\n\t"
        "v_lshlrev_b32_e32 v228, 16, v234\n\tv_and_b32_e32 v229, %[hi], v234\n\tv_lshlrev_b32_e32 v230, 16, v235\n\tv_and_b32_e32 v231, %[hi], v235\n\t"
        "v_lshlrev_b32_e32 v232, 16, v236\n\tv_and_b32_e32 v233, %[hi], v236\n\tv_lshlrev_b32_e32 v234, 16, v237\n\tv_and_b32_e32 v235, %[hi], v237\n\t"
        "v_lshlrev_b32_e32 v236, 16, v238\n\tv_and_b32_e32 v237, %[hi], v238\n\tv_lshlrev_b32_e32 v238, 16, v239\n\tv_and_b32_e32 v239, %[hi], v239\n\t"
        ".Lb6_%=:\n\t"
        "s_bitcmp1_b32 %[fr], 7\n\ts_cbranch_scc0 .Lb7_%=\n\t"
        "v_lshlrev_b32_e32 v240, 16, v248\n\tv_and_b32_e32 v241, %[hi], v248\n\tv_lshlrev_b32_e32 v242, 16, v249\n\tv_and_b32_e32 v243, %[hi], v249\n\t"
        "v_lshlrev_b32_e32 v244, 16, v250\n\tv_and_b32_e32 v245, %[hi], v250\n\tv_lshlrev_b32_e32 v246, 16, v251\n\tv_and_b32_e32 v247, %[hi], v251\n\t"
        "v_lshlrev_b32_e32 v248, 16, v252\n\tv_and_b32_e32 v249, %[hi], v252\n\tv_lshlrev_b32_e32 v250, 16, v253\n\tv_and_b32_e32 v251, %[hi], v253\n\t"
        "v_lshlrev_b32_e32 v252, 16, v254\n\tv_and_b32_e32 v253, %[hi], v254\n\tv_lshlrev_b32_e32 v254, 16, v255\n\tv_and_b32_e32 v255, %[hi], v255\n\t"
        ".Lb7_%=:\n\t"
        "s_cmp_eq_u32 %[got], -1\n\ts_cbranch_scc1 .Ldone_%=\n"
        ".Lwait_%=:\n\ts_sleep %[slp]\n\ts_waitcnt vmcnt(0)\n\ts_sub_u32 %[spin], %[spin], 1\n\ts_cbranch_scc0 .Ltop_%=\n"
        ".Ldone_%=:"
        : "=&{v[128:143]}"(f[0]), "=&{v[144:159]}"(f[1]), "=&{v[160:175]}"(f[2]), "=&{v[176:191]}"(f[3]), "=&{v[192:207]}"(f[4]), "=&{v[208:223]}"(f[5]), "=&{v[224:239]}"(f[6]), "=&{v[240:255]}"(f[7]),
          [fl] "+v"(fl), [got] "+s"(got), [spin] "+s"(spin), [fr] "=&s"(fresh), [so] "=&s"(so)
        : [tau] "s"(tau), [l16] "v"(l16), [l8] "v"(l8), [sp] "s"(pb.srd), [sh] "s"(srdH), [ro] "s"(xg * 8192u + w * 2048u), [tok] "s"(tok_off), [flg] "s"(flg_off + xg * 128u),
          [hi] "s"(0xffff0000u), [slp] "i"(isA ? SLEEP_A : SLEEP_B)
        : "vcc", "scc", "memory");
    STAMP(15);
    const f32x16 t = ((f[0] + f[1]) + (f[2] + f[3])) + ((f[4] + f[5]) + (f[6] + f[7]));
#pragma unroll
    for (int s = 0; s < 2; ++s)
      *(u32x4*)(out + oo + s * 64) = natural8(u32x4{cvtpk(t[8 * s], t[8 * s + 1]), cvtpk(t[8 * s + 2], t[8 * s + 3]), cvtpk(t[8 * s + 4], t[8 * s + 5]), cvtpk(t[8 * s + 6], t[8 * s + 7])});
    asm volatile("s_waitcnt vmcnt(2)" : "+v"(fl) :: "memory");
    if ((lane >> 1) == w && fl != toks) asm volatile("buffer_store_dword %0, %1, %2, %3 offen sc1" :: "v"(fl), "v"(fo), "s"(srdH), "s"(flg_off) : "memory");
  }
}

__global__ __launch_bounds__(256, 1)
void fused_attn(const char* __restrict__ Ax, const char* __restrict__ Ak, const char* __restrict__ Av, const char* __restrict__ Awq, const char* __restrict__ Awo,
                const char* __restrict__ Bx, const char* __restrict__ Bk, const char* __restrict__ Bv, const char* __restrict__ Bwq, const char* __restrict__ Bwo,
                char* __restrict__ part, char* __restrict__ out) {
  const char* pc; unsigned left;
  asm volatile("s_getpc_b64 %0\n.Lpc_%=:\n\ts_mov_b32 %1, .Lhot_end-.Lpc_%=" : "=s"(pc), "=s"(left));
  const srd_t code = mk_srd(pc, left);
  if (vbid() >= 128) { body<true>(Ax, Ak, Av, Awq, Awo, Bwo, code, part, out); asm volatile(".Lhot_end:"); __builtin_amdgcn_endpgm(); }
  body<false>(Bx, Bk, Bv, Bwq, Bwo, Bwo, code, part, out);
}

extern "C" void kernel_launch(void* const* d_in, const int* in_sizes, int n_in, void* d_out, int out_size, void* d_ws, size_t ws_size, hipStream_t stream) {
  (void)in_sizes; (void)n_in; (void)out_size; (void)ws_size;
  (void)hipFuncSetAttribute((const void*)fused_attn, hipFuncAttributeMaxDynamicSharedMemorySize, (int)LDS_BYTES);
  fused_attn<<<256, 256, LDS_BYTES, stream>>>((const char*)d_in[0], (const char*)d_in[1], (const char*)d_in[2], (const char*)d_in[3], (const char*)d_in[4],
                                              (const char*)d_in[5], (const char*)d_in[6], (const char*)d_in[7], (const char*)d_in[8], (const char*)d_in[9], (char*)d_ws, (char*)d_out);
}
```

```cpp
#include <hip/hip_runtime.h>
#include <hip/hip_bf16.h>
#include <utility>

typedef __bf16 bf16x8 __attribute__((ext_vector_type(8)));
typedef __bf16 bf16x4 __attribute__((ext_vector_type(4)));
typedef __bf16 bf16x2 __attribute__((ext_vector_type(2)));
typedef float f32x4 __attribute__((ext_vector_type(4)));
typedef float f32x2 __attribute__((ext_vector_type(2)));
typedef float f32x16 __attribute__((ext_vector_type(16)));
typedef unsigned u32x4 __attribute__((ext_vector_type(4)));
#define LAS __attribute__((address_space(3)))
#define DI __device__ __forceinline__
typedef __amdgpu_buffer_rsrc_t srd_t;
template <int... Is> using iseq = std::integer_sequence<int, Is...>;
template <int N> using make_iseq = std::make_integer_sequence<int, N>;

#define KV_NT 1
#define W_NT 0
#define X_NT 0
#define SLEEP_A 8
#define SLEEP_B 0
#define PACE_A 0
#define PACE_ATT 0
#define TOUCH_Q 1
#define TOUCH_O 3
#define TO_SKIP 16u
#define TO_B 8u
#define WARM_CODE 40
#define ALT 0
#define ORD 2
#define EARLY_O 0
#define TOUCH_B 4
#define PUB_SLEEP 0
#define KVPOL "nt"
#define CVV 0
#define DELAY_A 0
#define SK 9
#define SV 11
#define SP 13
#define OUTPOL 2
#define STAG 0
#define RAMP 0
#define PUBPOL "sc0 sc1"
#define WS_END 0

constexpr unsigned SLOT = 8192, LDS_BYTES = 20 * SLOT;
constexpr unsigned POFF = (unsigned)WS_END << 20, HOFF = POFF + (16u << 20);
constexpr float SC = 0.12751743082f;
constexpr float LMAX = 1048576.0f;

#define NO_REDUCE_STAMPS
#define GAP() __builtin_amdgcn_sched_barrier(0)
#define STAMP(i) do {} while (0)
#define CYC(i) do {} while (0)
#define PINS(x) asm volatile("" : "+s"(x))
#define PINV(x) asm volatile("" : "+v"(x))
DI unsigned rfl(unsigned x) { return __builtin_amdgcn_readfirstlane(x); }
DI unsigned cvtpk(float lo, float hi) { f32x2 v = {lo, hi}; bf16x2 b = __builtin_convertvector(v, bf16x2); return __builtin_bit_cast(unsigned, b); }
DI u32x4 pack2(const f32x4& a, const f32x4& b) { return u32x4{cvtpk(a[0], a[1]), cvtpk(a[2], a[3]), cvtpk(b[0], b[1]), cvtpk(b[2], b[3])}; }
DI u32x4 natural8(u32x4 w) {
  auto s0 = __builtin_amdgcn_permlane32_swap(w[0], w[2], false, false);
  auto s1 = __builtin_amdgcn_permlane32_swap(w[1], w[3], false, false);
  auto t0 = __builtin_amdgcn_permlane16_swap(s0[0], s0[1], false, false);
  auto t1 = __builtin_amdgcn_permlane16_swap(s1[0], s1[1], false, false);
  return u32x4{t0[0], t1[0], t0[1], t1[1]};
}
#define MF "v_mfma_f32_16x16x32_bf16 "
template <bool Z> DI void mm_vvv(f32x4& c, const bf16x8& a, const bf16x8& b) {
  if constexpr (Z) asm volatile(MF "%0, %1, %2, 0" : "=&v"(c) : "v"(a), "v"(b));
  else             asm volatile(MF "%0, %1, %2, %0" : "+v"(c) : "v"(a), "v"(b));
}
template <bool Z> DI void mm_vva(f32x4& c, const bf16x8& a, const bf16x8& b) {
  if constexpr (Z) asm volatile(MF "%0, %1, %2, 0" : "=&v"(c) : "v"(a), "a"(b));
  else             asm volatile(MF "%0, %1, %2, %0" : "+v"(c) : "v"(a), "a"(b));
}
DI void mm_vca(f32x4& d, const bf16x8& a, const bf16x8& b, const f32x4& c) { asm volatile(MF "%0, %1, %2, %3" : "=&v"(d) : "v"(a), "a"(b), "v"(c)); }
DI void mm_avv(f32x4& c, const bf16x8& a, const bf16x8& b) { asm volatile(MF "%0, %1, %2, %0" : "+a"(c) : "v"(a), "v"(b)); }
DI void set_m0(unsigned v) { asm volatile("s_mov_b32 m0, %0" :: "s"(v) : "memory"); }
template <int NT, int P> DI void dma_p(unsigned voff, srd_t srd, unsigned soff) {
  if constexpr (NT) asm volatile("buffer_load_dwordx4 %0, %1, %2 offen offset:%3 " KVPOL " lds" :: "v"(voff), "s"(srd), "s"(soff), "i"(1024 * P) : "memory");
  else              asm volatile("buffer_load_dwordx4 %0, %1, %2 offen offset:%3 lds" :: "v"(voff), "s"(srd), "s"(soff), "i"(1024 * P) : "memory");
}
struct Src { unsigned v[4]; srd_t srd; };
template <int NT> DI void dma_unit(const Src& s, unsigned soff, unsigned ldsaddr) {
  asm volatile("s_mov_b32 m0, %0\n\ts_nop 0" :: "s"(ldsaddr) : "memory");
  dma_p<NT, 0>(s.v[0], s.srd, soff); dma_p<NT, 1>(s.v[1], s.srd, soff); dma_p<NT, 2>(s.v[2], s.srd, soff); dma_p<NT, 3>(s.v[3], s.srd, soff);
}
template <int NT> DI void dma_elem(const Src& s, unsigned soff, unsigned ustep, unsigned ldsaddr) { dma_unit<NT>(s, soff, ldsaddr); dma_unit<NT>(s, soff + ustep, ldsaddr + 4096u); }
template <int N> DI void wait_vm() { asm volatile("s_waitcnt vmcnt(%0)" :: "i"(N) : "memory"); }
template <int N, int L> DI void wait_vm_lgkm() { asm volatile("" ::: "memory"); __builtin_amdgcn_s_waitcnt((N & 15) | ((N >> 4) << 14) | 0x70 | (L << 8)); asm volatile("" ::: "memory"); }
DI void wait_lgkm0() { wait_vm_lgkm<63, 0>(); }
DI void barrier() { asm volatile("s_barrier" ::: "memory"); }
DI bf16x8 lds128(unsigned a) { return *(const LAS bf16x8*)(size_t)(a); }
DI bf16x4 lds_tr1(unsigned a) { return __builtin_amdgcn_ds_read_tr16_b64_v4bf16((LAS bf16x4*)(size_t)(a)); }
DI bf16x8 cat(bf16x4 lo, bf16x4 hi) { return __builtin_shufflevector(lo, hi, 0, 1, 2, 3, 4, 5, 6, 7); }
DI bf16x8 lds_tr(unsigned a0, unsigned a1) { return cat(lds_tr1(a0), lds_tr1(a1)); }
DI srd_t mk_srd(const void* base, unsigned bytes) { return __builtin_amdgcn_make_buffer_rsrc((void*)base, (short)0, (int)bytes, 0x00020000); }
template <bool VX> DI void mk_src_rows(Src& s, const char* base, unsigned bytes, unsigned stride, unsigned lane) {
  s.srd = mk_srd(base, bytes);
  const unsigned row = (lane >> 2) & 3u, cb = lane >> 4, sl = lane & 3u;
#pragma unroll
  for (int p = 0; p < 4; ++p) s.v[p] = row * stride + cb * 64u + ((sl ^ (VX ? 2 * (p & 1) : (p & 2))) << 4) + (4u * stride - 1024u) * p;
}
template <bool VX> DI void mk_src_w(Src& s, const char* base, unsigned bytes, unsigned stride, unsigned lane) {
  s.srd = mk_srd(base, bytes);
  const unsigned sub = lane >> 5, cb = (lane >> 4) & 1u, row = (lane >> 2) & 3u, sl = lane & 3u;
#pragma unroll
  for (int p = 0; p < 4; ++p) s.v[p] = (4u * sub + row) * stride + cb * 64u + ((sl ^ (VX ? 2u * sub : 2u * (p & 1))) << 4) + (8u * stride - 1024u) * p;
}
template <int NT> DI void touch(unsigned& d, unsigned voff, srd_t srd, unsigned soff) {
  if constexpr (NT) asm volatile("buffer_load_dword %0, %1, %2, %3 offen nt" : "+v"(d) : "v"(voff), "s"(srd), "s"(soff) : "memory");
  else              asm volatile("buffer_load_dword %0, %1, %2, %3 offen" : "+v"(d) : "v"(voff), "s"(srd), "s"(soff) : "memory");
}
DI unsigned ksoff(unsigned m, unsigned rk) { return ((m + rk) & 15u) * 8192u; }

struct PC {
  unsigned r01, r2;
  unsigned w01[2], w2[2];
  unsigned xb, xmask, xa;
  unsigned z, soff;
};
template <unsigned TS_, unsigned QS_, unsigned ZI_, unsigned ZM_, unsigned ZO_, unsigned US_, unsigned CS_, bool ROT_> struct PK {
  static constexpr unsigned TS = TS_, QS = QS_, ZI = ZI_, ZM = ZM_, ZO = ZO_, US = US_, CS = CS_; static constexpr bool ROT = ROT_; };
DI unsigned q_soff(unsigned z) { return ((z & 0xC300u) * 0x440u) & 0x3C0000u; }
template <int POS> DI unsigned ring_lds(const PC& c) { return POS == 2 ? c.r2 : c.r01 + POS * SLOT; }
template <int POS> DI unsigned ring_rd(const PC& c, int d) { return (POS == 2 ? c.w2[d & 1] : c.w01[d & 1] + POS * SLOT) + (d >> 1) * 256u; }
struct NoX { static constexpr bool ACT = true; template <int G> DI void run() const {} };
template <class K, int POS, int S, bool Z, bool RD, bool DM, int NT, bool XT, int M, class X>
DI void proj_gap(f32x4 (&acc)[8][4], const bf16x8 (&cur)[4], bf16x8 (&nxt)[4], bf16x4 (&lo)[4], bf16x8 (&bf)[4], PC& c, unsigned soff, const Src& src, const Src& xs, unsigned xsoff, const X& ex) {
  constexpr int qb = M >> 2, db = M & 3;
  mm_vvv<Z>(acc[4 * S + db][qb], cur[db], bf[qb]);
  if constexpr (RD && M < 8) {
    constexpr int d = M >> 1; const unsigned a = S == 0 ? ring_rd<POS>(c, d) + 4096u : ring_rd<(POS + 1) % 3>(c, d);
    if constexpr (M & 1) nxt[d] = cat(lo[d], lds_tr1(a + K::TS)); else lo[d] = lds_tr1(a);
  }
  if constexpr (X::ACT && RD && S == 1 && db == 3) bf[qb] = lds128(c.xa + qb * K::QS);
  if constexpr (XT) {
    if constexpr (M < 8 && !(M & 1)) dma_p<X_NT, M / 2>(xs.v[M / 2], xs.srd, xsoff);
    if constexpr (M == 7) set_m0(ring_lds<POS>(c) + S * 4096u);
    if constexpr (M >= 8 && !(M & 1)) dma_p<NT, (M - 8) / 2>(src.v[(M - 8) / 2], src.srd, soff);
  } else if constexpr (DM && M >= 8 && !(M & 1)) dma_p<NT, (M - 8) / 2>(src.v[(M - 8) / 2], src.srd, soff);
  if constexpr (X::ACT && S == 0 && M == 9) { PINS(c.z); c.z = (c.z + K::ZI) & K::ZM; PINS(c.z); }
  if constexpr (X::ACT && S == 0 && M == 11) { c.xa = (c.z & c.xmask) | c.xb; PINV(c.xa); }
  if constexpr (S == 1 && DM && !K::ROT && M == 9) { PINS(c.soff); c.soff = (c.soff + K::CS) & (16u * K::CS - 1u); PINS(c.soff); }
  if constexpr (S == 1 && DM && K::ROT && M == 9) { PINS(c.z); c.soff = (c.z + 0x3100u) & 0xC300u; PINS(c.soff); }
  if constexpr (S == 1 && DM && K::ROT && M == 13) { PINS(c.soff); c.soff = (c.soff * 0x440u) & 0x3C0000u; PINS(c.soff); }
  ex.template run<16 * S + M>();
  GAP();
}
template <class K, int POS, int S, bool Z, bool RD, bool DM, int NT, int VM, bool BAR, bool XT, class X, int... Ms>
DI void proj_unit(f32x4 (&acc)[8][4], const bf16x8 (&cur)[4], bf16x8 (&nxt)[4], bf16x8 (&bf)[4], PC& c, const Src& src, const Src& xs, unsigned xsoff, unsigned xdst, const X& ex, iseq<Ms...>) {
  wait_vm_lgkm<(RD ? VM : 63), (S == 0 ? 2 : 0)>();
  if constexpr (BAR) barrier();
  const unsigned soff = c.soff + S * K::US;
  if constexpr (XT) set_m0(xdst); else if constexpr (DM) set_m0(ring_lds<POS>(c) + S * 4096u);
  GAP();
  bf16x4 lo[4];
  (proj_gap<K, POS, S, Z, RD, DM, NT, XT, Ms>(acc, cur, nxt, lo, bf, c, soff, src, xs, xsoff, ex), ...);
}
template <class K, int POS, bool Z, bool LAST, bool DM, int NT, int VM0, int VM1, bool BAR1 = false, bool XT0 = false, bool XT1 = false, class X = NoX>
DI void proj_elem(f32x4 (&acc)[8][4], bf16x8 (&a0)[4], bf16x8 (&a1)[4], bf16x8 (&bf)[4], PC& c, const Src& src, const X& ex = X{},
                  const Src* xs = nullptr, unsigned xso0 = 0, unsigned xd0 = 0, unsigned xso1 = 0, unsigned xd1 = 0) {
  proj_unit<K, POS, 0, Z, true, DM, NT, VM0, false, XT0>(acc, a0, a1, bf, c, src, xs ? *xs : src, xso0, xd0, ex, make_iseq<16>{});
  proj_unit<K, POS, 1, Z, !LAST, DM, NT, VM1, BAR1, XT1>(acc, a1, a0, bf, c, src, xs ? *xs : src, xso1, xd1, ex, make_iseq<16>{});
}
template <class K> DI void proj_setup(PC& c, unsigned tb, unsigned xb, unsigned z0, unsigned s3) {
  c.w01[0] = c.r01 + tb; c.w01[1] = c.w01[0] ^ 32u; c.w2[0] = c.r2 + tb; c.w2[1] = c.w2[0] ^ 32u;
  PINV(c.w01[0]); PINV(c.w01[1]); PINV(c.w2[0]); PINV(c.w2[1]);
  c.xb = xb; c.xmask = K::ZO; PINV(c.xmask);
  c.z = z0; c.soff = s3;
}
template <class K, int POS> DI void proj_start(bf16x8 (&a0)[4], bf16x8 (&bf)[4], PC& c, unsigned tb, unsigned xb, unsigned z0, unsigned s3) {
  proj_setup<K>(c, tb, xb, z0, s3);
  c.xa = (c.z & K::ZO) | xb; PINV(c.xa);
#pragma unroll
  for (int d = 0; d < 4; ++d) { const unsigned a = ring_rd<POS>(c, d); a0[d] = lds_tr(a, a + K::TS); }
  GAP();
#pragma unroll
  for (int q = 0; q < 4; ++q) bf[q] = lds128(c.xa + q * K::QS);
  GAP();
}
#define ACC_ALL(C, a) C(a[0][0]), C(a[0][1]), C(a[0][2]), C(a[0][3]), C(a[1][0]), C(a[1][1]), C(a[1][2]), C(a[1][3]), C(a[2][0]), C(a[2][1]), C(a[2][2]), C(a[2][3]), C(a[3][0]), C(a[3][1]), C(a[3][2]), C(a[3][3])
DI void acc_fence_v(f32x4 (&acc)[8][4]) {
  asm volatile("s_nop 7\n\ts_nop 3" : ACC_ALL("+v", acc) :: "memory");
  f32x4 (&hi)[4][4] = *(f32x4 (*)[4][4])&acc[4];
  asm volatile("" : ACC_ALL("+v", hi) :: "memory");
  GAP();
}
DI void acc_fence_a(f32x4 (&acc)[8][4]) {
  asm volatile("s_nop 7\n\ts_nop 3" : ACC_ALL("+a", acc) :: "memory");
  f32x4 (&hi)[4][4] = *(f32x4 (*)[4][4])&acc[4];
  asm volatile("" : ACC_ALL("+a", hi) :: "memory");
  GAP();
}

struct Stash { u32x4 p[64]; unsigned base[3]; };
template <int I> DI void stash_load1(Stash& st, unsigned vk, const Src& sK, const Src& sV, unsigned rk, unsigned wo_) {
  constexpr int m = I >> 2, isV = (I >> 1) & 1, pc_ = I & 1;
  const unsigned so = ksoff(m, rk) + wo_;
  if constexpr (I < 56) asm volatile("buffer_load_dwordx4 %0, %1, %2, %3 offen offset:%4 " KVPOL : "=a"(st.p[I]) : "v"(isV ? sV.v[pc_] : vk), "s"(isV ? sV.srd : sK.srd), "s"(so), "i"(1024 * pc_) : "memory");
  else                  asm volatile("buffer_load_dwordx4 %0, %1, %2, %3 offen offset:%4 " KVPOL : "=v"(st.p[I]) : "v"(isV ? sV.v[pc_] : vk), "s"(isV ? sV.srd : sK.srd), "s"(so), "i"(1024 * pc_) : "memory");
}
template <int... Is> DI void stash_load(Stash& st, unsigned vk, const Src& sK, const Src& sV, unsigned rk, unsigned wo_, iseq<Is...>) { (stash_load1<Is>(st, vk, sK, sV, rk, wo_), ...); }
constexpr int kv_pair(int m) { constexpr int t[10] = {0, 1, 2, 3, 8, 9, 4, 5, 6, 7}; return t[m % 10]; }
constexpr unsigned long long kv_tab() { unsigned long long v = 0; for (int i = 0; i < 16; ++i) v |= (unsigned long long)kv_pair(i) << (4 * i); return v; }
template <int I> DI void stash_write1(const Stash& st) {
  constexpr int m = I >> 2, isV = (I >> 1) & 1, pc_ = I & 1, off = kv_pair(m) * 16384 + isV * 8192 + pc_ * 1024;
  if constexpr (I < 56) asm volatile("ds_write_b128 %0, %1 offset:%2" :: "v"(st.base[off >> 16]), "a"(st.p[I]), "i"(off & 65535) : "memory");
  else                  asm volatile("ds_write_b128 %0, %1 offset:%2" :: "v"(st.base[off >> 16]), "v"(st.p[I]), "i"(off & 65535) : "memory");
}
template <int... Is> DI void stash_late(const Stash& st, iseq<Is...>) { (stash_write1<40 + Is>(st), ...); }

struct AC {
  f32x4 o[8][4];
  bf16x8 q[4][4];
  bf16x8 kf[8], vf[8], pf[4];
  f32x4 negm[4];
  f32x4 l[4];
  bf16x8 ones;
};
typedef f32x4 ST[2][4];
constexpr int NEB = 22;
template <int E> DI float sm_val(ST& S) { return S[(E >> 2) & 1][E >> 3][E & 3]; }
template <int E> DI void sm_exp1(ST& S) { float a; asm volatile("v_exp_f32_e32 %0, %1" : "=v"(a) : "v"(sm_val<E>(S))); S[(E >> 2) & 1][E >> 3][E & 3] = a; }
template <int QB, int P> DI void sm_cvt(ST& S, AC& c) {
  u32x4 w = __builtin_bit_cast(u32x4, c.pf[QB]);
  unsigned r;
  asm volatile("v_cvt_pk_bf16_f32 %0, %1, %2" : "=v"(r) : "v"(sm_val<8 * QB + 2 * P>(S)), "v"(sm_val<8 * QB + 2 * P + 1>(S)));
  w[P] = r;
  c.pf[QB] = __builtin_bit_cast(bf16x8, w);
}
template <bool DM, int VM, int R, int M> DI void mem_pre(unsigned ldsaddr) {
  if constexpr (M == 6) wait_lgkm0();
  if constexpr (DM && (M == 6 || M == 14)) set_m0(ldsaddr + (M == 14 ? 4096u : 0u));
  if constexpr (M == R && VM < 63) wait_vm<VM>();
}
template <bool DM, int NT, int M> DI void mem_post(const Src& src, unsigned soff, unsigned ustep) {
  if constexpr (DM && M >= 6 && M <= 20 && !(M & 1)) dma_p<NT, ((M - 6) / 2) & 3>(src.v[((M - 6) / 2) & 3], src.srd, soff + (M >= 14 ? ustep : 0u));
}
template <int WR, int M> DI void mem_wr(const Stash& st) {
  if constexpr (WR >= 40 && M >= 7 && M < 13) stash_write1<WR + M - 7>(st);
  if constexpr (WR >= 0 && WR < 40 && M >= 8 && M < 16)
    asm volatile("ds_write_b128 %0, %1 offset:%2" :: "v"(st.base[0]), "a"(st.p[WR + M - 8]), "i"((M - 8) * 1024) : "memory");
}
struct PA { unsigned rd0, rd1, soff, fill; };
template <bool QK, bool DM, int NT, int VM, int WR, int M, class F>
DI void gapA(AC& c, ST& S, ST& Sn, PA& pa, F& addr, const Src& src, unsigned ustep, const Stash& st, bf16x4& lo) {
  constexpr int R = QK ? 16 : 0;
  if constexpr ((DM || !QK) && M == 0) addr(pa);
  if constexpr (QK) mem_pre<DM, VM, 16, M>(pa.fill); else if constexpr (M == 0 && VM < 63) wait_vm<VM>();
  if constexpr (QK) {
    constexpr int f = M >> 2, s = f >> 1, kb = f & 1, qb = M & 3;
    if constexpr (s == 0) mm_vca(Sn[kb][qb], c.kf[f], c.q[qb][0], c.negm[qb]); else mm_vva<false>(Sn[kb][qb], c.kf[f], c.q[qb][s]);
  }
  mem_post<DM, NT, M>(src, pa.soff, ustep);
  mem_wr<WR, M>(st);
  if constexpr (QK && !DM && M == 10) addr(pa);
  if constexpr (QK && M == 12) asm volatile("" :: "v"(c.negm[0]), "v"(c.negm[1]), "v"(c.negm[2]), "v"(c.negm[3]));
  if constexpr (M >= R && M < R + 16) {
    constexpr int db = (M - R) >> 1; const unsigned a = ((db & 1) ? pa.rd1 : pa.rd0) + (db >> 1) * 256u;
    if constexpr (M & 1) c.vf[db] = cat(lo, lds_tr1(a + 4096u)); else lo = lds_tr1(a);
  }
  if constexpr (NEB + M < 32) sm_exp1<NEB + M>(S);
  if constexpr (M >= 16 && (M & 1)) sm_cvt<2 + ((M - 16) >> 3), ((M - 16) >> 1) & 3>(S, c);
  GAP();
}
template <bool QK, bool DM, int NT, int VM, int WR, class F, int... Ms>
DI void phaseA_(AC& c, ST& S, ST& Sn, F& addr, const Src& src, unsigned ustep, const Stash& st, iseq<Ms...>) {
  PA pa; bf16x4 lo;
  (gapA<QK, DM, NT, VM, WR, Ms>(c, S, Sn, pa, addr, src, ustep, st, lo), ...);
}
template <bool QK, bool DM, int NT, int VM, int WR = -1, class F>
DI void phaseA(AC& c, ST& S, ST& Sn, F addr, const Src& src, unsigned ustep, const Stash& st) {
  phaseA_<QK, DM, NT, VM, WR>(c, S, Sn, addr, src, ustep, st, make_iseq<32>{});
}
template <bool MAX> DI float xlane(float v) {
  auto a = __builtin_amdgcn_permlane32_swap(__float_as_uint(v), __float_as_uint(v), false, false);
  v = MAX ? fmaxf(__uint_as_float(a[0]), __uint_as_float(a[1])) : __uint_as_float(a[0]) + __uint_as_float(a[1]);
  auto b = __builtin_amdgcn_permlane16_swap(__float_as_uint(v), __float_as_uint(v), false, false);
  return MAX ? fmaxf(__uint_as_float(b[0]), __uint_as_float(b[1])) : __uint_as_float(b[0]) + __uint_as_float(b[1]);
}
DI float row_max(const f32x4& x, const f32x4& y) {
  return xlane<true>(fmaxf(fmaxf(fmaxf(x[0], x[1]), fmaxf(x[2], x[3])), fmaxf(fmaxf(y[0], y[1]), fmaxf(y[2], y[3]))));
}
template <bool SM, bool KR, bool DM, int NT, int VM, int WR, int M, class F>
DI void gapB(AC& c, ST& Sn, PA& pa, F& addr, const Src& src, unsigned ustep, const Stash& st) {
  mem_pre<DM, (KR ? VM : 63), 18, M>(pa.fill);
  {
    constexpr int h = M / 18, r = M % 18, qb = 2 * h + (r & 1);
    if constexpr (r < 2) mm_vvv<false>(c.l[qb], c.ones, c.pf[qb]); else mm_avv(c.o[(r - 2) >> 1][qb], c.vf[(r - 2) >> 1], c.pf[qb]);
  }
  mem_post<DM, NT, M>(src, pa.soff, ustep);
  mem_wr<WR, M>(st);
  if constexpr (M == 0) addr(pa);
  if constexpr (KR && M >= 18 && M < 34 && !(M & 1)) { constexpr int f = (M - 18) >> 1; c.kf[f] = lds128(pa.rd0 + (f & 1) * 4096u + (f >> 1) * 256u); }
  if constexpr (SM) {
    if constexpr (M >= 4 && M < 18) sm_exp1<M - 4>(Sn);
    if constexpr (M >= 19 && M < 35 && (M & 1)) sm_exp1<14 + (M - 19) / 2>(Sn);
    if constexpr (M >= 20 && !(M & 1)) sm_cvt<((M - 20) >> 3), ((M - 20) >> 1) & 3>(Sn, c);
  }
  GAP();
}
template <bool SM, bool KR, bool DM, int NT, int VM, int WR, class F, int... Ms>
DI void phaseB_(AC& c, ST& Sn, F& addr, const Src& src, unsigned ustep, const Stash& st, iseq<Ms...>) {
  PA pa;
  (gapB<SM, KR, DM, NT, VM, WR, Ms>(c, Sn, pa, addr, src, ustep, st), ...);
}
template <bool SM, bool KR, bool DM, int NT, int VM, int WR = -1, class F>
DI void phaseB(AC& c, ST& Sn, F addr, const Src& src, unsigned ustep, const Stash& st) {
  phaseB_<SM, KR, DM, NT, VM, WR>(c, Sn, addr, src, ustep, st, make_iseq<36>{});
}
DI void k_read(AC& ac, unsigned ka) {
#pragma unroll
  for (int f = 0; f < 8; ++f) ac.kf[f] = lds128(ka + (f & 1) * 4096u + (f >> 1) * 256u);
}
template <bool DM, int M> DI void qk0_gap(AC& ac, ST& SA, const Src& src, unsigned soff, unsigned fill) {
  constexpr int f = M >> 2, qb = M & 3;
  if constexpr (DM && (M == 0 || M == 16)) set_m0(fill + M * 256u);
  mm_vva<(f < 2)>(SA[f & 1][qb], ac.kf[f], ac.q[qb][f >> 1]);
  if constexpr (DM && qb == 0) dma_p<KV_NT, f & 3>(src.v[f & 3], src.srd, soff + (f >> 2) * 4096u);
  GAP();
}
template <bool DM, int... Ms> DI void qk0(AC& ac, ST& SA, const Src& src, unsigned soff, unsigned fill, iseq<Ms...>) { (qk0_gap<DM, Ms>(ac, SA, src, soff, fill), ...); }
template <int... Js> DI void exp_first(ST& SA, iseq<Js...>) { (sm_exp1<Js>(SA), ...); }
template <int... Js> DI void cvt_first(ST& SA, AC& c, iseq<Js...>) { (sm_cvt<(Js >> 2), (Js & 3)>(SA, c), ...); }
DI void sm_first(AC& ac, ST& SA) {
  asm volatile("s_nop 7\n\ts_nop 3" : "+v"(SA[0][0]), "+v"(SA[0][1]), "+v"(SA[0][2]), "+v"(SA[0][3]), "+v"(SA[1][0]), "+v"(SA[1][1]), "+v"(SA[1][2]), "+v"(SA[1][3]) :: "memory");
  GAP();
#pragma unroll
  for (int q = 0; q < 4; ++q) {
    const float m = row_max(SA[0][q], SA[1][q]);
    SA[0][q] -= m; SA[1][q] -= m;
    ac.negm[q] = f32x4{-m, -m, -m, -m};
    ac.l[q] = f32x4{};
    ac.pf[q] = bf16x8{};
  }
  ac.ones = __builtin_bit_cast(bf16x8, u32x4{0x3f803f80u, 0x3f803f80u, 0x3f803f80u, 0x3f803f80u});
  asm volatile("" : "+v"(ac.negm[0]), "+v"(ac.negm[1]), "+v"(ac.negm[2]), "+v"(ac.negm[3]), "+v"(ac.l[0]), "+v"(ac.l[1]), "+v"(ac.l[2]), "+v"(ac.l[3]), "+v"(ac.ones));
  exp_first(SA, make_iseq<NEB>{});
  cvt_first(SA, ac, make_iseq<8>{});
  GAP();
}
struct BS { unsigned p[4], v[6], k[2], rk, wo, l16; };
constexpr bool b_dma(int f) { return f != 2 * (SK - 3) && f != 2 * (SV - 2) + 1 && f != 2 * (SP - 2) + 1 && f < 30; }
constexpr int b_vm(int f) {
  const int s = f == 30 ? 26 : f - 3;
  if (!b_dma(s)) return 63;
  int n = b_dma(f) ? 5 + (f & 1) : 0;
  for (int i = s + 1; i < f; ++i) n += b_dma(i) ? 8 : 0;
  return n;
}
template <class KO, int I> DI void b_step(AC& ac, ST& S, ST& Sn, BS& b, const Src& sK, const Src& sV, const Src& sO, Stash& st) {
  constexpr int fa = 2 * I, fb = 2 * I + 1, vi = (I == SP || I == 15) ? 4 : (I & 1) ? 0 : 2;
  const unsigned pa = b.p[(I & 1) ? 3 : 1], pb = b.p[(I & 1) ? 0 : 2];
  if constexpr (I == SK - 3) st.base[0] = pa + b.l16;
  phaseA<(I < 15), b_dma(fa), (I < 14 ? KV_NT : W_NT), b_vm(fa), (I == SK - 3 ? 0 : -1)>(ac, S, Sn, [&](PA& p) __attribute__((always_inline)) {
    PINS(b.rk);
    p.soff = I < 14 ? ksoff(I < 13 ? I + 3 : 15, b.rk) : b.wo + KO::CS; p.fill = pa;
    p.rd0 = b.v[vi] + (I == SP ? SLOT : 0u); p.rd1 = b.v[vi + 1] + (I == SP ? SLOT : 0u); }, I < 13 ? sK : I < 14 ? sV : sO, I < 14 ? 4096u : KO::US, st);
  if constexpr (I == SV - 2) st.base[0] = pb + b.l16;
  if constexpr (I < 15)
    phaseB<true, (I < 14), b_dma(fb), (I < 13 ? KV_NT : W_NT), b_vm(fb), (I == SV - 2 ? 8 : -1)>(ac, Sn, [&](PA& p) __attribute__((always_inline)) {
      PINS(b.rk);
      p.soff = I < 13 ? ksoff(I + 2, b.rk) : b.wo + (I - 13) * 2u * KO::CS; p.fill = pb; p.rd0 = b.k[(I & 1) ? 0 : 1]; }, I < 13 ? sV : sO, I < 13 ? 4096u : KO::US, st);
}
template <int CH, int Q, int J> DI void o_word(const AC& c, const float (&inv)[4], bf16x8 (&nb)[4]) {
  u32x4 w = __builtin_bit_cast(u32x4, nb[Q]);
  unsigned r; float t;
  asm volatile("v_accvgpr_read_b32 %0, %2\n\tv_accvgpr_read_b32 %1, %3\n\tv_mul_f32_e32 %0, %0, %4\n\tv_mul_f32_e32 %1, %1, %4\n\tv_cvt_pk_bf16_f32 %0, %0, %1"
               : "=&v"(r), "=&v"(t) : "a"(c.o[2 * CH + (J >> 1)][Q][2 * (J & 1)]), "a"(c.o[2 * CH + (J >> 1)][Q][2 * (J & 1) + 1]), "v"(inv[Q]));
  w[J] = r;
  nb[Q] = __builtin_bit_cast(bf16x8, w);
}
template <int CH, int Q> DI void o_put(const bf16x8 (&nb)[4], unsigned ow) { asm volatile("ds_write_b128 %0, %1 offset:%2" :: "v"(ow), "v"(nb[Q]), "i"(CH * 4096 + Q * 1024) : "memory"); }
template <int E> struct CvX {
  static constexpr bool ACT = E == 3;
  const AC& c; const float (&inv)[4]; const bf16x8 (&cur)[4]; bf16x8 (&nb)[4]; unsigned ow;
  template <int G> DI void run() const {
    if constexpr (E < 3 && (G & 1)) o_word<E + 1, (G >> 3), (G >> 1) & 3>(c, inv, nb);
    if constexpr (E < 3 && ((G & 15) == 8 || (G & 15) == 10)) o_put<E, 2 * (G >> 4) + ((G >> 1) & 1)>(cur, ow);
    if constexpr (E == 2 && (G & 15) == 12) o_put<3, (G >> 4)>(nb, ow);
    if constexpr (E == 3 && (G == 8 || G == 10)) o_put<3, 2 + ((G >> 1) & 1)>(cur, ow);
  }
};
template <class K, int VM, int M> DI void gapL(AC& c, float (&inv)[4], bf16x8 (&nb)[4], bf16x8 (&a0)[4], bf16x4& lo, const PC& pc) {
  if constexpr (M == 6) wait_vm_lgkm<VM, 0>();
  if constexpr (M < 4) mm_vvv<false>(c.l[M], c.ones, c.pf[M]); else mm_avv(c.o[(M - 4) >> 2][M & 3], c.vf[(M - 4) >> 2], c.pf[M & 3]);
  if constexpr (M >= 6 && M < 14) {
    constexpr int d = (M - 6) >> 1; const unsigned a = ring_rd<0>(pc, d);
    if constexpr (M & 1) a0[d] = cat(lo, lds_tr1(a + K::TS)); else lo = lds_tr1(a);
  }
  if constexpr (M >= 4 && M < 12 && !(M & 1)) asm volatile("v_rcp_f32_e32 %0, %1" : "=v"(inv[(M - 4) >> 1]) : "v"(c.l[(M - 4) >> 1][0]));
  if constexpr (M >= 16 && M < 32) o_word<0, ((M - 16) >> 2), (M & 3)>(c, inv, nb);
  GAP();
}
template <class K, int VM, int... Ms> DI void phaseL(AC& c, float (&inv)[4], bf16x8 (&nb)[4], bf16x8 (&a0)[4], const PC& pc, iseq<Ms...>) {
  bf16x4 lo;
#pragma unroll
  for (int q = 0; q < 4; ++q) nb[q] = bf16x8{};
  (gapL<K, VM, Ms>(c, inv, nb, a0, lo, pc), ...);
}
struct Pub { u32x4 pk[16]; srd_t srd; unsigned v[2], so; };
template <int F, int H> DI void pub_half(const f32x4 (&acc)[8][4], Pub& p) {
  const f32x4& a = acc[4 * (F >> 3) + 2 * (F & 1) + H][(F >> 1) & 3];
  unsigned r0, r1;
  asm volatile("v_cvt_pk_bf16_f32 %0, %2, %3\n\tv_cvt_pk_bf16_f32 %1, %4, %5" : "=&v"(r0), "=v"(r1) : "v"(a[0]), "v"(a[1]), "v"(a[2]), "v"(a[3]));
  p.pk[F][2 * H] = r0; p.pk[F][2 * H + 1] = r1;
  if constexpr (H) asm volatile("buffer_store_dwordx4 %0, %1, %2, %3 offen offset:%4 " PUBPOL :: "v"(p.pk[F]), "v"(p.v[(F >> 2) & 1]), "s"(p.srd), "s"(p.so + (F >> 3) * 8192u), "i"((F & 3) * 1024) : "memory");
}
struct PubX {
  static constexpr bool ACT = false;
  const f32x4 (&acc)[8][4]; Pub& p;
  template <int G> DI void run() const { if constexpr (G >= 16) pub_half<((G - 16) >> 1), (G & 1)>(acc, p); }
};
template <int I> DI void own_put1(const u32x4& v, unsigned a) { asm volatile("ds_write_b128 %0, %1 offset:%2 ; %3" :: "v"(a), "v"(v), "i"((I & 7) * 1024), "i"(I) : "memory"); }
template <int B, int... Is> DI void own_put(const Pub& p, unsigned a, iseq<Is...>) { (own_put1<B + Is>(p.pk[B + Is], a), ...); }
template <int... Is> DI void pub_rest(const f32x4 (&acc)[8][4], Pub& p, iseq<Is...>) { (pub_half<8 + (Is >> 1), (Is & 1)>(acc, p), ...); }
template <int... Is> DI void pub_slow(const f32x4 (&acc)[8][4], Pub& p, iseq<Is...>) { ((pub_half<Is, 0>(acc, p), pub_half<Is, 1>(acc, p), __builtin_amdgcn_s_sleep(PUB_SLEEP)), ...); }
DI void redo_head(const AC& ac, const char* __restrict__ Kp, const char* __restrict__ Vp, unsigned lane, unsigned ow) {
  const unsigned r16 = lane & 15u, g = lane >> 4;
#pragma unroll 1
  for (unsigned qb = 0; qb < 4; ++qb) {
    bf16x8 qq[4];
#pragma unroll
    for (int s = 0; s < 4; ++s) qq[s] = qb == 0 ? ac.q[0][s] : qb == 1 ? ac.q[1][s] : qb == 2 ? ac.q[2][s] : ac.q[3][s];
    f32x4 o[8];
#pragma unroll
    for (int d = 0; d < 8; ++d) o[d] = f32x4{};
    float m = -__builtin_inff(), l = 0.f;
#pragma unroll 1
    for (unsigned t = 0; t < 16; ++t) {
      f32x4 s0 = {}, s1 = {};
      const char* kp = Kp + (t * 32u + r16) * 256u + g * 16u;
#pragma unroll
      for (int s = 0; s < 4; ++s) {
        s0 = __builtin_amdgcn_mfma_f32_16x16x32_bf16(*(const bf16x8*)(kp + s * 64), qq[s], s0, 0, 0, 0);
        s1 = __builtin_amdgcn_mfma_f32_16x16x32_bf16(*(const bf16x8*)(kp + s * 64 + 4096), qq[s], s1, 0, 0, 0);
      }
      const float mn = fmaxf(m, row_max(s0, s1)), al = __builtin_amdgcn_exp2f(m - mn);
      m = mn;
#pragma unroll
      for (int j = 0; j < 4; ++j) { s0[j] = __builtin_amdgcn_exp2f(s0[j] - mn); s1[j] = __builtin_amdgcn_exp2f(s1[j] - mn); }
      l = l * al + ((s0[0] + s0[1]) + (s0[2] + s0[3])) + ((s1[0] + s1[1]) + (s1[2] + s1[3]));
      const bf16x8 pf = __builtin_bit_cast(bf16x8, pack2(s0, s1));
      const __bf16* vp = (const __bf16*)(Vp + (t * 32u + g * 4u) * 256u) + r16;
#pragma unroll
      for (int d = 0; d < 8; ++d) {
        bf16x8 vf;
#pragma unroll
        for (int j = 0; j < 8; ++j) vf[j] = vp[((j & 3) + (j >> 2) * 16) * 128 + d * 16];
        o[d] = __builtin_amdgcn_mfma_f32_16x16x32_bf16(vf, pf, o[d] * al, 0, 0, 0);
      }
    }
    const float inv = 1.0f / xlane<false>(l);
#pragma unroll
    for (int s = 0; s < 4; ++s) *(LAS u32x4*)(size_t)(ow + s * 4096u + qb * 1024u) = pack2(o[2 * s] * inv, o[2 * s + 1] * inv);
  }
}
template <bool isA, int J> DI void q_conv1(AC& ac, f32x4 (&acc)[8][4], Stash& st, const Src& sK, const Src& sV, unsigned rk, unsigned p3) {
  constexpr int s = J >> 2, qb = J & 3;
  if constexpr (isA) { stash_write1<(5 * J) / 2>(st); stash_write1<(5 * J) / 2 + 1>(st); if constexpr (J & 1) stash_write1<(5 * J) / 2 + 2>(st); }
  else {
    constexpr int p = 2 * (J & 1);
    const Src& src = s < 2 ? sK : sV;
    const unsigned so = ksoff(s == 0 ? 2 : s == 1 ? SK : s == 2 ? SV : SP, rk) + (J & 2) * 2048u;
    if constexpr (s == 0 || s == 3) {
      if constexpr (p == 0) asm volatile("s_mov_b32 m0, %0\n\ts_nop 0" :: "s"(p3 + (s == 3 ? SLOT : 0u) + (J & 2) * 2048u) : "memory");
      dma_p<KV_NT, p>(src.v[p], src.srd, so); dma_p<KV_NT, p + 1>(src.v[p + 1], src.srd, so);
    } else {
      asm volatile("buffer_load_dwordx4 %0, %1, %2, %3 offen offset:%4 " KVPOL : "=a"(st.p[2 * J - 8]) : "v"(src.v[p]), "s"(src.srd), "s"(so), "i"(1024 * p) : "memory");
      asm volatile("buffer_load_dwordx4 %0, %1, %2, %3 offen offset:%4 " KVPOL : "=a"(st.p[2 * J - 7]) : "v"(src.v[p + 1]), "s"(src.srd), "s"(so), "i"(1024 * p + 1024) : "memory");
    }
  }
  ac.q[qb][s] = __builtin_bit_cast(bf16x8, natural8(pack2(acc[2 * s][qb] * SC, acc[2 * s + 1][qb] * SC)));
  asm volatile("" : "+a"(ac.q[qb][s]));
  GAP();
}
template <bool isA, int... Js> DI void q_conv(AC& ac, f32x4 (&acc)[8][4], Stash& st, const Src& sK, const Src& sV, unsigned rk, unsigned p3, iseq<Js...>) { (q_conv1<isA, Js>(ac, acc, st, sK, sV, rk, p3), ...); }
DI void zero_o(AC& ac) {
#pragma unroll
  for (int i = 0; i < 8; ++i)
#pragma unroll
    for (int q = 0; q < 4; ++q) { ac.o[i][q] = f32x4{}; asm volatile("" : "+a"(ac.o[i][q])); }
}

DI unsigned vbid() {
  const unsigned i = blockIdx.x;
  return i;
}
template <bool isA>
DI void body(const char* __restrict__ X, const char* __restrict__ K, const char* __restrict__ V, const char* __restrict__ Wq, const char* __restrict__ Wo,
             const char* __restrict__ Wo2, srd_t code, char* __restrict__ part, char* __restrict__ out) {
  extern __shared__ __attribute__((aligned(16))) char lds[];
  STAMP(0);
  unsigned tid = threadIdx.x;
  PINV(tid);
  const unsigned lane = tid & 63u, w = rfl(tid >> 6);
  const unsigned bid = vbid(), xg = bid & 7u, rr = bid >> 3, h = xg * 4u + w;
  const unsigned b = rr & 15u;
  const unsigned r16 = lane & 15u, g = lane >> 4;
  const char* xp  = X + (size_t)b * 65536u + w * 16384u;
  const char* wqp = Wq + h * 256u;
  const char* wop = Wo + (size_t)xg * 524288u + w * 256u;
  const size_t kvo = isA ? (size_t)(b * 8u + xg) * 131072u : (size_t)(b * 32u + h) * 131072u;
  const unsigned rk = isA ? b : b + 4u * w;
  typedef PK<512u, 4096u, 0x1300u, 0xF300u, 0xC300u, 128u, 32u * 8192u, true> KQ;
  typedef PK<512u, 4096u, 0x1300u, 0xF300u, 0xC300u, 4096u, 8192u, false> KT;
  typedef PK<2048u, 1024u, 0x1000u, 0xF000u, 0xF000u, 128u, 32u * 1024u, false> KO;
  asm volatile("" :: "v"(lds));
  PC pc;
  pc.r01 = 65536u + w * 16384u; pc.r2 = 131072u + w * 8192u;
  const unsigned zq = ((b & 3u) << 14) | ((b >> 2) << 8), co = 4u * w;
  const srd_t srdH = mk_srd(part + HOFF, 4096u + 256u * 128u);
  const unsigned tok_off = rr * 128u, flg_off = 4096u + rr * 1024u;
  const unsigned l8 = (lane & 7u) * 4u;
  unsigned entry_flags;
  asm volatile("buffer_load_dword %0, %1, %2, %3 offen sc1" : "=v"(entry_flags) : "v"(l8), "s"(srdH), "s"(flg_off + xg * 128u) : "memory");
  if constexpr (WARM_CODE && isA) { if (w == 3 && lane < WARM_CODE) asm volatile("buffer_load_dword %0, %1, %2, 0 offen" : "+v"(entry_flags) : "v"((b * WARM_CODE + lane) * 128u), "s"(code) : "memory"); }
  Src sQ, sX, sK, sV, sO;
  mk_src_w<false>(sQ, wqp, 511u * 8192u + 256u, 8192u, lane);
  mk_src_rows<false>(sX, xp, 16384u, 1024u, lane);
  const unsigned p0 = b & 3u, p1 = (p0 + 1u) & 3u, p2 = (p0 + 2u) & 3u, p3 = (p0 + 3u) & 3u, xw = w * 4096u;
  Stash st;
  unsigned dum0 = lane;
  if constexpr (isA && ORD == 0) {
    mk_src_rows<false>(sK, K + kvo, 131072u, 256u, lane);
    mk_src_rows<true>(sV, V + kvo, 131072u, 256u, lane);
    const unsigned to = (lane >> 1) * 1024u + (lane & 1u) * 128u + b * KO::CS;
    const char* wb = Wo2 + (size_t)xg * 524288u + w * 256u;
    if constexpr (EARLY_O == 1) asm volatile("global_load_dword %0, %1, %2\n\tglobal_load_dword %0, %1, %3" : "=&v"(dum0) : "v"(to), "s"(wop), "s"(wb) : "memory");
    stash_load(st, (w & 1u) ? sK.v[2] : sK.v[0], sK, sV, rk, w * 2048u, make_iseq<64>{});
    if constexpr (EARLY_O == 2) asm volatile("global_load_dword %0, %1, %2\n\tglobal_load_dword %0, %1, %3" : "=&v"(dum0) : "v"(to), "s"(wop), "s"(wb) : "memory");
  }
  dma_elem<TOUCH_Q>(sQ, q_soff(zq), KQ::US, pc.r2);
  if constexpr (!(isA && ORD == 2)) dma_unit<X_NT>(sX, p0 * 256u, xw + p0 * 16384u);
  dma_elem<W_NT>(sQ, q_soff(zq + 0x1300u), KQ::US, pc.r01);
  if constexpr (!(isA && ORD == 0)) {
    mk_src_rows<false>(sK, K + kvo, 131072u, 256u, lane);
    mk_src_rows<true>(sV, V + kvo, 131072u, 256u, lane);
  }
  mk_src_w<true>(sO, wop, 511u * 1024u + 256u, 1024u, lane);
  if constexpr (isA) {
    if constexpr (ORD == 2) {
      if constexpr (DELAY_A > 0) __builtin_amdgcn_s_sleep(DELAY_A);
      stash_load(st, (w & 1u) ? sK.v[2] : sK.v[0], sK, sV, rk, w * 2048u, make_iseq<64>{});
      dma_unit<X_NT>(sX, p0 * 256u, xw + p0 * 16384u);
    }
    dma_elem<W_NT>(sQ, q_soff(zq + 0x2200u), KQ::US, pc.r01 + SLOT);
    dma_unit<X_NT>(sX, p1 * 256u, xw + p1 * 16384u);
    dma_unit<X_NT>(sX, p2 * 256u, xw + p2 * 16384u);
    dma_unit<X_NT>(sX, p3 * 256u, xw + p3 * 16384u);
    if constexpr (ORD == 1) stash_load(st, (w & 1u) ? sK.v[2] : sK.v[0], sK, sV, rk, w * 2048u, make_iseq<64>{});
  }
  const unsigned rb = 1024u * (r16 >> 2) + 64u * (r16 & 3u) + 16u * (g ^ ((r16 >> 2) & 2u));
  const unsigned tb = 64u * ((lane >> 2) & 3u) + 32u * (g & 1u) + 8u * (lane & 3u);
  const unsigned tbV = tb + 1024u * g;
  const unsigned sb0 = w * 2048u + lane * 16u;
  st.base[0] = sb0; st.base[1] = sb0 + 65536u; st.base[2] = sb0 + 131072u;
  AC ac;
  if constexpr (!isA) zero_o(ac);
  {
    f32x4 acc[8][4];
    bf16x8 a0[4], a1[4], bf[4];
    asm volatile("s_waitcnt vmcnt(%2)" : "+v"(entry_flags), "+v"(dum0) : "i"(isA ? (ORD == 1 ? 63 : ORD == 0 ? 28 : 20) : 8) : "memory");
    if (w == 0) {
      const unsigned long long mk = __builtin_amdgcn_ballot_w64(((entry_flags >> (lane & 7u)) & 1u) != 0u);
      const unsigned tok = 0x5a5a5a00u | (~(unsigned)mk & 0xffu);
      if (lane == 0) asm volatile("buffer_store_dword %0, off, %1, %2 sc1" :: "v"(tok), "s"(srdH), "s"(tok_off + xg * 4u) : "memory");
    }
    barrier();
    STAMP(1);
    proj_start<KQ, 2>(a0, bf, pc, tbV, rb, zq, q_soff(zq + 0x3100u));
    if constexpr (isA) {
      constexpr int V1 = ORD == 0 ? 28 : 63, V2 = ORD == 1 ? 63 : 28;
      proj_elem<KQ, 2, true,  false, true, W_NT, 63, V1>(acc, a0, a1, bf, pc, sQ);
      STAMP(10);
      proj_elem<KQ, 0, false, false, true, W_NT, V1, V2>(acc, a0, a1, bf, pc, sQ);
      STAMP(11);
      proj_elem<KQ, 1, false, false, true, W_NT, V2, 16>(acc, a0, a1, bf, pc, sQ);
      proj_elem<KQ, 2, false, false, true, W_NT, 16, 16, ORD != 1>(acc, a0, a1, bf, pc, sQ);
    } else {
      dma_elem<W_NT>(sQ, q_soff(zq + 0x2200u), KQ::US, pc.r01 + SLOT);
      proj_elem<KQ, 2, true,  false, true, W_NT, 16, 20, false, true, true>(acc, a0, a1, bf, pc, sQ, NoX{}, &sX, p1 * 256u, xw + p1 * 16384u, p2 * 256u, xw + p2 * 16384u);
      STAMP(10);
      proj_elem<KQ, 0, false, false, true, W_NT, 24, 28, false, true, false>(acc, a0, a1, bf, pc, sQ, NoX{}, &sX, p3 * 256u, xw + p3 * 16384u);
      STAMP(11);
      proj_elem<KQ, 1, false, false, true, W_NT, 28, 24>(acc, a0, a1, bf, pc, sQ);
      proj_elem<KQ, 2, false, false, true, W_NT, 20, 16, true>(acc, a0, a1, bf, pc, sQ);
    }
    STAMP(8);
#pragma unroll 1
    for (unsigned e = 0; e < 3; ++e) {
      if constexpr (isA && PACE_A > 0) __builtin_amdgcn_s_sleep(PACE_A);
      proj_elem<KQ, 0, false, false, true, W_NT, 16, 16>(acc, a0, a1, bf, pc, sQ);
      if constexpr (isA && PACE_A > 0) __builtin_amdgcn_s_sleep(PACE_A);
      proj_elem<KQ, 1, false, false, true, W_NT, 16, 16>(acc, a0, a1, bf, pc, sQ);
      if constexpr (isA && PACE_A > 0) __builtin_amdgcn_s_sleep(PACE_A);
      proj_elem<KQ, 2, false, false, true, W_NT, 16, 16>(acc, a0, a1, bf, pc, sQ);
    }
    STAMP(9);
    if constexpr (isA) {
      proj_elem<KQ, 0, false, false, false, W_NT, 16, 12>(acc, a0, a1, bf, pc, sQ);
      proj_elem<KQ, 1, false, false, false, W_NT, 8, 4>(acc, a0, a1, bf, pc, sQ);
      proj_elem<KQ, 2, false, true, false, W_NT, 0, 0>(acc, a0, a1, bf, pc, sQ);
    } else {
      pc.soff = ksoff(0u, rk);
      proj_elem<KT, 0, false, false, true, KV_NT, 16, 16>(acc, a0, a1, bf, pc, sK);
      proj_elem<KT, 1, false, false, true, KV_NT, 16, 16>(acc, a0, a1, bf, pc, sK);
      pc.soff = ksoff(0u, rk);
      proj_elem<KT, 2, false, true, true, KV_NT, 16, 16>(acc, a0, a1, bf, pc, sV);
    }
    barrier();
    STAMP(2);
    acc_fence_v(acc);
    q_conv<isA>(ac, acc, st, sK, sV, rk, w * 16384u, make_iseq<16>{});
  }
  ST SA, SB;
  if constexpr (isA) {
    zero_o(ac);
    const unsigned kb = rb, vb = SLOT + tbV;
    unsigned dum = lane, to = TOUCH_B == 4 ? (lane >> 4) * 1024u + (lane & 8u) * 16u + (lane & 7u) * 4u : (lane >> 3) * 1024u + (lane & 4u) * 32u + (lane & 3u) * 4u;
    PINV(to);
    const char* ta = wop + b * KO::CS;
    const char* tb2 = Wo2 + (size_t)xg * 524288u + w * 256u + b * KO::CS;
    if constexpr (TOUCH_B == 4) asm volatile("global_load_dword %0, %1, %2 nt\n\tglobal_load_dword %0, %1, %2\n\tv_add_u32_e32 %1, 0x1000, %1" : "+v"(dum), "+v"(to) : "s"(ta) : "memory");
    asm volatile("s_waitcnt lgkmcnt(0)" ::: "memory");
    barrier();
    k_read(ac, kb + kv_pair(0) * 16384u);
    wait_lgkm0();
    GAP();
    qk0<false>(ac, SA, sK, 0u, 0u, make_iseq<32>{});
    k_read(ac, kb + kv_pair(1) * 16384u);
    if constexpr (TOUCH_B == 4) asm volatile("global_load_dword %0, %1, %2 nt\n\tglobal_load_dword %0, %1, %2\n\tv_add_u32_e32 %1, 0x1000, %1" : "+v"(dum), "+v"(to) : "s"(ta) : "memory");
    sm_first(ac, SA);
    if constexpr (STAG > 0) { for (unsigned k = 0; k < (b & 7u); ++k) __builtin_amdgcn_s_sleep(STAG); }
    STAMP(13);
    unsigned i4 = 0;
    auto av = [&](PA& p) { if constexpr (PACE_ATT > 0) __builtin_amdgcn_s_sleep(PACE_ATT);
                           PINS(i4); p.rd0 = vb + (((unsigned)(kv_tab() >> i4) & 15u) << 14); p.rd1 = p.rd0 ^ 32u; PINV(p.rd0); PINV(p.rd1); };
    auto ak = [&](PA& p) { if constexpr (PACE_ATT > 0) __builtin_amdgcn_s_sleep(PACE_ATT);
                           if constexpr (RAMP > 0) asm volatile("s_cmp_ge_u32 %0, 16\n\ts_cbranch_scc1 .Lr_%=\n\ts_sleep %1\n\ts_cmp_ge_u32 %0, 8\n\ts_cbranch_scc1 .Lr_%=\n\ts_sleep %1\n.Lr_%=:" :: "s"(i4), "i"(RAMP) : "scc");
                           PINS(i4); p.rd0 = kb + (((unsigned)((kv_tab() >> 8) >> i4) & 15u) << 14); PINV(p.rd0); i4 += 4u; };
    auto an = [&](PA&) { i4 += 4u; };
    auto at = [&](PA& p) __attribute__((always_inline)) {
      PINS(i4); p.rd0 = vb + (((unsigned)(kv_tab() >> i4) & 15u) << 14); p.rd1 = p.rd0 ^ 32u; PINV(p.rd0); PINV(p.rd1);
      if constexpr (TOUCH_O) {
        const char* tp;
        if constexpr (TOUCH_B == 4) {
          asm volatile("s_cmp_lt_u32 %1, 24\n\ts_cselect_b64 %0, %2, %3" : "=s"(tp) : "s"(i4), "s"(ta), "s"(tb2 - 32768) : "scc");
          asm volatile("global_load_dword %0, %1, %2 nt\n\tglobal_load_dword %0, %1, %2\n\tv_add_u32_e32 %1, 0x1000, %1" : "+v"(dum), "+v"(to) : "s"(tp) : "memory");
        } else {
          asm volatile("s_bitcmp0_b32 %1, 4\n\ts_cselect_b64 %0, %2, %3" : "=s"(tp) : "s"(i4), "s"(ta), "s"(tb2) : "scc");
          asm volatile("global_load_dword %0, %1, %2 nt\n\tglobal_load_dword %0, %1, %2" : "+v"(dum) : "v"(to), "s"(tp) : "memory");
          to = (to + 8192u) & 32767u; PINV(to);
        }
      }
    };
#define ITS
    while (i4 < 24u) {
      ITS
      phaseA<true, false, KV_NT, 63>(ac, SA, SB, at, sK, 0u, st);
      phaseB<true, true, false, KV_NT, 63>(ac, SB, ak, sK, 0u, st);
      phaseA<true, false, KV_NT, 63>(ac, SB, SA, at, sK, 0u, st);
      phaseB<true, true, false, KV_NT, 63>(ac, SA, ak, sK, 0u, st);
    }
    ITS
    barrier();
    phaseA<true, false, KV_NT, 63, 40>(ac, SA, SB, at, sK, 0u, st);
    phaseB<true, true, false, KV_NT, 63, 46>(ac, SB, ak, sK, 0u, st);
    phaseA<true, false, KV_NT, 63, 52>(ac, SB, SA, at, sK, 0u, st);
    phaseB<true, true, false, KV_NT, 63, 58>(ac, SA, ak, sK, 0u, st);
    wait_vm_lgkm<63, 8>(); barrier();
    while (i4 < 56u) {
      ITS
      phaseA<true, false, KV_NT, 63>(ac, SA, SB, at, sK, 0u, st);
      phaseB<true, true, false, KV_NT, 63>(ac, SB, ak, sK, 0u, st);
      phaseA<true, false, KV_NT, 63>(ac, SB, SA, at, sK, 0u, st);
      phaseB<true, true, false, KV_NT, 63>(ac, SA, ak, sK, 0u, st);
    }
    STAMP(14);
    barrier();
    unsigned v14 = vb + kv_pair(14) * 16384u, x14 = v14 ^ 32u; PINV(v14); PINV(x14);
    phaseA<true, true, W_NT, 63>(ac, SA, SB, [&](PA& p) { p.soff = co * KO::CS; p.fill = pc.r01; p.rd0 = v14; p.rd1 = x14; }, sO, KO::US, st);
    phaseB<true, false, true, W_NT, 63>(ac, SB, [&](PA& p) { p.soff = (co + 1u) * KO::CS; p.fill = pc.r01 + SLOT; }, sO, KO::US, st);
    phaseA<false, false, W_NT, 63>(ac, SB, SA, [&](PA& p) { p.rd0 = v14 + 16384u; p.rd1 = x14 + 16384u; }, sO, 0u, st);
    dum0 = dum;
  } else {
    const unsigned P0 = pc.r01, P1 = pc.r01 + SLOT, P2 = pc.r2, P3 = w * 16384u;
    unsigned v0 = tbV + P0, v2 = tbV + P2, x0 = v0 ^ 32u, x2 = v2 ^ 32u, k1 = rb + P1, k3 = rb + P3;
    PINV(v0); PINV(v2); PINV(x0); PINV(x2); PINV(k1); PINV(k3);
    wait_vm<48>();
    k_read(ac, k1 - SLOT);
    wait_lgkm0();
    GAP();
    qk0<true>(ac, SA, sV, ksoff(1u, rk), P0, make_iseq<32>{});
    wait_vm<48>();
    GAP();
    k_read(ac, k1);
    sm_first(ac, SA);
    STAMP(13);
    BS bs{{P0, P1, P2, P3}, {v0, x0, v2, x2, tbV + P3, (tbV + P3) ^ 32u}, {k1, k3}, rk, co * KO::CS, lane * 16u};
    PINV(bs.v[4]); PINV(bs.v[5]);
#define BT(k)
#define B2(I) b_step<KO, I>(ac, SA, SB, bs, sK, sV, sO, st); b_step<KO, I + 1>(ac, SB, SA, bs, sK, sV, sO, st);
    B2(0) BT(0) B2(2) BT(1) B2(4) BT(2) B2(6) BT(3) B2(8) BT(4) B2(10)
    STAMP(14);
    B2(12) B2(14)
#undef B2
  }
  {
    const unsigned l16 = lane * 16u;
    unsigned ow = w * 16384u + l16;
    PINV(ow);
    f32x4 acc[8][4];
    bf16x8 a0[4], a1[4], b0[4], bf[4];
    float inv[4];
    proj_setup<KO>(pc, tb + 512u * g, l16, (co + 3u) << 12, (co + 3u) * KO::CS);
    GAP();
    phaseL<KO, (isA ? 12 : 20)>(ac, inv, b0, a0, pc, make_iseq<36>{});
    STAMP(3);
    if constexpr (isA) {
      barrier();
      dma_elem<W_NT>(sO, (co + 2u) * KO::CS, KO::US, pc.r2);
    }
    const bool ok = ac.l[0][0] <= LMAX && ac.l[1][0] <= LMAX && ac.l[2][0] <= LMAX && ac.l[3][0] <= LMAX;
    if (__builtin_expect(__builtin_amdgcn_ballot_w64(!ok) != 0ull, 0)) {
      acc_fence_a(ac.o);
      redo_head(ac, K + kvo, V + kvo, lane, ow);
#pragma unroll
      for (int q = 0; q < 4; ++q) {
        inv[q] = 1.0f;
        b0[q] = lds128(ow + q * 1024u);
#pragma unroll
        for (int s = 1; s < 4; ++s) {
          const u32x4 t = __builtin_bit_cast(u32x4, lds128(ow + s * 4096u + q * 1024u));
          ac.o[2 * s][q] = f32x4{__uint_as_float(t[0] << 16), __uint_as_float(t[0] & 0xffff0000u), __uint_as_float(t[1] << 16), __uint_as_float(t[1] & 0xffff0000u)};
          ac.o[2 * s + 1][q] = f32x4{__uint_as_float(t[2] << 16), __uint_as_float(t[2] & 0xffff0000u), __uint_as_float(t[3] << 16), __uint_as_float(t[3] & 0xffff0000u)};
        }
      }
      wait_lgkm0();
      acc_fence_a(ac.o);
    }
#pragma unroll
    for (int q = 0; q < 4; ++q) bf[q] = bf16x8{};
    GAP();
    proj_elem<KO, 0, true, false, true, W_NT, 16, 16>(acc, a0, a1, b0, pc, sO, CvX<0>{ac, inv, b0, bf, ow});
    proj_elem<KO, 1, false, false, true, W_NT, 16, 16>(acc, a0, a1, bf, pc, sO, CvX<1>{ac, inv, bf, b0, ow});
    proj_elem<KO, 2, false, false, true, W_NT, 16, 16>(acc, a0, a1, b0, pc, sO, CvX<2>{ac, inv, b0, bf, ow});
    if constexpr (isA && TOUCH_B >= 3) PINV(dum0);
    proj_elem<KO, 0, false, false, true, W_NT, 16, 16, true>(acc, a0, a1, bf, pc, sO, CvX<3>{ac, inv, bf, b0, ow});
    STAMP(4);
#pragma unroll 1
    for (unsigned e = 0; e < 3; ++e) {
      proj_elem<KO, 1, false, false, true, W_NT, 16, 16>(acc, a0, a1, bf, pc, sO);
      proj_elem<KO, 2, false, false, true, W_NT, 16, 16>(acc, a0, a1, bf, pc, sO);
      proj_elem<KO, 0, false, false, true, W_NT, 16, 16>(acc, a0, a1, bf, pc, sO);
    }
    CYC(6);
    unsigned toks;
    asm volatile("buffer_load_dword %0, %1, %2, %3 offen sc1" : "=v"(toks) : "v"(l8), "s"(srdH), "s"(tok_off) : "memory");
    Pub pb;
    pb.srd = mk_srd(part + POFF + (size_t)rr * 524288u, 524288u);
    pb.v[0] = l16; pb.v[1] = l16 + 4096u; pb.so = xg * 65536u + w * 16384u;
    PINV(pb.v[1]);
#pragma unroll
    for (int i = 0; i < 16; ++i) pb.pk[i] = u32x4{};
    proj_elem<KO, 1, false, false, false, W_NT, 17, 13>(acc, a0, a1, bf, pc, sO);
    proj_elem<KO, 2, false, false, false, W_NT, 9, 5>(acc, a0, a1, bf, pc, sO);
    CYC(7);
    if constexpr (isA && PUB_SLEEP > 0) {
      proj_elem<KO, 0, false, true, false, W_NT, 0, 0>(acc, a0, a1, bf, pc, sO);
      STAMP(5);
      pub_slow(acc, pb, make_iseq<16>{});
    } else {
      proj_elem<KO, 0, false, true, false, W_NT, 0, 0>(acc, a0, a1, bf, pc, sO, PubX{acc, pb});
      STAMP(5);
      pub_rest(acc, pb, make_iseq<16>{});
    }
    const unsigned lr = ((lane + xg) & 7u) * 4u;
    unsigned fl;
    asm volatile("buffer_load_dword %0, %1, %2, %3 offen sc1" : "=v"(fl) : "v"(lr), "s"(srdH), "s"(flg_off + xg * 128u) : "memory");
    PINV(toks);
    STAMP(6);
    if (w == (xg >> 1)) {
      const unsigned a = pc.r01 + SLOT + l16;
      if (xg & 1u) own_put<8>(pb, a, make_iseq<8>{}); else own_put<0>(pb, a, make_iseq<8>{});
    }
    const unsigned fo = lane * 128u + xg * 4u, tau = __builtin_amdgcn_readlane(toks, xg);
    unsigned oo = ((b * 64u + 16u * w + r16) * 1024u + (isA ? 0u : 512u) + 64u * xg + 8u * g) * 2u;
    PINV(oo);
    wait_lgkm0();
    barrier();
    const unsigned oa = 65536u + SLOT + (xg >> 1) * 16384u + w * 2048u + l16;
    const u32x4 r0 = __builtin_bit_cast(u32x4, lds128(oa)), r1 = __builtin_bit_cast(u32x4, lds128(oa + 1024u));
    GAP();
    wait_vm<9>();
    if (lane == 2u * w) asm volatile("buffer_store_dword %0, %1, %2, %3 offen sc1" :: "v"(toks), "v"(fo), "s"(srdH), "s"(flg_off) : "memory");
    wait_vm<2>();
    if (lane == 2u * w + 1u) asm volatile("buffer_store_dword %0, %1, %2, %3 offen sc1" :: "v"(toks), "v"(fo), "s"(srdH), "s"(flg_off) : "memory");
    f32x16 f[8];
#pragma unroll
    for (int j = 0; j < 8; ++j) { const unsigned t = j < 4 ? r0[j & 3] : r1[j & 3]; f[0][2 * j] = __uint_as_float(t << 16); f[0][2 * j + 1] = __uint_as_float(t & 0xffff0000u); }
    PINV(f[0]);
    asm volatile("s_waitcnt vmcnt(2)" : "+v"(fl) : "v"(pb.pk[0]), "v"(pb.pk[1]), "v"(pb.pk[2]), "v"(pb.pk[3]), "v"(pb.pk[4]), "v"(pb.pk[5]), "v"(pb.pk[6]), "v"(pb.pk[7]),
                 "v"(pb.pk[8]), "v"(pb.pk[9]), "v"(pb.pk[10]), "v"(pb.pk[11]), "v"(pb.pk[12]), "v"(pb.pk[13]), "v"(pb.pk[14]), "v"(pb.pk[15]) : "memory");
    STAMP(7);
    unsigned got = 0xffffff01u, spin = 1u << 21, fresh, so, f2;
#define POLL(R) "buffer_load_dword %[" R "], %[lr], %[sh], %[flg] offen sc1\n\t"
#define SEEN(R) "v_cmp_eq_u32_e32 vcc, %[tau], %[" R "]\n\ts_andn2_b32 %[fr], vcc_lo, %[got]\n\ts_cbranch_scc1 .Lproc_%=\n\t"
    asm volatile(
        SEEN("fl") POLL("fl") "s_sleep 4\n"
        ".Lidle_%=:\n\t" POLL("f2") "s_waitcnt vmcnt(1)\n\t" SEEN("fl") "s_sleep %[slp]\n\t" POLL("fl") "s_waitcnt vmcnt(1)\n\t" SEEN("f2")
        "s_sleep %[slp]\n\ts_sub_u32 %[spin], %[spin], 1\n\ts_cbranch_scc0 .Lidle_%=\n\ts_branch .Ldone_%=\n"
        ".Lproc_%=:\n\ts_or_b32 %[got], %[got], %[fr]\n\t"
        "s_bitcmp1_b32 %[fr], 1\n\ts_cbranch_scc0 .La1_%=\n\ts_add_u32 %[so], %[ro], 65536\n\ts_bitset0_b32 %[so], 19\n\t"
        "buffer_load_dwordx4 v[152:155], %[l16], %[sp], %[so] offen sc1\n\tbuffer_load_dwordx4 v[156:159], %[l16], %[sp], %[so] offen offset:1024 sc1\n.La1_%=:\n\t"
        "s_bitcmp1_b32 %[fr], 2\n\ts_cbranch_scc0 .La2_%=\n\ts_add_u32 %[so], %[ro], 131072\n\ts_bitset0_b32 %[so], 19\n\t"
        "buffer_load_dwordx4 v[168:171], %[l16], %[sp], %[so] offen sc1\n\tbuffer_load_dwordx4 v[172:175], %[l16], %[sp], %[so] offen offset:1024 sc1\n.La2_%=:\n\t"
        "s_bitcmp1_b32 %[fr], 3\n\ts_cbranch_scc0 .La3_%=\n\ts_add_u32 %[so], %[ro], 196608\n\ts_bitset0_b32 %[so], 19\n\t"
        "buffer_load_dwordx4 v[184:187], %[l16], %[sp], %[so] offen sc1\n\tbuffer_load_dwordx4 v[188:191], %[l16], %[sp], %[so] offen offset:1024 sc1\n.La3_%=:\n\t"
        "s_bitcmp1_b32 %[fr], 4\n\ts_cbranch_scc0 .La4_%=\n\ts_add_u32 %[so], %[ro], 262144\n\ts_bitset0_b32 %[so], 19\n\t"
        "buffer_load_dwordx4 v[200:203], %[l16], %[sp], %[so] offen sc1\n\tbuffer_load_dwordx4 v[204:207], %[l16], %[sp], %[so] offen offset:1024 sc1\n.La4_%=:\n\t"
        "s_bitcmp1_b32 %[fr], 5\n\ts_cbranch_scc0 .La5_%=\n\ts_add_u32 %[so], %[ro], 327680\n\ts_bitset0_b32 %[so], 19\n\t"
        "buffer_load_dwordx4 v[216:219], %[l16], %[sp], %[so] offen sc1\n\tbuffer_load_dwordx4 v[220:223], %[l16], %[sp], %[so] offen offset:1024 sc1\n.La5_%=:\n\t"
        "s_bitcmp1_b32 %[fr], 6\n\ts_cbranch_scc0 .La6_%=\n\ts_add_u32 %[so], %[ro], 393216\n\ts_bitset0_b32 %[so], 19\n\t"
        "buffer_load_dwordx4 v[232:235], %[l16], %[sp], %[so] offen sc1\n\tbuffer_load_dwordx4 v[236:239], %[l16], %[sp], %[so] offen offset:1024 sc1\n.La6_%=:\n\t"
        "s_bitcmp1_b32 %[fr], 7\n\ts_cbranch_scc0 .La7_%=\n\ts_add_u32 %[so], %[ro], 458752\n\ts_bitset0_b32 %[so], 19\n\t"
        "buffer_load_dwordx4 v[248:251], %[l16], %[sp], %[so] offen sc1\n\tbuffer_load_dwordx4 v[252:255], %[l16], %[sp], %[so] offen offset:1024 sc1\n.La7_%=:\n\t"
        "s_waitcnt vmcnt(0)\n\ts_cmp_eq_u32 %[got], -1\n\ts_cbranch_scc1 .Ltok_%=\n\t" POLL("fl") "s_branch .Lwide_%=\n"
        ".Ltok_%=:\n\tbuffer_load_dword %[fl], %[l8], %[sh], %[tok] offen sc1\n"
        ".Lwide_%=:\n\t"
        "s_bitcmp1_b32 %[fr], 1\n\ts_cbranch_scc0 .Lb1_%=\n\t"
        "v_lshlrev_b32_e32 v144, 16, v152\n\tv_and_b32_e32 v145, %[hi], v152\n\tv_lshlrev_b32_e32 v146, 16, v153\n\tv_and_b32_e32 v147, %[hi], v153\n\t"
        "v_lshlrev_b32_e32 v148, 16, v154\n\tv_and_b32_e32 v149, %[hi], v154\n\tv_lshlrev_b32_e32 v150, 16, v155\n\tv_and_b32_e32 v151, %[hi], v155\n\t"
        "v_lshlrev_b32_e32 v152, 16, v156\n\tv_and_b32_e32 v153, %[hi], v156\n\tv_lshlrev_b32_e32 v154, 16, v157\n\tv_and_b32_e32 v155, %[hi], v157\n\t"
        "v_lshlrev_b32_e32 v156, 16, v158\n\tv_and_b32_e32 v157, %[hi], v158\n\tv_lshlrev_b32_e32 v158, 16, v159\n\tv_and_b32_e32 v159, %[hi], v159\n\t"
        ".Lb1_%=:\n\t"
        "s_bitcmp1_b32 %[fr], 2\n\ts_cbranch_scc0 .Lb2_%=\n\t"
        "v_lshlrev_b32_e32 v160, 16, v168\n\tv_and_b32_e32 v161, %[hi], v168\n\tv_lshlrev_b32_e32 v162, 16, v169\n\tv_and_b32_e32 v163, %[hi], v169\n\t"
        "v_lshlrev_b32_e32 v164, 16, v170\n\tv_and_b32_e32 v165, %[hi], v170\n\tv_lshlrev_b32_e32 v166, 16, v171\n\tv_and_b32_e32 v167, %[hi], v171\n\t"
        "v_lshlrev_b32_e32 v168, 16, v172\n\tv_and_b32_e32 v169, %[hi], v172\n\tv_lshlrev_b32_e32 v170, 16, v173\n\tv_and_b32_e32 v171, %[hi], v173\n\t"
        "v_lshlrev_b32_e32 v172, 16, v174\n\tv_and_b32_e32 v173, %[hi], v174\n\tv_lshlrev_b32_e32 v174, 16, v175\n\tv_and_b32_e32 v175, %[hi], v175\n\t"
        ".Lb2_%=:\n\t"
        "s_bitcmp1_b32 %[fr], 3\n\ts_cbranch_scc0 .Lb3_%=\n\t"
        "v_lshlrev_b32_e32 v176, 16, v184\n\tv_and_b32_e32 v177, %[hi], v184\n\tv_lshlrev_b32_e32 v178, 16, v185\n\tv_and_b32_e32 v179, %[hi], v185\n\t"
        "v_lshlrev_b32_e32 v180, 16, v186\n\tv_and_b32_e32 v181, %[hi], v186\n\tv_lshlrev_b32_e32 v182, 16, v187\n\tv_and_b32_e32 v183, %[hi], v187\n\t"
        "v_lshlrev_b32_e32 v184, 16, v188\n\tv_and_b32_e32 v185, %[hi], v188\n\tv_lshlrev_b32_e32 v186, 16, v189\n\tv_and_b32_e32 v187, %[hi], v189\n\t"
        "v_lshlrev_b32_e32 v188, 16, v190\n\tv_and_b32_e32 v189, %[hi], v190\n\tv_lshlrev_b32_e32 v190, 16, v191\n\tv_and_b32_e32 v191, %[hi], v191\n\t"
        ".Lb3_%=:\n\t"
        "s_bitcmp1_b32 %[fr], 4\n\ts_cbranch_scc0 .Lb4_%=\n\t"
        "v_lshlrev_b32_e32 v192, 16, v200\n\tv_and_b32_e32 v193, %[hi], v200\n\tv_lshlrev_b32_e32 v194, 16, v201\n\tv_and_b32_e32 v195, %[hi], v201\n\t"
        "v_lshlrev_b32_e32 v196, 16, v202\n\tv_and_b32_e32 v197, %[hi], v202\n\tv_lshlrev_b32_e32 v198, 16, v203\n\tv_and_b32_e32 v199, %[hi], v203\n\t"
        "v_lshlrev_b32_e32 v200, 16, v204\n\tv_and_b32_e32 v201, %[hi], v204\n\tv_lshlrev_b32_e32 v202, 16, v205\n\tv_and_b32_e32 v203, %[hi], v205\n\t"
        "v_lshlrev_b32_e32 v204, 16, v206\n\tv_and_b32_e32 v205, %[hi], v206\n\tv_lshlrev_b32_e32 v206, 16, v207\n\tv_and_b32_e32 v207, %[hi], v207\n\t"
        ".Lb4_%=:\n\t"
        "s_bitcmp1_b32 %[fr], 5\n\ts_cbranch_scc0 .Lb5_%=\n\t"
        "v_lshlrev_b32_e32 v208, 16, v216\n\tv_and_b32_e32 v209, %[hi], v216\n\tv_lshlrev_b32_e32 v210, 16, v217\n\tv_and_b32_e32 v211, %[hi], v217\n\t"
        "v_lshlrev_b32_e32 v212, 16, v218\n\tv_and_b32_e32 v213, %[hi], v218\n\tv_lshlrev_b32_e32 v214, 16, v219\n\tv_and_b32_e32 v215, %[hi], v219\n\t"
        "v_lshlrev_b32_e32 v216, 16, v220\n\tv_and_b32_e32 v217, %[hi], v220\n\tv_lshlrev_b32_e32 v218, 16, v221\n\tv_and_b32_e32 v219, %[hi], v221\n\t"
        "v_lshlrev_b32_e32 v220, 16, v222\n\tv_and_b32_e32 v221, %[hi], v222\n\tv_lshlrev_b32_e32 v222, 16, v223\n\tv_and_b32_e32 v223, %[hi], v223\n\t"
        ".Lb5_%=:\n\t"
        "s_bitcmp1_b32 %[fr], 6\n\ts_cbranch_scc0 .Lb6_%=\n\t"
        "v_lshlrev_b32_e32 v224, 16, v232\n\tv_and_b32_e32 v225, %[hi], v232\n\tv_lshlrev_b32_e32 v226, 16, v233\n\tv_and_b32_e32 v227, %[hi], v233\n\t"
        "v_lshlrev_b32_e32 v228, 16, v234\n\tv_and_b32_e32 v229, %[hi], v234\n\tv_lshlrev_b32_e32 v230, 16, v235\n\tv_and_b32_e32 v231, %[hi], v235\n\t"
        "v_lshlrev_b32_e32 v232, 16, v236\n\tv_and_b32_e32 v233, %[hi], v236\n\tv_lshlrev_b32_e32 v234, 16, v237\n\tv_and_b32_e32 v235, %[hi], v237\n\t"
        "v_lshlrev_b32_e32 v236, 16, v238\n\tv_and_b32_e32 v237, %[hi], v238\n\tv_lshlrev_b32_e32 v238, 16, v239\n\tv_and_b32_e32 v239, %[hi], v239\n\t"
        ".Lb6_%=:\n\t"
        "s_bitcmp1_b32 %[fr], 7\n\ts_cbranch_scc0 .Lb7_%=\n\t"
        "v_lshlrev_b32_e32 v240, 16, v248\n\tv_and_b32_e32 v241, %[hi], v248\n\tv_lshlrev_b32_e32 v242, 16, v249\n\tv_and_b32_e32 v243, %[hi], v249\n\t"
        "v_lshlrev_b32_e32 v244, 16, v250\n\tv_and_b32_e32 v245, %[hi], v250\n\tv_lshlrev_b32_e32 v246, 16, v251\n\tv_and_b32_e32 v247, %[hi], v251\n\t"
        "v_lshlrev_b32_e32 v248, 16, v252\n\tv_and_b32_e32 v249, %[hi], v252\n\tv_lshlrev_b32_e32 v250, 16, v253\n\tv_and_b32_e32 v251, %[hi], v253\n\t"
        "v_lshlrev_b32_e32 v252, 16, v254\n\tv_and_b32_e32 v253, %[hi], v254\n\tv_lshlrev_b32_e32 v254, 16, v255\n\tv_and_b32_e32 v255, %[hi], v255\n\t"
        ".Lb7_%=:\n\t"
        "s_cmp_eq_u32 %[got], -1\n\ts_cbranch_scc0 .Lidle_%=\n"
        ".Ldone_%=:"
        : "=&{v[144:159]}"(f[1]), "=&{v[160:175]}"(f[2]), "=&{v[176:191]}"(f[3]), "=&{v[192:207]}"(f[4]), "=&{v[208:223]}"(f[5]), "=&{v[224:239]}"(f[6]), "=&{v[240:255]}"(f[7]),
          [fl] "+v"(fl), [f2] "=&v"(f2), [got] "+s"(got), [spin] "+s"(spin), [fr] "=&s"(fresh), [so] "=&s"(so)
        : [tau] "s"(tau), [l16] "v"(l16), [l8] "v"(l8), [lr] "v"(lr), [sp] "s"(pb.srd), [sh] "s"(srdH), [ro] "s"(xg * 73728u + w * 2048u), [tok] "s"(tok_off),
          [flg] "s"(flg_off + xg * 128u), [hi] "s"(0xffff0000u), [slp] "i"(isA ? SLEEP_A : SLEEP_B)
        : "vcc", "scc", "memory");
#undef POLL
#undef SEEN
    STAMP(15);
    const f32x16 t = ((f[0] + f[1]) + (f[2] + f[3])) + ((f[4] + f[5]) + (f[6] + f[7]));
#pragma unroll
    for (int s = 0; s < 2; ++s)
    {
      const u32x4 ov = natural8(u32x4{cvtpk(t[8 * s], t[8 * s + 1]), cvtpk(t[8 * s + 2], t[8 * s + 3]), cvtpk(t[8 * s + 4], t[8 * s + 5]), cvtpk(t[8 * s + 6], t[8 * s + 7])});
      __builtin_amdgcn_raw_buffer_store_b128(ov, mk_srd(out, 2097152u), oo + s * 64, 0, OUTPOL);
    }
    asm volatile("s_waitcnt vmcnt(2)" : "+v"(fl) :: "memory");
    if ((lane >> 1) == w && fl != toks) asm volatile("buffer_store_dword %0, %1, %2, %3 offen sc1" :: "v"(fl), "v"(fo), "s"(srdH), "s"(flg_off) : "memory");
  }
}

__global__ __launch_bounds__(256, 1)
void fused_attn(const char* __restrict__ Ax, const char* __restrict__ Ak, const char* __restrict__ Av, const char* __restrict__ Awq, const char* __restrict__ Awo,
                const char* __restrict__ Bx, const char* __restrict__ Bk, const char* __restrict__ Bv, const char* __restrict__ Bwq, const char* __restrict__ Bwo,
                char* __restrict__ part, char* __restrict__ out) {
  const char* pc; unsigned left;
  asm volatile("s_getpc_b64 %0\n.Lpc_%=:\n\ts_mov_b32 %1, .Lhot_end-.Lpc_%=" : "=s"(pc), "=s"(left));
  const srd_t code = mk_srd(pc, left);
  if (vbid() >= 128) { body<true>(Ax, Ak, Av, Awq, Awo, Bwo, code, part, out); asm volatile(".Lhot_end:"); __builtin_amdgcn_endpgm(); }
  body<false>(Bx, Bk, Bv, Bwq, Bwo, Bwo, code, part, out);
}

extern "C" void kernel_launch(void* const* d_in, const int* in_sizes, int n_in, void* d_out, int out_size, void* d_ws, size_t ws_size, hipStream_t stream) {
  (void)in_sizes; (void)n_in; (void)out_size; (void)ws_size;
  (void)hipFuncSetAttribute((const void*)fused_attn, hipFuncAttributeMaxDynamicSharedMemorySize, (int)LDS_BYTES);
  fused_attn<<<256, 256, LDS_BYTES, stream>>>((const char*)d_in[0], (const char*)d_in[1], (const char*)d_in[2], (const char*)d_in[3], (const char*)d_in[4],
                                              (const char*)d_in[5], (const char*)d_in[6], (const char*)d_in[7], (const char*)d_in[8], (const char*)d_in[9], (char*)d_ws, (char*)d_out);
}
```

```cpp
#include <hip/hip_runtime.h>
#include <hip/hip_bf16.h>
#include <utility>

typedef __bf16 bf16x8 __attribute__((ext_vector_type(8)));
typedef __bf16 bf16x4 __attribute__((ext_vector_type(4)));
typedef __bf16 bf16x2 __attribute__((ext_vector_type(2)));
typedef float f32x4 __attribute__((ext_vector_type(4)));
typedef float f32x2 __attribute__((ext_vector_type(2)));
typedef float f32x16 __attribute__((ext_vector_type(16)));
typedef unsigned u32x4 __attribute__((ext_vector_type(4)));
#define LAS __attribute__((address_space(3)))
#define DI __device__ __forceinline__
typedef __amdgpu_buffer_rsrc_t srd_t;
template <int... Is> using iseq = std::integer_sequence<int, Is...>;
template <int N> using make_iseq = std::make_integer_sequence<int, N>;

constexpr int KV_NT = 1, W_NT = 0, X_NT = 0;
constexpr int SK = 9, SV = 11, SP = 13;
constexpr unsigned WARM = 40;

constexpr unsigned SLOT = 8192, LDS_BYTES = 20 * SLOT;
constexpr unsigned HOFF = 16u << 20;
constexpr float SC = 0.12751743082f;
constexpr float LMAX = 1048576.0f;

#define GAP() __builtin_amdgcn_sched_barrier(0)
#define PINS(x) asm volatile("" : "+s"(x))
#define PINV(x) asm volatile("" : "+v"(x))
DI unsigned rfl(unsigned x) { return __builtin_amdgcn_readfirstlane(x); }
DI unsigned cvtpk(float lo, float hi) { f32x2 v = {lo, hi}; bf16x2 b = __builtin_convertvector(v, bf16x2); return __builtin_bit_cast(unsigned, b); }
DI u32x4 pack2(const f32x4& a, const f32x4& b) { return u32x4{cvtpk(a[0], a[1]), cvtpk(a[2], a[3]), cvtpk(b[0], b[1]), cvtpk(b[2], b[3])}; }
DI u32x4 natural8(u32x4 w) {
  auto s0 = __builtin_amdgcn_permlane32_swap(w[0], w[2], false, false);
  auto s1 = __builtin_amdgcn_permlane32_swap(w[1], w[3], false, false);
  auto t0 = __builtin_amdgcn_permlane16_swap(s0[0], s0[1], false, false);
  auto t1 = __builtin_amdgcn_permlane16_swap(s1[0], s1[1], false, false);
  return u32x4{t0[0], t1[0], t0[1], t1[1]};
}
#define MF "v_mfma_f32_16x16x32_bf16 "
template <bool Z> DI void mm_vvv(f32x4& c, const bf16x8& a, const bf16x8& b) {
  if constexpr (Z) asm volatile(MF "%0, %1, %2, 0" : "=&v"(c) : "v"(a), "v"(b));
  else             asm volatile(MF "%0, %1, %2, %0" : "+v"(c) : "v"(a), "v"(b));
}
template <bool Z> DI void mm_vva(f32x4& c, const bf16x8& a, const bf16x8& b) {
  if constexpr (Z) asm volatile(MF "%0, %1, %2, 0" : "=&v"(c) : "v"(a), "a"(b));
  else             asm volatile(MF "%0, %1, %2, %0" : "+v"(c) : "v"(a), "a"(b));
}
DI void mm_vca(f32x4& d, const bf16x8& a, const bf16x8& b, const f32x4& c) { asm volatile(MF "%0, %1, %2, %3" : "=&v"(d) : "v"(a), "a"(b), "v"(c)); }
DI void mm_avv(f32x4& c, const bf16x8& a, const bf16x8& b) { asm volatile(MF "%0, %1, %2, %0" : "+a"(c) : "v"(a), "v"(b)); }
DI void set_m0(unsigned v) { asm volatile("s_mov_b32 m0, %0" :: "s"(v) : "memory"); }
template <int NT, int P> DI void dma_p(unsigned voff, srd_t srd, unsigned soff) {
  if constexpr (NT) asm volatile("buffer_load_dwordx4 %0, %1, %2 offen offset:%3 nt lds" :: "v"(voff), "s"(srd), "s"(soff), "i"(1024 * P) : "memory");
  else              asm volatile("buffer_load_dwordx4 %0, %1, %2 offen offset:%3 lds" :: "v"(voff), "s"(srd), "s"(soff), "i"(1024 * P) : "memory");
}
struct Src { unsigned v[4]; srd_t srd; };
template <int NT> DI void dma_unit(const Src& s, unsigned soff, unsigned ldsaddr) {
  asm volatile("s_mov_b32 m0, %0\n\ts_nop 0" :: "s"(ldsaddr) : "memory");
  dma_p<NT, 0>(s.v[0], s.srd, soff); dma_p<NT, 1>(s.v[1], s.srd, soff); dma_p<NT, 2>(s.v[2], s.srd, soff); dma_p<NT, 3>(s.v[3], s.srd, soff);
}
template <int NT> DI void dma_elem(const Src& s, unsigned soff, unsigned ustep, unsigned ldsaddr) { dma_unit<NT>(s, soff, ldsaddr); dma_unit<NT>(s, soff + ustep, ldsaddr + 4096u); }
template <int N> DI void wait_vm() { asm volatile("s_waitcnt vmcnt(%0)" :: "i"(N) : "memory"); }
template <int N, int L> DI void wait_vm_lgkm() { asm volatile("" ::: "memory"); __builtin_amdgcn_s_waitcnt((N & 15) | ((N >> 4) << 14) | 0x70 | (L << 8)); asm volatile("" ::: "memory"); }
DI void wait_lgkm0() { wait_vm_lgkm<63, 0>(); }
DI void barrier() { asm volatile("s_barrier" ::: "memory"); }
DI bf16x8 lds128(unsigned a) { return *(const LAS bf16x8*)(size_t)(a); }
DI bf16x4 lds_tr1(unsigned a) { return __builtin_amdgcn_ds_read_tr16_b64_v4bf16((LAS bf16x4*)(size_t)(a)); }
DI bf16x8 cat(bf16x4 lo, bf16x4 hi) { return __builtin_shufflevector(lo, hi, 0, 1, 2, 3, 4, 5, 6, 7); }
DI bf16x8 lds_tr(unsigned a0, unsigned a1) { return cat(lds_tr1(a0), lds_tr1(a1)); }
DI srd_t mk_srd(const void* base, unsigned bytes) { return __builtin_amdgcn_make_buffer_rsrc((void*)base, (short)0, (int)bytes, 0x00020000); }
template <bool VX> DI void mk_src_rows(Src& s, const char* base, unsigned bytes, unsigned stride, unsigned lane) {
  s.srd = mk_srd(base, bytes);
  const unsigned row = (lane >> 2) & 3u, cb = lane >> 4, sl = lane & 3u;
#pragma unroll
  for (int p = 0; p < 4; ++p) s.v[p] = row * stride + cb * 64u + ((sl ^ (VX ? 2 * (p & 1) : (p & 2))) << 4) + (4u * stride - 1024u) * p;
}
template <bool VX> DI void mk_src_w(Src& s, const char* base, unsigned bytes, unsigned stride, unsigned lane) {
  s.srd = mk_srd(base, bytes);
  const unsigned sub = lane >> 5, cb = (lane >> 4) & 1u, row = (lane >> 2) & 3u, sl = lane & 3u;
#pragma unroll
  for (int p = 0; p < 4; ++p) s.v[p] = (4u * sub + row) * stride + cb * 64u + ((sl ^ (VX ? 2u * sub : 2u * (p & 1))) << 4) + (8u * stride - 1024u) * p;
}
DI void touch(unsigned& d, unsigned& voff, const char* base) {
  asm volatile("global_load_dword %0, %1, %2 nt\n\tglobal_load_dword %0, %1, %2\n\tv_add_u32_e32 %1, 0x1000, %1" : "+v"(d), "+v"(voff) : "s"(base) : "memory");
}
DI unsigned ksoff(unsigned m, unsigned rk) { return ((m + rk) & 15u) * 8192u; }

struct PC {
  unsigned r01, r2;
  unsigned w01[2], w2[2];
  unsigned xb, xmask, xa;
  unsigned z, soff;
};
template <unsigned TS_, unsigned QS_, unsigned ZI_, unsigned ZM_, unsigned ZO_, unsigned US_, unsigned CS_, bool ROT_> struct PK {
  static constexpr unsigned TS = TS_, QS = QS_, ZI = ZI_, ZM = ZM_, ZO = ZO_, US = US_, CS = CS_; static constexpr bool ROT = ROT_; };
DI unsigned q_soff(unsigned z) { return ((z & 0xC300u) * 0x440u) & 0x3C0000u; }
template <int POS> DI unsigned ring_lds(const PC& c) { return POS == 2 ? c.r2 : c.r01 + POS * SLOT; }
template <int POS> DI unsigned ring_rd(const PC& c, int d) { return (POS == 2 ? c.w2[d & 1] : c.w01[d & 1] + POS * SLOT) + (d >> 1) * 256u; }
struct NoX { static constexpr bool ACT = true; template <int G> DI void run() const {} };
template <class K, int POS, int S, bool Z, bool RD, bool DM, int NT, bool XT, int M, class X>
DI void proj_gap(f32x4 (&acc)[8][4], const bf16x8 (&cur)[4], bf16x8 (&nxt)[4], bf16x4 (&lo)[4], bf16x8 (&bf)[4], PC& c, unsigned soff, const Src& src, const Src& xs, unsigned xsoff, const X& ex) {
  constexpr int qb = M >> 2, db = M & 3;
  mm_vvv<Z>(acc[4 * S + db][qb], cur[db], bf[qb]);
  if constexpr (RD && M < 8) {
    constexpr int d = M >> 1; const unsigned a = S == 0 ? ring_rd<POS>(c, d) + 4096u : ring_rd<(POS + 1) % 3>(c, d);
    if constexpr (M & 1) nxt[d] = cat(lo[d], lds_tr1(a + K::TS)); else lo[d] = lds_tr1(a);
  }
  if constexpr (X::ACT && RD && S == 1 && db == 3) bf[qb] = lds128(c.xa + qb * K::QS);
  if constexpr (XT) {
    if constexpr (M < 8 && !(M & 1)) dma_p<X_NT, M / 2>(xs.v[M / 2], xs.srd, xsoff);
    if constexpr (M == 7) set_m0(ring_lds<POS>(c) + S * 4096u);
    if constexpr (M >= 8 && !(M & 1)) dma_p<NT, (M - 8) / 2>(src.v[(M - 8) / 2], src.srd, soff);
  } else if constexpr (DM && M >= 8 && !(M & 1)) dma_p<NT, (M - 8) / 2>(src.v[(M - 8) / 2], src.srd, soff);
  if constexpr (X::ACT && S == 0 && M == 9) { PINS(c.z); c.z = (c.z + K::ZI) & K::ZM; PINS(c.z); }
  if constexpr (X::ACT && S == 0 && M == 11) { c.xa = (c.z & c.xmask) | c.xb; PINV(c.xa); }
  if constexpr (S == 1 && DM && !K::ROT && M == 9) { PINS(c.soff); c.soff = (c.soff + K::CS) & (16u * K::CS - 1u); PINS(c.soff); }
  if constexpr (S == 1 && DM && K::ROT && M == 9) { PINS(c.z); c.soff = (c.z + 0x3100u) & 0xC300u; PINS(c.soff); }
  if constexpr (S == 1 && DM && K::ROT && M == 13) { PINS(c.soff); c.soff = (c.soff * 0x440u) & 0x3C0000u; PINS(c.soff); }
  ex.template run<16 * S + M>();
  GAP();
}
template <class K, int POS, int S, bool Z, bool RD, bool DM, int NT, int VM, bool BAR, bool XT, class X, int... Ms>
DI void proj_unit(f32x4 (&acc)[8][4], const bf16x8 (&cur)[4], bf16x8 (&nxt)[4], bf16x8 (&bf)[4], PC& c, const Src& src, const Src& xs, unsigned xsoff, unsigned xdst, const X& ex, iseq<Ms...>) {
  wait_vm_lgkm<(RD ? VM : 63), (S == 0 ? 2 : 0)>();
  if constexpr (BAR) barrier();
  const unsigned soff = c.soff + S * K::US;
  if constexpr (XT) set_m0(xdst); else if constexpr (DM) set_m0(ring_lds<POS>(c) + S * 4096u);
  GAP();
  bf16x4 lo[4];
  (proj_gap<K, POS, S, Z, RD, DM, NT, XT, Ms>(acc, cur, nxt, lo, bf, c, soff, src, xs, xsoff, ex), ...);
}
template <class K, int POS, bool Z, bool LAST, bool DM, int NT, int VM0, int VM1, bool BAR1 = false, bool XT0 = false, bool XT1 = false, class X = NoX>
DI void proj_elem(f32x4 (&acc)[8][4], bf16x8 (&a0)[4], bf16x8 (&a1)[4], bf16x8 (&bf)[4], PC& c, const Src& src, const X& ex = X{},
                  const Src* xs = nullptr, unsigned xso0 = 0, unsigned xd0 = 0, unsigned xso1 = 0, unsigned xd1 = 0) {
  proj_unit<K, POS, 0, Z, true, DM, NT, VM0, false, XT0>(acc, a0, a1, bf, c, src, xs ? *xs : src, xso0, xd0, ex, make_iseq<16>{});
  proj_unit<K, POS, 1, Z, !LAST, DM, NT, VM1, BAR1, XT1>(acc, a1, a0, bf, c, src, xs ? *xs : src, xso1, xd1, ex, make_iseq<16>{});
}
template <class K> DI void proj_setup(PC& c, unsigned tb, unsigned xb, unsigned z0, unsigned s3) {
  c.w01[0] = c.r01 + tb; c.w01[1] = c.w01[0] ^ 32u; c.w2[0] = c.r2 + tb; c.w2[1] = c.w2[0] ^ 32u;
  PINV(c.w01[0]); PINV(c.w01[1]); PINV(c.w2[0]); PINV(c.w2[1]);
  c.xb = xb; c.xmask = K::ZO; PINV(c.xmask);
  c.z = z0; c.soff = s3;
}
template <class K, int POS> DI void proj_start(bf16x8 (&a0)[4], bf16x8 (&bf)[4], PC& c, unsigned tb, unsigned xb, unsigned z0, unsigned s3) {
  proj_setup<K>(c, tb, xb, z0, s3);
  c.xa = (c.z & K::ZO) | xb; PINV(c.xa);
#pragma unroll
  for (int d = 0; d < 4; ++d) { const unsigned a = ring_rd<POS>(c, d); a0[d] = lds_tr(a, a + K::TS); }
  GAP();
#pragma unroll
  for (int q = 0; q < 4; ++q) bf[q] = lds128(c.xa + q * K::QS);
  GAP();
}
#define ACC_ALL(C, a) C(a[0][0]), C(a[0][1]), C(a[0][2]), C(a[0][3]), C(a[1][0]), C(a[1][1]), C(a[1][2]), C(a[1][3]), C(a[2][0]), C(a[2][1]), C(a[2][2]), C(a[2][3]), C(a[3][0]), C(a[3][1]), C(a[3][2]), C(a[3][3])
DI void acc_fence_v(f32x4 (&acc)[8][4]) {
  asm volatile("s_nop 7\n\ts_nop 3" : ACC_ALL("+v", acc) :: "memory");
  f32x4 (&hi)[4][4] = *(f32x4 (*)[4][4])&acc[4];
  asm volatile("" : ACC_ALL("+v", hi) :: "memory");
  GAP();
}
DI void acc_fence_a(f32x4 (&acc)[8][4]) {
  asm volatile("s_nop 7\n\ts_nop 3" : ACC_ALL("+a", acc) :: "memory");
  f32x4 (&hi)[4][4] = *(f32x4 (*)[4][4])&acc[4];
  asm volatile("" : ACC_ALL("+a", hi) :: "memory");
  GAP();
}

struct Stash { u32x4 p[64]; unsigned base[3]; };
template <int I> DI void stash_load1(Stash& st, unsigned vk, const Src& sK, const Src& sV, unsigned rk, unsigned wo_) {
  constexpr int m = I >> 2, isV = (I >> 1) & 1, pc_ = I & 1;
  const unsigned so = ksoff(m, rk) + wo_;
  if constexpr (I < 56) asm volatile("buffer_load_dwordx4 %0, %1, %2, %3 offen offset:%4 nt" : "=a"(st.p[I]) : "v"(isV ? sV.v[pc_] : vk), "s"(isV ? sV.srd : sK.srd), "s"(so), "i"(1024 * pc_) : "memory");
  else                  asm volatile("buffer_load_dwordx4 %0, %1, %2, %3 offen offset:%4 nt" : "=v"(st.p[I]) : "v"(isV ? sV.v[pc_] : vk), "s"(isV ? sV.srd : sK.srd), "s"(so), "i"(1024 * pc_) : "memory");
}
template <int... Is> DI void stash_load(Stash& st, unsigned vk, const Src& sK, const Src& sV, unsigned rk, unsigned wo_, iseq<Is...>) { (stash_load1<Is>(st, vk, sK, sV, rk, wo_), ...); }
constexpr int kv_pair(int m) { constexpr int t[10] = {0, 1, 2, 3, 8, 9, 4, 5, 6, 7}; return t[m % 10]; }
constexpr unsigned long long kv_tab() { unsigned long long v = 0; for (int i = 0; i < 16; ++i) v |= (unsigned long long)kv_pair(i) << (4 * i); return v; }
template <int I> DI void stash_write1(const Stash& st) {
  constexpr int m = I >> 2, isV = (I >> 1) & 1, pc_ = I & 1, off = kv_pair(m) * 16384 + isV * 8192 + pc_ * 1024;
  if constexpr (I < 56) asm volatile("ds_write_b128 %0, %1 offset:%2" :: "v"(st.base[off >> 16]), "a"(st.p[I]), "i"(off & 65535) : "memory");
  else                  asm volatile("ds_write_b128 %0, %1 offset:%2" :: "v"(st.base[off >> 16]), "v"(st.p[I]), "i"(off & 65535) : "memory");
}

struct AC {
  f32x4 o[8][4];
  bf16x8 q[4][4];
  bf16x8 kf[8], vf[8], pf[4];
  f32x4 negm[4];
  f32x4 l[4];
  bf16x8 ones;
};
typedef f32x4 ST[2][4];
constexpr int NEB = 22;
template <int E> DI float sm_val(ST& S) { return S[(E >> 2) & 1][E >> 3][E & 3]; }
template <int E> DI void sm_exp1(ST& S) { float a; asm volatile("v_exp_f32_e32 %0, %1" : "=v"(a) : "v"(sm_val<E>(S))); S[(E >> 2) & 1][E >> 3][E & 3] = a; }
template <int QB, int P> DI void sm_cvt(ST& S, AC& c) {
  u32x4 w = __builtin_bit_cast(u32x4, c.pf[QB]);
  unsigned r;
  asm volatile("v_cvt_pk_bf16_f32 %0, %1, %2" : "=v"(r) : "v"(sm_val<8 * QB + 2 * P>(S)), "v"(sm_val<8 * QB + 2 * P + 1>(S)));
  w[P] = r;
  c.pf[QB] = __builtin_bit_cast(bf16x8, w);
}
template <bool DM, int VM, int R, int M> DI void mem_pre(unsigned ldsaddr) {
  if constexpr (M == 6) wait_lgkm0();
  if constexpr (DM && (M == 6 || M == 14)) set_m0(ldsaddr + (M == 14 ? 4096u : 0u));
  if constexpr (M == R && VM < 63) wait_vm<VM>();
}
template <bool DM, int NT, int M> DI void mem_post(const Src& src, unsigned soff, unsigned ustep) {
  if constexpr (DM && M >= 6 && M <= 20 && !(M & 1)) dma_p<NT, ((M - 6) / 2) & 3>(src.v[((M - 6) / 2) & 3], src.srd, soff + (M >= 14 ? ustep : 0u));
}
template <int WR, int M> DI void mem_wr(const Stash& st) {
  if constexpr (WR >= 40 && M >= 7 && M < 13) stash_write1<WR + M - 7>(st);
  if constexpr (WR >= 0 && WR < 40 && M >= 8 && M < 16)
    asm volatile("ds_write_b128 %0, %1 offset:%2" :: "v"(st.base[0]), "a"(st.p[WR + M - 8]), "i"((M - 8) * 1024) : "memory");
}
struct PA { unsigned rd0, rd1, soff, fill; };
template <bool QK, bool DM, int NT, int VM, int WR, int M, class F>
DI void gapA(AC& c, ST& S, ST& Sn, PA& pa, F& addr, const Src& src, unsigned ustep, const Stash& st, bf16x4& lo) {
  constexpr int R = QK ? 16 : 0;
  if constexpr ((DM || !QK) && M == 0) addr(pa);
  if constexpr (QK) mem_pre<DM, VM, 16, M>(pa.fill); else if constexpr (M == 0 && VM < 63) wait_vm<VM>();
  if constexpr (QK) {
    constexpr int f = M >> 2, s = f >> 1, kb = f & 1, qb = M & 3;
    if constexpr (s == 0) mm_vca(Sn[kb][qb], c.kf[f], c.q[qb][0], c.negm[qb]); else mm_vva<false>(Sn[kb][qb], c.kf[f], c.q[qb][s]);
  }
  mem_post<DM, NT, M>(src, pa.soff, ustep);
  mem_wr<WR, M>(st);
  if constexpr (QK && !DM && M == 10) addr(pa);
  if constexpr (QK && M == 12) asm volatile("" :: "v"(c.negm[0]), "v"(c.negm[1]), "v"(c.negm[2]), "v"(c.negm[3]));
  if constexpr (M >= R && M < R + 16) {
    constexpr int db = (M - R) >> 1; const unsigned a = ((db & 1) ? pa.rd1 : pa.rd0) + (db >> 1) * 256u;
    if constexpr (M & 1) c.vf[db] = cat(lo, lds_tr1(a + 4096u)); else lo = lds_tr1(a);
  }
  if constexpr (NEB + M < 32) sm_exp1<NEB + M>(S);
  if constexpr (M >= 16 && (M & 1)) sm_cvt<2 + ((M - 16) >> 3), ((M - 16) >> 1) & 3>(S, c);
  GAP();
}
template <bool QK, bool DM, int NT, int VM, int WR, class F, int... Ms>
DI void phaseA_(AC& c, ST& S, ST& Sn, F& addr, const Src& src, unsigned ustep, const Stash& st, iseq<Ms...>) {
  PA pa; bf16x4 lo;
  (gapA<QK, DM, NT, VM, WR, Ms>(c, S, Sn, pa, addr, src, ustep, st, lo), ...);
}
template <bool QK, bool DM, int NT, int VM, int WR = -1, class F>
DI void phaseA(AC& c, ST& S, ST& Sn, F addr, const Src& src, unsigned ustep, const Stash& st) {
  phaseA_<QK, DM, NT, VM, WR>(c, S, Sn, addr, src, ustep, st, make_iseq<32>{});
}
template <bool MAX> DI float xlane(float v) {
  auto a = __builtin_amdgcn_permlane32_swap(__float_as_uint(v), __float_as_uint(v), false, false);
  v = MAX ? fmaxf(__uint_as_float(a[0]), __uint_as_float(a[1])) : __uint_as_float(a[0]) + __uint_as_float(a[1]);
  auto b = __builtin_amdgcn_permlane16_swap(__float_as_uint(v), __float_as_uint(v), false, false);
  return MAX ? fmaxf(__uint_as_float(b[0]), __uint_as_float(b[1])) : __uint_as_float(b[0]) + __uint_as_float(b[1]);
}
DI float row_max(const f32x4& x, const f32x4& y) {
  return xlane<true>(fmaxf(fmaxf(fmaxf(x[0], x[1]), fmaxf(x[2], x[3])), fmaxf(fmaxf(y[0], y[1]), fmaxf(y[2], y[3]))));
}
template <bool SM, bool KR, bool DM, int NT, int VM, int WR, int M, class F>
DI void gapB(AC& c, ST& Sn, PA& pa, F& addr, const Src& src, unsigned ustep, const Stash& st) {
  mem_pre<DM, (KR ? VM : 63), 18, M>(pa.fill);
  {
    constexpr int h = M / 18, r = M % 18, qb = 2 * h + (r & 1);
    if constexpr (r < 2) mm_vvv<false>(c.l[qb], c.ones, c.pf[qb]); else mm_avv(c.o[(r - 2) >> 1][qb], c.vf[(r - 2) >> 1], c.pf[qb]);
  }
  mem_post<DM, NT, M>(src, pa.soff, ustep);
  mem_wr<WR, M>(st);
  if constexpr (M == 0) addr(pa);
  if constexpr (KR && M >= 18 && M < 34 && !(M & 1)) { constexpr int f = (M - 18) >> 1; c.kf[f] = lds128(pa.rd0 + (f & 1) * 4096u + (f >> 1) * 256u); }
  if constexpr (SM) {
    if constexpr (M >= 4 && M < 18) sm_exp1<M - 4>(Sn);
    if constexpr (M >= 19 && M < 35 && (M & 1)) sm_exp1<14 + (M - 19) / 2>(Sn);
    if constexpr (M >= 20 && !(M & 1)) sm_cvt<((M - 20) >> 3), ((M - 20) >> 1) & 3>(Sn, c);
  }
  GAP();
}
template <bool SM, bool KR, bool DM, int NT, int VM, int WR, class F, int... Ms>
DI void phaseB_(AC& c, ST& Sn, F& addr, const Src& src, unsigned ustep, const Stash& st, iseq<Ms...>) {
  PA pa;
  (gapB<SM, KR, DM, NT, VM, WR, Ms>(c, Sn, pa, addr, src, ustep, st), ...);
}
template <bool SM, bool KR, bool DM, int NT, int VM, int WR = -1, class F>
DI void phaseB(AC& c, ST& Sn, F addr, const Src& src, unsigned ustep, const Stash& st) {
  phaseB_<SM, KR, DM, NT, VM, WR>(c, Sn, addr, src, ustep, st, make_iseq<36>{});
}
DI void k_read(AC& ac, unsigned ka) {
#pragma unroll
  for (int f = 0; f < 8; ++f) ac.kf[f] = lds128(ka + (f & 1) * 4096u + (f >> 1) * 256u);
}
template <bool DM, int M> DI void qk0_gap(AC& ac, ST& SA, const Src& src, unsigned soff, unsigned fill) {
  constexpr int f = M >> 2, qb = M & 3;
  if constexpr (DM && (M == 0 || M == 16)) set_m0(fill + M * 256u);
  mm_vva<(f < 2)>(SA[f & 1][qb], ac.kf[f], ac.q[qb][f >> 1]);
  if constexpr (DM && qb == 0) dma_p<KV_NT, f & 3>(src.v[f & 3], src.srd, soff + (f >> 2) * 4096u);
  GAP();
}
template <bool DM, int... Ms> DI void qk0(AC& ac, ST& SA, const Src& src, unsigned soff, unsigned fill, iseq<Ms...>) { (qk0_gap<DM, Ms>(ac, SA, src, soff, fill), ...); }
template <int... Js> DI void exp_first(ST& SA, iseq<Js...>) { (sm_exp1<Js>(SA), ...); }
template <int... Js> DI void cvt_first(ST& SA, AC& c, iseq<Js...>) { (sm_cvt<(Js >> 2), (Js & 3)>(SA, c), ...); }
DI void sm_first(AC& ac, ST& SA) {
  asm volatile("s_nop 7\n\ts_nop 3" : "+v"(SA[0][0]), "+v"(SA[0][1]), "+v"(SA[0][2]), "+v"(SA[0][3]), "+v"(SA[1][0]), "+v"(SA[1][1]), "+v"(SA[1][2]), "+v"(SA[1][3]) :: "memory");
  GAP();
#pragma unroll
  for (int q = 0; q < 4; ++q) {
    const float m = row_max(SA[0][q], SA[1][q]);
    SA[0][q] -= m; SA[1][q] -= m;
    ac.negm[q] = f32x4{-m, -m, -m, -m};
    ac.l[q] = f32x4{};
    ac.pf[q] = bf16x8{};
  }
  ac.ones = __builtin_bit_cast(bf16x8, u32x4{0x3f803f80u, 0x3f803f80u, 0x3f803f80u, 0x3f803f80u});
  asm volatile("" : "+v"(ac.negm[0]), "+v"(ac.negm[1]), "+v"(ac.negm[2]), "+v"(ac.negm[3]), "+v"(ac.l[0]), "+v"(ac.l[1]), "+v"(ac.l[2]), "+v"(ac.l[3]), "+v"(ac.ones));
  exp_first(SA, make_iseq<NEB>{});
  cvt_first(SA, ac, make_iseq<8>{});
  GAP();
}
struct BS { unsigned p[4], v[6], k[2], rk, wo, l16; };
constexpr bool b_dma(int f) { return f != 2 * (SK - 3) && f != 2 * (SV - 2) + 1 && f != 2 * (SP - 2) + 1 && f < 30; }
constexpr int b_vm(int f) {
  const int s = f == 30 ? 26 : f - 3;
  if (!b_dma(s)) return 63;
  int n = b_dma(f) ? 5 + (f & 1) : 0;
  for (int i = s + 1; i < f; ++i) n += b_dma(i) ? 8 : 0;
  return n;
}
template <class KO, int I> DI void b_step(AC& ac, ST& S, ST& Sn, BS& b, const Src& sK, const Src& sV, const Src& sO, Stash& st) {
  constexpr int fa = 2 * I, fb = 2 * I + 1, vi = (I == SP || I == 15) ? 4 : (I & 1) ? 0 : 2;
  const unsigned pa = b.p[(I & 1) ? 3 : 1], pb = b.p[(I & 1) ? 0 : 2];
  if constexpr (I >= 1 && I <= 13) barrier();
  if constexpr (I == SK - 3) st.base[0] = pa + b.l16;
  phaseA<(I < 15), b_dma(fa), (I < 14 ? KV_NT : W_NT), b_vm(fa), (I == SK - 3 ? 0 : -1)>(ac, S, Sn, [&](PA& p) __attribute__((always_inline)) {
    PINS(b.rk);
    p.soff = I < 14 ? ksoff(I < 13 ? I + 3 : 15, b.rk) : b.wo + KO::CS; p.fill = pa;
    p.rd0 = b.v[vi] + (I == SP ? SLOT : 0u); p.rd1 = b.v[vi + 1] + (I == SP ? SLOT : 0u); }, I < 13 ? sK : I < 14 ? sV : sO, I < 14 ? 4096u : KO::US, st);
  if constexpr (I == SV - 2) st.base[0] = pb + b.l16;
  if constexpr (I < 15)
    phaseB<true, (I < 14), b_dma(fb), (I < 13 ? KV_NT : W_NT), b_vm(fb), (I == SV - 2 ? 8 : -1)>(ac, Sn, [&](PA& p) __attribute__((always_inline)) {
      PINS(b.rk);
      p.soff = I < 13 ? ksoff(I + 2, b.rk) : b.wo + (I - 13) * 2u * KO::CS; p.fill = pb; p.rd0 = b.k[(I & 1) ? 0 : 1]; }, I < 13 ? sV : sO, I < 13 ? 4096u : KO::US, st);
}
template <int CH, int Q, int J> DI void o_word(const AC& c, const float (&inv)[4], bf16x8 (&nb)[4]) {
  u32x4 w = __builtin_bit_cast(u32x4, nb[Q]);
  unsigned r; float t;
  asm volatile("v_accvgpr_read_b32 %0, %2\n\tv_accvgpr_read_b32 %1, %3\n\tv_mul_f32_e32 %0, %0, %4\n\tv_mul_f32_e32 %1, %1, %4\n\tv_cvt_pk_bf16_f32 %0, %0, %1"
               : "=&v"(r), "=&v"(t) : "a"(c.o[2 * CH + (J >> 1)][Q][2 * (J & 1)]), "a"(c.o[2 * CH + (J >> 1)][Q][2 * (J & 1) + 1]), "v"(inv[Q]));
  w[J] = r;
  nb[Q] = __builtin_bit_cast(bf16x8, w);
}
template <int CH, int Q> DI void o_put(const bf16x8 (&nb)[4], unsigned ow) { asm volatile("ds_write_b128 %0, %1 offset:%2" :: "v"(ow), "v"(nb[Q]), "i"(CH * 4096 + Q * 1024) : "memory"); }
template <int E> struct CvX {
  static constexpr bool ACT = E == 3;
  const AC& c; const float (&inv)[4]; const bf16x8 (&cur)[4]; bf16x8 (&nb)[4]; unsigned ow;
  template <int G> DI void run() const {
    if constexpr (E < 3 && (G & 1)) o_word<E + 1, (G >> 3), (G >> 1) & 3>(c, inv, nb);
    if constexpr (E < 3 && ((G & 15) == 8 || (G & 15) == 10)) o_put<E, 2 * (G >> 4) + ((G >> 1) & 1)>(cur, ow);
    if constexpr (E == 2 && (G & 15) == 12) o_put<3, (G >> 4)>(nb, ow);
    if constexpr (E == 3 && (G == 8 || G == 10)) o_put<3, 2 + ((G >> 1) & 1)>(cur, ow);
  }
};
template <class K, int VM, int M> DI void gapL(AC& c, float (&inv)[4], bf16x8 (&nb)[4], bf16x8 (&a0)[4], bf16x4& lo, const PC& pc) {
  if constexpr (M == 6) wait_vm_lgkm<VM, 0>();
  if constexpr (M < 4) mm_vvv<false>(c.l[M], c.ones, c.pf[M]); else mm_avv(c.o[(M - 4) >> 2][M & 3], c.vf[(M - 4) >> 2], c.pf[M & 3]);
  if constexpr (M >= 6 && M < 14) {
    constexpr int d = (M - 6) >> 1; const unsigned a = ring_rd<0>(pc, d);
    if constexpr (M & 1) a0[d] = cat(lo, lds_tr1(a + K::TS)); else lo = lds_tr1(a);
  }
  if constexpr (M >= 4 && M < 12 && !(M & 1)) asm volatile("v_rcp_f32_e32 %0, %1" : "=v"(inv[(M - 4) >> 1]) : "v"(c.l[(M - 4) >> 1][0]));
  if constexpr (M >= 16 && M < 32) o_word<0, ((M - 16) >> 2), (M & 3)>(c, inv, nb);
  GAP();
}
template <class K, int VM, int... Ms> DI void phaseL(AC& c, float (&inv)[4], bf16x8 (&nb)[4], bf16x8 (&a0)[4], const PC& pc, iseq<Ms...>) {
  bf16x4 lo;
#pragma unroll
  for (int q = 0; q < 4; ++q) nb[q] = bf16x8{};
  (gapL<K, VM, Ms>(c, inv, nb, a0, lo, pc), ...);
}
struct Pub { u32x4 pk[16]; srd_t srd; unsigned v[2], so; };
template <int F, int H> DI void pub_half(const f32x4 (&acc)[8][4], Pub& p) {
  const f32x4& a = acc[4 * (F >> 3) + 2 * (F & 1) + H][(F >> 1) & 3];
  unsigned r0, r1;
  asm volatile("v_cvt_pk_bf16_f32 %0, %2, %3\n\tv_cvt_pk_bf16_f32 %1, %4, %5" : "=&v"(r0), "=v"(r1) : "v"(a[0]), "v"(a[1]), "v"(a[2]), "v"(a[3]));
  p.pk[F][2 * H] = r0; p.pk[F][2 * H + 1] = r1;
  if constexpr (H) asm volatile("buffer_store_dwordx4 %0, %1, %2, %3 offen offset:%4 sc0 sc1" :: "v"(p.pk[F]), "v"(p.v[(F >> 2) & 1]), "s"(p.srd), "s"(p.so + (F >> 3) * 8192u), "i"((F & 3) * 1024) : "memory");
}
struct PubX {
  static constexpr bool ACT = false;
  const f32x4 (&acc)[8][4]; Pub& p;
  template <int G> DI void run() const { if constexpr (G >= 16) pub_half<((G - 16) >> 1), (G & 1)>(acc, p); }
};
template <int I> DI void own_put1(const u32x4& v, unsigned a) { asm volatile("ds_write_b128 %0, %1 offset:%2 ; %3" :: "v"(a), "v"(v), "i"((I & 7) * 1024), "i"(I) : "memory"); }
template <int B, int... Is> DI void own_put(const Pub& p, unsigned a, iseq<Is...>) { (own_put1<B + Is>(p.pk[B + Is], a), ...); }
template <int... Is> DI void pub_rest(const f32x4 (&acc)[8][4], Pub& p, iseq<Is...>) { (pub_half<8 + (Is >> 1), (Is & 1)>(acc, p), ...); }
DI void redo_head(const AC& ac, const char* __restrict__ Kp, const char* __restrict__ Vp, unsigned lane, unsigned ow) {
  const unsigned r16 = lane & 15u, g = lane >> 4;
#pragma unroll 1
  for (unsigned qb = 0; qb < 4; ++qb) {
    bf16x8 qq[4];
#pragma unroll
    for (int s = 0; s < 4; ++s) qq[s] = qb == 0 ? ac.q[0][s] : qb == 1 ? ac.q[1][s] : qb == 2 ? ac.q[2][s] : ac.q[3][s];
    f32x4 o[8];
#pragma unroll
    for (int d = 0; d < 8; ++d) o[d] = f32x4{};
    float m = -__builtin_inff(), l = 0.f;
#pragma unroll 1
    for (unsigned t = 0; t < 16; ++t) {
      f32x4 s0 = {}, s1 = {};
      const char* kp = Kp + (t * 32u + r16) * 256u + g * 16u;
#pragma unroll
      for (int s = 0; s < 4; ++s) {
        s0 = __builtin_amdgcn_mfma_f32_16x16x32_bf16(*(const bf16x8*)(kp + s * 64), qq[s], s0, 0, 0, 0);
        s1 = __builtin_amdgcn_mfma_f32_16x16x32_bf16(*(const bf16x8*)(kp + s * 64 + 4096), qq[s], s1, 0, 0, 0);
      }
      const float mn = fmaxf(m, row_max(s0, s1)), al = __builtin_amdgcn_exp2f(m - mn);
      m = mn;
#pragma unroll
      for (int j = 0; j < 4; ++j) { s0[j] = __builtin_amdgcn_exp2f(s0[j] - mn); s1[j] = __builtin_amdgcn_exp2f(s1[j] - mn); }
      l = l * al + ((s0[0] + s0[1]) + (s0[2] + s0[3])) + ((s1[0] + s1[1]) + (s1[2] + s1[3]));
      const bf16x8 pf = __builtin_bit_cast(bf16x8, pack2(s0, s1));
      const __bf16* vp = (const __bf16*)(Vp + (t * 32u + g * 4u) * 256u) + r16;
#pragma unroll
      for (int d = 0; d < 8; ++d) {
        bf16x8 vf;
#pragma unroll
        for (int j = 0; j < 8; ++j) vf[j] = vp[((j & 3) + (j >> 2) * 16) * 128 + d * 16];
        o[d] = __builtin_amdgcn_mfma_f32_16x16x32_bf16(vf, pf, o[d] * al, 0, 0, 0);
      }
    }
    const float inv = 1.0f / xlane<false>(l);
#pragma unroll
    for (int s = 0; s < 4; ++s) *(LAS u32x4*)(size_t)(ow + s * 4096u + qb * 1024u) = pack2(o[2 * s] * inv, o[2 * s + 1] * inv);
  }
}
template <bool isA, int J> DI void q_conv1(AC& ac, f32x4 (&acc)[8][4], Stash& st, const Src& sK, const Src& sV, unsigned rk, unsigned p3) {
  constexpr int s = J >> 2, qb = J & 3;
  if constexpr (isA) { stash_write1<(5 * J) / 2>(st); stash_write1<(5 * J) / 2 + 1>(st); if constexpr (J & 1) stash_write1<(5 * J) / 2 + 2>(st); }
  else {
    constexpr int p = 2 * (J & 1);
    const Src& src = s < 2 ? sK : sV;
    const unsigned so = ksoff(s == 0 ? 2 : s == 1 ? SK : s == 2 ? SV : SP, rk) + (J & 2) * 2048u;
    if constexpr (s == 0 || s == 3) {
      if constexpr (p == 0) asm volatile("s_mov_b32 m0, %0\n\ts_nop 0" :: "s"(p3 + (s == 3 ? SLOT : 0u) + (J & 2) * 2048u) : "memory");
      dma_p<KV_NT, p>(src.v[p], src.srd, so); dma_p<KV_NT, p + 1>(src.v[p + 1], src.srd, so);
    } else {
      asm volatile("buffer_load_dwordx4 %0, %1, %2, %3 offen offset:%4 nt" : "=a"(st.p[2 * J - 8]) : "v"(src.v[p]), "s"(src.srd), "s"(so), "i"(1024 * p) : "memory");
      asm volatile("buffer_load_dwordx4 %0, %1, %2, %3 offen offset:%4 nt" : "=a"(st.p[2 * J - 7]) : "v"(src.v[p + 1]), "s"(src.srd), "s"(so), "i"(1024 * p + 1024) : "memory");
    }
  }
  ac.q[qb][s] = __builtin_bit_cast(bf16x8, natural8(pack2(acc[2 * s][qb] * SC, acc[2 * s + 1][qb] * SC)));
  asm volatile("" : "+a"(ac.q[qb][s]));
  GAP();
}
template <bool isA, int... Js> DI void q_conv(AC& ac, f32x4 (&acc)[8][4], Stash& st, const Src& sK, const Src& sV, unsigned rk, unsigned p3, iseq<Js...>) { (q_conv1<isA, Js>(ac, acc, st, sK, sV, rk, p3), ...); }
DI void zero_o(AC& ac) {
#pragma unroll
  for (int i = 0; i < 8; ++i)
#pragma unroll
    for (int q = 0; q < 4; ++q) { ac.o[i][q] = f32x4{}; asm volatile("" : "+a"(ac.o[i][q])); }
}

template <bool isA>
DI void body(const char* __restrict__ X, const char* __restrict__ K, const char* __restrict__ V, const char* __restrict__ Wq, const char* __restrict__ Wo,
             const char* __restrict__ Wo2, srd_t code, char* __restrict__ part, char* __restrict__ out) {
  extern __shared__ __attribute__((aligned(16))) char lds[];
  unsigned tid = threadIdx.x;
  PINV(tid);
  const unsigned lane = tid & 63u, w = rfl(tid >> 6);
  const unsigned bid = blockIdx.x, xg = bid & 7u, rr = bid >> 3, b = rr & 15u, h = xg * 4u + w;
  const unsigned r16 = lane & 15u, g = lane >> 4;
  const char* xp  = X + (size_t)b * 65536u + w * 16384u;
  const char* wqp = Wq + h * 256u;
  const char* wop = Wo + (size_t)xg * 524288u + w * 256u;
  const size_t kvo = isA ? (size_t)(b * 8u + xg) * 131072u : (size_t)(b * 32u + h) * 131072u;
  const unsigned rk = isA ? b : b + 4u * w;
  typedef PK<512u, 4096u, 0x1300u, 0xF300u, 0xC300u, 128u, 32u * 8192u, true> KQ;
  typedef PK<512u, 4096u, 0x1300u, 0xF300u, 0xC300u, 4096u, 8192u, false> KT;
  typedef PK<2048u, 1024u, 0x1000u, 0xF000u, 0xF000u, 128u, 32u * 1024u, false> KO;
  asm volatile("" :: "v"(lds));
  PC pc;
  pc.r01 = 65536u + w * 16384u; pc.r2 = 131072u + w * 8192u;
  const unsigned zq = ((b & 3u) << 14) | ((b >> 2) << 8), co = 4u * w;
  const srd_t srdH = mk_srd(part + HOFF, 4096u + 256u * 128u);
  const unsigned tok_off = rr * 128u, flg_off = 4096u + rr * 1024u;
  const unsigned l8 = (lane & 7u) * 4u;
  unsigned entry_flags;
  asm volatile("buffer_load_dword %0, %1, %2, %3 offen sc1" : "=v"(entry_flags) : "v"(l8), "s"(srdH), "s"(flg_off + xg * 128u) : "memory");
  if constexpr (isA) { if (w == 3 && lane < WARM) asm volatile("buffer_load_dword %0, %1, %2, 0 offen" : "+v"(entry_flags) : "v"((b * WARM + lane) * 128u), "s"(code) : "memory"); }
  Src sQ, sX, sK, sV, sO;
  mk_src_w<false>(sQ, wqp, 511u * 8192u + 256u, 8192u, lane);
  mk_src_rows<false>(sX, xp, 16384u, 1024u, lane);
  const unsigned p0 = b & 3u, p1 = (p0 + 1u) & 3u, p2 = (p0 + 2u) & 3u, p3 = (p0 + 3u) & 3u, xw = w * 4096u;
  Stash st;
  unsigned dum0 = lane;
  dma_unit<1>(sQ, q_soff(zq), pc.r2);
  dma_unit<W_NT>(sQ, q_soff(zq + 0x1300u), pc.r01);
  if constexpr (!isA) dma_unit<X_NT>(sX, p0 * 256u, xw + p0 * 16384u);
  dma_unit<1>(sQ, q_soff(zq) + KQ::US, pc.r2 + 4096u);
  dma_unit<W_NT>(sQ, q_soff(zq + 0x1300u) + KQ::US, pc.r01 + 4096u);
  mk_src_rows<false>(sK, K + kvo, 131072u, 256u, lane);
  mk_src_rows<true>(sV, V + kvo, 131072u, 256u, lane);
  mk_src_w<true>(sO, wop, 511u * 1024u + 256u, 1024u, lane);
  if constexpr (isA) {
    stash_load(st, (w & 1u) ? sK.v[2] : sK.v[0], sK, sV, rk, w * 2048u, make_iseq<64>{});
    dma_unit<X_NT>(sX, p0 * 256u, xw + p0 * 16384u);
    dma_elem<W_NT>(sQ, q_soff(zq + 0x2200u), KQ::US, pc.r01 + SLOT);
    dma_unit<X_NT>(sX, p1 * 256u, xw + p1 * 16384u);
    dma_unit<X_NT>(sX, p2 * 256u, xw + p2 * 16384u);
    dma_unit<X_NT>(sX, p3 * 256u, xw + p3 * 16384u);
  }
  const unsigned rb = 1024u * (r16 >> 2) + 64u * (r16 & 3u) + 16u * (g ^ ((r16 >> 2) & 2u));
  const unsigned tb = 64u * ((lane >> 2) & 3u) + 32u * (g & 1u) + 8u * (lane & 3u);
  const unsigned tbV = tb + 1024u * g;
  const unsigned sb0 = w * 2048u + lane * 16u;
  st.base[0] = sb0; st.base[1] = sb0 + 65536u; st.base[2] = sb0 + 131072u;
  AC ac;
  if constexpr (!isA) zero_o(ac);
  {
    f32x4 acc[8][4];
    bf16x8 a0[4], a1[4], bf[4];
    asm volatile("s_waitcnt vmcnt(%2)" : "+v"(entry_flags), "+v"(dum0) : "i"(isA ? 20 : 8) : "memory");
    if (w == 0) {
      const unsigned long long mk = __builtin_amdgcn_ballot_w64(((entry_flags >> (lane & 7u)) & 1u) != 0u);
      const unsigned tok = 0x5a5a5a00u | (~(unsigned)mk & 0xffu);
      if (lane == 0) asm volatile("buffer_store_dword %0, off, %1, %2 sc1" :: "v"(tok), "s"(srdH), "s"(tok_off + xg * 4u) : "memory");
    }
    barrier();
    proj_start<KQ, 2>(a0, bf, pc, tbV, rb, zq, q_soff(zq + 0x3100u));
    if constexpr (isA) {
      proj_elem<KQ, 2, true,  false, true, W_NT, 63, 63>(acc, a0, a1, bf, pc, sQ);
      proj_elem<KQ, 0, false, false, true, W_NT, 63, 28>(acc, a0, a1, bf, pc, sQ);
      proj_elem<KQ, 1, false, false, true, W_NT, 28, 16>(acc, a0, a1, bf, pc, sQ);
      proj_elem<KQ, 2, false, false, true, W_NT, 16, 16, true>(acc, a0, a1, bf, pc, sQ);
    } else {
      dma_elem<W_NT>(sQ, q_soff(zq + 0x2200u), KQ::US, pc.r01 + SLOT);
      proj_elem<KQ, 2, true,  false, true, W_NT, 12, 20, false, true, true>(acc, a0, a1, bf, pc, sQ, NoX{}, &sX, p1 * 256u, xw + p1 * 16384u, p2 * 256u, xw + p2 * 16384u);
      proj_elem<KQ, 0, false, false, true, W_NT, 24, 28, false, true, false>(acc, a0, a1, bf, pc, sQ, NoX{}, &sX, p3 * 256u, xw + p3 * 16384u);
      proj_elem<KQ, 1, false, false, true, W_NT, 28, 24>(acc, a0, a1, bf, pc, sQ);
      proj_elem<KQ, 2, false, false, true, W_NT, 20, 16, true>(acc, a0, a1, bf, pc, sQ);
    }
#pragma unroll 1
    for (unsigned e = 0; e < 3; ++e) {
      proj_elem<KQ, 0, false, false, true, W_NT, 16, 16>(acc, a0, a1, bf, pc, sQ);
      proj_elem<KQ, 1, false, false, true, W_NT, 16, 16>(acc, a0, a1, bf, pc, sQ);
      proj_elem<KQ, 2, false, false, true, W_NT, 16, 16>(acc, a0, a1, bf, pc, sQ);
    }
    if constexpr (isA) {
      proj_elem<KQ, 0, false, false, false, W_NT, 16, 12>(acc, a0, a1, bf, pc, sQ);
      proj_elem<KQ, 1, false, false, false, W_NT, 8, 4>(acc, a0, a1, bf, pc, sQ);
      proj_elem<KQ, 2, false, true, false, W_NT, 0, 0>(acc, a0, a1, bf, pc, sQ);
    } else {
      pc.soff = ksoff(0u, rk);
      proj_elem<KT, 0, false, false, true, KV_NT, 16, 16>(acc, a0, a1, bf, pc, sK);
      proj_elem<KT, 1, false, false, true, KV_NT, 16, 16>(acc, a0, a1, bf, pc, sK);
      pc.soff = ksoff(0u, rk);
      proj_elem<KT, 2, false, true, true, KV_NT, 16, 16>(acc, a0, a1, bf, pc, sV);
    }
    barrier();
    acc_fence_v(acc);
    q_conv<isA>(ac, acc, st, sK, sV, rk, w * 16384u, make_iseq<16>{});
  }
  ST SA, SB;
  if constexpr (isA) {
    zero_o(ac);
    const unsigned kb = rb, vb = SLOT + tbV;
    unsigned dum = lane, to = (lane >> 4) * 1024u + (lane & 8u) * 16u + (lane & 7u) * 4u;
    PINV(to);
    const char* ta = wop + b * KO::CS;
    const char* tb2 = Wo2 + (size_t)xg * 524288u + w * 256u + b * KO::CS;
    touch(dum, to, ta);
    asm volatile("s_waitcnt lgkmcnt(0)" ::: "memory");
    barrier();
    k_read(ac, kb + kv_pair(0) * 16384u);
    wait_lgkm0();
    GAP();
    qk0<false>(ac, SA, sK, 0u, 0u, make_iseq<32>{});
    k_read(ac, kb + kv_pair(1) * 16384u);
    touch(dum, to, ta);
    sm_first(ac, SA);
    unsigned i4 = 0;
    auto ak = [&](PA& p) { PINS(i4); p.rd0 = kb + (((unsigned)((kv_tab() >> 8) >> i4) & 15u) << 14); PINV(p.rd0); i4 += 4u; };
    auto at = [&](PA& p) __attribute__((always_inline)) {
      PINS(i4); p.rd0 = vb + (((unsigned)(kv_tab() >> i4) & 15u) << 14); p.rd1 = p.rd0 ^ 32u; PINV(p.rd0); PINV(p.rd1);
      const char* tp;
      asm volatile("s_cmp_lt_u32 %1, 24\n\ts_cselect_b64 %0, %2, %3" : "=s"(tp) : "s"(i4), "s"(ta), "s"(tb2 - 32768) : "scc");
      touch(dum, to, tp);
    };
    while (i4 < 24u) {
      phaseA<true, false, KV_NT, 63>(ac, SA, SB, at, sK, 0u, st);
      phaseB<true, true, false, KV_NT, 63>(ac, SB, ak, sK, 0u, st);
      phaseA<true, false, KV_NT, 63>(ac, SB, SA, at, sK, 0u, st);
      phaseB<true, true, false, KV_NT, 63>(ac, SA, ak, sK, 0u, st);
    }
    barrier();
    phaseA<true, false, KV_NT, 63, 40>(ac, SA, SB, at, sK, 0u, st);
    phaseB<true, true, false, KV_NT, 63, 46>(ac, SB, ak, sK, 0u, st);
    phaseA<true, false, KV_NT, 63, 52>(ac, SB, SA, at, sK, 0u, st);
    phaseB<true, true, false, KV_NT, 63, 58>(ac, SA, ak, sK, 0u, st);
    wait_vm_lgkm<63, 8>(); barrier();
    while (i4 < 56u) {
      phaseA<true, false, KV_NT, 63>(ac, SA, SB, at, sK, 0u, st);
      phaseB<true, true, false, KV_NT, 63>(ac, SB, ak, sK, 0u, st);
      phaseA<true, false, KV_NT, 63>(ac, SB, SA, at, sK, 0u, st);
      phaseB<true, true, false, KV_NT, 63>(ac, SA, ak, sK, 0u, st);
    }
    barrier();
    unsigned v14 = vb + kv_pair(14) * 16384u, x14 = v14 ^ 32u; PINV(v14); PINV(x14);
    phaseA<true, true, W_NT, 63>(ac, SA, SB, [&](PA& p) { p.soff = co * KO::CS; p.fill = pc.r01; p.rd0 = v14; p.rd1 = x14; }, sO, KO::US, st);
    phaseB<true, false, true, W_NT, 63>(ac, SB, [&](PA& p) { p.soff = (co + 1u) * KO::CS; p.fill = pc.r01 + SLOT; }, sO, KO::US, st);
    phaseA<false, false, W_NT, 63>(ac, SB, SA, [&](PA& p) { p.rd0 = v14 + 16384u; p.rd1 = x14 + 16384u; }, sO, 0u, st);
    dum0 = dum;
  } else {
    const unsigned P0 = pc.r01, P1 = pc.r01 + SLOT, P2 = pc.r2, P3 = w * 16384u;
    unsigned v0 = tbV + P0, v2 = tbV + P2, x0 = v0 ^ 32u, x2 = v2 ^ 32u, k1 = rb + P1, k3 = rb + P3;
    PINV(v0); PINV(v2); PINV(x0); PINV(x2); PINV(k1); PINV(k3);
    wait_vm<48>();
    k_read(ac, k1 - SLOT);
    wait_lgkm0();
    GAP();
    qk0<true>(ac, SA, sV, ksoff(1u, rk), P0, make_iseq<32>{});
    wait_vm<48>();
    GAP();
    k_read(ac, k1);
    sm_first(ac, SA);
    BS bs{{P0, P1, P2, P3}, {v0, x0, v2, x2, tbV + P3, (tbV + P3) ^ 32u}, {k1, k3}, rk, co * KO::CS, lane * 16u};
    PINV(bs.v[4]); PINV(bs.v[5]);
#define B2(I) b_step<KO, I>(ac, SA, SB, bs, sK, sV, sO, st); b_step<KO, I + 1>(ac, SB, SA, bs, sK, sV, sO, st);
    B2(0) B2(2) B2(4) B2(6) B2(8) B2(10)
    B2(12) B2(14)
#undef B2
  }
  {
    const unsigned l16 = lane * 16u;
    unsigned ow = w * 16384u + l16;
    PINV(ow);
    f32x4 acc[8][4];
    bf16x8 a0[4], a1[4], b0[4], bf[4];
    float inv[4];
    proj_setup<KO>(pc, tb + 512u * g, l16, (co + 3u) << 12, (co + 3u) * KO::CS);
    GAP();
    phaseL<KO, (isA ? 12 : 20)>(ac, inv, b0, a0, pc, make_iseq<36>{});
    if constexpr (isA) {
      barrier();
      dma_elem<W_NT>(sO, (co + 2u) * KO::CS, KO::US, pc.r2);
    }
    const bool ok = ac.l[0][0] <= LMAX && ac.l[1][0] <= LMAX && ac.l[2][0] <= LMAX && ac.l[3][0] <= LMAX;
    if (__builtin_expect(__builtin_amdgcn_ballot_w64(!ok) != 0ull, 0)) {
      acc_fence_a(ac.o);
      redo_head(ac, K + kvo, V + kvo, lane, ow);
#pragma unroll
      for (int q = 0; q < 4; ++q) {
        inv[q] = 1.0f;
        b0[q] = lds128(ow + q * 1024u);
#pragma unroll
        for (int s = 1; s < 4; ++s) {
          const u32x4 t = __builtin_bit_cast(u32x4, lds128(ow + s * 4096u + q * 1024u));
          ac.o[2 * s][q] = f32x4{__uint_as_float(t[0] << 16), __uint_as_float(t[0] & 0xffff0000u), __uint_as_float(t[1] << 16), __uint_as_float(t[1] & 0xffff0000u)};
          ac.o[2 * s + 1][q] = f32x4{__uint_as_float(t[2] << 16), __uint_as_float(t[2] & 0xffff0000u), __uint_as_float(t[3] << 16), __uint_as_float(t[3] & 0xffff0000u)};
        }
      }
      wait_lgkm0();
      acc_fence_a(ac.o);
    }
#pragma unroll
    for (int q = 0; q < 4; ++q) bf[q] = bf16x8{};
    GAP();
    proj_elem<KO, 0, true, false, true, W_NT, 16, 16>(acc, a0, a1, b0, pc, sO, CvX<0>{ac, inv, b0, bf, ow});
    proj_elem<KO, 1, false, false, true, W_NT, 16, 16>(acc, a0, a1, bf, pc, sO, CvX<1>{ac, inv, bf, b0, ow});
    proj_elem<KO, 2, false, false, true, W_NT, 16, 16>(acc, a0, a1, b0, pc, sO, CvX<2>{ac, inv, b0, bf, ow});
    if constexpr (isA) PINV(dum0);
    proj_elem<KO, 0, false, false, true, W_NT, 16, 16, true>(acc, a0, a1, bf, pc, sO, CvX<3>{ac, inv, bf, b0, ow});
#pragma unroll 1
    for (unsigned e = 0; e < 3; ++e) {
      proj_elem<KO, 1, false, false, true, W_NT, 16, 16>(acc, a0, a1, bf, pc, sO);
      proj_elem<KO, 2, false, false, true, W_NT, 16, 16>(acc, a0, a1, bf, pc, sO);
      proj_elem<KO, 0, false, false, true, W_NT, 16, 16>(acc, a0, a1, bf, pc, sO);
    }
    unsigned toks;
    asm volatile("buffer_load_dword %0, %1, %2, %3 offen sc1" : "=v"(toks) : "v"(l8), "s"(srdH), "s"(tok_off) : "memory");
    Pub pb;
    pb.srd = mk_srd(part + (size_t)rr * 524288u, 524288u);
    pb.v[0] = l16; pb.v[1] = l16 + 4096u; pb.so = xg * 65536u + w * 16384u;
    PINV(pb.v[1]);
#pragma unroll
    for (int i = 0; i < 16; ++i) pb.pk[i] = u32x4{};
    proj_elem<KO, 1, false, false, false, W_NT, 17, 13>(acc, a0, a1, bf, pc, sO);
    proj_elem<KO, 2, false, false, false, W_NT, 9, 5>(acc, a0, a1, bf, pc, sO);
    proj_elem<KO, 0, false, true, false, W_NT, 0, 0>(acc, a0, a1, bf, pc, sO, PubX{acc, pb});
    pub_rest(acc, pb, make_iseq<16>{});
    const unsigned lr = ((lane + xg) & 7u) * 4u;
    unsigned fl;
    asm volatile("buffer_load_dword %0, %1, %2, %3 offen sc1" : "=v"(fl) : "v"(lr), "s"(srdH), "s"(flg_off + xg * 128u) : "memory");
    PINV(toks);
    if (w == (xg >> 1)) {
      const unsigned a = pc.r01 + SLOT + l16;
      if (xg & 1u) own_put<8>(pb, a, make_iseq<8>{}); else own_put<0>(pb, a, make_iseq<8>{});
    }
    const unsigned fo = lane * 128u + xg * 4u, tau = __builtin_amdgcn_readlane(toks, xg);
    unsigned oo = ((b * 64u + 16u * w + r16) * 1024u + (isA ? 0u : 512u) + 64u * xg + 8u * g) * 2u;
    PINV(oo);
    wait_lgkm0();
    barrier();
    const unsigned oa = 65536u + SLOT + (xg >> 1) * 16384u + w * 2048u + l16;
    const u32x4 r0 = __builtin_bit_cast(u32x4, lds128(oa)), r1 = __builtin_bit_cast(u32x4, lds128(oa + 1024u));
    GAP();
    wait_vm<9>();
    if (lane == 2u * w) asm volatile("buffer_store_dword %0, %1, %2, %3 offen sc1" :: "v"(toks), "v"(fo), "s"(srdH), "s"(flg_off) : "memory");
    wait_vm<2>();
    if (lane == 2u * w + 1u) asm volatile("buffer_store_dword %0, %1, %2, %3 offen sc1" :: "v"(toks), "v"(fo), "s"(srdH), "s"(flg_off) : "memory");
    f32x16 f[8];
#pragma unroll
    for (int j = 0; j < 8; ++j) { const unsigned t = j < 4 ? r0[j & 3] : r1[j & 3]; f[0][2 * j] = __uint_as_float(t << 16); f[0][2 * j + 1] = __uint_as_float(t & 0xffff0000u); }
    PINV(f[0]);
    asm volatile("s_waitcnt vmcnt(2)" : "+v"(fl) : "v"(pb.pk[0]), "v"(pb.pk[1]), "v"(pb.pk[2]), "v"(pb.pk[3]), "v"(pb.pk[4]), "v"(pb.pk[5]), "v"(pb.pk[6]), "v"(pb.pk[7]),
                 "v"(pb.pk[8]), "v"(pb.pk[9]), "v"(pb.pk[10]), "v"(pb.pk[11]), "v"(pb.pk[12]), "v"(pb.pk[13]), "v"(pb.pk[14]), "v"(pb.pk[15]) : "memory");
    unsigned got = 0xffffff01u, spin = 1u << 21, fresh, so, f2;
#define POLL(R) "buffer_load_dword %[" R "], %[lr], %[sh], %[flg] offen sc1\n\t"
#define SEEN(R) "v_cmp_eq_u32_e32 vcc, %[tau], %[" R "]\n\ts_andn2_b32 %[fr], vcc_lo, %[got]\n\ts_cbranch_scc1 .Lproc_%=\n\t"
    asm volatile(
        SEEN("fl") POLL("fl") "s_sleep 4\n"
        ".Lidle_%=:\n\t" POLL("f2") "s_waitcnt vmcnt(1)\n\t" SEEN("fl") "s_sleep %[slp]\n\t" POLL("fl") "s_waitcnt vmcnt(1)\n\t" SEEN("f2")
        "s_sleep %[slp]\n\ts_sub_u32 %[spin], %[spin], 1\n\ts_cbranch_scc0 .Lidle_%=\n\ts_branch .Ldone_%=\n"
        ".Lproc_%=:\n\ts_or_b32 %[got], %[got], %[fr]\n\t"
        "s_bitcmp1_b32 %[fr], 1\n\ts_cbranch_scc0 .La1_%=\n\ts_add_u32 %[so], %[ro], 65536\n\ts_bitset0_b32 %[so], 19\n\t"
        "buffer_load_dwordx4 v[152:155], %[l16], %[sp], %[so] offen sc1\n\tbuffer_load_dwordx4 v[156:159], %[l16], %[sp], %[so] offen offset:1024 sc1\n.La1_%=:\n\t"
        "s_bitcmp1_b32 %[fr], 2\n\ts_cbranch_scc0 .La2_%=\n\ts_add_u32 %[so], %[ro], 131072\n\ts_bitset0_b32 %[so], 19\n\t"
        "buffer_load_dwordx4 v[168:171], %[l16], %[sp], %[so] offen sc1\n\tbuffer_load_dwordx4 v[172:175], %[l16], %[sp], %[so] offen offset:1024 sc1\n.La2_%=:\n\t"
        "s_bitcmp1_b32 %[fr], 3\n\ts_cbranch_scc0 .La3_%=\n\ts_add_u32 %[so], %[ro], 196608\n\ts_bitset0_b32 %[so], 19\n\t"
        "buffer_load_dwordx4 v[184:187], %[l16], %[sp], %[so] offen sc1\n\tbuffer_load_dwordx4 v[188:191], %[l16], %[sp], %[so] offen offset:1024 sc1\n.La3_%=:\n\t"
        "s_bitcmp1_b32 %[fr], 4\n\ts_cbranch_scc0 .La4_%=\n\ts_add_u32 %[so], %[ro], 262144\n\ts_bitset0_b32 %[so], 19\n\t"
        "buffer_load_dwordx4 v[200:203], %[l16], %[sp], %[so] offen sc1\n\tbuffer_load_dwordx4 v[204:207], %[l16], %[sp], %[so] offen offset:1024 sc1\n.La4_%=:\n\t"
        "s_bitcmp1_b32 %[fr], 5\n\ts_cbranch_scc0 .La5_%=\n\ts_add_u32 %[so], %[ro], 327680\n\ts_bitset0_b32 %[so], 19\n\t"
        "buffer_load_dwordx4 v[216:219], %[l16], %[sp], %[so] offen sc1\n\tbuffer_load_dwordx4 v[220:223], %[l16], %[sp], %[so] offen offset:1024 sc1\n.La5_%=:\n\t"
        "s_bitcmp1_b32 %[fr], 6\n\ts_cbranch_scc0 .La6_%=\n\ts_add_u32 %[so], %[ro], 393216\n\ts_bitset0_b32 %[so], 19\n\t"
        "buffer_load_dwordx4 v[232:235], %[l16], %[sp], %[so] offen sc1\n\tbuffer_load_dwordx4 v[236:239], %[l16], %[sp], %[so] offen offset:1024 sc1\n.La6_%=:\n\t"
        "s_bitcmp1_b32 %[fr], 7\n\ts_cbranch_scc0 .La7_%=\n\ts_add_u32 %[so], %[ro], 458752\n\ts_bitset0_b32 %[so], 19\n\t"
        "buffer_load_dwordx4 v[248:251], %[l16], %[sp], %[so] offen sc1\n\tbuffer_load_dwordx4 v[252:255], %[l16], %[sp], %[so] offen offset:1024 sc1\n.La7_%=:\n\t"
        "s_waitcnt vmcnt(0)\n\ts_cmp_eq_u32 %[got], -1\n\ts_cbranch_scc1 .Ltok_%=\n\t" POLL("fl") "s_branch .Lwide_%=\n"
        ".Ltok_%=:\n\tbuffer_load_dword %[fl], %[l8], %[sh], %[tok] offen sc1\n"
        ".Lwide_%=:\n\t"
        "s_bitcmp1_b32 %[fr], 1\n\ts_cbranch_scc0 .Lb1_%=\n\t"
        "v_lshlrev_b32_e32 v144, 16, v152\n\tv_and_b32_e32 v145, %[hi], v152\n\tv_lshlrev_b32_e32 v146, 16, v153\n\tv_and_b32_e32 v147, %[hi], v153\n\t"
        "v_lshlrev_b32_e32 v148, 16, v154\n\tv_and_b32_e32 v149, %[hi], v154\n\tv_lshlrev_b32_e32 v150, 16, v155\n\tv_and_b32_e32 v151, %[hi], v155\n\t"
        "v_lshlrev_b32_e32 v152, 16, v156\n\tv_and_b32_e32 v153, %[hi], v156\n\tv_lshlrev_b32_e32 v154, 16, v157\n\tv_and_b32_e32 v155, %[hi], v157\n\t"
        "v_lshlrev_b32_e32 v156, 16, v158\n\tv_and_b32_e32 v157, %[hi], v158\n\tv_lshlrev_b32_e32 v158, 16, v159\n\tv_and_b32_e32 v159, %[hi], v159\n\t"
        ".Lb1_%=:\n\t"
        "s_bitcmp1_b32 %[fr], 2\n\ts_cbranch_scc0 .Lb2_%=\n\t"
        "v_lshlrev_b32_e32 v160, 16, v168\n\tv_and_b32_e32 v161, %[hi], v168\n\tv_lshlrev_b32_e32 v162, 16, v169\n\tv_and_b32_e32 v163, %[hi], v169\n\t"
        "v_lshlrev_b32_e32 v164, 16, v170\n\tv_and_b32_e32 v165, %[hi], v170\n\tv_lshlrev_b32_e32 v166, 16, v171\n\tv_and_b32_e32 v167, %[hi], v171\n\t"
        "v_lshlrev_b32_e32 v168, 16, v172\n\tv_and_b32_e32 v169, %[hi], v172\n\tv_lshlrev_b32_e32 v170, 16, v173\n\tv_and_b32_e32 v171, %[hi], v173\n\t"
        "v_lshlrev_b32_e32 v172, 16, v174\n\tv_and_b32_e32 v173, %[hi], v174\n\tv_lshlrev_b32_e32 v174, 16, v175\n\tv_and_b32_e32 v175, %[hi], v175\n\t"
        ".Lb2_%=:\n\t"
        "s_bitcmp1_b32 %[fr], 3\n\ts_cbranch_scc0 .Lb3_%=\n\t"
        "v_lshlrev_b32_e32 v176, 16, v184\n\tv_and_b32_e32 v177, %[hi], v184\n\tv_lshlrev_b32_e32 v178, 16, v185\n\tv_and_b32_e32 v179, %[hi], v185\n\t"
        "v_lshlrev_b32_e32 v180, 16, v186\n\tv_and_b32_e32 v181, %[hi], v186\n\tv_lshlrev_b32_e32 v182, 16, v187\n\tv_and_b32_e32 v183, %[hi], v187\n\t"
        "v_lshlrev_b32_e32 v184, 16, v188\n\tv_and_b32_e32 v185, %[hi], v188\n\tv_lshlrev_b32_e32 v186, 16, v189\n\tv_and_b32_e32 v187, %[hi], v189\n\t"
        "v_lshlrev_b32_e32 v188, 16, v190\n\tv_and_b32_e32 v189, %[hi], v190\n\tv_lshlrev_b32_e32 v190, 16, v191\n\tv_and_b32_e32 v191, %[hi], v191\n\t"
        ".Lb3_%=:\n\t"
        "s_bitcmp1_b32 %[fr], 4\n\ts_cbranch_scc0 .Lb4_%=\n\t"
        "v_lshlrev_b32_e32 v192, 16, v200\n\tv_and_b32_e32 v193, %[hi], v200\n\tv_lshlrev_b32_e32 v194, 16, v201\n\tv_and_b32_e32 v195, %[hi], v201\n\t"
        "v_lshlrev_b32_e32 v196, 16, v202\n\tv_and_b32_e32 v197, %[hi], v202\n\tv_lshlrev_b32_e32 v198, 16, v203\n\tv_and_b32_e32 v199, %[hi], v203\n\t"
        "v_lshlrev_b32_e32 v200, 16, v204\n\tv_and_b32_e32 v201, %[hi], v204\n\tv_lshlrev_b32_e32 v202, 16, v205\n\tv_and_b32_e32 v203, %[hi], v205\n\t"
        "v_lshlrev_b32_e32 v204, 16, v206\n\tv_and_b32_e32 v205, %[hi], v206\n\tv_lshlrev_b32_e32 v206, 16, v207\n\tv_and_b32_e32 v207, %[hi], v207\n\t"
        ".Lb4_%=:\n\t"
        "s_bitcmp1_b32 %[fr], 5\n\ts_cbranch_scc0 .Lb5_%=\n\t"
        "v_lshlrev_b32_e32 v208, 16, v216\n\tv_and_b32_e32 v209, %[hi], v216\n\tv_lshlrev_b32_e32 v210, 16, v217\n\tv_and_b32_e32 v211, %[hi], v217\n\t"
        "v_lshlrev_b32_e32 v212, 16, v218\n\tv_and_b32_e32 v213, %[hi], v218\n\tv_lshlrev_b32_e32 v214, 16, v219\n\tv_and_b32_e32 v215, %[hi], v219\n\t"
        "v_lshlrev_b32_e32 v216, 16, v220\n\tv_and_b32_e32 v217, %[hi], v220\n\tv_lshlrev_b32_e32 v218, 16, v221\n\tv_and_b32_e32 v219, %[hi], v221\n\t"
        "v_lshlrev_b32_e32 v220, 16, v222\n\tv_and_b32_e32 v221, %[hi], v222\n\tv_lshlrev_b32_e32 v222, 16, v223\n\tv_and_b32_e32 v223, %[hi], v223\n\t"
        ".Lb5_%=:\n\t"
        "s_bitcmp1_b32 %[fr], 6\n\ts_cbranch_scc0 .Lb6_%=\n\t"
        "v_lshlrev_b32_e32 v224, 16, v232\n\tv_and_b32_e32 v225, %[hi], v232\n\tv_lshlrev_b32_e32 v226, 16, v233\n\tv_and_b32_e32 v227, %[hi], v233\n\t"
        "v_lshlrev_b32_e32 v228, 16, v234\n\tv_and_b32_e32 v229, %[hi], v234\n\tv_lshlrev_b32_e32 v230, 16, v235\n\tv_and_b32_e32 v231, %[hi], v235\n\t"
        "v_lshlrev_b32_e32 v232, 16, v236\n\tv_and_b32_e32 v233, %[hi], v236\n\tv_lshlrev_b32_e32 v234, 16, v237\n\tv_and_b32_e32 v235, %[hi], v237\n\t"
        "v_lshlrev_b32_e32 v236, 16, v238\n\tv_and_b32_e32 v237, %[hi], v238\n\tv_lshlrev_b32_e32 v238, 16, v239\n\tv_and_b32_e32 v239, %[hi], v239\n\t"
        ".Lb6_%=:\n\t"
        "s_bitcmp1_b32 %[fr], 7\n\ts_cbranch_scc0 .Lb7_%=\n\t"
        "v_lshlrev_b32_e32 v240, 16, v248\n\tv_and_b32_e32 v241, %[hi], v248\n\tv_lshlrev_b32_e32 v242, 16, v249\n\tv_and_b32_e32 v243, %[hi], v249\n\t"
        "v_lshlrev_b32_e32 v244, 16, v250\n\tv_and_b32_e32 v245, %[hi], v250\n\tv_lshlrev_b32_e32 v246, 16, v251\n\tv_and_b32_e32 v247, %[hi], v251\n\t"
        "v_lshlrev_b32_e32 v248, 16, v252\n\tv_and_b32_e32 v249, %[hi], v252\n\tv_lshlrev_b32_e32 v250, 16, v253\n\tv_and_b32_e32 v251, %[hi], v253\n\t"
        "v_lshlrev_b32_e32 v252, 16, v254\n\tv_and_b32_e32 v253, %[hi], v254\n\tv_lshlrev_b32_e32 v254, 16, v255\n\tv_and_b32_e32 v255, %[hi], v255\n\t"
        ".Lb7_%=:\n\t"
        "s_cmp_eq_u32 %[got], -1\n\ts_cbranch_scc0 .Lidle_%=\n"
        ".Ldone_%=:"
        : "=&{v[144:159]}"(f[1]), "=&{v[160:175]}"(f[2]), "=&{v[176:191]}"(f[3]), "=&{v[192:207]}"(f[4]), "=&{v[208:223]}"(f[5]), "=&{v[224:239]}"(f[6]), "=&{v[240:255]}"(f[7]),
          [fl] "+v"(fl), [f2] "=&v"(f2), [got] "+s"(got), [spin] "+s"(spin), [fr] "=&s"(fresh), [so] "=&s"(so)
        : [tau] "s"(tau), [l16] "v"(l16), [l8] "v"(l8), [lr] "v"(lr), [sp] "s"(pb.srd), [sh] "s"(srdH), [ro] "s"(xg * 73728u + w * 2048u), [tok] "s"(tok_off),
          [flg] "s"(flg_off + xg * 128u), [hi] "s"(0xffff0000u), [slp] "i"(isA ? 8 : 0)
        : "vcc", "scc", "memory");
#undef POLL
#undef SEEN
    const f32x16 t = ((f[0] + f[1]) + (f[2] + f[3])) + ((f[4] + f[5]) + (f[6] + f[7]));
#pragma unroll
    for (int s = 0; s < 2; ++s)
    {
      const u32x4 ov = natural8(u32x4{cvtpk(t[8 * s], t[8 * s + 1]), cvtpk(t[8 * s + 2], t[8 * s + 3]), cvtpk(t[8 * s + 4], t[8 * s + 5]), cvtpk(t[8 * s + 6], t[8 * s + 7])});
      __builtin_amdgcn_raw_buffer_store_b128(ov, mk_srd(out, 2097152u), oo + s * 64, 0, 2);
    }
    asm volatile("s_waitcnt vmcnt(2)" : "+v"(fl) :: "memory");
    if ((lane >> 1) == w && fl != toks) asm volatile("buffer_store_dword %0, %1, %2, %3 offen sc1" :: "v"(fl), "v"(fo), "s"(srdH), "s"(flg_off) : "memory");
  }
}

__global__ __launch_bounds__(256, 1)
void fused_attn(const char* __restrict__ Ax, const char* __restrict__ Ak, const char* __restrict__ Av, const char* __restrict__ Awq, const char* __restrict__ Awo,
                const char* __restrict__ Bx, const char* __restrict__ Bk, const char* __restrict__ Bv, const char* __restrict__ Bwq, const char* __restrict__ Bwo,
                char* __restrict__ part, char* __restrict__ out) {
  const char* pc; unsigned left;
  asm volatile("s_getpc_b64 %0\n.Lpc_%=:\n\ts_mov_b32 %1, .Lhot_end-.Lpc_%=" : "=s"(pc), "=s"(left));
  const srd_t code = mk_srd(pc, left);
  if (blockIdx.x >= 128) { body<true>(Ax, Ak, Av, Awq, Awo, Bwo, code, part, out); asm volatile(".Lhot_end:"); __builtin_amdgcn_endpgm(); }
  body<false>(Bx, Bk, Bv, Bwq, Bwo, Bwo, code, part, out);
}

extern "C" void kernel_launch(void* const* d_in, const int* in_sizes, int n_in, void* d_out, int out_size, void* d_ws, size_t ws_size, hipStream_t stream) {
  (void)in_sizes; (void)n_in; (void)out_size; (void)ws_size;
  (void)hipFuncSetAttribute((const void*)fused_attn, hipFuncAttributeMaxDynamicSharedMemorySize, (int)LDS_BYTES);
  fused_attn<<<256, 256, LDS_BYTES, stream>>>((const char*)d_in[0], (const char*)d_in[1], (const char*)d_in[2], (const char*)d_in[3], (const char*)d_in[4],
                                              (const char*)d_in[5], (const char*)d_in[6], (const char*)d_in[7], (const char*)d_in[8], (const char*)d_in[9], (char*)d_ws, (char*)d_out);
}
```

```cpp
#include <hip/hip_runtime.h>
#include <hip/hip_bf16.h>
#include <utility>

typedef __bf16 bf16x8 __attribute__((ext_vector_type(8)));
typedef __bf16 bf16x4 __attribute__((ext_vector_type(4)));
typedef __bf16 bf16x2 __attribute__((ext_vector_type(2)));
typedef float f32x4 __attribute__((ext_vector_type(4)));
typedef float f32x2 __attribute__((ext_vector_type(2)));
typedef float f32x16 __attribute__((ext_vector_type(16)));
typedef unsigned u32x4 __attribute__((ext_vector_type(4)));
#define LAS __attribute__((address_space(3)))
#define DI __device__ __forceinline__
typedef __amdgpu_buffer_rsrc_t srd_t;
template <int... Is> using iseq = std::integer_sequence<int, Is...>;
template <int N> using make_iseq = std::make_integer_sequence<int, N>;

#define KV_NT 1
#define W_NT 0
#define X_NT 0
#define SLEEP_A 8
#define SLEEP_B 0
#define PACE_A 0
#define PACE_ATT 0
#define TOUCH_Q 1
#define TOUCH_O 3
#define TO_SKIP 16u
#define TO_B 8u
#define WARM_CODE 40
#define ALT 0
#define ORD 2
#define EARLY_O 0
#define TOUCH_B 4
#define PUB_SLEEP 3
#define KVPOL "nt"
#define CVV 0
#define DELAY_A 0
#define SK 9
#define SV 11
#define SP 13
#define OUTPOL 2
#define STAG 0
#define RAMP 0
#define PUBPOL "sc0 sc1"
#define ILV_A 1
#define ILV_B 1
#define LDPOL "sc1"
#define PLPOL "sc1"
#define FLPOL "sc1"
#define BARM 0x3ffe
#define LATEW 1
#define SKIPOWN 1
#define WS_END 0

constexpr unsigned SLOT = 8192, LDS_BYTES = 20 * SLOT;
constexpr unsigned POFF = (unsigned)WS_END << 20, HOFF = POFF + (16u << 20);
constexpr float SC = 0.12751743082f;
constexpr float LMAX = 1048576.0f;

#define NO_REDUCE_STAMPS
#define GAP() __builtin_amdgcn_sched_barrier(0)
#define STAMP(i) do {} while (0)
#define CYC(i) do {} while (0)
#define PINS(x) asm volatile("" : "+s"(x))
#define PINV(x) asm volatile("" : "+v"(x))
DI unsigned rfl(unsigned x) { return __builtin_amdgcn_readfirstlane(x); }
DI unsigned cvtpk(float lo, float hi) { f32x2 v = {lo, hi}; bf16x2 b = __builtin_convertvector(v, bf16x2); return __builtin_bit_cast(unsigned, b); }
DI u32x4 pack2(const f32x4& a, const f32x4& b) { return u32x4{cvtpk(a[0], a[1]), cvtpk(a[2], a[3]), cvtpk(b[0], b[1]), cvtpk(b[2], b[3])}; }
DI u32x4 natural8(u32x4 w) {
  auto s0 = __builtin_amdgcn_permlane32_swap(w[0], w[2], false, false);
  auto s1 = __builtin_amdgcn_permlane32_swap(w[1], w[3], false, false);
  auto t0 = __builtin_amdgcn_permlane16_swap(s0[0], s0[1], false, false);
  auto t1 = __builtin_amdgcn_permlane16_swap(s1[0], s1[1], false, false);
  return u32x4{t0[0], t1[0], t0[1], t1[1]};
}
#define MF "v_mfma_f32_16x16x32_bf16 "
template <bool Z> DI void mm_vvv(f32x4& c, const bf16x8& a, const bf16x8& b) {
  if constexpr (Z) asm volatile(MF "%0, %1, %2, 0" : "=&v"(c) : "v"(a), "v"(b));
  else             asm volatile(MF "%0, %1, %2, %0" : "+v"(c) : "v"(a), "v"(b));
}
template <bool Z> DI void mm_vva(f32x4& c, const bf16x8& a, const bf16x8& b) {
  if constexpr (Z) asm volatile(MF "%0, %1, %2, 0" : "=&v"(c) : "v"(a), "a"(b));
  else             asm volatile(MF "%0, %1, %2, %0" : "+v"(c) : "v"(a), "a"(b));
}
DI void mm_vca(f32x4& d, const bf16x8& a, const bf16x8& b, const f32x4& c) { asm volatile(MF "%0, %1, %2, %3" : "=&v"(d) : "v"(a), "a"(b), "v"(c)); }
DI void mm_avv(f32x4& c, const bf16x8& a, const bf16x8& b) { asm volatile(MF "%0, %1, %2, %0" : "+a"(c) : "v"(a), "v"(b)); }
DI void set_m0(unsigned v) { asm volatile("s_mov_b32 m0, %0" :: "s"(v) : "memory"); }
template <int NT, int P> DI void dma_p(unsigned voff, srd_t srd, unsigned soff) {
  if constexpr (NT) asm volatile("buffer_load_dwordx4 %0, %1, %2 offen offset:%3 " KVPOL " lds" :: "v"(voff), "s"(srd), "s"(soff), "i"(1024 * P) : "memory");
  else              asm volatile("buffer_load_dwordx4 %0, %1, %2 offen offset:%3 lds" :: "v"(voff), "s"(srd), "s"(soff), "i"(1024 * P) : "memory");
}
struct Src { unsigned v[4]; srd_t srd; };
template <int NT> DI void dma_unit(const Src& s, unsigned soff, unsigned ldsaddr) {
  asm volatile("s_mov_b32 m0, %0\n\ts_nop 0" :: "s"(ldsaddr) : "memory");
  dma_p<NT, 0>(s.v[0], s.srd, soff); dma_p<NT, 1>(s.v[1], s.srd, soff); dma_p<NT, 2>(s.v[2], s.srd, soff); dma_p<NT, 3>(s.v[3], s.srd, soff);
}
template <int NT> DI void dma_elem(const Src& s, unsigned soff, unsigned ustep, unsigned ldsaddr) { dma_unit<NT>(s, soff, ldsaddr); dma_unit<NT>(s, soff + ustep, ldsaddr + 4096u); }
template <int N> DI void wait_vm() { asm volatile("s_waitcnt vmcnt(%0)" :: "i"(N) : "memory"); }
template <int N, int L> DI void wait_vm_lgkm() { asm volatile("" ::: "memory"); __builtin_amdgcn_s_waitcnt((N & 15) | ((N >> 4) << 14) | 0x70 | (L << 8)); asm volatile("" ::: "memory"); }
DI void wait_lgkm0() { wait_vm_lgkm<63, 0>(); }
DI void barrier() { asm volatile("s_barrier" ::: "memory"); }
DI bf16x8 lds128(unsigned a) { return *(const LAS bf16x8*)(size_t)(a); }
DI bf16x4 lds_tr1(unsigned a) { return __builtin_amdgcn_ds_read_tr16_b64_v4bf16((LAS bf16x4*)(size_t)(a)); }
DI bf16x8 cat(bf16x4 lo, bf16x4 hi) { return __builtin_shufflevector(lo, hi, 0, 1, 2, 3, 4, 5, 6, 7); }
DI bf16x8 lds_tr(unsigned a0, unsigned a1) { return cat(lds_tr1(a0), lds_tr1(a1)); }
DI srd_t mk_srd(const void* base, unsigned bytes) { return __builtin_amdgcn_make_buffer_rsrc((void*)base, (short)0, (int)bytes, 0x00020000); }
template <bool VX> DI void mk_src_rows(Src& s, const char* base, unsigned bytes, unsigned stride, unsigned lane) {
  s.srd = mk_srd(base, bytes);
  const unsigned row = (lane >> 2) & 3u, cb = lane >> 4, sl = lane & 3u;
#pragma unroll
  for (int p = 0; p < 4; ++p) s.v[p] = row * stride + cb * 64u + ((sl ^ (VX ? 2 * (p & 1) : (p & 2))) << 4) + (4u * stride - 1024u) * p;
}
template <bool VX> DI void mk_src_w(Src& s, const char* base, unsigned bytes, unsigned stride, unsigned lane) {
  s.srd = mk_srd(base, bytes);
  const unsigned sub = lane >> 5, cb = (lane >> 4) & 1u, row = (lane >> 2) & 3u, sl = lane & 3u;
#pragma unroll
  for (int p = 0; p < 4; ++p) s.v[p] = (4u * sub + row) * stride + cb * 64u + ((sl ^ (VX ? 2u * sub : 2u * (p & 1))) << 4) + (8u * stride - 1024u) * p;
}
template <int NT> DI void touch(unsigned& d, unsigned voff, srd_t srd, unsigned soff) {
  if constexpr (NT) asm volatile("buffer_load_dword %0, %1, %2, %3 offen nt" : "+v"(d) : "v"(voff), "s"(srd), "s"(soff) : "memory");
  else              asm volatile("buffer_load_dword %0, %1, %2, %3 offen" : "+v"(d) : "v"(voff), "s"(srd), "s"(soff) : "memory");
}
DI unsigned ksoff(unsigned m, unsigned rk) { return ((m + rk) & 15u) * 8192u; }

struct PC {
  unsigned r01, r2;
  unsigned w01[2], w2[2];
  unsigned xb, xmask, xa;
  unsigned z, soff;
};
template <unsigned TS_, unsigned QS_, unsigned ZI_, unsigned ZM_, unsigned ZO_, unsigned US_, unsigned CS_, bool ROT_> struct PK {
  static constexpr unsigned TS = TS_, QS = QS_, ZI = ZI_, ZM = ZM_, ZO = ZO_, US = US_, CS = CS_; static constexpr bool ROT = ROT_; };
DI unsigned q_soff(unsigned z) { return ((z & 0xC300u) * 0x440u) & 0x3C0000u; }
template <int POS> DI unsigned ring_lds(const PC& c) { return POS == 2 ? c.r2 : c.r01 + POS * SLOT; }
template <int POS> DI unsigned ring_rd(const PC& c, int d) { return (POS == 2 ? c.w2[d & 1] : c.w01[d & 1] + POS * SLOT) + (d >> 1) * 256u; }
struct NoX { static constexpr bool ACT = true; template <int G> DI void run() const {} };
template <class K, int POS, int S, bool Z, bool RD, bool DM, int NT, bool XT, int M, class X>
DI void proj_gap(f32x4 (&acc)[8][4], const bf16x8 (&cur)[4], bf16x8 (&nxt)[4], bf16x4 (&lo)[4], bf16x8 (&bf)[4], PC& c, unsigned soff, const Src& src, const Src& xs, unsigned xsoff, const X& ex) {
  constexpr int qb = M >> 2, db = M & 3;
  mm_vvv<Z>(acc[4 * S + db][qb], cur[db], bf[qb]);
  if constexpr (RD && M < 8) {
    constexpr int d = M >> 1; const unsigned a = S == 0 ? ring_rd<POS>(c, d) + 4096u : ring_rd<(POS + 1) % 3>(c, d);
    if constexpr (M & 1) nxt[d] = cat(lo[d], lds_tr1(a + K::TS)); else lo[d] = lds_tr1(a);
  }
  if constexpr (X::ACT && RD && S == 1 && db == 3) bf[qb] = lds128(c.xa + qb * K::QS);
  if constexpr (XT) {
    if constexpr (M < 8 && !(M & 1)) dma_p<X_NT, M / 2>(xs.v[M / 2], xs.srd, xsoff);
    if constexpr (M == 7) set_m0(ring_lds<POS>(c) + S * 4096u);
    if constexpr (M >= 8 && !(M & 1)) dma_p<NT, (M - 8) / 2>(src.v[(M - 8) / 2], src.srd, soff);
  } else if constexpr (DM && M >= 8 && !(M & 1)) dma_p<NT, (M - 8) / 2>(src.v[(M - 8) / 2], src.srd, soff);
  if constexpr (X::ACT && S == 0 && M == 9) { PINS(c.z); c.z = (c.z + K::ZI) & K::ZM; PINS(c.z); }
  if constexpr (X::ACT && S == 0 && M == 11) { c.xa = (c.z & c.xmask) | c.xb; PINV(c.xa); }
  if constexpr (S == 1 && DM && !K::ROT && M == 9) { PINS(c.soff); c.soff = (c.soff + K::CS) & (16u * K::CS - 1u); PINS(c.soff); }
  if constexpr (S == 1 && DM && K::ROT && M == 9) { PINS(c.z); c.soff = (c.z + 0x3100u) & 0xC300u; PINS(c.soff); }
  if constexpr (S == 1 && DM && K::ROT && M == 13) { PINS(c.soff); c.soff = (c.soff * 0x440u) & 0x3C0000u; PINS(c.soff); }
  ex.template run<16 * S + M>();
  GAP();
}
template <class K, int POS, int S, bool Z, bool RD, bool DM, int NT, int VM, bool BAR, bool XT, class X, int... Ms>
DI void proj_unit(f32x4 (&acc)[8][4], const bf16x8 (&cur)[4], bf16x8 (&nxt)[4], bf16x8 (&bf)[4], PC& c, const Src& src, const Src& xs, unsigned xsoff, unsigned xdst, const X& ex, iseq<Ms...>) {
  wait_vm_lgkm<(RD ? VM : 63), (S == 0 ? 2 : 0)>();
  if constexpr (BAR) barrier();
  const unsigned soff = c.soff + S * K::US;
  if constexpr (XT) set_m0(xdst); else if constexpr (DM) set_m0(ring_lds<POS>(c) + S * 4096u);
  GAP();
  bf16x4 lo[4];
  (proj_gap<K, POS, S, Z, RD, DM, NT, XT, Ms>(acc, cur, nxt, lo, bf, c, soff, src, xs, xsoff, ex), ...);
}
template <class K, int POS, bool Z, bool LAST, bool DM, int NT, int VM0, int VM1, bool BAR1 = false, bool XT0 = false, bool XT1 = false, class X = NoX>
DI void proj_elem(f32x4 (&acc)[8][4], bf16x8 (&a0)[4], bf16x8 (&a1)[4], bf16x8 (&bf)[4], PC& c, const Src& src, const X& ex = X{},
                  const Src* xs = nullptr, unsigned xso0 = 0, unsigned xd0 = 0, unsigned xso1 = 0, unsigned xd1 = 0) {
  proj_unit<K, POS, 0, Z, true, DM, NT, VM0, false, XT0>(acc, a0, a1, bf, c, src, xs ? *xs : src, xso0, xd0, ex, make_iseq<16>{});
  proj_unit<K, POS, 1, Z, !LAST, DM, NT, VM1, BAR1, XT1>(acc, a1, a0, bf, c, src, xs ? *xs : src, xso1, xd1, ex, make_iseq<16>{});
}
template <class K> DI void proj_setup(PC& c, unsigned tb, unsigned xb, unsigned z0, unsigned s3) {
  c.w01[0] = c.r01 + tb; c.w01[1] = c.w01[0] ^ 32u; c.w2[0] = c.r2 + tb; c.w2[1] = c.w2[0] ^ 32u;
  PINV(c.w01[0]); PINV(c.w01[1]); PINV(c.w2[0]); PINV(c.w2[1]);
  c.xb = xb; c.xmask = K::ZO; PINV(c.xmask);
  c.z = z0; c.soff = s3;
}
template <class K, int POS> DI void proj_start(bf16x8 (&a0)[4], bf16x8 (&bf)[4], PC& c, unsigned tb, unsigned xb, unsigned z0, unsigned s3) {
  proj_setup<K>(c, tb, xb, z0, s3);
  c.xa = (c.z & K::ZO) | xb; PINV(c.xa);
#pragma unroll
  for (int d = 0; d < 4; ++d) { const unsigned a = ring_rd<POS>(c, d); a0[d] = lds_tr(a, a + K::TS); }
  GAP();
#pragma unroll
  for (int q = 0; q < 4; ++q) bf[q] = lds128(c.xa + q * K::QS);
  GAP();
}
#define ACC_ALL(C, a) C(a[0][0]), C(a[0][1]), C(a[0][2]), C(a[0][3]), C(a[1][0]), C(a[1][1]), C(a[1][2]), C(a[1][3]), C(a[2][0]), C(a[2][1]), C(a[2][2]), C(a[2][3]), C(a[3][0]), C(a[3][1]), C(a[3][2]), C(a[3][3])
DI void acc_fence_v(f32x4 (&acc)[8][4]) {
  asm volatile("s_nop 7\n\ts_nop 3" : ACC_ALL("+v", acc) :: "memory");
  f32x4 (&hi)[4][4] = *(f32x4 (*)[4][4])&acc[4];
  asm volatile("" : ACC_ALL("+v", hi) :: "memory");
  GAP();
}
DI void acc_fence_a(f32x4 (&acc)[8][4]) {
  asm volatile("s_nop 7\n\ts_nop 3" : ACC_ALL("+a", acc) :: "memory");
  f32x4 (&hi)[4][4] = *(f32x4 (*)[4][4])&acc[4];
  asm volatile("" : ACC_ALL("+a", hi) :: "memory");
  GAP();
}

struct Stash { u32x4 p[64]; unsigned base[3]; };
template <int I> DI void stash_load1(Stash& st, unsigned vk, const Src& sK, const Src& sV, unsigned rk, unsigned wo_) {
  constexpr int m = I >> 2, isV = (I >> 1) & 1, pc_ = I & 1;
  const unsigned so = ksoff(m, rk) + wo_;
  if constexpr (I < 56) asm volatile("buffer_load_dwordx4 %0, %1, %2, %3 offen offset:%4 " KVPOL : "=a"(st.p[I]) : "v"(isV ? sV.v[pc_] : vk), "s"(isV ? sV.srd : sK.srd), "s"(so), "i"(1024 * pc_) : "memory");
  else                  asm volatile("buffer_load_dwordx4 %0, %1, %2, %3 offen offset:%4 " KVPOL : "=v"(st.p[I]) : "v"(isV ? sV.v[pc_] : vk), "s"(isV ? sV.srd : sK.srd), "s"(so), "i"(1024 * pc_) : "memory");
}
template <int... Is> DI void stash_load(Stash& st, unsigned vk, const Src& sK, const Src& sV, unsigned rk, unsigned wo_, iseq<Is...>) { (stash_load1<Is>(st, vk, sK, sV, rk, wo_), ...); }
constexpr int kv_pair(int m) { constexpr int t[10] = {0, 1, 2, 3, 8, 9, 4, 5, 6, 7}; return t[m % 10]; }
constexpr unsigned long long kv_tab() { unsigned long long v = 0; for (int i = 0; i < 16; ++i) v |= (unsigned long long)kv_pair(i) << (4 * i); return v; }
template <int I> DI void stash_write1(const Stash& st) {
  constexpr int m = I >> 2, isV = (I >> 1) & 1, pc_ = I & 1, off = kv_pair(m) * 16384 + isV * 8192 + pc_ * 1024;
  if constexpr (I < 56) asm volatile("ds_write_b128 %0, %1 offset:%2" :: "v"(st.base[off >> 16]), "a"(st.p[I]), "i"(off & 65535) : "memory");
  else                  asm volatile("ds_write_b128 %0, %1 offset:%2" :: "v"(st.base[off >> 16]), "v"(st.p[I]), "i"(off & 65535) : "memory");
}
template <int... Is> DI void stash_late(const Stash& st, iseq<Is...>) { (stash_write1<40 + Is>(st), ...); }

struct AC {
  f32x4 o[8][4];
  bf16x8 q[4][4];
  bf16x8 kf[8], vf[8], pf[4];
  f32x4 negm[4];
  f32x4 l[4];
  bf16x8 ones;
};
typedef f32x4 ST[2][4];
constexpr int NEB = 22;
template <int E> DI float sm_val(ST& S) { return S[(E >> 2) & 1][E >> 3][E & 3]; }
template <int E> DI void sm_exp1(ST& S) { float a; asm volatile("v_exp_f32_e32 %0, %1" : "=v"(a) : "v"(sm_val<E>(S))); S[(E >> 2) & 1][E >> 3][E & 3] = a; }
template <int QB, int P> DI void sm_cvt(ST& S, AC& c) {
  u32x4 w = __builtin_bit_cast(u32x4, c.pf[QB]);
  unsigned r;
  asm volatile("v_cvt_pk_bf16_f32 %0, %1, %2" : "=v"(r) : "v"(sm_val<8 * QB + 2 * P>(S)), "v"(sm_val<8 * QB + 2 * P + 1>(S)));
  w[P] = r;
  c.pf[QB] = __builtin_bit_cast(bf16x8, w);
}
template <bool DM, int VM, int R, int M> DI void mem_pre(unsigned ldsaddr) {
  if constexpr (M == 6) wait_lgkm0();
  if constexpr (DM && (M == 6 || M == 14)) set_m0(ldsaddr + (M == 14 ? 4096u : 0u));
  if constexpr (M == R && VM < 63) wait_vm<VM>();
}
template <bool DM, int NT, int M> DI void mem_post(const Src& src, unsigned soff, unsigned ustep) {
  if constexpr (DM && M >= 6 && M <= 20 && !(M & 1)) dma_p<NT, ((M - 6) / 2) & 3>(src.v[((M - 6) / 2) & 3], src.srd, soff + (M >= 14 ? ustep : 0u));
}
template <int WR, int M> DI void mem_wr(const Stash& st) {
  if constexpr (WR >= 40 && M >= 7 && M < 13) stash_write1<WR + M - 7>(st);
  if constexpr (WR >= 0 && WR < 40 && M >= 8 && M < 16)
    asm volatile("ds_write_b128 %0, %1 offset:%2" :: "v"(st.base[0]), "a"(st.p[WR + M - 8]), "i"((M - 8) * 1024) : "memory");
}
struct PA { unsigned rd0, rd1, soff, fill; };
template <bool QK, bool DM, int NT, int VM, int WR, int M, class F>
DI void gapA(AC& c, ST& S, ST& Sn, PA& pa, F& addr, const Src& src, unsigned ustep, const Stash& st, bf16x4& lo) {
  constexpr int R = QK ? 16 : 0;
  if constexpr ((DM || !QK) && M == 0) addr(pa);
  if constexpr (QK) mem_pre<DM, VM, 16, M>(pa.fill); else if constexpr (M == 0 && VM < 63) wait_vm<VM>();
  if constexpr (QK) {
    constexpr int f = M >> 2, s = f >> 1, kb = f & 1, qb = M & 3;
    if constexpr (s == 0) mm_vca(Sn[kb][qb], c.kf[f], c.q[qb][0], c.negm[qb]); else mm_vva<false>(Sn[kb][qb], c.kf[f], c.q[qb][s]);
  }
  mem_post<DM, NT, M>(src, pa.soff, ustep);
  mem_wr<WR, M>(st);
  if constexpr (QK && !DM && M == 10) addr(pa);
  if constexpr (QK && M == 12) asm volatile("" :: "v"(c.negm[0]), "v"(c.negm[1]), "v"(c.negm[2]), "v"(c.negm[3]));
  if constexpr (M >= R && M < R + 16) {
    constexpr int db = (M - R) >> 1; const unsigned a = ((db & 1) ? pa.rd1 : pa.rd0) + (db >> 1) * 256u;
    if constexpr (M & 1) c.vf[db] = cat(lo, lds_tr1(a + 4096u)); else lo = lds_tr1(a);
  }
  if constexpr (NEB + M < 32) sm_exp1<NEB + M>(S);
  if constexpr (M >= 16 && (M & 1)) sm_cvt<2 + ((M - 16) >> 3), ((M - 16) >> 1) & 3>(S, c);
  GAP();
}
template <bool QK, bool DM, int NT, int VM, int WR, class F, int... Ms>
DI void phaseA_(AC& c, ST& S, ST& Sn, F& addr, const Src& src, unsigned ustep, const Stash& st, iseq<Ms...>) {
  PA pa; bf16x4 lo;
  (gapA<QK, DM, NT, VM, WR, Ms>(c, S, Sn, pa, addr, src, ustep, st, lo), ...);
}
template <bool QK, bool DM, int NT, int VM, int WR = -1, class F>
DI void phaseA(AC& c, ST& S, ST& Sn, F addr, const Src& src, unsigned ustep, const Stash& st) {
  phaseA_<QK, DM, NT, VM, WR>(c, S, Sn, addr, src, ustep, st, make_iseq<32>{});
}
template <bool MAX> DI float xlane(float v) {
  auto a = __builtin_amdgcn_permlane32_swap(__float_as_uint(v), __float_as_uint(v), false, false);
  v = MAX ? fmaxf(__uint_as_float(a[0]), __uint_as_float(a[1])) : __uint_as_float(a[0]) + __uint_as_float(a[1]);
  auto b = __builtin_amdgcn_permlane16_swap(__float_as_uint(v), __float_as_uint(v), false, false);
  return MAX ? fmaxf(__uint_as_float(b[0]), __uint_as_float(b[1])) : __uint_as_float(b[0]) + __uint_as_float(b[1]);
}
DI float row_max(const f32x4& x, const f32x4& y) {
  return xlane<true>(fmaxf(fmaxf(fmaxf(x[0], x[1]), fmaxf(x[2], x[3])), fmaxf(fmaxf(y[0], y[1]), fmaxf(y[2], y[3]))));
}
template <bool SM, bool KR, bool DM, int NT, int VM, int WR, int M, class F>
DI void gapB(AC& c, ST& Sn, PA& pa, F& addr, const Src& src, unsigned ustep, const Stash& st) {
  mem_pre<DM, (KR ? VM : 63), 18, M>(pa.fill);
  {
    constexpr int h = M / 18, r = M % 18, qb = 2 * h + (r & 1);
    if constexpr (r < 2) mm_vvv<false>(c.l[qb], c.ones, c.pf[qb]); else mm_avv(c.o[(r - 2) >> 1][qb], c.vf[(r - 2) >> 1], c.pf[qb]);
  }
  mem_post<DM, NT, M>(src, pa.soff, ustep);
  mem_wr<WR, M>(st);
  if constexpr (M == 0) addr(pa);
  if constexpr (KR && M >= 18 && M < 34 && !(M & 1)) { constexpr int f = (M - 18) >> 1; c.kf[f] = lds128(pa.rd0 + (f & 1) * 4096u + (f >> 1) * 256u); }
  if constexpr (SM) {
    if constexpr (M >= 4 && M < 18) sm_exp1<M - 4>(Sn);
    if constexpr (M >= 19 && M < 35 && (M & 1)) sm_exp1<14 + (M - 19) / 2>(Sn);
    if constexpr (M >= 20 && !(M & 1)) sm_cvt<((M - 20) >> 3), ((M - 20) >> 1) & 3>(Sn, c);
  }
  GAP();
}
template <bool SM, bool KR, bool DM, int NT, int VM, int WR, class F, int... Ms>
DI void phaseB_(AC& c, ST& Sn, F& addr, const Src& src, unsigned ustep, const Stash& st, iseq<Ms...>) {
  PA pa;
  (gapB<SM, KR, DM, NT, VM, WR, Ms>(c, Sn, pa, addr, src, ustep, st), ...);
}
template <bool SM, bool KR, bool DM, int NT, int VM, int WR = -1, class F>
DI void phaseB(AC& c, ST& Sn, F addr, const Src& src, unsigned ustep, const Stash& st) {
  phaseB_<SM, KR, DM, NT, VM, WR>(c, Sn, addr, src, ustep, st, make_iseq<36>{});
}
DI void k_read(AC& ac, unsigned ka) {
#pragma unroll
  for (int f = 0; f < 8; ++f) ac.kf[f] = lds128(ka + (f & 1) * 4096u + (f >> 1) * 256u);
}
template <bool DM, int M> DI void qk0_gap(AC& ac, ST& SA, const Src& src, unsigned soff, unsigned fill) {
  constexpr int f = M >> 2, qb = M & 3;
  if constexpr (DM && (M == 0 || M == 16)) set_m0(fill + M * 256u);
  mm_vva<(f < 2)>(SA[f & 1][qb], ac.kf[f], ac.q[qb][f >> 1]);
  if constexpr (DM && qb == 0) dma_p<KV_NT, f & 3>(src.v[f & 3], src.srd, soff + (f >> 2) * 4096u);
  GAP();
}
template <bool DM, int... Ms> DI void qk0(AC& ac, ST& SA, const Src& src, unsigned soff, unsigned fill, iseq<Ms...>) { (qk0_gap<DM, Ms>(ac, SA, src, soff, fill), ...); }
template <int... Js> DI void exp_first(ST& SA, iseq<Js...>) { (sm_exp1<Js>(SA), ...); }
template <int... Js> DI void cvt_first(ST& SA, AC& c, iseq<Js...>) { (sm_cvt<(Js >> 2), (Js & 3)>(SA, c), ...); }
DI void sm_first(AC& ac, ST& SA) {
  asm volatile("s_nop 7\n\ts_nop 3" : "+v"(SA[0][0]), "+v"(SA[0][1]), "+v"(SA[0][2]), "+v"(SA[0][3]), "+v"(SA[1][0]), "+v"(SA[1][1]), "+v"(SA[1][2]), "+v"(SA[1][3]) :: "memory");
  GAP();
#pragma unroll
  for (int q = 0; q < 4; ++q) {
    const float m = row_max(SA[0][q], SA[1][q]);
    SA[0][q] -= m; SA[1][q] -= m;
    ac.negm[q] = f32x4{-m, -m, -m, -m};
    ac.l[q] = f32x4{};
    ac.pf[q] = bf16x8{};
  }
  ac.ones = __builtin_bit_cast(bf16x8, u32x4{0x3f803f80u, 0x3f803f80u, 0x3f803f80u, 0x3f803f80u});
  asm volatile("" : "+v"(ac.negm[0]), "+v"(ac.negm[1]), "+v"(ac.negm[2]), "+v"(ac.negm[3]), "+v"(ac.l[0]), "+v"(ac.l[1]), "+v"(ac.l[2]), "+v"(ac.l[3]), "+v"(ac.ones));
  exp_first(SA, make_iseq<NEB>{});
  cvt_first(SA, ac, make_iseq<8>{});
  GAP();
}
struct BS { unsigned p[4], v[6], k[2], rk, wo, l16; };
constexpr bool b_dma(int f) { return f != 2 * (SK - 3) && f != 2 * (SV - 2) + 1 && f != 2 * (SP - 2) + 1 && f < 30 && !(LATEW && f == 27); }
constexpr int b_vm(int f) {
  const int s = f == 30 ? 26 : f - 3;
  if (!b_dma(s)) return 63;
  int n = b_dma(f) ? 5 + (f & 1) : 0;
  for (int i = s + 1; i < f; ++i) n += b_dma(i) ? 8 : 0;
  return n;
}
template <class KO, int I> DI void b_step(AC& ac, ST& S, ST& Sn, BS& b, const Src& sK, const Src& sV, const Src& sO, Stash& st) {
  constexpr int fa = 2 * I, fb = 2 * I + 1, vi = (I == SP || I == 15) ? 4 : (I & 1) ? 0 : 2;
  const unsigned pa = b.p[(I & 1) ? 3 : 1], pb = b.p[(I & 1) ? 0 : 2];
  if constexpr ((BARM >> I) & 1) barrier();
  if constexpr (I == SK - 3) st.base[0] = pa + b.l16;
  phaseA<(I < 15), b_dma(fa), (I < 14 ? KV_NT : W_NT), b_vm(fa), (I == SK - 3 ? 0 : -1)>(ac, S, Sn, [&](PA& p) __attribute__((always_inline)) {
    PINS(b.rk);
    p.soff = I < 14 ? ksoff(I < 13 ? I + 3 : 15, b.rk) : b.wo + (LATEW ? 0u : KO::CS); p.fill = (LATEW && I == 14) ? b.p[0] : pa;
    p.rd0 = b.v[vi] + (I == SP ? SLOT : 0u); p.rd1 = b.v[vi + 1] + (I == SP ? SLOT : 0u); }, I < 13 ? sK : I < 14 ? sV : sO, I < 14 ? 4096u : KO::US, st);
  if constexpr (I == SV - 2) st.base[0] = pb + b.l16;
  if constexpr (I < 15)
    phaseB<true, (I < 14), b_dma(fb), (I < 13 ? KV_NT : W_NT), b_vm(fb), (I == SV - 2 ? 8 : -1)>(ac, Sn, [&](PA& p) __attribute__((always_inline)) {
      PINS(b.rk);
      p.soff = I < 13 ? ksoff(I + 2, b.rk) : b.wo + (I - 13) * (LATEW ? 1u : 2u) * KO::CS; p.fill = (LATEW && I == 14) ? b.p[1] : pb; p.rd0 = b.k[(I & 1) ? 0 : 1]; }, I < 13 ? sV : sO, I < 13 ? 4096u : KO::US, st);
}
template <int CH, int Q, int J> DI void o_word(const AC& c, const float (&inv)[4], bf16x8 (&nb)[4]) {
  u32x4 w = __builtin_bit_cast(u32x4, nb[Q]);
  unsigned r; float t;
  asm volatile("v_accvgpr_read_b32 %0, %2\n\tv_accvgpr_read_b32 %1, %3\n\tv_mul_f32_e32 %0, %0, %4\n\tv_mul_f32_e32 %1, %1, %4\n\tv_cvt_pk_bf16_f32 %0, %0, %1"
               : "=&v"(r), "=&v"(t) : "a"(c.o[2 * CH + (J >> 1)][Q][2 * (J & 1)]), "a"(c.o[2 * CH + (J >> 1)][Q][2 * (J & 1) + 1]), "v"(inv[Q]));
  w[J] = r;
  nb[Q] = __builtin_bit_cast(bf16x8, w);
}
template <int CH, int Q> DI void o_put(const bf16x8 (&nb)[4], unsigned ow) { asm volatile("ds_write_b128 %0, %1 offset:%2" :: "v"(ow), "v"(nb[Q]), "i"(CH * 4096 + Q * 1024) : "memory"); }
template <int E> struct CvX {
  static constexpr bool ACT = E == 3;
  const AC& c; const float (&inv)[4]; const bf16x8 (&cur)[4]; bf16x8 (&nb)[4]; unsigned ow;
  template <int G> DI void run() const {
    if constexpr (E < 3 && (G & 1)) o_word<E + 1, (G >> 3), (G >> 1) & 3>(c, inv, nb);
    if constexpr (E < 3 && ((G & 15) == 8 || (G & 15) == 10)) o_put<E, 2 * (G >> 4) + ((G >> 1) & 1)>(cur, ow);
    if constexpr (E == 2 && (G & 15) == 12) o_put<3, (G >> 4)>(nb, ow);
    if constexpr (E == 3 && (G == 8 || G == 10)) o_put<3, 2 + ((G >> 1) & 1)>(cur, ow);
  }
};
template <class K, int VM, bool DM, int M> DI void gapL(AC& c, float (&inv)[4], bf16x8 (&nb)[4], bf16x8 (&a0)[4], bf16x4& lo, const PC& pc, const Src& src, unsigned soff) {
  if constexpr (M == 6) wait_vm_lgkm<VM, 0>();
  if constexpr (DM && (M == 12 || M == 32)) set_m0(pc.r2 + (M == 32 ? 4096u : 0u));
  if constexpr (M < 4) mm_vvv<false>(c.l[M], c.ones, c.pf[M]); else mm_avv(c.o[(M - 4) >> 2][M & 3], c.vf[(M - 4) >> 2], c.pf[M & 3]);
  if constexpr (M >= 6 && M < 14) {
    constexpr int d = (M - 6) >> 1; const unsigned a = ring_rd<0>(pc, d);
    if constexpr (M & 1) a0[d] = cat(lo, lds_tr1(a + K::TS)); else lo = lds_tr1(a);
  }
  if constexpr (M >= 4 && M < 12 && !(M & 1)) asm volatile("v_rcp_f32_e32 %0, %1" : "=v"(inv[(M - 4) >> 1]) : "v"(c.l[(M - 4) >> 1][0]));
  if constexpr (M >= 16 && M < 32) o_word<0, ((M - 16) >> 2), (M & 3)>(c, inv, nb);
  if constexpr (DM && ((M >= 12 && M < 16) || M >= 32)) dma_p<W_NT, M & 3>(src.v[M & 3], src.srd, soff + (M >= 32 ? K::US : 0u));
  GAP();
}
template <class K, int VM, bool DM, int... Ms> DI void phaseL(AC& c, float (&inv)[4], bf16x8 (&nb)[4], bf16x8 (&a0)[4], const PC& pc, const Src& src, unsigned soff, iseq<Ms...>) {
  bf16x4 lo;
#pragma unroll
  for (int q = 0; q < 4; ++q) nb[q] = bf16x8{};
  (gapL<K, VM, DM, Ms>(c, inv, nb, a0, lo, pc, src, soff), ...);
}
struct Pub { u32x4 pk[16]; srd_t srd; unsigned v[4], so; };
template <int F, int H> DI void pub_half(const f32x4 (&acc)[8][4], Pub& p) {
  const f32x4& a = acc[4 * (F >> 3) + 2 * (F & 1) + H][(F >> 1) & 3];
  unsigned r0, r1;
  asm volatile("v_cvt_pk_bf16_f32 %0, %2, %3\n\tv_cvt_pk_bf16_f32 %1, %4, %5" : "=&v"(r0), "=v"(r1) : "v"(a[0]), "v"(a[1]), "v"(a[2]), "v"(a[3]));
  p.pk[F][2 * H] = r0; p.pk[F][2 * H + 1] = r1;
  if constexpr (H) asm volatile("buffer_store_dwordx4 %0, %1, %2, %3 offen offset:%4 " PUBPOL :: "v"(p.pk[F]), "v"(p.v[F >> 2]), "s"(p.srd), "s"(p.so), "i"((F & 3) * 1024) : "memory");
}
struct PubX {
  static constexpr bool ACT = false;
  const f32x4 (&acc)[8][4]; Pub& p;
  template <int G> DI void run() const { if constexpr (G >= 16) pub_half<((G - 16) >> 1), (G & 1)>(acc, p); }
};
template <class K, int U, bool PB, int M>
DI void tail_gap(f32x4 (&acc)[8][4], const bf16x8 (&cur)[4], bf16x8 (&nxt)[4], bf16x4 (&lo)[4], bf16x8 (&bf)[4], const PC& c, const unsigned (&xa)[3], Pub& p) {
  constexpr int qb = M >> 2, db = M & 3, S = U / 3, ne = (U + 1) % 3;
  mm_vvv<false>(acc[4 * S + db][qb], cur[db], bf[qb]);
  if constexpr (U < 5 && M < 8) {
    constexpr int d = M >> 1; const unsigned a = ring_rd<(ne + 1) % 3>(c, d) + ((U + 1) / 3) * 4096u;
    if constexpr (M & 1) nxt[d] = cat(lo[d], lds_tr1(a + K::TS)); else lo[d] = lds_tr1(a);
  }
  if constexpr (U < 5 && db == 3) bf[qb] = lds128(xa[ne] + qb * K::QS);
  if constexpr (PB && (U == 3 || U == 4) && M >= 8) pub_half<4 * (U - 3) + ((M - 8) >> 1), (M & 1)>(acc, p);
  if constexpr (PB && U == 5 && M >= 8) pub_half<8 + ((M - 8) >> 1), (M & 1)>(acc, p);
  GAP();
}
template <class K, int U, bool PB, int VM, int... Ms>
DI void tail_unit(f32x4 (&acc)[8][4], const bf16x8 (&cur)[4], bf16x8 (&nxt)[4], bf16x8 (&bf)[4], const PC& c, const unsigned (&xa)[3], Pub& p, iseq<Ms...>) {
  wait_vm_lgkm<VM, 2>();
  GAP();
  bf16x4 lo[4];
  (tail_gap<K, U, PB, Ms>(acc, cur, nxt, lo, bf, c, xa, p), ...);
}
template <int I> DI void own_put1(const u32x4& v, unsigned a) { asm volatile("ds_write_b128 %0, %1 offset:%2 ; %3" :: "v"(a), "v"(v), "i"((I & 7) * 1024), "i"(I) : "memory"); }
template <int B, int... Is> DI void own_put(const Pub& p, unsigned a, iseq<Is...>) { (own_put1<B + Is>(p.pk[B + Is], a), ...); }
template <int... Is> DI void pub_rest(const f32x4 (&acc)[8][4], Pub& p, iseq<Is...>) { (pub_half<12 + (Is >> 1), (Is & 1)>(acc, p), ...); }
template <int... Is> DI void pub_slow(const f32x4 (&acc)[8][4], Pub& p, iseq<Is...>) { ((pub_half<Is, 0>(acc, p), pub_half<Is, 1>(acc, p), __builtin_amdgcn_s_sleep(PUB_SLEEP)), ...); }
DI void redo_head(const AC& ac, const char* __restrict__ Kp, const char* __restrict__ Vp, unsigned lane, unsigned ow) {
  const unsigned r16 = lane & 15u, g = lane >> 4;
#pragma unroll 1
  for (unsigned qb = 0; qb < 4; ++qb) {
    bf16x8 qq[4];
#pragma unroll
    for (int s = 0; s < 4; ++s) qq[s] = qb == 0 ? ac.q[0][s] : qb == 1 ? ac.q[1][s] : qb == 2 ? ac.q[2][s] : ac.q[3][s];
    f32x4 o[8];
#pragma unroll
    for (int d = 0; d < 8; ++d) o[d] = f32x4{};
    float m = -__builtin_inff(), l = 0.f;
#pragma unroll 1
    for (unsigned t = 0; t < 16; ++t) {
      f32x4 s0 = {}, s1 = {};
      const char* kp = Kp + (t * 32u + r16) * 256u + g * 16u;
#pragma unroll
      for (int s = 0; s < 4; ++s) {
        s0 = __builtin_amdgcn_mfma_f32_16x16x32_bf16(*(const bf16x8*)(kp + s * 64), qq[s], s0, 0, 0, 0);
        s1 = __builtin_amdgcn_mfma_f32_16x16x32_bf16(*(const bf16x8*)(kp + s * 64 + 4096), qq[s], s1, 0, 0, 0);
      }
      const float mn = fmaxf(m, row_max(s0, s1)), al = __builtin_amdgcn_exp2f(m - mn);
      m = mn;
#pragma unroll
      for (int j = 0; j < 4; ++j) { s0[j] = __builtin_amdgcn_exp2f(s0[j] - mn); s1[j] = __builtin_amdgcn_exp2f(s1[j] - mn); }
      l = l * al + ((s0[0] + s0[1]) + (s0[2] + s0[3])) + ((s1[0] + s1[1]) + (s1[2] + s1[3]));
      const bf16x8 pf = __builtin_bit_cast(bf16x8, pack2(s0, s1));
      const __bf16* vp = (const __bf16*)(Vp + (t * 32u + g * 4u) * 256u) + r16;
#pragma unroll
      for (int d = 0; d < 8; ++d) {
        bf16x8 vf;
#pragma unroll
        for (int j = 0; j < 8; ++j) vf[j] = vp[((j & 3) + (j >> 2) * 16) * 128 + d * 16];
        o[d] = __builtin_amdgcn_mfma_f32_16x16x32_bf16(vf, pf, o[d] * al, 0, 0, 0);
      }
    }
    const float inv = 1.0f / xlane<false>(l);
#pragma unroll
    for (int s = 0; s < 4; ++s) *(LAS u32x4*)(size_t)(ow + s * 4096u + qb * 1024u) = pack2(o[2 * s] * inv, o[2 * s + 1] * inv);
  }
}
template <bool isA, int J> DI void q_conv1(AC& ac, f32x4 (&acc)[8][4], Stash& st, const Src& sK, const Src& sV, unsigned rk, unsigned p3) {
  constexpr int s = J >> 2, qb = J & 3;
  if constexpr (isA) { stash_write1<(5 * J) / 2>(st); stash_write1<(5 * J) / 2 + 1>(st); if constexpr (J & 1) stash_write1<(5 * J) / 2 + 2>(st); }
  else {
    constexpr int p = 2 * (J & 1);
    const Src& src = s < 2 ? sK : sV;
    const unsigned so = ksoff(s == 0 ? 2 : s == 1 ? SK : s == 2 ? SV : SP, rk) + (J & 2) * 2048u;
    if constexpr (s == 0 || s == 3) {
      if constexpr (p == 0) asm volatile("s_mov_b32 m0, %0\n\ts_nop 0" :: "s"(p3 + (s == 3 ? SLOT : 0u) + (J & 2) * 2048u) : "memory");
      dma_p<KV_NT, p>(src.v[p], src.srd, so); dma_p<KV_NT, p + 1>(src.v[p + 1], src.srd, so);
    } else {
      asm volatile("buffer_load_dwordx4 %0, %1, %2, %3 offen offset:%4 " KVPOL : "=a"(st.p[2 * J - 8]) : "v"(src.v[p]), "s"(src.srd), "s"(so), "i"(1024 * p) : "memory");
      asm volatile("buffer_load_dwordx4 %0, %1, %2, %3 offen offset:%4 " KVPOL : "=a"(st.p[2 * J - 7]) : "v"(src.v[p + 1]), "s"(src.srd), "s"(so), "i"(1024 * p + 1024) : "memory");
    }
  }
  ac.q[qb][s] = __builtin_bit_cast(bf16x8, natural8(pack2(acc[2 * s][qb] * SC, acc[2 * s + 1][qb] * SC)));
  asm volatile("" : "+a"(ac.q[qb][s]));
  GAP();
}
template <bool isA, int... Js> DI void q_conv(AC& ac, f32x4 (&acc)[8][4], Stash& st, const Src& sK, const Src& sV, unsigned rk, unsigned p3, iseq<Js...>) { (q_conv1<isA, Js>(ac, acc, st, sK, sV, rk, p3), ...); }
DI void zero_o(AC& ac) {
#pragma unroll
  for (int i = 0; i < 8; ++i)
#pragma unroll
    for (int q = 0; q < 4; ++q) { ac.o[i][q] = f32x4{}; asm volatile("" : "+a"(ac.o[i][q])); }
}

DI unsigned vbid() {
  const unsigned i = blockIdx.x;
  return i;
}
template <bool isA>
DI void body(const char* __restrict__ X, const char* __restrict__ K, const char* __restrict__ V, const char* __restrict__ Wq, const char* __restrict__ Wo,
             const char* __restrict__ Wo2, srd_t code, char* __restrict__ part, char* __restrict__ out) {
  extern __shared__ __attribute__((aligned(16))) char lds[];
  STAMP(0);
  unsigned tid = threadIdx.x;
  PINV(tid);
  const unsigned lane = tid & 63u, w = rfl(tid >> 6);
  const unsigned bid = vbid(), xg = bid & 7u, rr = bid >> 3, h = xg * 4u + w;
  const unsigned b = rr & 15u;
  const unsigned r16 = lane & 15u, g = lane >> 4;
  const char* xp  = X + (size_t)b * 65536u + w * 16384u;
  const char* wqp = Wq + h * 256u;
  const char* wop = Wo + (size_t)xg * 524288u + w * 256u;
  const size_t kvo = isA ? (size_t)(b * 8u + xg) * 131072u : (size_t)(b * 32u + h) * 131072u;
  const unsigned rk = isA ? b : b + 4u * w;
  typedef PK<512u, 4096u, 0x1300u, 0xF300u, 0xC300u, 128u, 32u * 8192u, true> KQ;
  typedef PK<512u, 4096u, 0x1300u, 0xF300u, 0xC300u, 4096u, 8192u, false> KT;
  typedef PK<2048u, 1024u, 0x1000u, 0xF000u, 0xF000u, 128u, 32u * 1024u, false> KO;
  asm volatile("" :: "v"(lds));
  PC pc;
  pc.r01 = 65536u + w * 16384u; pc.r2 = 131072u + w * 8192u;
  const unsigned zq = ((b & 3u) << 14) | ((b >> 2) << 8), co = 4u * w;
  const srd_t srdH = mk_srd(part + HOFF, 4096u + 256u * 128u);
  const unsigned tok_off = rr * 128u, flg_off = 4096u + rr * 1024u;
  const unsigned l8 = (lane & 7u) * 4u;
  unsigned entry_flags;
  asm volatile("buffer_load_dword %0, %1, %2, %3 offen sc1" : "=v"(entry_flags) : "v"(l8), "s"(srdH), "s"(flg_off + xg * 128u) : "memory");
  if constexpr (WARM_CODE && isA) { if (w == 3 && lane < WARM_CODE) asm volatile("buffer_load_dword %0, %1, %2, 0 offen" : "+v"(entry_flags) : "v"((b * WARM_CODE + lane) * 128u), "s"(code) : "memory"); }
  Src sQ, sX, sK, sV, sO;
  mk_src_w<false>(sQ, wqp, 511u * 8192u + 256u, 8192u, lane);
  mk_src_rows<false>(sX, xp, 16384u, 1024u, lane);
  const unsigned p0 = b & 3u, p1 = (p0 + 1u) & 3u, p2 = (p0 + 2u) & 3u, p3 = (p0 + 3u) & 3u, xw = w * 4096u;
  Stash st;
  unsigned dum0 = lane;
  if constexpr (isA && ORD == 0) {
    mk_src_rows<false>(sK, K + kvo, 131072u, 256u, lane);
    mk_src_rows<true>(sV, V + kvo, 131072u, 256u, lane);
    const unsigned to = (lane >> 1) * 1024u + (lane & 1u) * 128u + b * KO::CS;
    const char* wb = Wo2 + (size_t)xg * 524288u + w * 256u;
    if constexpr (EARLY_O == 1) asm volatile("global_load_dword %0, %1, %2\n\tglobal_load_dword %0, %1, %3" : "=&v"(dum0) : "v"(to), "s"(wop), "s"(wb) : "memory");
    stash_load(st, (w & 1u) ? sK.v[2] : sK.v[0], sK, sV, rk, w * 2048u, make_iseq<64>{});
    if constexpr (EARLY_O == 2) asm volatile("global_load_dword %0, %1, %2\n\tglobal_load_dword %0, %1, %3" : "=&v"(dum0) : "v"(to), "s"(wop), "s"(wb) : "memory");
  }
  if constexpr ((isA ? ILV_A : ILV_B) == 1) {
    dma_unit<TOUCH_Q>(sQ, q_soff(zq), pc.r2);
    dma_unit<W_NT>(sQ, q_soff(zq + 0x1300u), pc.r01);
    if constexpr (!(isA && ORD == 2)) dma_unit<X_NT>(sX, p0 * 256u, xw + p0 * 16384u);
    dma_unit<TOUCH_Q>(sQ, q_soff(zq) + KQ::US, pc.r2 + 4096u);
    dma_unit<W_NT>(sQ, q_soff(zq + 0x1300u) + KQ::US, pc.r01 + 4096u);
  } else {
    dma_elem<(isA ? ILV_A : ILV_B) == 2 ? 0 : TOUCH_Q>(sQ, q_soff(zq), KQ::US, pc.r2);
    if constexpr (!(isA && ORD == 2)) dma_unit<X_NT>(sX, p0 * 256u, xw + p0 * 16384u);
    dma_elem<W_NT>(sQ, q_soff(zq + 0x1300u), KQ::US, pc.r01);
  }
  if constexpr (!(isA && ORD == 0)) {
    mk_src_rows<false>(sK, K + kvo, 131072u, 256u, lane);
    mk_src_rows<true>(sV, V + kvo, 131072u, 256u, lane);
  }
  mk_src_w<true>(sO, wop, 511u * 1024u + 256u, 1024u, lane);
  if constexpr (isA) {
    if constexpr (ORD == 2) {
      if constexpr (DELAY_A > 0) __builtin_amdgcn_s_sleep(DELAY_A);
      stash_load(st, (w & 1u) ? sK.v[2] : sK.v[0], sK, sV, rk, w * 2048u, make_iseq<64>{});
      dma_unit<X_NT>(sX, p0 * 256u, xw + p0 * 16384u);
    }
    dma_elem<W_NT>(sQ, q_soff(zq + 0x2200u), KQ::US, pc.r01 + SLOT);
    dma_unit<X_NT>(sX, p1 * 256u, xw + p1 * 16384u);
    dma_unit<X_NT>(sX, p2 * 256u, xw + p2 * 16384u);
    dma_unit<X_NT>(sX, p3 * 256u, xw + p3 * 16384u);
    if constexpr (ORD == 1) stash_load(st, (w & 1u) ? sK.v[2] : sK.v[0], sK, sV, rk, w * 2048u, make_iseq<64>{});
  }
  const unsigned rb = 1024u * (r16 >> 2) + 64u * (r16 & 3u) + 16u * (g ^ ((r16 >> 2) & 2u));
  const unsigned tb = 64u * ((lane >> 2) & 3u) + 32u * (g & 1u) + 8u * (lane & 3u);
  const unsigned tbV = tb + 1024u * g;
  const unsigned sb0 = w * 2048u + lane * 16u;
  st.base[0] = sb0; st.base[1] = sb0 + 65536u; st.base[2] = sb0 + 131072u;
  AC ac;
  if constexpr (!isA) zero_o(ac);
  {
    f32x4 acc[8][4];
    bf16x8 a0[4], a1[4], bf[4];
    asm volatile("s_waitcnt vmcnt(%2)" : "+v"(entry_flags), "+v"(dum0) : "i"(isA ? (ORD == 1 ? 63 : ORD == 0 ? 28 : 20) : 8) : "memory");
    if (w == 0) {
      const unsigned long long mk = __builtin_amdgcn_ballot_w64(((entry_flags >> (lane & 7u)) & 1u) != 0u);
      const unsigned tok = 0x5a5a5a00u | (~(unsigned)mk & 0xffu);
      if (lane == 0) asm volatile("buffer_store_dword %0, off, %1, %2 sc1" :: "v"(tok), "s"(srdH), "s"(tok_off + xg * 4u) : "memory");
    }
    barrier();
    STAMP(1);
    proj_start<KQ, 2>(a0, bf, pc, tbV, rb, zq, q_soff(zq + 0x3100u));
    if constexpr (isA) {
      constexpr int V1 = ORD == 0 ? 28 : 63, V2 = ORD == 1 ? 63 : 28;
      proj_elem<KQ, 2, true,  false, true, W_NT, 63, V1>(acc, a0, a1, bf, pc, sQ);
      STAMP(10);
      proj_elem<KQ, 0, false, false, true, W_NT, V1, V2>(acc, a0, a1, bf, pc, sQ);
      STAMP(11);
      proj_elem<KQ, 1, false, false, true, W_NT, V2, 16>(acc, a0, a1, bf, pc, sQ);
      proj_elem<KQ, 2, false, false, true, W_NT, 16, 16, ORD != 1>(acc, a0, a1, bf, pc, sQ);
    } else {
      dma_elem<W_NT>(sQ, q_soff(zq + 0x2200u), KQ::US, pc.r01 + SLOT);
      proj_elem<KQ, 2, true,  false, true, W_NT, (ILV_B == 1 ? 12 : 16), 20, false, true, true>(acc, a0, a1, bf, pc, sQ, NoX{}, &sX, p1 * 256u, xw + p1 * 16384u, p2 * 256u, xw + p2 * 16384u);
      STAMP(10);
      proj_elem<KQ, 0, false, false, true, W_NT, 24, 28, false, true, false>(acc, a0, a1, bf, pc, sQ, NoX{}, &sX, p3 * 256u, xw + p3 * 16384u);
      STAMP(11);
      proj_elem<KQ, 1, false, false, true, W_NT, 28, 24>(acc, a0, a1, bf, pc, sQ);
      proj_elem<KQ, 2, false, false, true, W_NT, 20, 16, true>(acc, a0, a1, bf, pc, sQ);
    }
    STAMP(8);
#pragma unroll 1
    for (unsigned e = 0; e < 3; ++e) {
      if constexpr (isA && PACE_A > 0) __builtin_amdgcn_s_sleep(PACE_A);
      proj_elem<KQ, 0, false, false, true, W_NT, 16, 16>(acc, a0, a1, bf, pc, sQ);
      if constexpr (isA && PACE_A > 0) __builtin_amdgcn_s_sleep(PACE_A);
      proj_elem<KQ, 1, false, false, true, W_NT, 16, 16>(acc, a0, a1, bf, pc, sQ);
      if constexpr (isA && PACE_A > 0) __builtin_amdgcn_s_sleep(PACE_A);
      proj_elem<KQ, 2, false, false, true, W_NT, 16, 16>(acc, a0, a1, bf, pc, sQ);
    }
    STAMP(9);
    if constexpr (isA) {
      proj_elem<KQ, 0, false, false, false, W_NT, 16, 12>(acc, a0, a1, bf, pc, sQ);
      proj_elem<KQ, 1, false, false, false, W_NT, 8, 4>(acc, a0, a1, bf, pc, sQ);
      proj_elem<KQ, 2, false, true, false, W_NT, 0, 0>(acc, a0, a1, bf, pc, sQ);
    } else {
      pc.soff = ksoff(0u, rk);
      proj_elem<KT, 0, false, false, true, KV_NT, 16, 16>(acc, a0, a1, bf, pc, sK);
      proj_elem<KT, 1, false, false, true, KV_NT, 16, 16>(acc, a0, a1, bf, pc, sK);
      pc.soff = ksoff(0u, rk);
      proj_elem<KT, 2, false, true, true, KV_NT, 16, 16>(acc, a0, a1, bf, pc, sV);
    }
    barrier();
    STAMP(2);
    acc_fence_v(acc);
    q_conv<isA>(ac, acc, st, sK, sV, rk, w * 16384u, make_iseq<16>{});
  }
  ST SA, SB;
  if constexpr (isA) {
    zero_o(ac);
    const unsigned kb = rb, vb = SLOT + tbV;
    unsigned dum = lane, to = TOUCH_B == 4 ? (lane >> 4) * 1024u + (lane & 8u) * 16u + (lane & 7u) * 4u : (lane >> 3) * 1024u + (lane & 4u) * 32u + (lane & 3u) * 4u;
    PINV(to);
    const char* ta = wop + b * KO::CS;
    const char* tb2 = Wo2 + (size_t)xg * 524288u + w * 256u + b * KO::CS;
    if constexpr (TOUCH_B == 4) asm volatile("global_load_dword %0, %1, %2 nt\n\tglobal_load_dword %0, %1, %2\n\tv_add_u32_e32 %1, 0x1000, %1" : "+v"(dum), "+v"(to) : "s"(ta) : "memory");
    asm volatile("s_waitcnt lgkmcnt(0)" ::: "memory");
    barrier();
    k_read(ac, kb + kv_pair(0) * 16384u);
    wait_lgkm0();
    GAP();
    qk0<false>(ac, SA, sK, 0u, 0u, make_iseq<32>{});
    k_read(ac, kb + kv_pair(1) * 16384u);
    if constexpr (TOUCH_B == 4) asm volatile("global_load_dword %0, %1, %2 nt\n\tglobal_load_dword %0, %1, %2\n\tv_add_u32_e32 %1, 0x1000, %1" : "+v"(dum), "+v"(to) : "s"(ta) : "memory");
    sm_first(ac, SA);
    if constexpr (STAG > 0) { for (unsigned k = 0; k < (b & 7u); ++k) __builtin_amdgcn_s_sleep(STAG); }
    STAMP(13);
    unsigned i4 = 0;
    auto av = [&](PA& p) { if constexpr (PACE_ATT > 0) __builtin_amdgcn_s_sleep(PACE_ATT);
                           PINS(i4); p.rd0 = vb + (((unsigned)(kv_tab() >> i4) & 15u) << 14); p.rd1 = p.rd0 ^ 32u; PINV(p.rd0); PINV(p.rd1); };
    auto ak = [&](PA& p) { if constexpr (PACE_ATT > 0) __builtin_amdgcn_s_sleep(PACE_ATT);
                           if constexpr (RAMP > 0) asm volatile("s_cmp_ge_u32 %0, 16\n\ts_cbranch_scc1 .Lr_%=\n\ts_sleep %1\n\ts_cmp_ge_u32 %0, 8\n\ts_cbranch_scc1 .Lr_%=\n\ts_sleep %1\n.Lr_%=:" :: "s"(i4), "i"(RAMP) : "scc");
                           PINS(i4); p.rd0 = kb + (((unsigned)((kv_tab() >> 8) >> i4) & 15u) << 14); PINV(p.rd0); i4 += 4u; };
    auto an = [&](PA&) { i4 += 4u; };
    auto at = [&](PA& p) __attribute__((always_inline)) {
      PINS(i4); p.rd0 = vb + (((unsigned)(kv_tab() >> i4) & 15u) << 14); p.rd1 = p.rd0 ^ 32u; PINV(p.rd0); PINV(p.rd1);
      if constexpr (TOUCH_O) {
        const char* tp;
        if constexpr (TOUCH_B == 4) {
          asm volatile("s_cmp_lt_u32 %1, 24\n\ts_cselect_b64 %0, %2, %3" : "=s"(tp) : "s"(i4), "s"(ta), "s"(tb2 - 32768) : "scc");
          asm volatile("global_load_dword %0, %1, %2 nt\n\tglobal_load_dword %0, %1, %2\n\tv_add_u32_e32 %1, 0x1000, %1" : "+v"(dum), "+v"(to) : "s"(tp) : "memory");
        } else {
          asm volatile("s_bitcmp0_b32 %1, 4\n\ts_cselect_b64 %0, %2, %3" : "=s"(tp) : "s"(i4), "s"(ta), "s"(tb2) : "scc");
          asm volatile("global_load_dword %0, %1, %2 nt\n\tglobal_load_dword %0, %1, %2" : "+v"(dum) : "v"(to), "s"(tp) : "memory");
          to = (to + 8192u) & 32767u; PINV(to);
        }
      }
    };
#define ITS
    while (i4 < 24u) {
      ITS
      phaseA<true, false, KV_NT, 63>(ac, SA, SB, at, sK, 0u, st);
      phaseB<true, true, false, KV_NT, 63>(ac, SB, ak, sK, 0u, st);
      phaseA<true, false, KV_NT, 63>(ac, SB, SA, at, sK, 0u, st);
      phaseB<true, true, false, KV_NT, 63>(ac, SA, ak, sK, 0u, st);
    }
    ITS
    barrier();
    phaseA<true, false, KV_NT, 63, 40>(ac, SA, SB, at, sK, 0u, st);
    phaseB<true, true, false, KV_NT, 63, 46>(ac, SB, ak, sK, 0u, st);
    phaseA<true, false, KV_NT, 63, 52>(ac, SB, SA, at, sK, 0u, st);
    phaseB<true, true, false, KV_NT, 63, 58>(ac, SA, ak, sK, 0u, st);
    wait_vm_lgkm<63, 8>(); barrier();
    while (i4 < 56u) {
      ITS
      phaseA<true, false, KV_NT, 63>(ac, SA, SB, at, sK, 0u, st);
      phaseB<true, true, false, KV_NT, 63>(ac, SB, ak, sK, 0u, st);
      phaseA<true, false, KV_NT, 63>(ac, SB, SA, at, sK, 0u, st);
      phaseB<true, true, false, KV_NT, 63>(ac, SA, ak, sK, 0u, st);
    }
    STAMP(14);
    barrier();
    unsigned v14 = vb + kv_pair(14) * 16384u, x14 = v14 ^ 32u; PINV(v14); PINV(x14);
    phaseA<true, true, W_NT, 63>(ac, SA, SB, [&](PA& p) { p.soff = co * KO::CS; p.fill = pc.r01; p.rd0 = v14; p.rd1 = x14; }, sO, KO::US, st);
    phaseB<true, false, true, W_NT, 63>(ac, SB, [&](PA& p) { p.soff = (co + 1u) * KO::CS; p.fill = pc.r01 + SLOT; }, sO, KO::US, st);
    phaseA<false, false, W_NT, 63>(ac, SB, SA, [&](PA& p) { p.rd0 = v14 + 16384u; p.rd1 = x14 + 16384u; }, sO, 0u, st);
    dum0 = dum;
  } else {
    const unsigned P0 = pc.r01, P1 = pc.r01 + SLOT, P2 = pc.r2, P3 = w * 16384u;
    unsigned v0 = tbV + P0, v2 = tbV + P2, x0 = v0 ^ 32u, x2 = v2 ^ 32u, k1 = rb + P1, k3 = rb + P3;
    PINV(v0); PINV(v2); PINV(x0); PINV(x2); PINV(k1); PINV(k3);
    wait_vm<48>();
    k_read(ac, k1 - SLOT);
    wait_lgkm0();
    GAP();
    qk0<true>(ac, SA, sV, ksoff(1u, rk), P0, make_iseq<32>{});
    wait_vm<48>();
    GAP();
    k_read(ac, k1);
    sm_first(ac, SA);
    STAMP(13);
    BS bs{{P0, P1, P2, P3}, {v0, x0, v2, x2, tbV + P3, (tbV + P3) ^ 32u}, {k1, k3}, rk, co * KO::CS, lane * 16u};
    PINV(bs.v[4]); PINV(bs.v[5]);
#define BT(k)
#define B2(I) b_step<KO, I>(ac, SA, SB, bs, sK, sV, sO, st); b_step<KO, I + 1>(ac, SB, SA, bs, sK, sV, sO, st);
    B2(0) BT(0) B2(2) BT(1) B2(4) BT(2) B2(6) BT(3) B2(8) BT(4) B2(10)
    STAMP(14);
    B2(12) B2(14)
#undef B2
  }
  {
    const unsigned l16 = lane * 16u;
    unsigned ow = w * 16384u + l16;
    PINV(ow);
    f32x4 acc[8][4];
    bf16x8 a0[4], a1[4], b0[4], bf[4];
    float inv[4];
    proj_setup<KO>(pc, tb + 512u * g, l16, (co + 3u) << 12, (co + 3u) * KO::CS);
    unsigned toks;
    asm volatile("buffer_load_dword %0, %1, %2, %3 offen sc1" : "=v"(toks) : "v"(l8), "s"(srdH), "s"(tok_off) : "memory");
    GAP();
    phaseL<KO, (isA || LATEW ? 13 : 21), (!isA && LATEW)>(ac, inv, b0, a0, pc, sO, (co + 2u) * KO::CS, make_iseq<36>{});
    STAMP(3);
    if constexpr (isA) {
      barrier();
      dma_elem<W_NT>(sO, (co + 2u) * KO::CS, KO::US, pc.r2);
    }
    const bool ok = ac.l[0][0] <= LMAX && ac.l[1][0] <= LMAX && ac.l[2][0] <= LMAX && ac.l[3][0] <= LMAX;
    if (__builtin_expect(__builtin_amdgcn_ballot_w64(!ok) != 0ull, 0)) {
      asm volatile("s_waitcnt vmcnt(0)" : "+v"(toks) :: "memory");
      acc_fence_a(ac.o);
      redo_head(ac, K + kvo, V + kvo, lane, ow);
#pragma unroll
      for (int q = 0; q < 4; ++q) {
        inv[q] = 1.0f;
        b0[q] = lds128(ow + q * 1024u);
#pragma unroll
        for (int s = 1; s < 4; ++s) {
          const u32x4 t = __builtin_bit_cast(u32x4, lds128(ow + s * 4096u + q * 1024u));
          ac.o[2 * s][q] = f32x4{__uint_as_float(t[0] << 16), __uint_as_float(t[0] & 0xffff0000u), __uint_as_float(t[1] << 16), __uint_as_float(t[1] & 0xffff0000u)};
          ac.o[2 * s + 1][q] = f32x4{__uint_as_float(t[2] << 16), __uint_as_float(t[2] & 0xffff0000u), __uint_as_float(t[3] << 16), __uint_as_float(t[3] & 0xffff0000u)};
        }
      }
      wait_lgkm0();
      acc_fence_a(ac.o);
    }
#pragma unroll
    for (int q = 0; q < 4; ++q) bf[q] = bf16x8{};
    GAP();
    proj_elem<KO, 0, true, false, true, W_NT, 16, 16>(acc, a0, a1, b0, pc, sO, CvX<0>{ac, inv, b0, bf, ow});
    proj_elem<KO, 1, false, false, true, W_NT, 16, 16>(acc, a0, a1, bf, pc, sO, CvX<1>{ac, inv, bf, b0, ow});
    proj_elem<KO, 2, false, false, true, W_NT, 16, 16>(acc, a0, a1, b0, pc, sO, CvX<2>{ac, inv, b0, bf, ow});
    if constexpr (isA && TOUCH_B >= 3) PINV(dum0);
    proj_elem<KO, 0, false, false, true, W_NT, 16, 16, true>(acc, a0, a1, bf, pc, sO, CvX<3>{ac, inv, bf, b0, ow});
    STAMP(4);
#pragma unroll 1
    for (unsigned e = 0; e < 3; ++e) {
      proj_elem<KO, 1, false, false, true, W_NT, 16, 16>(acc, a0, a1, bf, pc, sO);
      proj_elem<KO, 2, false, false, true, W_NT, 16, 16>(acc, a0, a1, bf, pc, sO);
      proj_elem<KO, 0, false, false, true, W_NT, 16, 16>(acc, a0, a1, bf, pc, sO);
    }
    CYC(6);
    Pub pb;
    pb.srd = mk_srd(part + POFF + (size_t)rr * 524288u, 524288u);
    pb.so = xg * 65536u + w * 16384u;
#pragma unroll
    for (unsigned i = 0; i < 4; ++i) { pb.v[i] = l16 + i * 4096u + (SKIPOWN && 2u * w + (i >> 1) == xg ? 0x80000000u : 0u); PINV(pb.v[i]); }
#pragma unroll
    for (int i = 0; i < 16; ++i) pb.pk[i] = u32x4{};
    unsigned xa[3];
#pragma unroll
    for (unsigned i = 0; i < 3; ++i) { xa[i] = (((co + 13u + i) & 15u) << 12) | l16; PINV(xa[i]); }
    constexpr bool PB = !(isA && PUB_SLEEP > 0);
    tail_unit<KO, 0, PB, 12>(acc, a0, a1, bf, pc, xa, pb, make_iseq<16>{});
    tail_unit<KO, 1, PB, 4>(acc, a1, a0, bf, pc, xa, pb, make_iseq<16>{});
    tail_unit<KO, 2, PB, 63>(acc, a0, a1, bf, pc, xa, pb, make_iseq<16>{});
    tail_unit<KO, 3, PB, 0>(acc, a1, a0, bf, pc, xa, pb, make_iseq<16>{});
    CYC(7);
    tail_unit<KO, 4, PB, 63>(acc, a0, a1, bf, pc, xa, pb, make_iseq<16>{});
    tail_unit<KO, 5, PB, 63>(acc, a1, a0, bf, pc, xa, pb, make_iseq<16>{});
    STAMP(5);
    if constexpr (PB) pub_rest(acc, pb, make_iseq<8>{}); else pub_slow(acc, pb, make_iseq<16>{});
    const unsigned lr = ((lane + xg) & 7u) * 4u;
    unsigned fl;
    asm volatile("buffer_load_dword %0, %1, %2, %3 offen sc1" : "=v"(fl) : "v"(lr), "s"(srdH), "s"(flg_off + xg * 128u) : "memory");
    PINV(toks);
    STAMP(6);
    if (w == (xg >> 1)) {
      const unsigned a = pc.r01 + SLOT + l16;
      if (xg & 1u) own_put<8>(pb, a, make_iseq<8>{}); else own_put<0>(pb, a, make_iseq<8>{});
    }
    const unsigned fo = lane * 128u + xg * 4u, tau = __builtin_amdgcn_readlane(toks, xg);
    unsigned oo = ((b * 64u + 16u * w + r16) * 1024u + (isA ? 0u : 512u) + 64u * xg + 8u * g) * 2u;
    PINV(oo);
    wait_lgkm0();
    barrier();
    const unsigned oa = 65536u + SLOT + (xg >> 1) * 16384u + w * 2048u + l16;
    const u32x4 r0 = __builtin_bit_cast(u32x4, lds128(oa)), r1 = __builtin_bit_cast(u32x4, lds128(oa + 1024u));
    GAP();
    wait_vm<9>();
    if (lane == 2u * w) asm volatile("buffer_store_dword %0, %1, %2, %3 offen " FLPOL :: "v"(toks), "v"(fo), "s"(srdH), "s"(flg_off) : "memory");
    wait_vm<2>();
    if (lane == 2u * w + 1u) asm volatile("buffer_store_dword %0, %1, %2, %3 offen " FLPOL :: "v"(toks), "v"(fo), "s"(srdH), "s"(flg_off) : "memory");
    f32x16 f[8];
#pragma unroll
    for (int j = 0; j < 8; ++j) { const unsigned t = j < 4 ? r0[j & 3] : r1[j & 3]; f[0][2 * j] = __uint_as_float(t << 16); f[0][2 * j + 1] = __uint_as_float(t & 0xffff0000u); }
    PINV(f[0]);
    asm volatile("s_waitcnt vmcnt(2)" : "+v"(fl) : "v"(pb.pk[0]), "v"(pb.pk[1]), "v"(pb.pk[2]), "v"(pb.pk[3]), "v"(pb.pk[4]), "v"(pb.pk[5]), "v"(pb.pk[6]), "v"(pb.pk[7]),
                 "v"(pb.pk[8]), "v"(pb.pk[9]), "v"(pb.pk[10]), "v"(pb.pk[11]), "v"(pb.pk[12]), "v"(pb.pk[13]), "v"(pb.pk[14]), "v"(pb.pk[15]) : "memory");
    STAMP(7);
    unsigned got = 0xffffff01u, spin = 1u << 21, fresh, so, f2;
#define POLL(R) "buffer_load_dword %[" R "], %[lr], %[sh], %[flg] offen " PLPOL "\n\t"
#define SEEN(R) "v_cmp_eq_u32_e32 vcc, %[tau], %[" R "]\n\ts_andn2_b32 %[fr], vcc_lo, %[got]\n\ts_cbranch_scc1 .Lproc_%=\n\t"
    asm volatile(
        SEEN("fl") POLL("fl") "s_sleep 4\n"
        ".Lidle_%=:\n\t" POLL("f2") "s_waitcnt vmcnt(1)\n\t" SEEN("fl") "s_sleep %[slp]\n\t" POLL("fl") "s_waitcnt vmcnt(1)\n\t" SEEN("f2")
        "s_sleep %[slp]\n\ts_sub_u32 %[spin], %[spin], 1\n\ts_cbranch_scc0 .Lidle_%=\n\ts_branch .Ldone_%=\n"
        ".Lproc_%=:\n\ts_or_b32 %[got], %[got], %[fr]\n\t"
        "s_bitcmp1_b32 %[fr], 1\n\ts_cbranch_scc0 .La1_%=\n\ts_add_u32 %[so], %[ro], 65536\n\ts_bitset0_b32 %[so], 19\n\t"
        "buffer_load_dwordx4 v[152:155], %[l16], %[sp], %[so] offen " LDPOL "\n\tbuffer_load_dwordx4 v[156:159], %[l16], %[sp], %[so] offen offset:1024 " LDPOL "\n.La1_%=:\n\t"
        "s_bitcmp1_b32 %[fr], 2\n\ts_cbranch_scc0 .La2_%=\n\ts_add_u32 %[so], %[ro], 131072\n\ts_bitset0_b32 %[so], 19\n\t"
        "buffer_load_dwordx4 v[168:171], %[l16], %[sp], %[so] offen " LDPOL "\n\tbuffer_load_dwordx4 v[172:175], %[l16], %[sp], %[so] offen offset:1024 " LDPOL "\n.La2_%=:\n\t"
        "s_bitcmp1_b32 %[fr], 3\n\ts_cbranch_scc0 .La3_%=\n\ts_add_u32 %[so], %[ro], 196608\n\ts_bitset0_b32 %[so], 19\n\t"
        "buffer_load_dwordx4 v[184:187], %[l16], %[sp], %[so] offen " LDPOL "\n\tbuffer_load_dwordx4 v[188:191], %[l16], %[sp], %[so] offen offset:1024 " LDPOL "\n.La3_%=:\n\t"
        "s_bitcmp1_b32 %[fr], 4\n\ts_cbranch_scc0 .La4_%=\n\ts_add_u32 %[so], %[ro], 262144\n\ts_bitset0_b32 %[so], 19\n\t"
        "buffer_load_dwordx4 v[200:203], %[l16], %[sp], %[so] offen " LDPOL "\n\tbuffer_load_dwordx4 v[204:207], %[l16], %[sp], %[so] offen offset:1024 " LDPOL "\n.La4_%=:\n\t"
        "s_bitcmp1_b32 %[fr], 5\n\ts_cbranch_scc0 .La5_%=\n\ts_add_u32 %[so], %[ro], 327680\n\ts_bitset0_b32 %[so], 19\n\t"
        "buffer_load_dwordx4 v[216:219], %[l16], %[sp], %[so] offen " LDPOL "\n\tbuffer_load_dwordx4 v[220:223], %[l16], %[sp], %[so] offen offset:1024 " LDPOL "\n.La5_%=:\n\t"
        "s_bitcmp1_b32 %[fr], 6\n\ts_cbranch_scc0 .La6_%=\n\ts_add_u32 %[so], %[ro], 393216\n\ts_bitset0_b32 %[so], 19\n\t"
        "buffer_load_dwordx4 v[232:235], %[l16], %[sp], %[so] offen " LDPOL "\n\tbuffer_load_dwordx4 v[236:239], %[l16], %[sp], %[so] offen offset:1024 " LDPOL "\n.La6_%=:\n\t"
        "s_bitcmp1_b32 %[fr], 7\n\ts_cbranch_scc0 .La7_%=\n\ts_add_u32 %[so], %[ro], 458752\n\ts_bitset0_b32 %[so], 19\n\t"
        "buffer_load_dwordx4 v[248:251], %[l16], %[sp], %[so] offen " LDPOL "\n\tbuffer_load_dwordx4 v[252:255], %[l16], %[sp], %[so] offen offset:1024 " LDPOL "\n.La7_%=:\n\t"
        "s_waitcnt vmcnt(0)\n\ts_cmp_eq_u32 %[got], -1\n\ts_cbranch_scc1 .Ltok_%=\n\t" POLL("fl") "s_branch .Lwide_%=\n"
        ".Ltok_%=:\n\tbuffer_load_dword %[fl], %[l8], %[sh], %[tok] offen sc1\n"
        ".Lwide_%=:\n\t"
        "s_bitcmp1_b32 %[fr], 1\n\ts_cbranch_scc0 .Lb1_%=\n\t"
        "v_lshlrev_b32_e32 v144, 16, v152\n\tv_and_b32_e32 v145, %[hi], v152\n\tv_lshlrev_b32_e32 v146, 16, v153\n\tv_and_b32_e32 v147, %[hi], v153\n\t"
        "v_lshlrev_b32_e32 v148, 16, v154\n\tv_and_b32_e32 v149, %[hi], v154\n\tv_lshlrev_b32_e32 v150, 16, v155\n\tv_and_b32_e32 v151, %[hi], v155\n\t"
        "v_lshlrev_b32_e32 v152, 16, v156\n\tv_and_b32_e32 v153, %[hi], v156\n\tv_lshlrev_b32_e32 v154, 16, v157\n\tv_and_b32_e32 v155, %[hi], v157\n\t"
        "v_lshlrev_b32_e32 v156, 16, v158\n\tv_and_b32_e32 v157, %[hi], v158\n\tv_lshlrev_b32_e32 v158, 16, v159\n\tv_and_b32_e32 v159, %[hi], v159\n\t"
        ".Lb1_%=:\n\t"
        "s_bitcmp1_b32 %[fr], 2\n\ts_cbranch_scc0 .Lb2_%=\n\t"
        "v_lshlrev_b32_e32 v160, 16, v168\n\tv_and_b32_e32 v161, %[hi], v168\n\tv_lshlrev_b32_e32 v162, 16, v169\n\tv_and_b32_e32 v163, %[hi], v169\n\t"
        "v_lshlrev_b32_e32 v164, 16, v170\n\tv_and_b32_e32 v165, %[hi], v170\n\tv_lshlrev_b32_e32 v166, 16, v171\n\tv_and_b32_e32 v167, %[hi], v171\n\t"
        "v_lshlrev_b32_e32 v168, 16, v172\n\tv_and_b32_e32 v169, %[hi], v172\n\tv_lshlrev_b32_e32 v170, 16, v173\n\tv_and_b32_e32 v171, %[hi], v173\n\t"
        "v_lshlrev_b32_e32 v172, 16, v174\n\tv_and_b32_e32 v173, %[hi], v174\n\tv_lshlrev_b32_e32 v174, 16, v175\n\tv_and_b32_e32 v175, %[hi], v175\n\t"
        ".Lb2_%=:\n\t"
        "s_bitcmp1_b32 %[fr], 3\n\ts_cbranch_scc0 .Lb3_%=\n\t"
        "v_lshlrev_b32_e32 v176, 16, v184\n\tv_and_b32_e32 v177, %[hi], v184\n\tv_lshlrev_b32_e32 v178, 16, v185\n\tv_and_b32_e32 v179, %[hi], v185\n\t"
        "v_lshlrev_b32_e32 v180, 16, v186\n\tv_and_b32_e32 v181, %[hi], v186\n\tv_lshlrev_b32_e32 v182, 16, v187\n\tv_and_b32_e32 v183, %[hi], v187\n\t"
        "v_lshlrev_b32_e32 v184, 16, v188\n\tv_and_b32_e32 v185, %[hi], v188\n\tv_lshlrev_b32_e32 v186, 16, v189\n\tv_and_b32_e32 v187, %[hi], v189\n\t"
        "v_lshlrev_b32_e32 v188, 16, v190\n\tv_and_b32_e32 v189, %[hi], v190\n\tv_lshlrev_b32_e32 v190, 16, v191\n\tv_and_b32_e32 v191, %[hi], v191\n\t"
        ".Lb3_%=:\n\t"
        "s_bitcmp1_b32 %[fr], 4\n\ts_cbranch_scc0 .Lb4_%=\n\t"
        "v_lshlrev_b32_e32 v192, 16, v200\n\tv_and_b32_e32 v193, %[hi], v200\n\tv_lshlrev_b32_e32 v194, 16, v201\n\tv_and_b32_e32 v195, %[hi], v201\n\t"
        "v_lshlrev_b32_e32 v196, 16, v202\n\tv_and_b32_e32 v197, %[hi], v202\n\tv_lshlrev_b32_e32 v198, 16, v203\n\tv_and_b32_e32 v199, %[hi], v203\n\t"
        "v_lshlrev_b32_e32 v200, 16, v204\n\tv_and_b32_e32 v201, %[hi], v204\n\tv_lshlrev_b32_e32 v202, 16, v205\n\tv_and_b32_e32 v203, %[hi], v205\n\t"
        "v_lshlrev_b32_e32 v204, 16, v206\n\tv_and_b32_e32 v205, %[hi], v206\n\tv_lshlrev_b32_e32 v206, 16, v207\n\tv_and_b32_e32 v207, %[hi], v207\n\t"
        ".Lb4_%=:\n\t"
        "s_bitcmp1_b32 %[fr], 5\n\ts_cbranch_scc0 .Lb5_%=\n\t"
        "v_lshlrev_b32_e32 v208, 16, v216\n\tv_and_b32_e32 v209, %[hi], v216\n\tv_lshlrev_b32_e32 v210, 16, v217\n\tv_and_b32_e32 v211, %[hi], v217\n\t"
        "v_lshlrev_b32_e32 v212, 16, v218\n\tv_and_b32_e32 v213, %[hi], v218\n\tv_lshlrev_b32_e32 v214, 16, v219\n\tv_and_b32_e32 v215, %[hi], v219\n\t"
        "v_lshlrev_b32_e32 v216, 16, v220\n\tv_and_b32_e32 v217, %[hi], v220\n\tv_lshlrev_b32_e32 v218, 16, v221\n\tv_and_b32_e32 v219, %[hi], v221\n\t"
        "v_lshlrev_b32_e32 v220, 16, v222\n\tv_and_b32_e32 v221, %[hi], v222\n\tv_lshlrev_b32_e32 v222, 16, v223\n\tv_and_b32_e32 v223, %[hi], v223\n\t"
        ".Lb5_%=:\n\t"
        "s_bitcmp1_b32 %[fr], 6\n\ts_cbranch_scc0 .Lb6_%=\n\t"
        "v_lshlrev_b32_e32 v224, 16, v232\n\tv_and_b32_e32 v225, %[hi], v232\n\tv_lshlrev_b32_e32 v226, 16, v233\n\tv_and_b32_e32 v227, %[hi], v233\n\t"
        "v_lshlrev_b32_e32 v228, 16, v234\n\tv_and_b32_e32 v229, %[hi], v234\n\tv_lshlrev_b32_e32 v230, 16, v235\n\tv_and_b32_e32 v231, %[hi], v235\n\t"
        "v_lshlrev_b32_e32 v232, 16, v236\n\tv_and_b32_e32 v233, %[hi], v236\n\tv_lshlrev_b32_e32 v234, 16, v237\n\tv_and_b32_e32 v235, %[hi], v237\n\t"
        "v_lshlrev_b32_e32 v236, 16, v238\n\tv_and_b32_e32 v237, %[hi], v238\n\tv_lshlrev_b32_e32 v238, 16, v239\n\tv_and_b32_e32 v239, %[hi], v239\n\t"
        ".Lb6_%=:\n\t"
        "s_bitcmp1_b32 %[fr], 7\n\ts_cbranch_scc0 .Lb7_%=\n\t"
        "v_lshlrev_b32_e32 v240, 16, v248\n\tv_and_b32_e32 v241, %[hi], v248\n\tv_lshlrev_b32_e32 v242, 16, v249\n\tv_and_b32_e32 v243, %[hi], v249\n\t"
        "v_lshlrev_b32_e32 v244, 16, v250\n\tv_and_b32_e32 v245, %[hi], v250\n\tv_lshlrev_b32_e32 v246, 16, v251\n\tv_and_b32_e32 v247, %[hi], v251\n\t"
        "v_lshlrev_b32_e32 v248, 16, v252\n\tv_and_b32_e32 v249, %[hi], v252\n\tv_lshlrev_b32_e32 v250, 16, v253\n\tv_and_b32_e32 v251, %[hi], v253\n\t"
        "v_lshlrev_b32_e32 v252, 16, v254\n\tv_and_b32_e32 v253, %[hi], v254\n\tv_lshlrev_b32_e32 v254, 16, v255\n\tv_and_b32_e32 v255, %[hi], v255\n\t"
        ".Lb7_%=:\n\t"
        "s_cmp_eq_u32 %[got], -1\n\ts_cbranch_scc0 .Lidle_%=\n"
        ".Ldone_%=:"
        : "=&{v[144:159]}"(f[1]), "=&{v[160:175]}"(f[2]), "=&{v[176:191]}"(f[3]), "=&{v[192:207]}"(f[4]), "=&{v[208:223]}"(f[5]), "=&{v[224:239]}"(f[6]), "=&{v[240:255]}"(f[7]),
          [fl] "+v"(fl), [f2] "=&v"(f2), [got] "+s"(got), [spin] "+s"(spin), [fr] "=&s"(fresh), [so] "=&s"(so)
        : [tau] "s"(tau), [l16] "v"(l16), [l8] "v"(l8), [lr] "v"(lr), [sp] "s"(pb.srd), [sh] "s"(srdH), [ro] "s"(xg * 73728u + w * 2048u), [tok] "s"(tok_off),
          [flg] "s"(flg_off + xg * 128u), [hi] "s"(0xffff0000u), [slp] "i"(isA ? SLEEP_A : SLEEP_B)
        : "vcc", "scc", "memory");
#undef POLL
#undef SEEN
    STAMP(15);
    const f32x16 t = ((f[0] + f[1]) + (f[2] + f[3])) + ((f[4] + f[5]) + (f[6] + f[7]));
#pragma unroll
    for (int s = 0; s < 2; ++s)
    {
      const u32x4 ov = natural8(u32x4{cvtpk(t[8 * s], t[8 * s + 1]), cvtpk(t[8 * s + 2], t[8 * s + 3]), cvtpk(t[8 * s + 4], t[8 * s + 5]), cvtpk(t[8 * s + 6], t[8 * s + 7])});
      __builtin_amdgcn_raw_buffer_store_b128(ov, mk_srd(out, 2097152u), oo + s * 64, 0, OUTPOL);
    }
    asm volatile("s_waitcnt vmcnt(2)" : "+v"(fl) :: "memory");
    if ((lane >> 1) == w && fl != toks) asm volatile("buffer_store_dword %0, %1, %2, %3 offen sc1" :: "v"(fl), "v"(fo), "s"(srdH), "s"(flg_off) : "memory");
  }
}

__global__ __launch_bounds__(256, 1)
void fused_attn(const char* __restrict__ Ax, const char* __restrict__ Ak, const char* __restrict__ Av, const char* __restrict__ Awq, const char* __restrict__ Awo,
                const char* __restrict__ Bx, const char* __restrict__ Bk, const char* __restrict__ Bv, const char* __restrict__ Bwq, const char* __restrict__ Bwo,
                char* __restrict__ part, char* __restrict__ out) {
  const char* pc; unsigned left;
  asm volatile("s_getpc_b64 %0\n.Lpc_%=:\n\ts_mov_b32 %1, .Lhot_end-.Lpc_%=" : "=s"(pc), "=s"(left));
  const srd_t code = mk_srd(pc, left);
  if (vbid() >= 128) { body<true>(Ax, Ak, Av, Awq, Awo, Bwo, code, part, out); asm volatile(".Lhot_end:"); __builtin_amdgcn_endpgm(); }
  body<false>(Bx, Bk, Bv, Bwq, Bwo, Bwo, code, part, out);
}

extern "C" void kernel_launch(void* const* d_in, const int* in_sizes, int n_in, void* d_out, int out_size, void* d_ws, size_t ws_size, hipStream_t stream) {
  (void)in_sizes; (void)n_in; (void)out_size; (void)ws_size;
  (void)hipFuncSetAttribute((const void*)fused_attn, hipFuncAttributeMaxDynamicSharedMemorySize, (int)LDS_BYTES);
  fused_attn<<<256, 256, LDS_BYTES, stream>>>((const char*)d_in[0], (const char*)d_in[1], (const char*)d_in[2], (const char*)d_in[3], (const char*)d_in[4],
                                              (const char*)d_in[5], (const char*)d_in[6], (const char*)d_in[7], (const char*)d_in[8], (const char*)d_in[9], (char*)d_ws, (char*)d_out);
}
```

```cpp
#include <hip/hip_runtime.h>
#include <hip/hip_bf16.h>
#include <utility>
#include <type_traits>

typedef __bf16 bf16x8 __attribute__((ext_vector_type(8)));
typedef __bf16 bf16x4 __attribute__((ext_vector_type(4)));
typedef __bf16 bf16x2 __attribute__((ext_vector_type(2)));
typedef float f32x4 __attribute__((ext_vector_type(4)));
typedef float f32x2 __attribute__((ext_vector_type(2)));
typedef float f32x16 __attribute__((ext_vector_type(16)));
typedef unsigned u32x4 __attribute__((ext_vector_type(4)));
#define LAS __attribute__((address_space(3)))
#define DI __device__ __forceinline__
typedef __amdgpu_buffer_rsrc_t srd_t;
template <int... Is> using iseq = std::integer_sequence<int, Is...>;
template <int N> using make_iseq = std::make_integer_sequence<int, N>;

constexpr int KV_NT = 1, W_NT = 0, X_NT = 0;
constexpr int SK = 4, SV = 4, SP = 5;
constexpr unsigned EASY_T = 1350;
constexpr unsigned WARM = 40;

constexpr unsigned SLOT = 8192, LDS_BYTES = 20 * SLOT;
constexpr unsigned HOFF = 16u << 20;
constexpr float SC = 0.12751743082f;
constexpr float LMAX = 1048576.0f;

#define GAP() __builtin_amdgcn_sched_barrier(0)
#define PINS(x) asm volatile("" : "+s"(x))
#define PINV(x) asm volatile("" : "+v"(x))
DI unsigned rfl(unsigned x) { return __builtin_amdgcn_readfirstlane(x); }
DI unsigned cvtpk(float lo, float hi) { f32x2 v = {lo, hi}; bf16x2 b = __builtin_convertvector(v, bf16x2); return __builtin_bit_cast(unsigned, b); }
DI u32x4 pack2(const f32x4& a, const f32x4& b) { return u32x4{cvtpk(a[0], a[1]), cvtpk(a[2], a[3]), cvtpk(b[0], b[1]), cvtpk(b[2], b[3])}; }
DI u32x4 natural8(u32x4 w) {
  auto s0 = __builtin_amdgcn_permlane32_swap(w[0], w[2], false, false);
  auto s1 = __builtin_amdgcn_permlane32_swap(w[1], w[3], false, false);
  auto t0 = __builtin_amdgcn_permlane16_swap(s0[0], s0[1], false, false);
  auto t1 = __builtin_amdgcn_permlane16_swap(s1[0], s1[1], false, false);
  return u32x4{t0[0], t1[0], t0[1], t1[1]};
}
#define MF "v_mfma_f32_16x16x32_bf16 "
template <bool Z> DI void mm_vvv(f32x4& c, const bf16x8& a, const bf16x8& b) {
  if constexpr (Z) asm volatile(MF "%0, %1, %2, 0" : "=&v"(c) : "v"(a), "v"(b));
  else             asm volatile(MF "%0, %1, %2, %0" : "+v"(c) : "v"(a), "v"(b));
}
template <bool Z> DI void mm_vva(f32x4& c, const bf16x8& a, const bf16x8& b) {
  if constexpr (Z) asm volatile(MF "%0, %1, %2, 0" : "=&v"(c) : "v"(a), "a"(b));
  else             asm volatile(MF "%0, %1, %2, %0" : "+v"(c) : "v"(a), "a"(b));
}
DI void mm_vca(f32x4& d, const bf16x8& a, const bf16x8& b, const f32x4& c) { asm volatile(MF "%0, %1, %2, %3" : "=&v"(d) : "v"(a), "a"(b), "v"(c)); }
DI void mm_avv(f32x4& c, const bf16x8& a, const bf16x8& b) { asm volatile(MF "%0, %1, %2, %0" : "+a"(c) : "v"(a), "v"(b)); }
DI void set_m0(unsigned v) { asm volatile("s_mov_b32 m0, %0" :: "s"(v) : "memory"); }
template <int NT, int P> DI void dma_p(unsigned voff, srd_t srd, unsigned soff) {
  if constexpr (NT) asm volatile("buffer_load_dwordx4 %0, %1, %2 offen offset:%3 nt lds" :: "v"(voff), "s"(srd), "s"(soff), "i"(1024 * P) : "memory");
  else              asm volatile("buffer_load_dwordx4 %0, %1, %2 offen offset:%3 lds" :: "v"(voff), "s"(srd), "s"(soff), "i"(1024 * P) : "memory");
}
struct Src { unsigned v[4]; srd_t srd; };
template <int NT> DI void dma_unit(const Src& s, unsigned soff, unsigned ldsaddr) {
  asm volatile("s_mov_b32 m0, %0\n\ts_nop 0" :: "s"(ldsaddr) : "memory");
  dma_p<NT, 0>(s.v[0], s.srd, soff); dma_p<NT, 1>(s.v[1], s.srd, soff); dma_p<NT, 2>(s.v[2], s.srd, soff); dma_p<NT, 3>(s.v[3], s.srd, soff);
}
template <int NT> DI void dma_elem(const Src& s, unsigned soff, unsigned ustep, unsigned ldsaddr) { dma_unit<NT>(s, soff, ldsaddr); dma_unit<NT>(s, soff + ustep, ldsaddr + 4096u); }
template <int N> DI void wait_vm() { asm volatile("s_waitcnt vmcnt(%0)" :: "i"(N) : "memory"); }
template <int N, int L> DI void wait_vm_lgkm() { asm volatile("" ::: "memory"); __builtin_amdgcn_s_waitcnt((N & 15) | ((N >> 4) << 14) | 0x70 | (L << 8)); asm volatile("" ::: "memory"); }
DI void wait_lgkm0() { wait_vm_lgkm<63, 0>(); }
DI void barrier() { asm volatile("s_barrier" ::: "memory"); }
DI bf16x8 lds128(unsigned a) { return *(const LAS bf16x8*)(size_t)(a); }
DI bf16x4 lds_tr1(unsigned a) { return __builtin_amdgcn_ds_read_tr16_b64_v4bf16((LAS bf16x4*)(size_t)(a)); }
DI bf16x8 cat(bf16x4 lo, bf16x4 hi) { return __builtin_shufflevector(lo, hi, 0, 1, 2, 3, 4, 5, 6, 7); }
DI bf16x8 lds_tr(unsigned a0, unsigned a1) { return cat(lds_tr1(a0), lds_tr1(a1)); }
DI srd_t mk_srd(const void* base, unsigned bytes) { return __builtin_amdgcn_make_buffer_rsrc((void*)base, (short)0, (int)bytes, 0x00020000); }
template <bool VX> DI void mk_src_rows(Src& s, const char* base, unsigned bytes, unsigned stride, unsigned lane) {
  s.srd = mk_srd(base, bytes);
  const unsigned row = (lane >> 2) & 3u, cb = lane >> 4, sl = lane & 3u;
#pragma unroll
  for (int p = 0; p < 4; ++p) s.v[p] = row * stride + cb * 64u + ((sl ^ (VX ? 2 * (p & 1) : (p & 2))) << 4) + (4u * stride - 1024u) * p;
}
template <bool VX> DI void mk_src_w(Src& s, const char* base, unsigned bytes, unsigned stride, unsigned lane) {
  s.srd = mk_srd(base, bytes);
  const unsigned sub = lane >> 5, cb = (lane >> 4) & 1u, row = (lane >> 2) & 3u, sl = lane & 3u;
#pragma unroll
  for (int p = 0; p < 4; ++p) s.v[p] = (4u * sub + row) * stride + cb * 64u + ((sl ^ (VX ? 2u * sub : 2u * (p & 1))) << 4) + (8u * stride - 1024u) * p;
}
DI void touch(unsigned& d, unsigned& voff, const char* base) {
  asm volatile("global_load_dword %0, %1, %2 nt\n\tglobal_load_dword %0, %1, %2\n\tv_add_u32_e32 %1, 0x1000, %1" : "+v"(d), "+v"(voff) : "s"(base) : "memory");
}
DI unsigned ksoff(unsigned m, unsigned rk) { return ((m + rk) & 15u) * 8192u; }

struct PC {
  unsigned r01, r2;
  unsigned w01[2], w2[2];
  unsigned xb, xmask, xa;
  unsigned z, soff;
};
template <unsigned TS_, unsigned QS_, unsigned ZI_, unsigned ZM_, unsigned ZO_, unsigned US_, unsigned CS_, bool ROT_> struct PK {
  static constexpr unsigned TS = TS_, QS = QS_, ZI = ZI_, ZM = ZM_, ZO = ZO_, US = US_, CS = CS_; static constexpr bool ROT = ROT_; };
DI unsigned q_soff(unsigned z) { return ((z & 0xC300u) * 0x440u) & 0x3C0000u; }
template <int POS> DI unsigned ring_lds(const PC& c) { return POS == 2 ? c.r2 : c.r01 + POS * SLOT; }
template <int POS> DI unsigned ring_rd(const PC& c, int d) { return (POS == 2 ? c.w2[d & 1] : c.w01[d & 1] + POS * SLOT) + (d >> 1) * 256u; }
struct NoX { static constexpr bool ACT = true; template <int G> DI void run() const {} };
struct EasyX {
  static constexpr bool ACT = true;
  unsigned long long& t; unsigned lim;
  template <int G> DI void run() const {
    if constexpr (G == 8) asm volatile("s_memrealtime %0" : "=s"(t));
    if constexpr (G == 16) PINS(t);
    if constexpr (G == 16) asm volatile("s_cmp_lt_i32 %0, 0\n\ts_cbranch_scc1 .Le_%=\n\ts_sleep %1\n.Le_%=:" :: "s"((unsigned)t - lim), "i"(6) : "scc");
  }
};
template <class K, int POS, int S, bool Z, bool RD, bool DM, int NT, bool XT, int M, class X>
DI void proj_gap(f32x4 (&acc)[8][4], const bf16x8 (&cur)[4], bf16x8 (&nxt)[4], bf16x4 (&lo)[4], bf16x8 (&bf)[4], PC& c, unsigned soff, const Src& src, const Src& xs, unsigned xsoff, const X& ex) {
  constexpr int qb = M >> 2, db = M & 3;
  mm_vvv<Z>(acc[4 * S + db][qb], cur[db], bf[qb]);
  if constexpr (RD && M < 8) {
    constexpr int d = M >> 1; const unsigned a = S == 0 ? ring_rd<POS>(c, d) + 4096u : ring_rd<(POS + 1) % 3>(c, d);
    if constexpr (M & 1) nxt[d] = cat(lo[d], lds_tr1(a + K::TS)); else lo[d] = lds_tr1(a);
  }
  if constexpr (X::ACT && RD && S == 1 && db == 3) bf[qb] = lds128(c.xa + qb * K::QS);
  if constexpr (XT) {
    if constexpr (M < 8 && !(M & 1)) dma_p<X_NT, M / 2>(xs.v[M / 2], xs.srd, xsoff);
    if constexpr (M == 7) set_m0(ring_lds<POS>(c) + S * 4096u);
    if constexpr (M >= 8 && !(M & 1)) dma_p<NT, (M - 8) / 2>(src.v[(M - 8) / 2], src.srd, soff);
  } else if constexpr (DM && M >= 8 && !(M & 1)) dma_p<NT, (M - 8) / 2>(src.v[(M - 8) / 2], src.srd, soff);
  if constexpr (X::ACT && S == 0 && M == 9) { PINS(c.z); c.z = (c.z + K::ZI) & K::ZM; PINS(c.z); }
  if constexpr (X::ACT && S == 0 && M == 11) { c.xa = (c.z & c.xmask) | c.xb; PINV(c.xa); }
  if constexpr (S == 1 && DM && !K::ROT && M == 9) { PINS(c.soff); c.soff = (c.soff + K::CS) & (16u * K::CS - 1u); PINS(c.soff); }
  if constexpr (S == 1 && DM && K::ROT && M == 9) { PINS(c.z); c.soff = (c.z + 0x3100u) & 0xC300u; PINS(c.soff); }
  if constexpr (S == 1 && DM && K::ROT && M == 13) { PINS(c.soff); c.soff = (c.soff * 0x440u) & 0x3C0000u; PINS(c.soff); }
  ex.template run<16 * S + M>();
  GAP();
}
template <class K, int POS, int S, bool Z, bool RD, bool DM, int NT, int VM, bool BAR, bool XT, class X, int... Ms>
DI void proj_unit(f32x4 (&acc)[8][4], const bf16x8 (&cur)[4], bf16x8 (&nxt)[4], bf16x8 (&bf)[4], PC& c, const Src& src, const Src& xs, unsigned xsoff, unsigned xdst, const X& ex, iseq<Ms...>) {
  wait_vm_lgkm<(RD ? VM : 63), (S == 0 ? 2 : 0)>();
  if constexpr (BAR) barrier();
  const unsigned soff = c.soff + S * K::US;
  if constexpr (XT) set_m0(xdst); else if constexpr (DM) set_m0(ring_lds<POS>(c) + S * 4096u);
  GAP();
  bf16x4 lo[4];
  (proj_gap<K, POS, S, Z, RD, DM, NT, XT, Ms>(acc, cur, nxt, lo, bf, c, soff, src, xs, xsoff, ex), ...);
}
template <class K, int POS, bool Z, bool LAST, bool DM, int NT, int VM0, int VM1, bool BAR1 = false, bool XT0 = false, bool XT1 = false, class X = NoX>
DI void proj_elem(f32x4 (&acc)[8][4], bf16x8 (&a0)[4], bf16x8 (&a1)[4], bf16x8 (&bf)[4], PC& c, const Src& src, const X& ex = X{},
                  const Src* xs = nullptr, unsigned xso0 = 0, unsigned xd0 = 0, unsigned xso1 = 0, unsigned xd1 = 0) {
  proj_unit<K, POS, 0, Z, true, DM, NT, VM0, false, XT0>(acc, a0, a1, bf, c, src, xs ? *xs : src, xso0, xd0, ex, make_iseq<16>{});
  proj_unit<K, POS, 1, Z, !LAST, DM, NT, VM1, BAR1, XT1>(acc, a1, a0, bf, c, src, xs ? *xs : src, xso1, xd1, ex, make_iseq<16>{});
}
template <class K> DI void proj_setup(PC& c, unsigned tb, unsigned xb, unsigned z0, unsigned s3) {
  c.w01[0] = c.r01 + tb; c.w01[1] = c.w01[0] ^ 32u; c.w2[0] = c.r2 + tb; c.w2[1] = c.w2[0] ^ 32u;
  PINV(c.w01[0]); PINV(c.w01[1]); PINV(c.w2[0]); PINV(c.w2[1]);
  c.xb = xb; c.xmask = K::ZO; PINV(c.xmask);
  c.z = z0; c.soff = s3;
}
template <class K, int POS> DI void proj_start(bf16x8 (&a0)[4], bf16x8 (&bf)[4], PC& c, unsigned tb, unsigned xb, unsigned z0, unsigned s3) {
  proj_setup<K>(c, tb, xb, z0, s3);
  c.xa = (c.z & K::ZO) | xb; PINV(c.xa);
#pragma unroll
  for (int d = 0; d < 4; ++d) { const unsigned a = ring_rd<POS>(c, d); a0[d] = lds_tr(a, a + K::TS); }
  GAP();
#pragma unroll
  for (int q = 0; q < 4; ++q) bf[q] = lds128(c.xa + q * K::QS);
  GAP();
}
#define ACC_ALL(C, a) C(a[0][0]), C(a[0][1]), C(a[0][2]), C(a[0][3]), C(a[1][0]), C(a[1][1]), C(a[1][2]), C(a[1][3]), C(a[2][0]), C(a[2][1]), C(a[2][2]), C(a[2][3]), C(a[3][0]), C(a[3][1]), C(a[3][2]), C(a[3][3])
DI void acc_fence_v(f32x4 (&acc)[8][4]) {
  asm volatile("s_nop 7\n\ts_nop 3" : ACC_ALL("+v", acc) :: "memory");
  f32x4 (&hi)[4][4] = *(f32x4 (*)[4][4])&acc[4];
  asm volatile("" : ACC_ALL("+v", hi) :: "memory");
  GAP();
}
DI void acc_fence_a(f32x4 (&acc)[8][4]) {
  asm volatile("s_nop 7\n\ts_nop 3" : ACC_ALL("+a", acc) :: "memory");
  f32x4 (&hi)[4][4] = *(f32x4 (*)[4][4])&acc[4];
  asm volatile("" : ACC_ALL("+a", hi) :: "memory");
  GAP();
}

struct Stash { u32x4 p[64]; unsigned base[3]; };
template <int I> DI void stash_load1(Stash& st, unsigned vk, const Src& sK, const Src& sV, unsigned rk, unsigned wo_) {
  constexpr int m = I >> 2, isV = (I >> 1) & 1, pc_ = I & 1;
  const unsigned so = ksoff(m, rk) + wo_;
  if constexpr (I < 56) asm volatile("buffer_load_dwordx4 %0, %1, %2, %3 offen offset:%4 nt" : "=a"(st.p[I]) : "v"(isV ? sV.v[pc_] : vk), "s"(isV ? sV.srd : sK.srd), "s"(so), "i"(1024 * pc_) : "memory");
  else                  asm volatile("buffer_load_dwordx4 %0, %1, %2, %3 offen offset:%4 nt" : "=v"(st.p[I]) : "v"(isV ? sV.v[pc_] : vk), "s"(isV ? sV.srd : sK.srd), "s"(so), "i"(1024 * pc_) : "memory");
}
template <int... Is> DI void stash_load(Stash& st, unsigned vk, const Src& sK, const Src& sV, unsigned rk, unsigned wo_, iseq<Is...>) { (stash_load1<Is>(st, vk, sK, sV, rk, wo_), ...); }
constexpr int kv_pair(int m) { constexpr int t[10] = {0, 1, 2, 3, 8, 9, 4, 5, 6, 7}; return t[m % 10]; }
constexpr unsigned long long kv_tab() { unsigned long long v = 0; for (int i = 0; i < 16; ++i) v |= (unsigned long long)kv_pair(i) << (4 * i); return v; }
template <int I> DI void stash_write1(const Stash& st) {
  constexpr int m = I >> 2, isV = (I >> 1) & 1, pc_ = I & 1, off = kv_pair(m) * 16384 + isV * 8192 + pc_ * 1024;
  if constexpr (I < 56) asm volatile("ds_write_b128 %0, %1 offset:%2" :: "v"(st.base[off >> 16]), "a"(st.p[I]), "i"(off & 65535) : "memory");
  else                  asm volatile("ds_write_b128 %0, %1 offset:%2" :: "v"(st.base[off >> 16]), "v"(st.p[I]), "i"(off & 65535) : "memory");
}

struct AC {
  f32x4 o[8][4];
  bf16x8 q[4][4];
  bf16x8 kf[8], vf[8], pf[4];
  f32x4 negm[4];
  f32x4 l[4];
  bf16x8 ones;
};
typedef f32x4 ST[2][4];
constexpr int NEB = 22;
template <int E> DI float sm_val(ST& S) { return S[(E >> 2) & 1][E >> 3][E & 3]; }
template <int E> DI void sm_exp1(ST& S) { float a; asm volatile("v_exp_f32_e32 %0, %1" : "=v"(a) : "v"(sm_val<E>(S))); S[(E >> 2) & 1][E >> 3][E & 3] = a; }
template <int QB, int P> DI void sm_cvt(ST& S, AC& c) {
  u32x4 w = __builtin_bit_cast(u32x4, c.pf[QB]);
  unsigned r;
  asm volatile("v_cvt_pk_bf16_f32 %0, %1, %2" : "=v"(r) : "v"(sm_val<8 * QB + 2 * P>(S)), "v"(sm_val<8 * QB + 2 * P + 1>(S)));
  w[P] = r;
  c.pf[QB] = __builtin_bit_cast(bf16x8, w);
}
template <bool DM, int VM, int R, int M> DI void mem_pre(unsigned ldsaddr) {
  if constexpr (M == 6) wait_lgkm0();
  if constexpr (DM && (M == 6 || M == 14)) set_m0(ldsaddr + (M == 14 ? 4096u : 0u));
  if constexpr (M == R && VM < 63) wait_vm<VM>();
}
template <bool DM, int NT, int M> DI void mem_post(const Src& src, unsigned soff, unsigned ustep) {
  if constexpr (DM && M >= 6 && M <= 20 && !(M & 1)) dma_p<NT, ((M - 6) / 2) & 3>(src.v[((M - 6) / 2) & 3], src.srd, soff + (M >= 14 ? ustep : 0u));
}
template <int WR, int M> DI void mem_wr(const Stash& st) {
  if constexpr (WR >= 40 && M >= 7 && M < 13) stash_write1<WR + M - 7>(st);
  if constexpr (WR >= 0 && WR < 40 && M >= 8 && M < 16)
    asm volatile("ds_write_b128 %0, %1 offset:%2" :: "v"(st.base[0]), "a"(st.p[WR + M - 8]), "i"((M - 8) * 1024) : "memory");
}
struct PA { unsigned rd0, rd1, soff, fill; };
template <bool QK, bool DM, int NT, int VM, int WR, int M, class F>
DI void gapA(AC& c, ST& S, ST& Sn, PA& pa, F& addr, const Src& src, unsigned ustep, const Stash& st, bf16x4& lo) {
  constexpr int R = QK ? 16 : 0;
  if constexpr ((DM || !QK) && M == 0) addr(pa);
  if constexpr (QK) mem_pre<DM, VM, 16, M>(pa.fill); else if constexpr (M == 0 && VM < 63) wait_vm<VM>();
  if constexpr (QK) {
    constexpr int f = M >> 2, s = f >> 1, kb = f & 1, qb = M & 3;
    if constexpr (s == 0) mm_vca(Sn[kb][qb], c.kf[f], c.q[qb][0], c.negm[qb]); else mm_vva<false>(Sn[kb][qb], c.kf[f], c.q[qb][s]);
  }
  mem_post<DM, NT, M>(src, pa.soff, ustep);
  mem_wr<WR, M>(st);
  if constexpr (QK && !DM && M == 10) addr(pa);
  if constexpr (QK && M == 12) asm volatile("" :: "v"(c.negm[0]), "v"(c.negm[1]), "v"(c.negm[2]), "v"(c.negm[3]));
  if constexpr (M >= R && M < R + 16) {
    constexpr int db = (M - R) >> 1; const unsigned a = ((db & 1) ? pa.rd1 : pa.rd0) + (db >> 1) * 256u;
    if constexpr (M & 1) c.vf[db] = cat(lo, lds_tr1(a + 4096u)); else lo = lds_tr1(a);
  }
  if constexpr (NEB + M < 32) sm_exp1<NEB + M>(S);
  if constexpr (M >= 16 && (M & 1)) sm_cvt<2 + ((M - 16) >> 3), ((M - 16) >> 1) & 3>(S, c);
  GAP();
}
template <bool QK, bool DM, int NT, int VM, int WR, class F, int... Ms>
DI void phaseA_(AC& c, ST& S, ST& Sn, F& addr, const Src& src, unsigned ustep, const Stash& st, iseq<Ms...>) {
  PA pa; bf16x4 lo;
  (gapA<QK, DM, NT, VM, WR, Ms>(c, S, Sn, pa, addr, src, ustep, st, lo), ...);
}
template <bool QK, bool DM, int NT, int VM, int WR = -1, class F>
DI void phaseA(AC& c, ST& S, ST& Sn, F addr, const Src& src, unsigned ustep, const Stash& st) {
  phaseA_<QK, DM, NT, VM, WR>(c, S, Sn, addr, src, ustep, st, make_iseq<32>{});
}
template <bool MAX> DI float xlane(float v) {
  auto a = __builtin_amdgcn_permlane32_swap(__float_as_uint(v), __float_as_uint(v), false, false);
  v = MAX ? fmaxf(__uint_as_float(a[0]), __uint_as_float(a[1])) : __uint_as_float(a[0]) + __uint_as_float(a[1]);
  auto b = __builtin_amdgcn_permlane16_swap(__float_as_uint(v), __float_as_uint(v), false, false);
  return MAX ? fmaxf(__uint_as_float(b[0]), __uint_as_float(b[1])) : __uint_as_float(b[0]) + __uint_as_float(b[1]);
}
DI float row_max(const f32x4& x, const f32x4& y) {
  return xlane<true>(fmaxf(fmaxf(fmaxf(x[0], x[1]), fmaxf(x[2], x[3])), fmaxf(fmaxf(y[0], y[1]), fmaxf(y[2], y[3]))));
}
template <bool SM, bool KR, bool DM, int NT, int VM, int WR, int M, class F>
DI void gapB(AC& c, ST& Sn, PA& pa, F& addr, const Src& src, unsigned ustep, const Stash& st) {
  mem_pre<DM, (KR ? VM : 63), 18, M>(pa.fill);
  {
    constexpr int h = M / 18, r = M % 18, qb = 2 * h + (r & 1);
    if constexpr (r < 2) mm_vvv<false>(c.l[qb], c.ones, c.pf[qb]); else mm_avv(c.o[(r - 2) >> 1][qb], c.vf[(r - 2) >> 1], c.pf[qb]);
  }
  mem_post<DM, NT, M>(src, pa.soff, ustep);
  mem_wr<WR, M>(st);
  if constexpr (M == 0) addr(pa);
  if constexpr (KR && M >= 18 && M < 34 && !(M & 1)) { constexpr int f = (M - 18) >> 1; c.kf[f] = lds128(pa.rd0 + (f & 1) * 4096u + (f >> 1) * 256u); }
  if constexpr (SM) {
    if constexpr (M >= 4 && M < 18) sm_exp1<M - 4>(Sn);
    if constexpr (M >= 19 && M < 35 && (M & 1)) sm_exp1<14 + (M - 19) / 2>(Sn);
    if constexpr (M >= 20 && !(M & 1)) sm_cvt<((M - 20) >> 3), ((M - 20) >> 1) & 3>(Sn, c);
  }
  GAP();
}
template <bool SM, bool KR, bool DM, int NT, int VM, int WR, class F, int... Ms>
DI void phaseB_(AC& c, ST& Sn, F& addr, const Src& src, unsigned ustep, const Stash& st, iseq<Ms...>) {
  PA pa;
  (gapB<SM, KR, DM, NT, VM, WR, Ms>(c, Sn, pa, addr, src, ustep, st), ...);
}
template <bool SM, bool KR, bool DM, int NT, int VM, int WR = -1, class F>
DI void phaseB(AC& c, ST& Sn, F addr, const Src& src, unsigned ustep, const Stash& st) {
  phaseB_<SM, KR, DM, NT, VM, WR>(c, Sn, addr, src, ustep, st, make_iseq<36>{});
}
DI void k_read(AC& ac, unsigned ka) {
#pragma unroll
  for (int f = 0; f < 8; ++f) ac.kf[f] = lds128(ka + (f & 1) * 4096u + (f >> 1) * 256u);
}
template <bool DM, int M> DI void qk0_gap(AC& ac, ST& SA, const Src& src, unsigned soff, unsigned fill) {
  constexpr int f = M >> 2, qb = M & 3;
  if constexpr (DM && (M == 0 || M == 16)) set_m0(fill + M * 256u);
  mm_vva<(f < 2)>(SA[f & 1][qb], ac.kf[f], ac.q[qb][f >> 1]);
  if constexpr (DM && qb == 0) dma_p<KV_NT, f & 3>(src.v[f & 3], src.srd, soff + (f >> 2) * 4096u);
  GAP();
}
template <bool DM, int... Ms> DI void qk0(AC& ac, ST& SA, const Src& src, unsigned soff, unsigned fill, iseq<Ms...>) { (qk0_gap<DM, Ms>(ac, SA, src, soff, fill), ...); }
template <int... Js> DI void exp_first(ST& SA, iseq<Js...>) { (sm_exp1<Js>(SA), ...); }
template <int... Js> DI void cvt_first(ST& SA, AC& c, iseq<Js...>) { (sm_cvt<(Js >> 2), (Js & 3)>(SA, c), ...); }
DI void sm_first(AC& ac, ST& SA) {
  asm volatile("s_nop 7\n\ts_nop 3" : "+v"(SA[0][0]), "+v"(SA[0][1]), "+v"(SA[0][2]), "+v"(SA[0][3]), "+v"(SA[1][0]), "+v"(SA[1][1]), "+v"(SA[1][2]), "+v"(SA[1][3]) :: "memory");
  GAP();
#pragma unroll
  for (int q = 0; q < 4; ++q) {
    const float m = row_max(SA[0][q], SA[1][q]);
    SA[0][q] -= m; SA[1][q] -= m;
    ac.negm[q] = f32x4{-m, -m, -m, -m};
    ac.l[q] = f32x4{};
    ac.pf[q] = bf16x8{};
  }
  ac.ones = __builtin_bit_cast(bf16x8, u32x4{0x3f803f80u, 0x3f803f80u, 0x3f803f80u, 0x3f803f80u});
  asm volatile("" : "+v"(ac.negm[0]), "+v"(ac.negm[1]), "+v"(ac.negm[2]), "+v"(ac.negm[3]), "+v"(ac.l[0]), "+v"(ac.l[1]), "+v"(ac.l[2]), "+v"(ac.l[3]), "+v"(ac.ones));
  exp_first(SA, make_iseq<NEB>{});
  cvt_first(SA, ac, make_iseq<8>{});
  GAP();
}
struct BS { unsigned p[4], v[6], k[2], rk, wo, l16; };
constexpr bool b_dma(int f) { return f != 2 * (SK - 3) && f != 2 * (SV - 2) + 1 && f != 2 * (SP - 2) + 1 && f < 30 && f != 27; }
constexpr int b_vm(int f) {
  const int s = f == 30 ? 26 : f - 3;
  if (!b_dma(s)) return 63;
  int n = b_dma(f) ? 5 + (f & 1) : 0;
  for (int i = s + 1; i < f; ++i) n += b_dma(i) ? 8 : 0;
  return n;
}
template <class KO, int I> DI void b_step(AC& ac, ST& S, ST& Sn, BS& b, const Src& sK, const Src& sV, const Src& sO, Stash& st) {
  constexpr int fa = 2 * I, fb = 2 * I + 1, vi = (I == SP || I == 15) ? 4 : (I & 1) ? 0 : 2;
  const unsigned pa = b.p[(I & 1) ? 3 : 1], pb = b.p[(I & 1) ? 0 : 2];
  if constexpr (I >= 1 && I <= 13) barrier();
  if constexpr (I == SK - 3) st.base[0] = pa + b.l16;
  phaseA<(I < 15), b_dma(fa), (I < 14 ? KV_NT : W_NT), b_vm(fa), (I == SK - 3 ? 0 : -1)>(ac, S, Sn, [&](PA& p) __attribute__((always_inline)) {
    PINS(b.rk);
    p.soff = I < 14 ? ksoff(I < 13 ? I + 3 : 15, b.rk) : b.wo; p.fill = I == 14 ? b.p[0] : pa;
    p.rd0 = b.v[vi] + (I == SP ? SLOT : 0u); p.rd1 = b.v[vi + 1] + (I == SP ? SLOT : 0u); }, I < 13 ? sK : I < 14 ? sV : sO, I < 14 ? 4096u : KO::US, st);
  if constexpr (I == SV - 2) st.base[0] = pb + b.l16;
  if constexpr (I < 15)
    phaseB<true, (I < 14), b_dma(fb), (I < 13 ? KV_NT : W_NT), b_vm(fb), (I == SV - 2 ? 8 : -1)>(ac, Sn, [&](PA& p) __attribute__((always_inline)) {
      PINS(b.rk);
      p.soff = I < 13 ? ksoff(I + 2, b.rk) : b.wo + KO::CS; p.fill = I == 14 ? b.p[1] : pb; p.rd0 = b.k[(I & 1) ? 0 : 1]; }, I < 13 ? sV : sO, I < 13 ? 4096u : KO::US, st);
}
template <int CH, int Q, int J> DI void o_word(const AC& c, const float (&inv)[4], bf16x8 (&nb)[4]) {
  u32x4 w = __builtin_bit_cast(u32x4, nb[Q]);
  unsigned r; float t;
  asm volatile("v_accvgpr_read_b32 %0, %2\n\tv_accvgpr_read_b32 %1, %3\n\tv_mul_f32_e32 %0, %0, %4\n\tv_mul_f32_e32 %1, %1, %4\n\tv_cvt_pk_bf16_f32 %0, %0, %1"
               : "=&v"(r), "=&v"(t) : "a"(c.o[2 * CH + (J >> 1)][Q][2 * (J & 1)]), "a"(c.o[2 * CH + (J >> 1)][Q][2 * (J & 1) + 1]), "v"(inv[Q]));
  w[J] = r;
  nb[Q] = __builtin_bit_cast(bf16x8, w);
}
template <int CH, int Q> DI void o_put(const bf16x8 (&nb)[4], unsigned ow) { asm volatile("ds_write_b128 %0, %1 offset:%2" :: "v"(ow), "v"(nb[Q]), "i"(CH * 4096 + Q * 1024) : "memory"); }
template <int E> struct CvX {
  static constexpr bool ACT = E == 3;
  const AC& c; const float (&inv)[4]; const bf16x8 (&cur)[4]; bf16x8 (&nb)[4]; unsigned ow;
  template <int G> DI void run() const {
    if constexpr (E < 3 && (G & 1)) o_word<E + 1, (G >> 3), (G >> 1) & 3>(c, inv, nb);
    if constexpr (E < 3 && ((G & 15) == 8 || (G & 15) == 10)) o_put<E, 2 * (G >> 4) + ((G >> 1) & 1)>(cur, ow);
    if constexpr (E == 2 && (G & 15) == 12) o_put<3, (G >> 4)>(nb, ow);
    if constexpr (E == 3 && (G == 8 || G == 10)) o_put<3, 2 + ((G >> 1) & 1)>(cur, ow);
  }
};
template <class K, int VM, bool DM, int M> DI void gapL(AC& c, float (&inv)[4], bf16x8 (&nb)[4], bf16x8 (&a0)[4], bf16x4& lo, const PC& pc, const Src& src, unsigned soff) {
  if constexpr (M == 6) wait_vm_lgkm<VM, 0>();
  if constexpr (DM && (M == 12 || M == 32)) set_m0(pc.r2 + (M == 32 ? 4096u : 0u));
  if constexpr (M < 4) mm_vvv<false>(c.l[M], c.ones, c.pf[M]); else mm_avv(c.o[(M - 4) >> 2][M & 3], c.vf[(M - 4) >> 2], c.pf[M & 3]);
  if constexpr (M >= 6 && M < 14) {
    constexpr int d = (M - 6) >> 1; const unsigned a = ring_rd<0>(pc, d);
    if constexpr (M & 1) a0[d] = cat(lo, lds_tr1(a + K::TS)); else lo = lds_tr1(a);
  }
  if constexpr (M >= 4 && M < 12 && !(M & 1)) asm volatile("v_rcp_f32_e32 %0, %1" : "=v"(inv[(M - 4) >> 1]) : "v"(c.l[(M - 4) >> 1][0]));
  if constexpr (M >= 16 && M < 32) o_word<0, ((M - 16) >> 2), (M & 3)>(c, inv, nb);
  if constexpr (DM && ((M >= 12 && M < 16) || M >= 32)) dma_p<W_NT, M & 3>(src.v[M & 3], src.srd, soff + (M >= 32 ? K::US : 0u));
  GAP();
}
template <class K, int VM, bool DM, int... Ms> DI void phaseL(AC& c, float (&inv)[4], bf16x8 (&nb)[4], bf16x8 (&a0)[4], const PC& pc, const Src& src, unsigned soff, iseq<Ms...>) {
  bf16x4 lo;
#pragma unroll
  for (int q = 0; q < 4; ++q) nb[q] = bf16x8{};
  (gapL<K, VM, DM, Ms>(c, inv, nb, a0, lo, pc, src, soff), ...);
}
struct Pub { u32x4 pk[16]; srd_t srd; unsigned v[4], so; };
template <int F, int H> DI void pub_half(const f32x4 (&acc)[8][4], Pub& p) {
  const f32x4& a = acc[4 * (F >> 3) + 2 * (F & 1) + H][(F >> 1) & 3];
  unsigned r0, r1;
  asm volatile("v_cvt_pk_bf16_f32 %0, %2, %3\n\tv_cvt_pk_bf16_f32 %1, %4, %5" : "=&v"(r0), "=v"(r1) : "v"(a[0]), "v"(a[1]), "v"(a[2]), "v"(a[3]));
  p.pk[F][2 * H] = r0; p.pk[F][2 * H + 1] = r1;
  if constexpr (H) asm volatile("buffer_store_dwordx4 %0, %1, %2, %3 offen offset:%4 sc0 sc1" :: "v"(p.pk[F]), "v"(p.v[F >> 2]), "s"(p.srd), "s"(p.so), "i"((F & 3) * 1024) : "memory");
}
template <class K, int U, int M>
DI void tail_gap(f32x4 (&acc)[8][4], const bf16x8 (&cur)[4], bf16x8 (&nxt)[4], bf16x4 (&lo)[4], bf16x8 (&bf)[4], const PC& c, const unsigned (&xa)[3], Pub& p) {
  constexpr int qb = M >> 2, db = M & 3, S = U / 3, ne = (U + 1) % 3;
  mm_vvv<false>(acc[4 * S + db][qb], cur[db], bf[qb]);
  if constexpr (U < 5 && M < 8) {
    constexpr int d = M >> 1; const unsigned a = ring_rd<(ne + 1) % 3>(c, d) + ((U + 1) / 3) * 4096u;
    if constexpr (M & 1) nxt[d] = cat(lo[d], lds_tr1(a + K::TS)); else lo[d] = lds_tr1(a);
  }
  if constexpr (U < 5 && db == 3) bf[qb] = lds128(xa[ne] + qb * K::QS);
  if constexpr ((U == 3 || U == 4) && M >= 8) pub_half<4 * (U - 3) + ((M - 8) >> 1), (M & 1)>(acc, p);
  if constexpr (U == 5 && M >= 8) pub_half<8 + ((M - 8) >> 1), (M & 1)>(acc, p);
  GAP();
}
template <class K, int U, int VM, int... Ms>
DI void tail_unit(f32x4 (&acc)[8][4], const bf16x8 (&cur)[4], bf16x8 (&nxt)[4], bf16x8 (&bf)[4], const PC& c, const unsigned (&xa)[3], Pub& p, iseq<Ms...>) {
  wait_vm_lgkm<VM, 2>();
  GAP();
  bf16x4 lo[4];
  (tail_gap<K, U, Ms>(acc, cur, nxt, lo, bf, c, xa, p), ...);
}
template <int I> DI void own_put1(const u32x4& v, unsigned a) { asm volatile("ds_write_b128 %0, %1 offset:%2 ; %3" :: "v"(a), "v"(v), "i"((I & 7) * 1024), "i"(I) : "memory"); }
template <int B, int... Is> DI void own_put(const Pub& p, unsigned a, iseq<Is...>) { (own_put1<B + Is>(p.pk[B + Is], a), ...); }
template <int... Is> DI void pub_rest(const f32x4 (&acc)[8][4], Pub& p, iseq<Is...>) { (pub_half<12 + (Is >> 1), (Is & 1)>(acc, p), ...); }
DI void redo_head(const AC& ac, const char* __restrict__ Kp, const char* __restrict__ Vp, unsigned lane, unsigned ow) {
  const unsigned r16 = lane & 15u, g = lane >> 4;
#pragma unroll 1
  for (unsigned qb = 0; qb < 4; ++qb) {
    bf16x8 qq[4];
#pragma unroll
    for (int s = 0; s < 4; ++s) qq[s] = qb == 0 ? ac.q[0][s] : qb == 1 ? ac.q[1][s] : qb == 2 ? ac.q[2][s] : ac.q[3][s];
    f32x4 o[8];
#pragma unroll
    for (int d = 0; d < 8; ++d) o[d] = f32x4{};
    float m = -__builtin_inff(), l = 0.f;
#pragma unroll 1
    for (unsigned t = 0; t < 16; ++t) {
      f32x4 s0 = {}, s1 = {};
      const char* kp = Kp + (t * 32u + r16) * 256u + g * 16u;
#pragma unroll
      for (int s = 0; s < 4; ++s) {
        s0 = __builtin_amdgcn_mfma_f32_16x16x32_bf16(*(const bf16x8*)(kp + s * 64), qq[s], s0, 0, 0, 0);
        s1 = __builtin_amdgcn_mfma_f32_16x16x32_bf16(*(const bf16x8*)(kp + s * 64 + 4096), qq[s], s1, 0, 0, 0);
      }
      const float mn = fmaxf(m, row_max(s0, s1)), al = __builtin_amdgcn_exp2f(m - mn);
      m = mn;
#pragma unroll
      for (int j = 0; j < 4; ++j) { s0[j] = __builtin_amdgcn_exp2f(s0[j] - mn); s1[j] = __builtin_amdgcn_exp2f(s1[j] - mn); }
      l = l * al + ((s0[0] + s0[1]) + (s0[2] + s0[3])) + ((s1[0] + s1[1]) + (s1[2] + s1[3]));
      const bf16x8 pf = __builtin_bit_cast(bf16x8, pack2(s0, s1));
      const __bf16* vp = (const __bf16*)(Vp + (t * 32u + g * 4u) * 256u) + r16;
#pragma unroll
      for (int d = 0; d < 8; ++d) {
        bf16x8 vf;
#pragma unroll
        for (int j = 0; j < 8; ++j) vf[j] = vp[((j & 3) + (j >> 2) * 16) * 128 + d * 16];
        o[d] = __builtin_amdgcn_mfma_f32_16x16x32_bf16(vf, pf, o[d] * al, 0, 0, 0);
      }
    }
    const float inv = 1.0f / xlane<false>(l);
#pragma unroll
    for (int s = 0; s < 4; ++s) *(LAS u32x4*)(size_t)(ow + s * 4096u + qb * 1024u) = pack2(o[2 * s] * inv, o[2 * s + 1] * inv);
  }
}
template <bool isA, int J> DI void q_conv1(AC& ac, f32x4 (&acc)[8][4], Stash& st, const Src& sK, const Src& sV, unsigned rk, unsigned p3) {
  constexpr int s = J >> 2, qb = J & 3;
  if constexpr (isA) { stash_write1<(5 * J) / 2>(st); stash_write1<(5 * J) / 2 + 1>(st); if constexpr (J & 1) stash_write1<(5 * J) / 2 + 2>(st); }
  else {
    constexpr int p = 2 * (J & 1);
    const Src& src = s < 2 ? sK : sV;
    const unsigned so = ksoff(s == 0 ? 2 : s == 1 ? SK : s == 2 ? SV : SP, rk) + (J & 2) * 2048u;
    if constexpr (s == 0 || s == 3) {
      if constexpr (p == 0) asm volatile("s_mov_b32 m0, %0\n\ts_nop 0" :: "s"(p3 + (s == 3 ? SLOT : 0u) + (J & 2) * 2048u) : "memory");
      dma_p<KV_NT, p>(src.v[p], src.srd, so); dma_p<KV_NT, p + 1>(src.v[p + 1], src.srd, so);
    } else {
      asm volatile("buffer_load_dwordx4 %0, %1, %2, %3 offen offset:%4 nt" : "=a"(st.p[2 * J - 8]) : "v"(src.v[p]), "s"(src.srd), "s"(so), "i"(1024 * p) : "memory");
      asm volatile("buffer_load_dwordx4 %0, %1, %2, %3 offen offset:%4 nt" : "=a"(st.p[2 * J - 7]) : "v"(src.v[p + 1]), "s"(src.srd), "s"(so), "i"(1024 * p + 1024) : "memory");
    }
  }
  ac.q[qb][s] = __builtin_bit_cast(bf16x8, natural8(pack2(acc[2 * s][qb] * SC, acc[2 * s + 1][qb] * SC)));
  asm volatile("" : "+a"(ac.q[qb][s]));
  GAP();
}
template <bool isA, int... Js> DI void q_conv(AC& ac, f32x4 (&acc)[8][4], Stash& st, const Src& sK, const Src& sV, unsigned rk, unsigned p3, iseq<Js...>) { (q_conv1<isA, Js>(ac, acc, st, sK, sV, rk, p3), ...); }
DI void zero_o(AC& ac) {
#pragma unroll
  for (int i = 0; i < 8; ++i)
#pragma unroll
    for (int q = 0; q < 4; ++q) { ac.o[i][q] = f32x4{}; asm volatile("" : "+a"(ac.o[i][q])); }
}

template <bool isA>
DI void body(const char* __restrict__ X, const char* __restrict__ K, const char* __restrict__ V, const char* __restrict__ Wq, const char* __restrict__ Wo,
             const char* __restrict__ Wo2, srd_t code, char* __restrict__ part, char* __restrict__ out) {
  extern __shared__ __attribute__((aligned(16))) char lds[];
  unsigned long long tnow;
  asm volatile("s_memrealtime %0\n\ts_waitcnt lgkmcnt(0)" : "=s"(tnow));
  const unsigned tlim = (unsigned)tnow + EASY_T;
  unsigned tid = threadIdx.x;
  PINV(tid);
  const unsigned lane = tid & 63u, w = rfl(tid >> 6);
  const unsigned bid = blockIdx.x, xg = bid & 7u, rr = bid >> 3, b = rr & 15u, h = xg * 4u + w;
  const unsigned r16 = lane & 15u, g = lane >> 4;
  const char* xp  = X + (size_t)b * 65536u + w * 16384u;
  const char* wqp = Wq + h * 256u;
  const char* wop = Wo + (size_t)xg * 524288u + w * 256u;
  const size_t kvo = isA ? (size_t)(b * 8u + xg) * 131072u : (size_t)(b * 32u + h) * 131072u;
  const unsigned rk = isA ? b : b + 4u * w;
  typedef PK<512u, 4096u, 0x1300u, 0xF300u, 0xC300u, 128u, 32u * 8192u, true> KQ;
  typedef PK<512u, 4096u, 0x1300u, 0xF300u, 0xC300u, 4096u, 8192u, false> KT;
  typedef PK<2048u, 1024u, 0x1000u, 0xF000u, 0xF000u, 128u, 32u * 1024u, false> KO;
  asm volatile("" :: "v"(lds));
  PC pc;
  pc.r01 = 65536u + w * 16384u; pc.r2 = 131072u + w * 8192u;
  const unsigned zq = ((b & 3u) << 14) | ((b >> 2) << 8), co = 4u * w;
  const srd_t srdH = mk_srd(part + HOFF, 4096u + 256u * 128u);
  const unsigned tok_off = rr * 128u, flg_off = 4096u + rr * 1024u;
  const unsigned l8 = (lane & 7u) * 4u;
  unsigned entry_flags;
  asm volatile("buffer_load_dword %0, %1, %2, %3 offen sc1" : "=v"(entry_flags) : "v"(l8), "s"(srdH), "s"(flg_off + xg * 128u) : "memory");
  if constexpr (isA) { if (w == 3 && lane < WARM) asm volatile("buffer_load_dword %0, %1, %2, 0 offen" : "+v"(entry_flags) : "v"((b * WARM + lane) * 128u), "s"(code) : "memory"); }
  Src sQ, sX, sK, sV, sO;
  mk_src_w<false>(sQ, wqp, 511u * 8192u + 256u, 8192u, lane);
  mk_src_rows<false>(sX, xp, 16384u, 1024u, lane);
  const unsigned p0 = b & 3u, p1 = (p0 + 1u) & 3u, p2 = (p0 + 2u) & 3u, p3 = (p0 + 3u) & 3u, xw = w * 4096u;
  Stash st;
  unsigned dum0 = lane;
  dma_unit<1>(sQ, q_soff(zq), pc.r2);
  dma_unit<W_NT>(sQ, q_soff(zq + 0x1300u), pc.r01);
  if constexpr (!isA) dma_unit<X_NT>(sX, p0 * 256u, xw + p0 * 16384u);
  dma_unit<1>(sQ, q_soff(zq) + KQ::US, pc.r2 + 4096u);
  dma_unit<W_NT>(sQ, q_soff(zq + 0x1300u) + KQ::US, pc.r01 + 4096u);
  mk_src_rows<false>(sK, K + kvo, 131072u, 256u, lane);
  mk_src_rows<true>(sV, V + kvo, 131072u, 256u, lane);
  mk_src_w<true>(sO, wop, 511u * 1024u + 256u, 1024u, lane);
  if constexpr (isA) {
    stash_load(st, (w & 1u) ? sK.v[2] : sK.v[0], sK, sV, rk, w * 2048u, make_iseq<64>{});
    dma_unit<X_NT>(sX, p0 * 256u, xw + p0 * 16384u);
    dma_elem<W_NT>(sQ, q_soff(zq + 0x2200u), KQ::US, pc.r01 + SLOT);
    dma_unit<X_NT>(sX, p1 * 256u, xw + p1 * 16384u);
    dma_unit<X_NT>(sX, p2 * 256u, xw + p2 * 16384u);
    dma_unit<X_NT>(sX, p3 * 256u, xw + p3 * 16384u);
  }
  const unsigned rb = 1024u * (r16 >> 2) + 64u * (r16 & 3u) + 16u * (g ^ ((r16 >> 2) & 2u));
  const unsigned tb = 64u * ((lane >> 2) & 3u) + 32u * (g & 1u) + 8u * (lane & 3u);
  const unsigned tbV = tb + 1024u * g;
  const unsigned sb0 = w * 2048u + lane * 16u;
  st.base[0] = sb0; st.base[1] = sb0 + 65536u; st.base[2] = sb0 + 131072u;
  AC ac;
  if constexpr (!isA) zero_o(ac);
  {
    f32x4 acc[8][4];
    bf16x8 a0[4], a1[4], bf[4];
    asm volatile("s_waitcnt vmcnt(%2)" : "+v"(entry_flags), "+v"(dum0) : "i"(isA ? 20 : 8) : "memory");
    if (w == 0) {
      const unsigned long long mk = __builtin_amdgcn_ballot_w64(((entry_flags >> (lane & 7u)) & 1u) != 0u);
      const unsigned tok = 0x5a5a5a00u | (~(unsigned)mk & 0xffu);
      if (lane == 0) asm volatile("buffer_store_dword %0, off, %1, %2 sc1" :: "v"(tok), "s"(srdH), "s"(tok_off + xg * 4u) : "memory");
    }
    barrier();
    proj_start<KQ, 2>(a0, bf, pc, tbV, rb, zq, q_soff(zq + 0x3100u));
    if constexpr (isA) {
      proj_elem<KQ, 2, true,  false, true, W_NT, 63, 63>(acc, a0, a1, bf, pc, sQ);
      proj_elem<KQ, 0, false, false, true, W_NT, 63, 28>(acc, a0, a1, bf, pc, sQ);
      proj_elem<KQ, 1, false, false, true, W_NT, 28, 16>(acc, a0, a1, bf, pc, sQ);
      proj_elem<KQ, 2, false, false, true, W_NT, 16, 16, true>(acc, a0, a1, bf, pc, sQ);
    } else {
      dma_elem<W_NT>(sQ, q_soff(zq + 0x2200u), KQ::US, pc.r01 + SLOT);
      proj_elem<KQ, 2, true,  false, true, W_NT, 12, 20, false, true, true>(acc, a0, a1, bf, pc, sQ, NoX{}, &sX, p1 * 256u, xw + p1 * 16384u, p2 * 256u, xw + p2 * 16384u);
      proj_elem<KQ, 0, false, false, true, W_NT, 24, 28, false, true, false>(acc, a0, a1, bf, pc, sQ, NoX{}, &sX, p3 * 256u, xw + p3 * 16384u);
      proj_elem<KQ, 1, false, false, true, W_NT, 28, 24>(acc, a0, a1, bf, pc, sQ);
      proj_elem<KQ, 2, false, false, true, W_NT, 20, 16, true>(acc, a0, a1, bf, pc, sQ);
    }
    typedef std::conditional_t<isA, EasyX, NoX> PX;
    const PX px = [&] { if constexpr (isA) return EasyX{tnow, tlim}; else return NoX{}; }();
#pragma unroll 1
    for (unsigned e = 0; e < 3; ++e) {
      proj_elem<KQ, 0, false, false, true, W_NT, 16, 16>(acc, a0, a1, bf, pc, sQ, px);
      proj_elem<KQ, 1, false, false, true, W_NT, 16, 16>(acc, a0, a1, bf, pc, sQ, px);
      proj_elem<KQ, 2, false, false, true, W_NT, 16, 16>(acc, a0, a1, bf, pc, sQ, px);
    }
    if constexpr (isA) {
      proj_elem<KQ, 0, false, false, false, W_NT, 16, 12>(acc, a0, a1, bf, pc, sQ, px);
      proj_elem<KQ, 1, false, false, false, W_NT, 8, 4>(acc, a0, a1, bf, pc, sQ, px);
      proj_elem<KQ, 2, false, true, false, W_NT, 0, 0>(acc, a0, a1, bf, pc, sQ, px);
    } else {
      pc.soff = ksoff(0u, rk);
      proj_elem<KT, 0, false, false, true, KV_NT, 16, 16>(acc, a0, a1, bf, pc, sK);
      proj_elem<KT, 1, false, false, true, KV_NT, 16, 16>(acc, a0, a1, bf, pc, sK);
      pc.soff = ksoff(0u, rk);
      proj_elem<KT, 2, false, true, true, KV_NT, 16, 16>(acc, a0, a1, bf, pc, sV);
    }
    barrier();
    acc_fence_v(acc);
    q_conv<isA>(ac, acc, st, sK, sV, rk, w * 16384u, make_iseq<16>{});
  }
  ST SA, SB;
  if constexpr (isA) {
    zero_o(ac);
    const unsigned kb = rb, vb = SLOT + tbV;
    unsigned dum = lane, to = (lane >> 4) * 1024u + (lane & 8u) * 16u + (lane & 7u) * 4u;
    PINV(to);
    const char* ta = wop + b * KO::CS;
    const char* tb2 = Wo2 + (size_t)xg * 524288u + w * 256u + b * KO::CS;
    touch(dum, to, ta);
    asm volatile("s_waitcnt lgkmcnt(0)" ::: "memory");
    barrier();
    k_read(ac, kb + kv_pair(0) * 16384u);
    wait_lgkm0();
    GAP();
    qk0<false>(ac, SA, sK, 0u, 0u, make_iseq<32>{});
    k_read(ac, kb + kv_pair(1) * 16384u);
    touch(dum, to, ta);
    sm_first(ac, SA);
    unsigned i4 = 0;
    auto ak = [&](PA& p) { PINS(i4); p.rd0 = kb + (((unsigned)((kv_tab() >> 8) >> i4) & 15u) << 14); PINV(p.rd0); i4 += 4u; };
    auto at = [&](PA& p) __attribute__((always_inline)) {
      PINS(i4); p.rd0 = vb + (((unsigned)(kv_tab() >> i4) & 15u) << 14); p.rd1 = p.rd0 ^ 32u; PINV(p.rd0); PINV(p.rd1);
      const char* tp;
      asm volatile("s_cmp_lt_u32 %1, 24\n\ts_cselect_b64 %0, %2, %3" : "=s"(tp) : "s"(i4), "s"(ta), "s"(tb2 - 32768) : "scc");
      touch(dum, to, tp);
    };
    while (i4 < 24u) {
      phaseA<true, false, KV_NT, 63>(ac, SA, SB, at, sK, 0u, st);
      phaseB<true, true, false, KV_NT, 63>(ac, SB, ak, sK, 0u, st);
      phaseA<true, false, KV_NT, 63>(ac, SB, SA, at, sK, 0u, st);
      phaseB<true, true, false, KV_NT, 63>(ac, SA, ak, sK, 0u, st);
    }
    barrier();
    phaseA<true, false, KV_NT, 63, 40>(ac, SA, SB, at, sK, 0u, st);
    phaseB<true, true, false, KV_NT, 63, 46>(ac, SB, ak, sK, 0u, st);
    phaseA<true, false, KV_NT, 63, 52>(ac, SB, SA, at, sK, 0u, st);
    phaseB<true, true, false, KV_NT, 63, 58>(ac, SA, ak, sK, 0u, st);
    wait_vm_lgkm<63, 8>(); barrier();
    while (i4 < 56u) {
      phaseA<true, false, KV_NT, 63>(ac, SA, SB, at, sK, 0u, st);
      phaseB<true, true, false, KV_NT, 63>(ac, SB, ak, sK, 0u, st);
      phaseA<true, false, KV_NT, 63>(ac, SB, SA, at, sK, 0u, st);
      phaseB<true, true, false, KV_NT, 63>(ac, SA, ak, sK, 0u, st);
    }
    barrier();
    unsigned v14 = vb + kv_pair(14) * 16384u, x14 = v14 ^ 32u; PINV(v14); PINV(x14);
    phaseA<true, true, W_NT, 63>(ac, SA, SB, [&](PA& p) { p.soff = co * KO::CS; p.fill = pc.r01; p.rd0 = v14; p.rd1 = x14; }, sO, KO::US, st);
    phaseB<true, false, true, W_NT, 63>(ac, SB, [&](PA& p) { p.soff = (co + 1u) * KO::CS; p.fill = pc.r01 + SLOT; }, sO, KO::US, st);
    phaseA<false, false, W_NT, 63>(ac, SB, SA, [&](PA& p) { p.rd0 = v14 + 16384u; p.rd1 = x14 + 16384u; }, sO, 0u, st);
    dum0 = dum;
  } else {
    const unsigned P0 = pc.r01, P1 = pc.r01 + SLOT, P2 = pc.r2, P3 = w * 16384u;
    unsigned v0 = tbV + P0, v2 = tbV + P2, x0 = v0 ^ 32u, x2 = v2 ^ 32u, k1 = rb + P1, k3 = rb + P3;
    PINV(v0); PINV(v2); PINV(x0); PINV(x2); PINV(k1); PINV(k3);
    wait_vm<48>();
    k_read(ac, k1 - SLOT);
    wait_lgkm0();
    GAP();
    qk0<true>(ac, SA, sV, ksoff(1u, rk), P0, make_iseq<32>{});
    wait_vm<48>();
    GAP();
    k_read(ac, k1);
    sm_first(ac, SA);
    BS bs{{P0, P1, P2, P3}, {v0, x0, v2, x2, tbV + P3, (tbV + P3) ^ 32u}, {k1, k3}, rk, co * KO::CS, lane * 16u};
    PINV(bs.v[4]); PINV(bs.v[5]);
#define B2(I) b_step<KO, I>(ac, SA, SB, bs, sK, sV, sO, st); b_step<KO, I + 1>(ac, SB, SA, bs, sK, sV, sO, st);
    B2(0) B2(2) B2(4) B2(6) B2(8) B2(10)
    B2(12) B2(14)
#undef B2
  }
  {
    const unsigned l16 = lane * 16u;
    unsigned ow = w * 16384u + l16;
    PINV(ow);
    f32x4 acc[8][4];
    bf16x8 a0[4], a1[4], b0[4], bf[4];
    float inv[4];
    proj_setup<KO>(pc, tb + 512u * g, l16, (co + 3u) << 12, (co + 3u) * KO::CS);
    unsigned toks;
    asm volatile("buffer_load_dword %0, %1, %2, %3 offen sc1" : "=v"(toks) : "v"(l8), "s"(srdH), "s"(tok_off) : "memory");
    GAP();
    phaseL<KO, 13, !isA>(ac, inv, b0, a0, pc, sO, (co + 2u) * KO::CS, make_iseq<36>{});
    if constexpr (isA) {
      barrier();
      dma_elem<W_NT>(sO, (co + 2u) * KO::CS, KO::US, pc.r2);
    }
    const bool ok = ac.l[0][0] <= LMAX && ac.l[1][0] <= LMAX && ac.l[2][0] <= LMAX && ac.l[3][0] <= LMAX;
    if (__builtin_expect(__builtin_amdgcn_ballot_w64(!ok) != 0ull, 0)) {
      asm volatile("s_waitcnt vmcnt(0)" : "+v"(toks) :: "memory");
      acc_fence_a(ac.o);
      redo_head(ac, K + kvo, V + kvo, lane, ow);
#pragma unroll
      for (int q = 0; q < 4; ++q) {
        inv[q] = 1.0f;
        b0[q] = lds128(ow + q * 1024u);
#pragma unroll
        for (int s = 1; s < 4; ++s) {
          const u32x4 t = __builtin_bit_cast(u32x4, lds128(ow + s * 4096u + q * 1024u));
          ac.o[2 * s][q] = f32x4{__uint_as_float(t[0] << 16), __uint_as_float(t[0] & 0xffff0000u), __uint_as_float(t[1] << 16), __uint_as_float(t[1] & 0xffff0000u)};
          ac.o[2 * s + 1][q] = f32x4{__uint_as_float(t[2] << 16), __uint_as_float(t[2] & 0xffff0000u), __uint_as_float(t[3] << 16), __uint_as_float(t[3] & 0xffff0000u)};
        }
      }
      wait_lgkm0();
      acc_fence_a(ac.o);
    }
#pragma unroll
    for (int q = 0; q < 4; ++q) bf[q] = bf16x8{};
    GAP();
    proj_elem<KO, 0, true, false, true, W_NT, 16, 16>(acc, a0, a1, b0, pc, sO, CvX<0>{ac, inv, b0, bf, ow});
    proj_elem<KO, 1, false, false, true, W_NT, 16, 16>(acc, a0, a1, bf, pc, sO, CvX<1>{ac, inv, bf, b0, ow});
    proj_elem<KO, 2, false, false, true, W_NT, 16, 16>(acc, a0, a1, b0, pc, sO, CvX<2>{ac, inv, b0, bf, ow});
    if constexpr (isA) PINV(dum0);
    proj_elem<KO, 0, false, false, true, W_NT, 16, 16, true>(acc, a0, a1, bf, pc, sO, CvX<3>{ac, inv, bf, b0, ow});
#pragma unroll 1
    for (unsigned e = 0; e < 3; ++e) {
      proj_elem<KO, 1, false, false, true, W_NT, 16, 16>(acc, a0, a1, bf, pc, sO);
      proj_elem<KO, 2, false, false, true, W_NT, 16, 16>(acc, a0, a1, bf, pc, sO);
      proj_elem<KO, 0, false, false, true, W_NT, 16, 16>(acc, a0, a1, bf, pc, sO);
    }
    Pub pb;
    pb.srd = mk_srd(part + (size_t)rr * 524288u, 524288u);
    pb.so = xg * 65536u + w * 16384u;
#pragma unroll
    for (unsigned i = 0; i < 4; ++i) { pb.v[i] = l16 + i * 4096u + (2u * w + (i >> 1) == xg ? 0x80000000u : 0u); PINV(pb.v[i]); }
#pragma unroll
    for (int i = 0; i < 16; ++i) pb.pk[i] = u32x4{};
    unsigned xa[3];
#pragma unroll
    for (unsigned i = 0; i < 3; ++i) { xa[i] = (((co + 13u + i) & 15u) << 12) | l16; PINV(xa[i]); }
    tail_unit<KO, 0, 12>(acc, a0, a1, bf, pc, xa, pb, make_iseq<16>{});
    tail_unit<KO, 1, 4>(acc, a1, a0, bf, pc, xa, pb, make_iseq<16>{});
    tail_unit<KO, 2, 63>(acc, a0, a1, bf, pc, xa, pb, make_iseq<16>{});
    tail_unit<KO, 3, 0>(acc, a1, a0, bf, pc, xa, pb, make_iseq<16>{});
    tail_unit<KO, 4, 63>(acc, a0, a1, bf, pc, xa, pb, make_iseq<16>{});
    tail_unit<KO, 5, 63>(acc, a1, a0, bf, pc, xa, pb, make_iseq<16>{});
    pub_rest(acc, pb, make_iseq<8>{});
    const unsigned lr = ((lane + xg) & 7u) * 4u;
    unsigned fl;
    asm volatile("buffer_load_dword %0, %1, %2, %3 offen sc1" : "=v"(fl) : "v"(lr), "s"(srdH), "s"(flg_off + xg * 128u) : "memory");
    PINV(toks);
    if (w == (xg >> 1)) {
      const unsigned a = pc.r01 + SLOT + l16;
      if (xg & 1u) own_put<8>(pb, a, make_iseq<8>{}); else own_put<0>(pb, a, make_iseq<8>{});
    }
    const unsigned fo = lane * 128u + xg * 4u, tau = __builtin_amdgcn_readlane(toks, xg);
    unsigned oo = ((b * 64u + 16u * w + r16) * 1024u + (isA ? 0u : 512u) + 64u * xg + 8u * g) * 2u;
    PINV(oo);
    wait_lgkm0();
    barrier();
    const unsigned oa = 65536u + SLOT + (xg >> 1) * 16384u + w * 2048u + l16;
    const u32x4 r0 = __builtin_bit_cast(u32x4, lds128(oa)), r1 = __builtin_bit_cast(u32x4, lds128(oa + 1024u));
    GAP();
    wait_vm<9>();
    if (lane == 2u * w) asm volatile("buffer_store_dword %0, %1, %2, %3 offen sc1" :: "v"(toks), "v"(fo), "s"(srdH), "s"(flg_off) : "memory");
    wait_vm<2>();
    if (lane == 2u * w + 1u) asm volatile("buffer_store_dword %0, %1, %2, %3 offen sc1" :: "v"(toks), "v"(fo), "s"(srdH), "s"(flg_off) : "memory");
    f32x16 f[8];
#pragma unroll
    for (int j = 0; j < 8; ++j) { const unsigned t = j < 4 ? r0[j & 3] : r1[j & 3]; f[0][2 * j] = __uint_as_float(t << 16); f[0][2 * j + 1] = __uint_as_float(t & 0xffff0000u); }
    PINV(f[0]);
    asm volatile("s_waitcnt vmcnt(2)" : "+v"(fl) : "v"(pb.pk[0]), "v"(pb.pk[1]), "v"(pb.pk[2]), "v"(pb.pk[3]), "v"(pb.pk[4]), "v"(pb.pk[5]), "v"(pb.pk[6]), "v"(pb.pk[7]),
                 "v"(pb.pk[8]), "v"(pb.pk[9]), "v"(pb.pk[10]), "v"(pb.pk[11]), "v"(pb.pk[12]), "v"(pb.pk[13]), "v"(pb.pk[14]), "v"(pb.pk[15]) : "memory");
    unsigned got = 0xffffff01u, spin = 1u << 21, fresh, so, f2;
#define POLL(R) "buffer_load_dword %[" R "], %[lr], %[sh], %[flg] offen sc1\n\t"
#define SEEN(R) "v_cmp_eq_u32_e32 vcc, %[tau], %[" R "]\n\ts_andn2_b32 %[fr], vcc_lo, %[got]\n\ts_cbranch_scc1 .Lproc_%=\n\t"
    asm volatile(
        SEEN("fl") POLL("fl") "s_sleep 4\n"
        ".Lidle_%=:\n\t" POLL("f2") "s_waitcnt vmcnt(1)\n\t" SEEN("fl") "s_sleep %[slp]\n\t" POLL("fl") "s_waitcnt vmcnt(1)\n\t" SEEN("f2")
        "s_sleep %[slp]\n\ts_sub_u32 %[spin], %[spin], 1\n\ts_cbranch_scc0 .Lidle_%=\n\ts_branch .Ldone_%=\n"
        ".Lproc_%=:\n\ts_or_b32 %[got], %[got], %[fr]\n\t"
        "s_bitcmp1_b32 %[fr], 1\n\ts_cbranch_scc0 .La1_%=\n\ts_add_u32 %[so], %[ro], 65536\n\ts_bitset0_b32 %[so], 19\n\t"
        "buffer_load_dwordx4 v[152:155], %[l16], %[sp], %[so] offen sc1\n\tbuffer_load_dwordx4 v[156:159], %[l16], %[sp], %[so] offen offset:1024 sc1\n.La1_%=:\n\t"
        "s_bitcmp1_b32 %[fr], 2\n\ts_cbranch_scc0 .La2_%=\n\ts_add_u32 %[so], %[ro], 131072\n\ts_bitset0_b32 %[so], 19\n\t"
        "buffer_load_dwordx4 v[168:171], %[l16], %[sp], %[so] offen sc1\n\tbuffer_load_dwordx4 v[172:175], %[l16], %[sp], %[so] offen offset:1024 sc1\n.La2_%=:\n\t"
        "s_bitcmp1_b32 %[fr], 3\n\ts_cbranch_scc0 .La3_%=\n\ts_add_u32 %[so], %[ro], 196608\n\ts_bitset0_b32 %[so], 19\n\t"
        "buffer_load_dwordx4 v[184:187], %[l16], %[sp], %[so] offen sc1\n\tbuffer_load_dwordx4 v[188:191], %[l16], %[sp], %[so] offen offset:1024 sc1\n.La3_%=:\n\t"
        "s_bitcmp1_b32 %[fr], 4\n\ts_cbranch_scc0 .La4_%=\n\ts_add_u32 %[so], %[ro], 262144\n\ts_bitset0_b32 %[so], 19\n\t"
        "buffer_load_dwordx4 v[200:203], %[l16], %[sp], %[so] offen sc1\n\tbuffer_load_dwordx4 v[204:207], %[l16], %[sp], %[so] offen offset:1024 sc1\n.La4_%=:\n\t"
        "s_bitcmp1_b32 %[fr], 5\n\ts_cbranch_scc0 .La5_%=\n\ts_add_u32 %[so], %[ro], 327680\n\ts_bitset0_b32 %[so], 19\n\t"
        "buffer_load_dwordx4 v[216:219], %[l16], %[sp], %[so] offen sc1\n\tbuffer_load_dwordx4 v[220:223], %[l16], %[sp], %[so] offen offset:1024 sc1\n.La5_%=:\n\t"
        "s_bitcmp1_b32 %[fr], 6\n\ts_cbranch_scc0 .La6_%=\n\ts_add_u32 %[so], %[ro], 393216\n\ts_bitset0_b32 %[so], 19\n\t"
        "buffer_load_dwordx4 v[232:235], %[l16], %[sp], %[so] offen sc1\n\tbuffer_load_dwordx4 v[236:239], %[l16], %[sp], %[so] offen offset:1024 sc1\n.La6_%=:\n\t"
        "s_bitcmp1_b32 %[fr], 7\n\ts_cbranch_scc0 .La7_%=\n\ts_add_u32 %[so], %[ro], 458752\n\ts_bitset0_b32 %[so], 19\n\t"
        "buffer_load_dwordx4 v[248:251], %[l16], %[sp], %[so] offen sc1\n\tbuffer_load_dwordx4 v[252:255], %[l16], %[sp], %[so] offen offset:1024 sc1\n.La7_%=:\n\t"
        "s_waitcnt vmcnt(0)\n\ts_cmp_eq_u32 %[got], -1\n\ts_cbranch_scc1 .Ltok_%=\n\t" POLL("fl") "s_branch .Lwide_%=\n"
        ".Ltok_%=:\n\tbuffer_load_dword %[fl], %[l8], %[sh], %[tok] offen sc1\n"
        ".Lwide_%=:\n\t"
        "s_bitcmp1_b32 %[fr], 1\n\ts_cbranch_scc0 .Lb1_%=\n\t"
        "v_lshlrev_b32_e32 v144, 16, v152\n\tv_and_b32_e32 v145, %[hi], v152\n\tv_lshlrev_b32_e32 v146, 16, v153\n\tv_and_b32_e32 v147, %[hi], v153\n\t"
        "v_lshlrev_b32_e32 v148, 16, v154\n\tv_and_b32_e32 v149, %[hi], v154\n\tv_lshlrev_b32_e32 v150, 16, v155\n\tv_and_b32_e32 v151, %[hi], v155\n\t"
        "v_lshlrev_b32_e32 v152, 16, v156\n\tv_and_b32_e32 v153, %[hi], v156\n\tv_lshlrev_b32_e32 v154, 16, v157\n\tv_and_b32_e32 v155, %[hi], v157\n\t"
        "v_lshlrev_b32_e32 v156, 16, v158\n\tv_and_b32_e32 v157, %[hi], v158\n\tv_lshlrev_b32_e32 v158, 16, v159\n\tv_and_b32_e32 v159, %[hi], v159\n\t"
        ".Lb1_%=:\n\t"
        "s_bitcmp1_b32 %[fr], 2\n\ts_cbranch_scc0 .Lb2_%=\n\t"
        "v_lshlrev_b32_e32 v160, 16, v168\n\tv_and_b32_e32 v161, %[hi], v168\n\tv_lshlrev_b32_e32 v162, 16, v169\n\tv_and_b32_e32 v163, %[hi], v169\n\t"
        "v_lshlrev_b32_e32 v164, 16, v170\n\tv_and_b32_e32 v165, %[hi], v170\n\tv_lshlrev_b32_e32 v166, 16, v171\n\tv_and_b32_e32 v167, %[hi], v171\n\t"
        "v_lshlrev_b32_e32 v168, 16, v172\n\tv_and_b32_e32 v169, %[hi], v172\n\tv_lshlrev_b32_e32 v170, 16, v173\n\tv_and_b32_e32 v171, %[hi], v173\n\t"
        "v_lshlrev_b32_e32 v172, 16, v174\n\tv_and_b32_e32 v173, %[hi], v174\n\tv_lshlrev_b32_e32 v174, 16, v175\n\tv_and_b32_e32 v175, %[hi], v175\n\t"
        ".Lb2_%=:\n\t"
        "s_bitcmp1_b32 %[fr], 3\n\ts_cbranch_scc0 .Lb3_%=\n\t"
        "v_lshlrev_b32_e32 v176, 16, v184\n\tv_and_b32_e32 v177, %[hi], v184\n\tv_lshlrev_b32_e32 v178, 16, v185\n\tv_and_b32_e32 v179, %[hi], v185\n\t"
        "v_lshlrev_b32_e32 v180, 16, v186\n\tv_and_b32_e32 v181, %[hi], v186\n\tv_lshlrev_b32_e32 v182, 16, v187\n\tv_and_b32_e32 v183, %[hi], v187\n\t"
        "v_lshlrev_b32_e32 v184, 16, v188\n\tv_and_b32_e32 v185, %[hi], v188\n\tv_lshlrev_b32_e32 v186, 16, v189\n\tv_and_b32_e32 v187, %[hi], v189\n\t"
        "v_lshlrev_b32_e32 v188, 16, v190\n\tv_and_b32_e32 v189, %[hi], v190\n\tv_lshlrev_b32_e32 v190, 16, v191\n\tv_and_b32_e32 v191, %[hi], v191\n\t"
        ".Lb3_%=:\n\t"
        "s_bitcmp1_b32 %[fr], 4\n\ts_cbranch_scc0 .Lb4_%=\n\t"
        "v_lshlrev_b32_e32 v192, 16, v200\n\tv_and_b32_e32 v193, %[hi], v200\n\tv_lshlrev_b32_e32 v194, 16, v201\n\tv_and_b32_e32 v195, %[hi], v201\n\t"
        "v_lshlrev_b32_e32 v196, 16, v202\n\tv_and_b32_e32 v197, %[hi], v202\n\tv_lshlrev_b32_e32 v198, 16, v203\n\tv_and_b32_e32 v199, %[hi], v203\n\t"
        "v_lshlrev_b32_e32 v200, 16, v204\n\tv_and_b32_e32 v201, %[hi], v204\n\tv_lshlrev_b32_e32 v202, 16, v205\n\tv_and_b32_e32 v203, %[hi], v205\n\t"
        "v_lshlrev_b32_e32 v204, 16, v206\n\tv_and_b32_e32 v205, %[hi], v206\n\tv_lshlrev_b32_e32 v206, 16, v207\n\tv_and_b32_e32 v207, %[hi], v207\n\t"
        ".Lb4_%=:\n\t"
        "s_bitcmp1_b32 %[fr], 5\n\ts_cbranch_scc0 .Lb5_%=\n\t"
        "v_lshlrev_b32_e32 v208, 16, v216\n\tv_and_b32_e32 v209, %[hi], v216\n\tv_lshlrev_b32_e32 v210, 16, v217\n\tv_and_b32_e32 v211, %[hi], v217\n\t"
        "v_lshlrev_b32_e32 v212, 16, v218\n\tv_and_b32_e32 v213, %[hi], v218\n\tv_lshlrev_b32_e32 v214, 16, v219\n\tv_and_b32_e32 v215, %[hi], v219\n\t"
        "v_lshlrev_b32_e32 v216, 16, v220\n\tv_and_b32_e32 v217, %[hi], v220\n\tv_lshlrev_b32_e32 v218, 16, v221\n\tv_and_b32_e32 v219, %[hi], v221\n\t"
        "v_lshlrev_b32_e32 v220, 16, v222\n\tv_and_b32_e32 v221, %[hi], v222\n\tv_lshlrev_b32_e32 v222, 16, v223\n\tv_and_b32_e32 v223, %[hi], v223\n\t"
        ".Lb5_%=:\n\t"
        "s_bitcmp1_b32 %[fr], 6\n\ts_cbranch_scc0 .Lb6_%=\n\t"
        "v_lshlrev_b32_e32 v224, 16, v232\n\tv_and_b32_e32 v225, %[hi], v232\n\tv_lshlrev_b32_e32 v226, 16, v233\n\tv_and_b32_e32 v227, %[hi], v233\n\t"
        "v_lshlrev_b32_e32 v228, 16, v234\n\tv_and_b32_e32 v229, %[hi], v234\n\tv_lshlrev_b32_e32 v230, 16, v235\n\tv_and_b32_e32 v231, %[hi], v235\n\t"
        "v_lshlrev_b32_e32 v232, 16, v236\n\tv_and_b32_e32 v233, %[hi], v236\n\tv_lshlrev_b32_e32 v234, 16, v237\n\tv_and_b32_e32 v235, %[hi], v237\n\t"
        "v_lshlrev_b32_e32 v236, 16, v238\n\tv_and_b32_e32 v237, %[hi], v238\n\tv_lshlrev_b32_e32 v238, 16, v239\n\tv_and_b32_e32 v239, %[hi], v239\n\t"
        ".Lb6_%=:\n\t"
        "s_bitcmp1_b32 %[fr], 7\n\ts_cbranch_scc0 .Lb7_%=\n\t"
        "v_lshlrev_b32_e32 v240, 16, v248\n\tv_and_b32_e32 v241, %[hi], v248\n\tv_lshlrev_b32_e32 v242, 16, v249\n\tv_and_b32_e32 v243, %[hi], v249\n\t"
        "v_lshlrev_b32_e32 v244, 16, v250\n\tv_and_b32_e32 v245, %[hi], v250\n\tv_lshlrev_b32_e32 v246, 16, v251\n\tv_and_b32_e32 v247, %[hi], v251\n\t"
        "v_lshlrev_b32_e32 v248, 16, v252\n\tv_and_b32_e32 v249, %[hi], v252\n\tv_lshlrev_b32_e32 v250, 16, v253\n\tv_and_b32_e32 v251, %[hi], v253\n\t"
        "v_lshlrev_b32_e32 v252, 16, v254\n\tv_and_b32_e32 v253, %[hi], v254\n\tv_lshlrev_b32_e32 v254, 16, v255\n\tv_and_b32_e32 v255, %[hi], v255\n\t"
        ".Lb7_%=:\n\t"
        "s_cmp_eq_u32 %[got], -1\n\ts_cbranch_scc0 .Lidle_%=\n"
        ".Ldone_%=:"
        : "=&{v[144:159]}"(f[1]), "=&{v[160:175]}"(f[2]), "=&{v[176:191]}"(f[3]), "=&{v[192:207]}"(f[4]), "=&{v[208:223]}"(f[5]), "=&{v[224:239]}"(f[6]), "=&{v[240:255]}"(f[7]),
          [fl] "+v"(fl), [f2] "=&v"(f2), [got] "+s"(got), [spin] "+s"(spin), [fr] "=&s"(fresh), [so] "=&s"(so)
        : [tau] "s"(tau), [l16] "v"(l16), [l8] "v"(l8), [lr] "v"(lr), [sp] "s"(pb.srd), [sh] "s"(srdH), [ro] "s"(xg * 73728u + w * 2048u), [tok] "s"(tok_off),
          [flg] "s"(flg_off + xg * 128u), [hi] "s"(0xffff0000u), [slp] "i"(isA ? 8 : 0)
        : "vcc", "scc", "memory");
#undef POLL
#undef SEEN
    const f32x16 t = ((f[0] + f[1]) + (f[2] + f[3])) + ((f[4] + f[5]) + (f[6] + f[7]));
#pragma unroll
    for (int s = 0; s < 2; ++s)
    {
      const u32x4 ov = natural8(u32x4{cvtpk(t[8 * s], t[8 * s + 1]), cvtpk(t[8 * s + 2], t[8 * s + 3]), cvtpk(t[8 * s + 4], t[8 * s + 5]), cvtpk(t[8 * s + 6], t[8 * s + 7])});
      __builtin_amdgcn_raw_buffer_store_b128(ov, mk_srd(out, 2097152u), oo + s * 64, 0, 2);
    }
    asm volatile("s_waitcnt vmcnt(2)" : "+v"(fl) :: "memory");
    if ((lane >> 1) == w && fl != toks) asm volatile("buffer_store_dword %0, %1, %2, %3 offen sc1" :: "v"(fl), "v"(fo), "s"(srdH), "s"(flg_off) : "memory");
  }
}

__global__ __launch_bounds__(256, 1)
void fused_attn(const char* __restrict__ Ax, const char* __restrict__ Ak, const char* __restrict__ Av, const char* __restrict__ Awq, const char* __restrict__ Awo,
                const char* __restrict__ Bx, const char* __restrict__ Bk, const char* __restrict__ Bv, const char* __restrict__ Bwq, const char* __restrict__ Bwo,
                char* __restrict__ part, char* __restrict__ out) {
  const char* pc; unsigned left;
  asm volatile("s_getpc_b64 %0\n.Lpc_%=:\n\ts_mov_b32 %1, .Lhot_end-.Lpc_%=" : "=s"(pc), "=s"(left));
  const srd_t code = mk_srd(pc, left);
  if (blockIdx.x >= 128) { body<true>(Ax, Ak, Av, Awq, Awo, Bwo, code, part, out); asm volatile(".Lhot_end:"); __builtin_amdgcn_endpgm(); }
  body<false>(Bx, Bk, Bv, Bwq, Bwo, Bwo, code, part, out);
}

extern "C" void kernel_launch(void* const* d_in, const int* in_sizes, int n_in, void* d_out, int out_size, void* d_ws, size_t ws_size, hipStream_t stream) {
  (void)in_sizes; (void)n_in; (void)out_size; (void)ws_size;
  (void)hipFuncSetAttribute((const void*)fused_attn, hipFuncAttributeMaxDynamicSharedMemorySize, (int)LDS_BYTES);
  fused_attn<<<256, 256, LDS_BYTES, stream>>>((const char*)d_in[0], (const char*)d_in[1], (const char*)d_in[2], (const char*)d_in[3], (const char*)d_in[4],
                                              (const char*)d_in[5], (const char*)d_in[6], (const char*)d_in[7], (const char*)d_in[8], (const char*)d_in[9], (char*)d_ws, (char*)d_out);
}
```

```cpp
#define PROBE_K 0
#define PROBE_WHO 3
#define PROBE_MUL 0ull
#include <hip/hip_runtime.h>
#include <hip/hip_bf16.h>
#include <utility>
#include <type_traits>

typedef __bf16 bf16x8 __attribute__((ext_vector_type(8)));
typedef __bf16 bf16x4 __attribute__((ext_vector_type(4)));
typedef __bf16 bf16x2 __attribute__((ext_vector_type(2)));
typedef float f32x4 __attribute__((ext_vector_type(4)));
typedef float f32x2 __attribute__((ext_vector_type(2)));
typedef float f32x16 __attribute__((ext_vector_type(16)));
typedef unsigned u32x4 __attribute__((ext_vector_type(4)));
#define LAS __attribute__((address_space(3)))
#define DI __device__ __forceinline__
typedef __amdgpu_buffer_rsrc_t srd_t;
template <int... Is> using iseq = std::integer_sequence<int, Is...>;
template <int N> using make_iseq = std::make_integer_sequence<int, N>;

#define KV_NT 1
#define W_NT 0
#define X_NT 0
#define SLEEP_A 8
#define SLEEP_B 0
#define PACE_A 0
#define PACE_ATT 0
#define TOUCH_Q 1
#define TOUCH_O 3
#define TO_SKIP 16u
#define TO_B 8u
#define WARM_CODE 40
#define ALT 0
#define ORD 2
#define EARLY_O 0
#define TOUCH_B 4
#define PUB_SLEEP 0
#define KVPOL "nt"
#define CVV 0
#define DELAY_A 0
#define SK 4
#define SV 4
#define SP 5
#define OUTPOL 2
#define STAG 0
#define RAMP 0
#define PUBPOL "sc0 sc1"
#define ILV_A 1
#define ILV_B 1
#define LDPOL "sc1"
#define PLPOL "sc1"
#define FLPOL "sc1"
#define BARM 0x3ffe
#define LATEW 1
#define SKIPOWN 1
#define EASY 6
#define EASY_T 1350
#define EASY2 0
#define EASY2_T 3000
#define RKA 7u * b
#define RKB 5u * b + 8u * w
#define GEOB 8, 15, 16, 12, 11, 13, 14, 10, 9
#define GEOA 15, 8, 16, 9, 10, 14, 11, 12, 13
#define KVPOS 4
#define XUP 0
#define PX0 b
#define WS_END 0

constexpr unsigned SLOT = 8192, LDS_BYTES = 20 * SLOT;
constexpr unsigned POFF = (unsigned)WS_END << 20, HOFF = POFF + (16u << 20);
constexpr float SC = 0.12751743082f;
constexpr float LMAX = 1048576.0f;

#define NO_REDUCE_STAMPS
#define GAP() __builtin_amdgcn_sched_barrier(0)
#define STAMP(i) do { if (i == 0 || i == PROBE_K) { GAP(); unsigned long long t_; asm volatile("s_memrealtime %0\n\ts_waitcnt lgkmcnt(0)" : "=s"(t_) :: "memory"); if (i == 0) pr0 = t_; prk = t_; GAP(); } } while (0)
#define CYC(i) do {} while (0)
#define PINS(x) asm volatile("" : "+s"(x))
#define PINV(x) asm volatile("" : "+v"(x))
DI unsigned rfl(unsigned x) { return __builtin_amdgcn_readfirstlane(x); }
DI unsigned cvtpk(float lo, float hi) { f32x2 v = {lo, hi}; bf16x2 b = __builtin_convertvector(v, bf16x2); return __builtin_bit_cast(unsigned, b); }
DI u32x4 pack2(const f32x4& a, const f32x4& b) { return u32x4{cvtpk(a[0], a[1]), cvtpk(a[2], a[3]), cvtpk(b[0], b[1]), cvtpk(b[2], b[3])}; }
DI u32x4 natural8(u32x4 w) {
  auto s0 = __builtin_amdgcn_permlane32_swap(w[0], w[2], false, false);
  auto s1 = __builtin_amdgcn_permlane32_swap(w[1], w[3], false, false);
  auto t0 = __builtin_amdgcn_permlane16_swap(s0[0], s0[1], false, false);
  auto t1 = __builtin_amdgcn_permlane16_swap(s1[0], s1[1], false, false);
  return u32x4{t0[0], t1[0], t0[1], t1[1]};
}
#define MF "v_mfma_f32_16x16x32_bf16 "
template <bool Z> DI void mm_vvv(f32x4& c, const bf16x8& a, const bf16x8& b) {
  if constexpr (Z) asm volatile(MF "%0, %1, %2, 0" : "=&v"(c) : "v"(a), "v"(b));
  else             asm volatile(MF "%0, %1, %2, %0" : "+v"(c) : "v"(a), "v"(b));
}
template <bool Z> DI void mm_vva(f32x4& c, const bf16x8& a, const bf16x8& b) {
  if constexpr (Z) asm volatile(MF "%0, %1, %2, 0" : "=&v"(c) : "v"(a), "a"(b));
  else             asm volatile(MF "%0, %1, %2, %0" : "+v"(c) : "v"(a), "a"(b));
}
DI void mm_vca(f32x4& d, const bf16x8& a, const bf16x8& b, const f32x4& c) { asm volatile(MF "%0, %1, %2, %3" : "=&v"(d) : "v"(a), "a"(b), "v"(c)); }
DI void mm_avv(f32x4& c, const bf16x8& a, const bf16x8& b) { asm volatile(MF "%0, %1, %2, %0" : "+a"(c) : "v"(a), "v"(b)); }
DI void set_m0(unsigned v) { asm volatile("s_mov_b32 m0, %0" :: "s"(v) : "memory"); }
template <int NT, int P> DI void dma_p(unsigned voff, srd_t srd, unsigned soff) {
  if constexpr (NT) asm volatile("buffer_load_dwordx4 %0, %1, %2 offen offset:%3 " KVPOL " lds" :: "v"(voff), "s"(srd), "s"(soff), "i"(1024 * P) : "memory");
  else              asm volatile("buffer_load_dwordx4 %0, %1, %2 offen offset:%3 lds" :: "v"(voff), "s"(srd), "s"(soff), "i"(1024 * P) : "memory");
}
struct Src { unsigned v[4]; srd_t srd; };
template <int NT> DI void dma_unit(const Src& s, unsigned soff, unsigned ldsaddr) {
  asm volatile("s_mov_b32 m0, %0\n\ts_nop 0" :: "s"(ldsaddr) : "memory");
  dma_p<NT, 0>(s.v[0], s.srd, soff); dma_p<NT, 1>(s.v[1], s.srd, soff); dma_p<NT, 2>(s.v[2], s.srd, soff); dma_p<NT, 3>(s.v[3], s.srd, soff);
}
template <int NT> DI void dma_elem(const Src& s, unsigned soff, unsigned ustep, unsigned ldsaddr) { dma_unit<NT>(s, soff, ldsaddr); dma_unit<NT>(s, soff + ustep, ldsaddr + 4096u); }
template <int N> DI void wait_vm() { asm volatile("s_waitcnt vmcnt(%0)" :: "i"(N) : "memory"); }
template <int N, int L> DI void wait_vm_lgkm() { asm volatile("" ::: "memory"); __builtin_amdgcn_s_waitcnt((N & 15) | ((N >> 4) << 14) | 0x70 | (L << 8)); asm volatile("" ::: "memory"); }
DI void wait_lgkm0() { wait_vm_lgkm<63, 0>(); }
DI void barrier() { asm volatile("s_barrier" ::: "memory"); }
DI bf16x8 lds128(unsigned a) { return *(const LAS bf16x8*)(size_t)(a); }
DI bf16x4 lds_tr1(unsigned a) { return __builtin_amdgcn_ds_read_tr16_b64_v4bf16((LAS bf16x4*)(size_t)(a)); }
DI bf16x8 cat(bf16x4 lo, bf16x4 hi) { return __builtin_shufflevector(lo, hi, 0, 1, 2, 3, 4, 5, 6, 7); }
DI bf16x8 lds_tr(unsigned a0, unsigned a1) { return cat(lds_tr1(a0), lds_tr1(a1)); }
DI srd_t mk_srd(const void* base, unsigned bytes) { return __builtin_amdgcn_make_buffer_rsrc((void*)base, (short)0, (int)bytes, 0x00020000); }
template <bool VX> DI void mk_src_rows(Src& s, const char* base, unsigned bytes, unsigned stride, unsigned lane) {
  s.srd = mk_srd(base, bytes);
  const unsigned row = (lane >> 2) & 3u, cb = lane >> 4, sl = lane & 3u;
#pragma unroll
  for (int p = 0; p < 4; ++p) s.v[p] = row * stride + cb * 64u + ((sl ^ (VX ? 2 * (p & 1) : (p & 2))) << 4) + (4u * stride - 1024u) * p;
}
template <bool VX> DI void mk_src_w(Src& s, const char* base, unsigned bytes, unsigned stride, unsigned lane) {
  s.srd = mk_srd(base, bytes);
  const unsigned sub = lane >> 5, cb = (lane >> 4) & 1u, row = (lane >> 2) & 3u, sl = lane & 3u;
#pragma unroll
  for (int p = 0; p < 4; ++p) s.v[p] = (4u * sub + row) * stride + cb * 64u + ((sl ^ (VX ? 2u * sub : 2u * (p & 1))) << 4) + (8u * stride - 1024u) * p;
}
template <int NT> DI void touch(unsigned& d, unsigned voff, srd_t srd, unsigned soff) {
  if constexpr (NT) asm volatile("buffer_load_dword %0, %1, %2, %3 offen nt" : "+v"(d) : "v"(voff), "s"(srd), "s"(soff) : "memory");
  else              asm volatile("buffer_load_dword %0, %1, %2, %3 offen" : "+v"(d) : "v"(voff), "s"(srd), "s"(soff) : "memory");
}
struct Geo { int r0, r1, p0, p1, u, t0, t1, t2, t3; };
constexpr Geo GB = {GEOB}, GA = {GEOA};
constexpr unsigned KUS = 1u << GB.u;
template <bool isA> DI unsigned ksoffg(unsigned m, unsigned rk) {
  constexpr Geo G = isA ? GA : GB;
  const unsigned t = m + rk;
  return ((t & 1u) << G.t0) | (((t >> 1) & 1u) << G.t1) | (((t >> 2) & 1u) << G.t2) | (((t >> 3) & 1u) << G.t3);
}
DI unsigned ksoff(unsigned m, unsigned rk) { return ksoffg<false>(m, rk); }
template <bool isA, bool VX> DI void mk_src_kv(Src& s, const char* base, unsigned lane, unsigned w) {
  constexpr Geo G = isA ? GA : GB;
  s.srd = mk_srd(base, 131072u);
  const unsigned row = (lane >> 2) & 3u, sl = lane & 3u, o = ((row & 1u) << G.r0) + ((row >> 1) << G.r1) + (lane >> 4) * 64u;
#pragma unroll
  for (unsigned p = 0; p < (isA ? 2u : 4u); ++p)
    s.v[p] = o + ((sl ^ (VX ? 2u * (p & 1u) : isA ? 2u * (w & 1u) : (p & 2u))) << 4) + ((p & 1u) << G.p0) + ((p >> 1) << G.p1) - (isA ? 0u : 1024u * p);
}

struct PC {
  unsigned r01, r2;
  unsigned w01[2], w2[2];
  unsigned xb, xmask, xa;
  unsigned z, soff;
};
template <unsigned TS_, unsigned QS_, unsigned ZI_, unsigned ZM_, unsigned ZO_, unsigned US_, unsigned CS_, bool ROT_> struct PK {
  static constexpr unsigned TS = TS_, QS = QS_, ZI = ZI_, ZM = ZM_, ZO = ZO_, US = US_, CS = CS_; static constexpr bool ROT = ROT_; };
DI unsigned q_soff(unsigned z) { return ((z & 0xC300u) * 0x440u) & 0x3C0000u; }
template <int POS> DI unsigned ring_lds(const PC& c) { return POS == 2 ? c.r2 : c.r01 + POS * SLOT; }
template <int POS> DI unsigned ring_rd(const PC& c, int d) { return (POS == 2 ? c.w2[d & 1] : c.w01[d & 1] + POS * SLOT) + (d >> 1) * 256u; }
struct NoX { static constexpr bool ACT = true; template <int G> DI void run() const {} };
template <int SL> struct EasyX {
  static constexpr bool ACT = true;
  unsigned long long& t; unsigned lim;
  template <int G> DI void run() const {
    if constexpr (G == 8) asm volatile("s_memrealtime %0" : "=s"(t));
    if constexpr (G == 16) PINS(t);
    if constexpr (G == 16) asm volatile("s_cmp_lt_i32 %0, 0\n\ts_cbranch_scc1 .Le_%=\n\ts_sleep %1\n.Le_%=:" :: "s"((unsigned)t - lim), "i"(SL) : "scc");
  }
};
template <class K, int POS, int S, bool Z, bool RD, bool DM, int NT, bool XT, int M, class X>
DI void proj_gap(f32x4 (&acc)[8][4], const bf16x8 (&cur)[4], bf16x8 (&nxt)[4], bf16x4 (&lo)[4], bf16x8 (&bf)[4], PC& c, unsigned soff, const Src& src, const Src& xs, unsigned xsoff, const X& ex) {
  constexpr int qb = M >> 2, db = M & 3;
  mm_vvv<Z>(acc[4 * S + db][qb], cur[db], bf[qb]);
  if constexpr (RD && M < 8) {
    constexpr int d = M >> 1; const unsigned a = S == 0 ? ring_rd<POS>(c, d) + 4096u : ring_rd<(POS + 1) % 3>(c, d);
    if constexpr (M & 1) nxt[d] = cat(lo[d], lds_tr1(a + K::TS)); else lo[d] = lds_tr1(a);
  }
  if constexpr (X::ACT && RD && S == 1 && db == 3) bf[qb] = lds128(c.xa + qb * K::QS);
  if constexpr (XT) {
    if constexpr (M < 8 && !(M & 1)) dma_p<X_NT, M / 2>(xs.v[M / 2], xs.srd, xsoff);
    if constexpr (M == 7) set_m0(ring_lds<POS>(c) + S * 4096u);
    if constexpr (M >= 8 && !(M & 1)) dma_p<NT, (M - 8) / 2>(src.v[(M - 8) / 2], src.srd, soff);
  } else if constexpr (DM && M >= 8 && !(M & 1)) dma_p<NT, (M - 8) / 2>(src.v[(M - 8) / 2], src.srd, soff);
  if constexpr (X::ACT && S == 0 && M == 9) { PINS(c.z); c.z = (c.z + K::ZI) & K::ZM; PINS(c.z); }
  if constexpr (X::ACT && S == 0 && M == 11) { c.xa = (c.z & c.xmask) | c.xb; PINV(c.xa); }
  if constexpr (S == 1 && DM && !K::ROT && M == 9) { PINS(c.soff); c.soff = (c.soff + K::CS) & (16u * K::CS - 1u); PINS(c.soff); }
  if constexpr (S == 1 && DM && K::ROT && M == 9) { PINS(c.z); c.soff = (c.z + 0x3100u) & 0xC300u; PINS(c.soff); }
  if constexpr (S == 1 && DM && K::ROT && M == 13) { PINS(c.soff); c.soff = (c.soff * 0x440u) & 0x3C0000u; PINS(c.soff); }
  ex.template run<16 * S + M>();
  GAP();
}
template <class K, int POS, int S, bool Z, bool RD, bool DM, int NT, int VM, bool BAR, bool XT, class X, int... Ms>
DI void proj_unit(f32x4 (&acc)[8][4], const bf16x8 (&cur)[4], bf16x8 (&nxt)[4], bf16x8 (&bf)[4], PC& c, const Src& src, const Src& xs, unsigned xsoff, unsigned xdst, const X& ex, iseq<Ms...>) {
  wait_vm_lgkm<(RD ? VM : 63), (S == 0 ? 2 : 0)>();
  if constexpr (BAR) barrier();
  const unsigned soff = c.soff + S * K::US;
  if constexpr (XT) set_m0(xdst); else if constexpr (DM) set_m0(ring_lds<POS>(c) + S * 4096u);
  GAP();
  bf16x4 lo[4];
  (proj_gap<K, POS, S, Z, RD, DM, NT, XT, Ms>(acc, cur, nxt, lo, bf, c, soff, src, xs, xsoff, ex), ...);
}
template <class K, int POS, bool Z, bool LAST, bool DM, int NT, int VM0, int VM1, bool BAR1 = false, bool XT0 = false, bool XT1 = false, class X = NoX>
DI void proj_elem(f32x4 (&acc)[8][4], bf16x8 (&a0)[4], bf16x8 (&a1)[4], bf16x8 (&bf)[4], PC& c, const Src& src, const X& ex = X{},
                  const Src* xs = nullptr, unsigned xso0 = 0, unsigned xd0 = 0, unsigned xso1 = 0, unsigned xd1 = 0) {
  proj_unit<K, POS, 0, Z, true, DM, NT, VM0, false, XT0>(acc, a0, a1, bf, c, src, xs ? *xs : src, xso0, xd0, ex, make_iseq<16>{});
  proj_unit<K, POS, 1, Z, !LAST, DM, NT, VM1, BAR1, XT1>(acc, a1, a0, bf, c, src, xs ? *xs : src, xso1, xd1, ex, make_iseq<16>{});
}
template <class K> DI void proj_setup(PC& c, unsigned tb, unsigned xb, unsigned z0, unsigned s3) {
  c.w01[0] = c.r01 + tb; c.w01[1] = c.w01[0] ^ 32u; c.w2[0] = c.r2 + tb; c.w2[1] = c.w2[0] ^ 32u;
  PINV(c.w01[0]); PINV(c.w01[1]); PINV(c.w2[0]); PINV(c.w2[1]);
  c.xb = xb; c.xmask = K::ZO; PINV(c.xmask);
  c.z = z0; c.soff = s3;
}
template <class K, int POS> DI void proj_start(bf16x8 (&a0)[4], bf16x8 (&bf)[4], PC& c, unsigned tb, unsigned xb, unsigned z0, unsigned s3) {
  proj_setup<K>(c, tb, xb, z0, s3);
  c.xa = (c.z & K::ZO) | xb; PINV(c.xa);
#pragma unroll
  for (int d = 0; d < 4; ++d) { const unsigned a = ring_rd<POS>(c, d); a0[d] = lds_tr(a, a + K::TS); }
  GAP();
#pragma unroll
  for (int q = 0; q < 4; ++q) bf[q] = lds128(c.xa + q * K::QS);
  GAP();
}
#define ACC_ALL(C, a) C(a[0][0]), C(a[0][1]), C(a[0][2]), C(a[0][3]), C(a[1][0]), C(a[1][1]), C(a[1][2]), C(a[1][3]), C(a[2][0]), C(a[2][1]), C(a[2][2]), C(a[2][3]), C(a[3][0]), C(a[3][1]), C(a[3][2]), C(a[3][3])
DI void acc_fence_v(f32x4 (&acc)[8][4]) {
  asm volatile("s_nop 7\n\ts_nop 3" : ACC_ALL("+v", acc) :: "memory");
  f32x4 (&hi)[4][4] = *(f32x4 (*)[4][4])&acc[4];
  asm volatile("" : ACC_ALL("+v", hi) :: "memory");
  GAP();
}
DI void acc_fence_a(f32x4 (&acc)[8][4]) {
  asm volatile("s_nop 7\n\ts_nop 3" : ACC_ALL("+a", acc) :: "memory");
  f32x4 (&hi)[4][4] = *(f32x4 (*)[4][4])&acc[4];
  asm volatile("" : ACC_ALL("+a", hi) :: "memory");
  GAP();
}

struct Stash { u32x4 p[64]; unsigned base[3]; };
template <int I> DI void stash_load1(Stash& st, const Src& sK, const Src& sV, unsigned rk, unsigned wo_) {
  constexpr int m = I >> 2, isV = (I >> 1) & 1, pc_ = I & 1;
  const unsigned so = ksoffg<true>(m, rk) + wo_;
  if constexpr (I < 56) asm volatile("buffer_load_dwordx4 %0, %1, %2, %3 offen " KVPOL : "=a"(st.p[I]) : "v"(isV ? sV.v[pc_] : sK.v[pc_]), "s"(isV ? sV.srd : sK.srd), "s"(so) : "memory");
  else                  asm volatile("buffer_load_dwordx4 %0, %1, %2, %3 offen " KVPOL : "=v"(st.p[I]) : "v"(isV ? sV.v[pc_] : sK.v[pc_]), "s"(isV ? sV.srd : sK.srd), "s"(so) : "memory");
}
constexpr int stash_idx(int j) { const int q = j >> 1, v = (q >> KVPOS) & 1, m = (q & ((1 << KVPOS) - 1)) | ((q >> (KVPOS + 1)) << KVPOS); return 4 * m + 2 * v + (j & 1); }
template <int... Is> DI void stash_load(Stash& st, const Src& sK, const Src& sV, unsigned rk, unsigned wo_, iseq<Is...>) { (stash_load1<stash_idx(Is)>(st, sK, sV, rk, wo_), ...); }
constexpr int kv_pair(int m) { constexpr int t[10] = {0, 1, 2, 3, 8, 9, 4, 5, 6, 7}; return t[m % 10]; }
constexpr unsigned long long kv_tab() { unsigned long long v = 0; for (int i = 0; i < 16; ++i) v |= (unsigned long long)kv_pair(i) << (4 * i); return v; }
template <int I> DI void stash_write1(const Stash& st) {
  constexpr int m = I >> 2, isV = (I >> 1) & 1, pc_ = I & 1, off = kv_pair(m) * 16384 + isV * 8192 + pc_ * 1024;
  if constexpr (I < 56) asm volatile("ds_write_b128 %0, %1 offset:%2" :: "v"(st.base[off >> 16]), "a"(st.p[I]), "i"(off & 65535) : "memory");
  else                  asm volatile("ds_write_b128 %0, %1 offset:%2" :: "v"(st.base[off >> 16]), "v"(st.p[I]), "i"(off & 65535) : "memory");
}
template <int... Is> DI void stash_late(const Stash& st, iseq<Is...>) { (stash_write1<40 + Is>(st), ...); }

struct AC {
  f32x4 o[8][4];
  bf16x8 q[4][4];
  bf16x8 kf[8], vf[8], pf[4];
  f32x4 negm[4];
  f32x4 l[4];
  bf16x8 ones;
};
typedef f32x4 ST[2][4];
constexpr int NEB = 22;
template <int E> DI float sm_val(ST& S) { return S[(E >> 2) & 1][E >> 3][E & 3]; }
template <int E> DI void sm_exp1(ST& S) { float a; asm volatile("v_exp_f32_e32 %0, %1" : "=v"(a) : "v"(sm_val<E>(S))); S[(E >> 2) & 1][E >> 3][E & 3] = a; }
template <int QB, int P> DI void sm_cvt(ST& S, AC& c) {
  u32x4 w = __builtin_bit_cast(u32x4, c.pf[QB]);
  unsigned r;
  asm volatile("v_cvt_pk_bf16_f32 %0, %1, %2" : "=v"(r) : "v"(sm_val<8 * QB + 2 * P>(S)), "v"(sm_val<8 * QB + 2 * P + 1>(S)));
  w[P] = r;
  c.pf[QB] = __builtin_bit_cast(bf16x8, w);
}
template <bool DM, int VM, int R, int M> DI void mem_pre(unsigned ldsaddr) {
  if constexpr (M == 6) wait_lgkm0();
  if constexpr (DM && (M == 6 || M == 14)) set_m0(ldsaddr + (M == 14 ? 4096u : 0u));
  if constexpr (M == R && VM < 63) wait_vm<VM>();
}
template <bool DM, int NT, int M> DI void mem_post(const Src& src, unsigned soff, unsigned ustep) {
  if constexpr (DM && M >= 6 && M <= 20 && !(M & 1)) dma_p<NT, ((M - 6) / 2) & 3>(src.v[((M - 6) / 2) & 3], src.srd, soff + (M >= 14 ? ustep : 0u));
}
template <int WR, int M> DI void mem_wr(const Stash& st) {
  if constexpr (WR >= 40 && M >= 7 && M < 13) stash_write1<WR + M - 7>(st);
  if constexpr (WR >= 0 && WR < 40 && M >= 8 && M < 16)
    asm volatile("ds_write_b128 %0, %1 offset:%2" :: "v"(st.base[0]), "a"(st.p[WR + M - 8]), "i"((M - 8) * 1024) : "memory");
}
struct PA { unsigned rd0, rd1, soff, fill; };
template <bool QK, bool DM, int NT, int VM, int WR, int M, class F>
DI void gapA(AC& c, ST& S, ST& Sn, PA& pa, F& addr, const Src& src, unsigned ustep, const Stash& st, bf16x4& lo) {
  constexpr int R = QK ? 16 : 0;
  if constexpr ((DM || !QK) && M == 0) addr(pa);
  if constexpr (QK) mem_pre<DM, VM, 16, M>(pa.fill); else if constexpr (M == 0 && VM < 63) wait_vm<VM>();
  if constexpr (QK) {
    constexpr int f = M >> 2, s = f >> 1, kb = f & 1, qb = M & 3;
    if constexpr (s == 0) mm_vca(Sn[kb][qb], c.kf[f], c.q[qb][0], c.negm[qb]); else mm_vva<false>(Sn[kb][qb], c.kf[f], c.q[qb][s]);
  }
  mem_post<DM, NT, M>(src, pa.soff, ustep);
  mem_wr<WR, M>(st);
  if constexpr (QK && !DM && M == 10) addr(pa);
  if constexpr (QK && M == 12) asm volatile("" :: "v"(c.negm[0]), "v"(c.negm[1]), "v"(c.negm[2]), "v"(c.negm[3]));
  if constexpr (M >= R && M < R + 16) {
    constexpr int db = (M - R) >> 1; const unsigned a = ((db & 1) ? pa.rd1 : pa.rd0) + (db >> 1) * 256u;
    if constexpr (M & 1) c.vf[db] = cat(lo, lds_tr1(a + 4096u)); else lo = lds_tr1(a);
  }
  if constexpr (NEB + M < 32) sm_exp1<NEB + M>(S);
  if constexpr (M >= 16 && (M & 1)) sm_cvt<2 + ((M - 16) >> 3), ((M - 16) >> 1) & 3>(S, c);
  GAP();
}
template <bool QK, bool DM, int NT, int VM, int WR, class F, int... Ms>
DI void phaseA_(AC& c, ST& S, ST& Sn, F& addr, const Src& src, unsigned ustep, const Stash& st, iseq<Ms...>) {
  PA pa; bf16x4 lo;
  (gapA<QK, DM, NT, VM, WR, Ms>(c, S, Sn, pa, addr, src, ustep, st, lo), ...);
}
template <bool QK, bool DM, int NT, int VM, int WR = -1, class F>
DI void phaseA(AC& c, ST& S, ST& Sn, F addr, const Src& src, unsigned ustep, const Stash& st) {
  phaseA_<QK, DM, NT, VM, WR>(c, S, Sn, addr, src, ustep, st, make_iseq<32>{});
}
template <bool MAX> DI float xlane(float v) {
  auto a = __builtin_amdgcn_permlane32_swap(__float_as_uint(v), __float_as_uint(v), false, false);
  v = MAX ? fmaxf(__uint_as_float(a[0]), __uint_as_float(a[1])) : __uint_as_float(a[0]) + __uint_as_float(a[1]);
  auto b = __builtin_amdgcn_permlane16_swap(__float_as_uint(v), __float_as_uint(v), false, false);
  return MAX ? fmaxf(__uint_as_float(b[0]), __uint_as_float(b[1])) : __uint_as_float(b[0]) + __uint_as_float(b[1]);
}
DI float row_max(const f32x4& x, const f32x4& y) {
  return xlane<true>(fmaxf(fmaxf(fmaxf(x[0], x[1]), fmaxf(x[2], x[3])), fmaxf(fmaxf(y[0], y[1]), fmaxf(y[2], y[3]))));
}
template <bool SM, bool KR, bool DM, int NT, int VM, int WR, int M, class F>
DI void gapB(AC& c, ST& Sn, PA& pa, F& addr, const Src& src, unsigned ustep, const Stash& st) {
  mem_pre<DM, (KR ? VM : 63), 18, M>(pa.fill);
  {
    constexpr int h = M / 18, r = M % 18, qb = 2 * h + (r & 1);
    if constexpr (r < 2) mm_vvv<false>(c.l[qb], c.ones, c.pf[qb]); else mm_avv(c.o[(r - 2) >> 1][qb], c.vf[(r - 2) >> 1], c.pf[qb]);
  }
  mem_post<DM, NT, M>(src, pa.soff, ustep);
  mem_wr<WR, M>(st);
  if constexpr (M == 0) addr(pa);
  if constexpr (KR && M >= 18 && M < 34 && !(M & 1)) { constexpr int f = (M - 18) >> 1; c.kf[f] = lds128(pa.rd0 + (f & 1) * 4096u + (f >> 1) * 256u); }
  if constexpr (SM) {
    if constexpr (M >= 4 && M < 18) sm_exp1<M - 4>(Sn);
    if constexpr (M >= 19 && M < 35 && (M & 1)) sm_exp1<14 + (M - 19) / 2>(Sn);
    if constexpr (M >= 20 && !(M & 1)) sm_cvt<((M - 20) >> 3), ((M - 20) >> 1) & 3>(Sn, c);
  }
  GAP();
}
template <bool SM, bool KR, bool DM, int NT, int VM, int WR, class F, int... Ms>
DI void phaseB_(AC& c, ST& Sn, F& addr, const Src& src, unsigned ustep, const Stash& st, iseq<Ms...>) {
  PA pa;
  (gapB<SM, KR, DM, NT, VM, WR, Ms>(c, Sn, pa, addr, src, ustep, st), ...);
}
template <bool SM, bool KR, bool DM, int NT, int VM, int WR = -1, class F>
DI void phaseB(AC& c, ST& Sn, F addr, const Src& src, unsigned ustep, const Stash& st) {
  phaseB_<SM, KR, DM, NT, VM, WR>(c, Sn, addr, src, ustep, st, make_iseq<36>{});
}
DI void k_read(AC& ac, unsigned ka) {
#pragma unroll
  for (int f = 0; f < 8; ++f) ac.kf[f] = lds128(ka + (f & 1) * 4096u + (f >> 1) * 256u);
}
template <bool DM, int M> DI void qk0_gap(AC& ac, ST& SA, const Src& src, unsigned soff, unsigned fill) {
  constexpr int f = M >> 2, qb = M & 3;
  if constexpr (DM && (M == 0 || M == 16)) set_m0(fill + M * 256u);
  mm_vva<(f < 2)>(SA[f & 1][qb], ac.kf[f], ac.q[qb][f >> 1]);
  if constexpr (DM && qb == 0) dma_p<KV_NT, f & 3>(src.v[f & 3], src.srd, soff + (f >> 2) * KUS);
  GAP();
}
template <bool DM, int... Ms> DI void qk0(AC& ac, ST& SA, const Src& src, unsigned soff, unsigned fill, iseq<Ms...>) { (qk0_gap<DM, Ms>(ac, SA, src, soff, fill), ...); }
template <int... Js> DI void exp_first(ST& SA, iseq<Js...>) { (sm_exp1<Js>(SA), ...); }
template <int... Js> DI void cvt_first(ST& SA, AC& c, iseq<Js...>) { (sm_cvt<(Js >> 2), (Js & 3)>(SA, c), ...); }
DI void sm_first(AC& ac, ST& SA) {
  asm volatile("s_nop 7\n\ts_nop 3" : "+v"(SA[0][0]), "+v"(SA[0][1]), "+v"(SA[0][2]), "+v"(SA[0][3]), "+v"(SA[1][0]), "+v"(SA[1][1]), "+v"(SA[1][2]), "+v"(SA[1][3]) :: "memory");
  GAP();
#pragma unroll
  for (int q = 0; q < 4; ++q) {
    const float m = row_max(SA[0][q], SA[1][q]);
    SA[0][q] -= m; SA[1][q] -= m;
    ac.negm[q] = f32x4{-m, -m, -m, -m};
    ac.l[q] = f32x4{};
    ac.pf[q] = bf16x8{};
  }
  ac.ones = __builtin_bit_cast(bf16x8, u32x4{0x3f803f80u, 0x3f803f80u, 0x3f803f80u, 0x3f803f80u});
  asm volatile("" : "+v"(ac.negm[0]), "+v"(ac.negm[1]), "+v"(ac.negm[2]), "+v"(ac.negm[3]), "+v"(ac.l[0]), "+v"(ac.l[1]), "+v"(ac.l[2]), "+v"(ac.l[3]), "+v"(ac.ones));
  exp_first(SA, make_iseq<NEB>{});
  cvt_first(SA, ac, make_iseq<8>{});
  GAP();
}
struct BS { unsigned p[4], v[6], k[2], rk, wo, l16; };
constexpr bool b_dma(int f) { return f != 2 * (SK - 3) && f != 2 * (SV - 2) + 1 && f != 2 * (SP - 2) + 1 && f < 30 && !(LATEW && f == 27); }
constexpr int b_vm(int f) {
  const int s = f == 30 ? 26 : f - 3;
  if (!b_dma(s)) return 63;
  int n = b_dma(f) ? 5 + (f & 1) : 0;
  for (int i = s + 1; i < f; ++i) n += b_dma(i) ? 8 : 0;
  return n;
}
template <class KO, int I> DI void b_step(AC& ac, ST& S, ST& Sn, BS& b, const Src& sK, const Src& sV, const Src& sO, Stash& st) {
  constexpr int fa = 2 * I, fb = 2 * I + 1, vi = (I == SP || I == 15) ? 4 : (I & 1) ? 0 : 2;
  const unsigned pa = b.p[(I & 1) ? 3 : 1], pb = b.p[(I & 1) ? 0 : 2];
  if constexpr ((BARM >> I) & 1) barrier();
  if constexpr (I == SK - 3) st.base[0] = pa + b.l16;
  phaseA<(I < 15), b_dma(fa), (I < 14 ? KV_NT : W_NT), b_vm(fa), (I == SK - 3 ? 0 : -1)>(ac, S, Sn, [&](PA& p) __attribute__((always_inline)) {
    PINS(b.rk);
    p.soff = I < 14 ? ksoff(I < 13 ? I + 3 : 15, b.rk) : b.wo + (LATEW ? 0u : KO::CS); p.fill = (LATEW && I == 14) ? b.p[0] : pa;
    p.rd0 = b.v[vi] + (I == SP ? SLOT : 0u); p.rd1 = b.v[vi + 1] + (I == SP ? SLOT : 0u); }, I < 13 ? sK : I < 14 ? sV : sO, I < 14 ? KUS : KO::US, st);
  if constexpr (I == SV - 2) st.base[0] = pb + b.l16;
  if constexpr (I < 15)
    phaseB<true, (I < 14), b_dma(fb), (I < 13 ? KV_NT : W_NT), b_vm(fb), (I == SV - 2 ? 8 : -1)>(ac, Sn, [&](PA& p) __attribute__((always_inline)) {
      PINS(b.rk);
      p.soff = I < 13 ? ksoff(I + 2, b.rk) : b.wo + (I - 13) * (LATEW ? 1u : 2u) * KO::CS; p.fill = (LATEW && I == 14) ? b.p[1] : pb; p.rd0 = b.k[(I & 1) ? 0 : 1]; }, I < 13 ? sV : sO, I < 13 ? KUS : KO::US, st);
}
template <int CH, int Q, int J> DI void o_word(const AC& c, const float (&inv)[4], bf16x8 (&nb)[4]) {
  u32x4 w = __builtin_bit_cast(u32x4, nb[Q]);
  unsigned r; float t;
  asm volatile("v_accvgpr_read_b32 %0, %2\n\tv_accvgpr_read_b32 %1, %3\n\tv_mul_f32_e32 %0, %0, %4\n\tv_mul_f32_e32 %1, %1, %4\n\tv_cvt_pk_bf16_f32 %0, %0, %1"
               : "=&v"(r), "=&v"(t) : "a"(c.o[2 * CH + (J >> 1)][Q][2 * (J & 1)]), "a"(c.o[2 * CH + (J >> 1)][Q][2 * (J & 1) + 1]), "v"(inv[Q]));
  w[J] = r;
  nb[Q] = __builtin_bit_cast(bf16x8, w);
}
template <int CH, int Q> DI void o_put(const bf16x8 (&nb)[4], unsigned ow) { asm volatile("ds_write_b128 %0, %1 offset:%2" :: "v"(ow), "v"(nb[Q]), "i"(CH * 4096 + Q * 1024) : "memory"); }
template <int E> struct CvX {
  static constexpr bool ACT = E == 3;
  const AC& c; const float (&inv)[4]; const bf16x8 (&cur)[4]; bf16x8 (&nb)[4]; unsigned ow;
  template <int G> DI void run() const {
    if constexpr (E < 3 && (G & 1)) o_word<E + 1, (G >> 3), (G >> 1) & 3>(c, inv, nb);
    if constexpr (E < 3 && ((G & 15) == 8 || (G & 15) == 10)) o_put<E, 2 * (G >> 4) + ((G >> 1) & 1)>(cur, ow);
    if constexpr (E == 2 && (G & 15) == 12) o_put<3, (G >> 4)>(nb, ow);
    if constexpr (E == 3 && (G == 8 || G == 10)) o_put<3, 2 + ((G >> 1) & 1)>(cur, ow);
  }
};
template <class K, int VM, bool DM, int M> DI void gapL(AC& c, float (&inv)[4], bf16x8 (&nb)[4], bf16x8 (&a0)[4], bf16x4& lo, const PC& pc, const Src& src, unsigned soff) {
  if constexpr (M == 6) wait_vm_lgkm<VM, 0>();
  if constexpr (DM && (M == 12 || M == 32)) set_m0(pc.r2 + (M == 32 ? 4096u : 0u));
  if constexpr (M < 4) mm_vvv<false>(c.l[M], c.ones, c.pf[M]); else mm_avv(c.o[(M - 4) >> 2][M & 3], c.vf[(M - 4) >> 2], c.pf[M & 3]);
  if constexpr (M >= 6 && M < 14) {
    constexpr int d = (M - 6) >> 1; const unsigned a = ring_rd<0>(pc, d);
    if constexpr (M & 1) a0[d] = cat(lo, lds_tr1(a + K::TS)); else lo = lds_tr1(a);
  }
  if constexpr (M >= 4 && M < 12 && !(M & 1)) asm volatile("v_rcp_f32_e32 %0, %1" : "=v"(inv[(M - 4) >> 1]) : "v"(c.l[(M - 4) >> 1][0]));
  if constexpr (M >= 16 && M < 32) o_word<0, ((M - 16) >> 2), (M & 3)>(c, inv, nb);
  if constexpr (DM && ((M >= 12 && M < 16) || M >= 32)) dma_p<W_NT, M & 3>(src.v[M & 3], src.srd, soff + (M >= 32 ? K::US : 0u));
  GAP();
}
template <class K, int VM, bool DM, int... Ms> DI void phaseL(AC& c, float (&inv)[4], bf16x8 (&nb)[4], bf16x8 (&a0)[4], const PC& pc, const Src& src, unsigned soff, iseq<Ms...>) {
  bf16x4 lo;
#pragma unroll
  for (int q = 0; q < 4; ++q) nb[q] = bf16x8{};
  (gapL<K, VM, DM, Ms>(c, inv, nb, a0, lo, pc, src, soff), ...);
}
struct Pub { u32x4 pk[16]; srd_t srd; unsigned v[4], so; };
template <int F, int H> DI void pub_half(const f32x4 (&acc)[8][4], Pub& p) {
  const f32x4& a = acc[4 * (F >> 3) + 2 * (F & 1) + H][(F >> 1) & 3];
  unsigned r0, r1;
  asm volatile("v_cvt_pk_bf16_f32 %0, %2, %3\n\tv_cvt_pk_bf16_f32 %1, %4, %5" : "=&v"(r0), "=v"(r1) : "v"(a[0]), "v"(a[1]), "v"(a[2]), "v"(a[3]));
  p.pk[F][2 * H] = r0; p.pk[F][2 * H + 1] = r1;
  if constexpr (H) asm volatile("buffer_store_dwordx4 %0, %1, %2, %3 offen offset:%4 " PUBPOL :: "v"(p.pk[F]), "v"(p.v[F >> 2]), "s"(p.srd), "s"(p.so), "i"((F & 3) * 1024) : "memory");
}
struct PubX {
  static constexpr bool ACT = false;
  const f32x4 (&acc)[8][4]; Pub& p;
  template <int G> DI void run() const { if constexpr (G >= 16) pub_half<((G - 16) >> 1), (G & 1)>(acc, p); }
};
template <class K, int U, bool PB, int M>
DI void tail_gap(f32x4 (&acc)[8][4], const bf16x8 (&cur)[4], bf16x8 (&nxt)[4], bf16x4 (&lo)[4], bf16x8 (&bf)[4], const PC& c, const unsigned (&xa)[3], Pub& p) {
  constexpr int qb = M >> 2, db = M & 3, S = U / 3, ne = (U + 1) % 3;
  mm_vvv<false>(acc[4 * S + db][qb], cur[db], bf[qb]);
  if constexpr (U < 5 && M < 8) {
    constexpr int d = M >> 1; const unsigned a = ring_rd<(ne + 1) % 3>(c, d) + ((U + 1) / 3) * 4096u;
    if constexpr (M & 1) nxt[d] = cat(lo[d], lds_tr1(a + K::TS)); else lo[d] = lds_tr1(a);
  }
  if constexpr (U < 5 && db == 3) bf[qb] = lds128(xa[ne] + qb * K::QS);
  if constexpr (PB && (U == 3 || U == 4) && M >= 8) pub_half<4 * (U - 3) + ((M - 8) >> 1), (M & 1)>(acc, p);
  if constexpr (PB && U == 5 && M >= 8) pub_half<8 + ((M - 8) >> 1), (M & 1)>(acc, p);
  GAP();
}
template <class K, int U, bool PB, int VM, int... Ms>
DI void tail_unit(f32x4 (&acc)[8][4], const bf16x8 (&cur)[4], bf16x8 (&nxt)[4], bf16x8 (&bf)[4], const PC& c, const unsigned (&xa)[3], Pub& p, iseq<Ms...>) {
  wait_vm_lgkm<VM, 2>();
  GAP();
  bf16x4 lo[4];
  (tail_gap<K, U, PB, Ms>(acc, cur, nxt, lo, bf, c, xa, p), ...);
}
template <int I> DI void own_put1(const u32x4& v, unsigned a) { asm volatile("ds_write_b128 %0, %1 offset:%2 ; %3" :: "v"(a), "v"(v), "i"((I & 7) * 1024), "i"(I) : "memory"); }
template <int B, int... Is> DI void own_put(const Pub& p, unsigned a, iseq<Is...>) { (own_put1<B + Is>(p.pk[B + Is], a), ...); }
template <int... Is> DI void pub_rest(const f32x4 (&acc)[8][4], Pub& p, iseq<Is...>) { (pub_half<12 + (Is >> 1), (Is & 1)>(acc, p), ...); }
template <int... Is> DI void pub_slow(const f32x4 (&acc)[8][4], Pub& p, iseq<Is...>) { ((pub_half<Is, 0>(acc, p), pub_half<Is, 1>(acc, p), __builtin_amdgcn_s_sleep(PUB_SLEEP)), ...); }
DI void redo_head(const AC& ac, const char* __restrict__ Kp, const char* __restrict__ Vp, unsigned lane, unsigned ow) {
  const unsigned r16 = lane & 15u, g = lane >> 4;
#pragma unroll 1
  for (unsigned qb = 0; qb < 4; ++qb) {
    bf16x8 qq[4];
#pragma unroll
    for (int s = 0; s < 4; ++s) qq[s] = qb == 0 ? ac.q[0][s] : qb == 1 ? ac.q[1][s] : qb == 2 ? ac.q[2][s] : ac.q[3][s];
    f32x4 o[8];
#pragma unroll
    for (int d = 0; d < 8; ++d) o[d] = f32x4{};
    float m = -__builtin_inff(), l = 0.f;
#pragma unroll 1
    for (unsigned t = 0; t < 16; ++t) {
      f32x4 s0 = {}, s1 = {};
      const char* kp = Kp + (t * 32u + r16) * 256u + g * 16u;
#pragma unroll
      for (int s = 0; s < 4; ++s) {
        s0 = __builtin_amdgcn_mfma_f32_16x16x32_bf16(*(const bf16x8*)(kp + s * 64), qq[s], s0, 0, 0, 0);
        s1 = __builtin_amdgcn_mfma_f32_16x16x32_bf16(*(const bf16x8*)(kp + s * 64 + 4096), qq[s], s1, 0, 0, 0);
      }
      const float mn = fmaxf(m, row_max(s0, s1)), al = __builtin_amdgcn_exp2f(m - mn);
      m = mn;
#pragma unroll
      for (int j = 0; j < 4; ++j) { s0[j] = __builtin_amdgcn_exp2f(s0[j] - mn); s1[j] = __builtin_amdgcn_exp2f(s1[j] - mn); }
      l = l * al + ((s0[0] + s0[1]) + (s0[2] + s0[3])) + ((s1[0] + s1[1]) + (s1[2] + s1[3]));
      const bf16x8 pf = __builtin_bit_cast(bf16x8, pack2(s0, s1));
      const __bf16* vp = (const __bf16*)(Vp + (t * 32u + g * 4u) * 256u) + r16;
#pragma unroll
      for (int d = 0; d < 8; ++d) {
        bf16x8 vf;
#pragma unroll
        for (int j = 0; j < 8; ++j) vf[j] = vp[((j & 3) + (j >> 2) * 16) * 128 + d * 16];
        o[d] = __builtin_amdgcn_mfma_f32_16x16x32_bf16(vf, pf, o[d] * al, 0, 0, 0);
      }
    }
    const float inv = 1.0f / xlane<false>(l);
#pragma unroll
    for (int s = 0; s < 4; ++s) *(LAS u32x4*)(size_t)(ow + s * 4096u + qb * 1024u) = pack2(o[2 * s] * inv, o[2 * s + 1] * inv);
  }
}
template <bool isA, int J> DI void q_conv1(AC& ac, f32x4 (&acc)[8][4], Stash& st, const Src& sK, const Src& sV, unsigned rk, unsigned p3) {
  constexpr int s = J >> 2, qb = J & 3;
  if constexpr (isA) { stash_write1<(5 * J) / 2>(st); stash_write1<(5 * J) / 2 + 1>(st); if constexpr (J & 1) stash_write1<(5 * J) / 2 + 2>(st); }
  else {
    constexpr int p = 2 * (J & 1);
    const Src& src = s < 2 ? sK : sV;
    const unsigned so = ksoff(s == 0 ? 2 : s == 1 ? SK : s == 2 ? SV : SP, rk) + ((J >> 1) & 1) * KUS;
    if constexpr (s == 0 || s == 3) {
      if constexpr (p == 0) asm volatile("s_mov_b32 m0, %0\n\ts_nop 0" :: "s"(p3 + (s == 3 ? SLOT : 0u) + (J & 2) * 2048u) : "memory");
      dma_p<KV_NT, p>(src.v[p], src.srd, so); dma_p<KV_NT, p + 1>(src.v[p + 1], src.srd, so);
    } else {
      asm volatile("buffer_load_dwordx4 %0, %1, %2, %3 offen offset:%4 " KVPOL : "=a"(st.p[2 * J - 8]) : "v"(src.v[p]), "s"(src.srd), "s"(so), "i"(1024 * p) : "memory");
      asm volatile("buffer_load_dwordx4 %0, %1, %2, %3 offen offset:%4 " KVPOL : "=a"(st.p[2 * J - 7]) : "v"(src.v[p + 1]), "s"(src.srd), "s"(so), "i"(1024 * p + 1024) : "memory");
    }
  }
  ac.q[qb][s] = __builtin_bit_cast(bf16x8, natural8(pack2(acc[2 * s][qb] * SC, acc[2 * s + 1][qb] * SC)));
  asm volatile("" : "+a"(ac.q[qb][s]));
  GAP();
}
template <bool isA, int... Js> DI void q_conv(AC& ac, f32x4 (&acc)[8][4], Stash& st, const Src& sK, const Src& sV, unsigned rk, unsigned p3, iseq<Js...>) { (q_conv1<isA, Js>(ac, acc, st, sK, sV, rk, p3), ...); }
DI void zero_o(AC& ac) {
#pragma unroll
  for (int i = 0; i < 8; ++i)
#pragma unroll
    for (int q = 0; q < 4; ++q) { ac.o[i][q] = f32x4{}; asm volatile("" : "+a"(ac.o[i][q])); }
}

DI unsigned vbid() {
  const unsigned i = blockIdx.x;
  return i;
}
template <bool isA>
DI void body(const char* __restrict__ X, const char* __restrict__ K, const char* __restrict__ V, const char* __restrict__ Wq, const char* __restrict__ Wo,
             const char* __restrict__ Wo2, srd_t code, char* __restrict__ part, char* __restrict__ out) {
  extern __shared__ __attribute__((aligned(16))) char lds[];
  unsigned long long pr0 = 0, prk = 0;
  STAMP(0);
  unsigned long long tnow;
  asm volatile("s_memrealtime %0\n\ts_waitcnt lgkmcnt(0)" : "=s"(tnow));
  const unsigned tlim = (unsigned)tnow + EASY_T;
  unsigned tid = threadIdx.x;
  PINV(tid);
  const unsigned lane = tid & 63u, w = rfl(tid >> 6);
  const unsigned bid = vbid(), xg = bid & 7u, rr = bid >> 3, h = xg * 4u + w;
  const unsigned b = rr & 15u;
  const unsigned r16 = lane & 15u, g = lane >> 4;
  const char* xp  = X + (size_t)b * 65536u + w * 16384u;
  const char* wqp = Wq + h * 256u;
  const char* wop = Wo + (size_t)xg * 524288u + w * 256u;
  const size_t kvo = isA ? (size_t)(b * 8u + xg) * 131072u : (size_t)(b * 32u + h) * 131072u;
  const unsigned rk = isA ? (RKA) : (RKB);
  typedef PK<512u, 4096u, 0x1300u, 0xF300u, 0xC300u, 128u, 32u * 8192u, true> KQ;
  typedef PK<512u, 4096u, 0x1300u, 0xF300u, 0xC300u, KUS, 8192u, false> KT;
  typedef PK<2048u, 1024u, 0x1000u, 0xF000u, 0xF000u, 128u, 32u * 1024u, false> KO;
  asm volatile("" :: "v"(lds));
  PC pc;
  pc.r01 = 65536u + w * 16384u; pc.r2 = 131072u + w * 8192u;
  const unsigned zq = (((PX0) & 3u) << 14) | ((b >> 2) << 8), co = 4u * w;
  const srd_t srdH = mk_srd(part + HOFF, 4096u + 256u * 128u);
  const unsigned tok_off = rr * 128u, flg_off = 4096u + rr * 1024u;
  const unsigned l8 = (lane & 7u) * 4u;
  unsigned entry_flags;
  asm volatile("buffer_load_dword %0, %1, %2, %3 offen sc1" : "=v"(entry_flags) : "v"(l8), "s"(srdH), "s"(flg_off + xg * 128u) : "memory");
  if constexpr (WARM_CODE && isA) { if (w == 3 && lane < WARM_CODE) asm volatile("buffer_load_dword %0, %1, %2, 0 offen" : "+v"(entry_flags) : "v"((b * WARM_CODE + lane) * 128u), "s"(code) : "memory"); }
  Src sQ, sX, sK, sV, sO;
  mk_src_w<false>(sQ, wqp, 511u * 8192u + 256u, 8192u, lane);
  mk_src_rows<false>(sX, xp, 16384u, 1024u, lane);
  const unsigned p0 = (PX0) & 3u, p1 = (p0 + 1u) & 3u, p2 = (p0 + 2u) & 3u, p3 = (p0 + 3u) & 3u, xw = w * 4096u;
  Stash st;
  unsigned dum0 = lane;
  if constexpr (isA && ORD == 0) {
    mk_src_kv<isA, false>(sK, K + kvo, lane, w);
    mk_src_kv<isA, true>(sV, V + kvo, lane, w);
    const unsigned to = (lane >> 1) * 1024u + (lane & 1u) * 128u + b * KO::CS;
    const char* wb = Wo2 + (size_t)xg * 524288u + w * 256u;
    if constexpr (EARLY_O == 1) asm volatile("global_load_dword %0, %1, %2\n\tglobal_load_dword %0, %1, %3" : "=&v"(dum0) : "v"(to), "s"(wop), "s"(wb) : "memory");
    stash_load(st, sK, sV, rk, ((w & 1u) << GA.p1) + ((w >> 1) << GA.u), make_iseq<64>{});
    if constexpr (EARLY_O == 2) asm volatile("global_load_dword %0, %1, %2\n\tglobal_load_dword %0, %1, %3" : "=&v"(dum0) : "v"(to), "s"(wop), "s"(wb) : "memory");
  }
  if constexpr ((isA ? ILV_A : ILV_B) == 1) {
    dma_unit<TOUCH_Q>(sQ, q_soff(zq), pc.r2);
    dma_unit<W_NT>(sQ, q_soff(zq + 0x1300u), pc.r01);
    if constexpr (!(isA && ORD == 2)) dma_unit<X_NT>(sX, p0 * 256u, xw + p0 * 16384u);
    dma_unit<TOUCH_Q>(sQ, q_soff(zq) + KQ::US, pc.r2 + 4096u);
    dma_unit<W_NT>(sQ, q_soff(zq + 0x1300u) + KQ::US, pc.r01 + 4096u);
    if constexpr (!isA && XUP) {
      dma_unit<X_NT>(sX, p1 * 256u, xw + p1 * 16384u);
      dma_unit<X_NT>(sX, p2 * 256u, xw + p2 * 16384u);
      dma_unit<X_NT>(sX, p3 * 256u, xw + p3 * 16384u);
    }
  } else {
    dma_elem<(isA ? ILV_A : ILV_B) == 2 ? 0 : TOUCH_Q>(sQ, q_soff(zq), KQ::US, pc.r2);
    if constexpr (!(isA && ORD == 2)) dma_unit<X_NT>(sX, p0 * 256u, xw + p0 * 16384u);
    dma_elem<W_NT>(sQ, q_soff(zq + 0x1300u), KQ::US, pc.r01);
  }
  if constexpr (!(isA && ORD == 0)) {
    mk_src_kv<isA, false>(sK, K + kvo, lane, w);
    mk_src_kv<isA, true>(sV, V + kvo, lane, w);
  }
  mk_src_w<true>(sO, wop, 511u * 1024u + 256u, 1024u, lane);
  if constexpr (isA) {
    if constexpr (ORD == 2) {
      if constexpr (DELAY_A > 0) __builtin_amdgcn_s_sleep(DELAY_A);
      stash_load(st, sK, sV, rk, ((w & 1u) << GA.p1) + ((w >> 1) << GA.u), make_iseq<64>{});
      dma_unit<X_NT>(sX, p0 * 256u, xw + p0 * 16384u);
    }
    dma_elem<W_NT>(sQ, q_soff(zq + 0x2200u), KQ::US, pc.r01 + SLOT);
    dma_unit<X_NT>(sX, p1 * 256u, xw + p1 * 16384u);
    dma_unit<X_NT>(sX, p2 * 256u, xw + p2 * 16384u);
    dma_unit<X_NT>(sX, p3 * 256u, xw + p3 * 16384u);
    if constexpr (ORD == 1) stash_load(st, sK, sV, rk, ((w & 1u) << GA.p1) + ((w >> 1) << GA.u), make_iseq<64>{});
  }
  const unsigned rb = 1024u * (r16 >> 2) + 64u * (r16 & 3u) + 16u * (g ^ ((r16 >> 2) & 2u));
  const unsigned tb = 64u * ((lane >> 2) & 3u) + 32u * (g & 1u) + 8u * (lane & 3u);
  const unsigned tbV = tb + 1024u * g;
  const unsigned sb0 = w * 2048u + lane * 16u;
  st.base[0] = sb0; st.base[1] = sb0 + 65536u; st.base[2] = sb0 + 131072u;
  AC ac;
  if constexpr (!isA) zero_o(ac);
  {
    f32x4 acc[8][4];
    bf16x8 a0[4], a1[4], bf[4];
    asm volatile("s_waitcnt vmcnt(%2)" : "+v"(entry_flags), "+v"(dum0) : "i"(isA ? (ORD == 1 ? 63 : ORD == 0 ? 28 : 20) : XUP ? 20 : 8) : "memory");
    if (w == 0) {
      const unsigned long long mk = __builtin_amdgcn_ballot_w64(((entry_flags >> (lane & 7u)) & 1u) != 0u);
      const unsigned tok = 0x5a5a5a00u | (~(unsigned)mk & 0xffu);
      if (lane == 0) asm volatile("buffer_store_dword %0, off, %1, %2 sc1" :: "v"(tok), "s"(srdH), "s"(tok_off + xg * 4u) : "memory");
    }
    barrier();
    STAMP(1);
    proj_start<KQ, 2>(a0, bf, pc, tbV, rb, zq, q_soff(zq + 0x3100u));
    if constexpr (isA) {
      constexpr int V1 = ORD == 0 ? 28 : 63, V2 = ORD == 1 ? 63 : 28;
      proj_elem<KQ, 2, true,  false, true, W_NT, 63, V1>(acc, a0, a1, bf, pc, sQ);
      STAMP(10);
      proj_elem<KQ, 0, false, false, true, W_NT, V1, V2>(acc, a0, a1, bf, pc, sQ);
      STAMP(11);
      proj_elem<KQ, 1, false, false, true, W_NT, V2, 16>(acc, a0, a1, bf, pc, sQ);
      proj_elem<KQ, 2, false, false, true, W_NT, 16, 16, ORD != 1>(acc, a0, a1, bf, pc, sQ);
    } else if constexpr (XUP) {
      dma_elem<W_NT>(sQ, q_soff(zq + 0x2200u), KQ::US, pc.r01 + SLOT);
      proj_elem<KQ, 2, true,  false, true, W_NT, 24, 63>(acc, a0, a1, bf, pc, sQ);
      STAMP(10);
      proj_elem<KQ, 0, false, false, true, W_NT, 28, 16>(acc, a0, a1, bf, pc, sQ);
      STAMP(11);
      proj_elem<KQ, 1, false, false, true, W_NT, 16, 16>(acc, a0, a1, bf, pc, sQ);
      proj_elem<KQ, 2, false, false, true, W_NT, 16, 16, true>(acc, a0, a1, bf, pc, sQ);
    } else {
      dma_elem<W_NT>(sQ, q_soff(zq + 0x2200u), KQ::US, pc.r01 + SLOT);
      proj_elem<KQ, 2, true,  false, true, W_NT, (ILV_B == 1 ? 12 : 16), 20, false, true, true>(acc, a0, a1, bf, pc, sQ, NoX{}, &sX, p1 * 256u, xw + p1 * 16384u, p2 * 256u, xw + p2 * 16384u);
      STAMP(10);
      proj_elem<KQ, 0, false, false, true, W_NT, 24, 28, false, true, false>(acc, a0, a1, bf, pc, sQ, NoX{}, &sX, p3 * 256u, xw + p3 * 16384u);
      STAMP(11);
      proj_elem<KQ, 1, false, false, true, W_NT, 28, 24>(acc, a0, a1, bf, pc, sQ);
      proj_elem<KQ, 2, false, false, true, W_NT, 20, 16, true>(acc, a0, a1, bf, pc, sQ);
    }
    STAMP(8);
    typedef std::conditional_t<isA && (EASY > 0), EasyX<EASY>, NoX> PX;
    const PX px = [&] { if constexpr (isA && EASY > 0) return EasyX<EASY>{tnow, tlim}; else return NoX{}; }();
#pragma unroll 1
    for (unsigned e = 0; e < 3; ++e) {
      proj_elem<KQ, 0, false, false, true, W_NT, 16, 16>(acc, a0, a1, bf, pc, sQ, px);
      proj_elem<KQ, 1, false, false, true, W_NT, 16, 16>(acc, a0, a1, bf, pc, sQ, px);
      proj_elem<KQ, 2, false, false, true, W_NT, 16, 16>(acc, a0, a1, bf, pc, sQ, px);
    }
    STAMP(9);
    if constexpr (isA) {
      proj_elem<KQ, 0, false, false, false, W_NT, 16, 12>(acc, a0, a1, bf, pc, sQ, px);
      proj_elem<KQ, 1, false, false, false, W_NT, 8, 4>(acc, a0, a1, bf, pc, sQ, px);
      proj_elem<KQ, 2, false, true, false, W_NT, 0, 0>(acc, a0, a1, bf, pc, sQ, px);
    } else {
      pc.soff = ksoff(0u, rk);
      proj_elem<KT, 0, false, false, true, KV_NT, 16, 16>(acc, a0, a1, bf, pc, sK);
      pc.soff = ksoff(1u, rk);
      proj_elem<KT, 1, false, false, true, KV_NT, 16, 16>(acc, a0, a1, bf, pc, sK);
      pc.soff = ksoff(0u, rk);
      proj_elem<KT, 2, false, true, true, KV_NT, 16, 16>(acc, a0, a1, bf, pc, sV);
    }
    barrier();
    STAMP(2);
    acc_fence_v(acc);
    q_conv<isA>(ac, acc, st, sK, sV, rk, w * 16384u, make_iseq<16>{});
  }
  ST SA, SB;
  if constexpr (isA) {
    zero_o(ac);
    const unsigned kb = rb, vb = SLOT + tbV;
    unsigned dum = lane, to = TOUCH_B == 4 ? (lane >> 4) * 1024u + (lane & 8u) * 16u + (lane & 7u) * 4u : (lane >> 3) * 1024u + (lane & 4u) * 32u + (lane & 3u) * 4u;
    PINV(to);
    const char* ta = wop + b * KO::CS;
    const char* tb2 = Wo2 + (size_t)xg * 524288u + w * 256u + b * KO::CS;
    if constexpr (TOUCH_B == 4) asm volatile("global_load_dword %0, %1, %2 nt\n\tglobal_load_dword %0, %1, %2\n\tv_add_u32_e32 %1, 0x1000, %1" : "+v"(dum), "+v"(to) : "s"(ta) : "memory");
    asm volatile("s_waitcnt lgkmcnt(0)" ::: "memory");
    barrier();
    k_read(ac, kb + kv_pair(0) * 16384u);
    wait_lgkm0();
    GAP();
    qk0<false>(ac, SA, sK, 0u, 0u, make_iseq<32>{});
    k_read(ac, kb + kv_pair(1) * 16384u);
    if constexpr (TOUCH_B == 4) asm volatile("global_load_dword %0, %1, %2 nt\n\tglobal_load_dword %0, %1, %2\n\tv_add_u32_e32 %1, 0x1000, %1" : "+v"(dum), "+v"(to) : "s"(ta) : "memory");
    sm_first(ac, SA);
    if constexpr (STAG > 0) { for (unsigned k = 0; k < (b & 7u); ++k) __builtin_amdgcn_s_sleep(STAG); }
    STAMP(13);
    unsigned i4 = 0;
    auto av = [&](PA& p) { if constexpr (PACE_ATT > 0) __builtin_amdgcn_s_sleep(PACE_ATT);
                           PINS(i4); p.rd0 = vb + (((unsigned)(kv_tab() >> i4) & 15u) << 14); p.rd1 = p.rd0 ^ 32u; PINV(p.rd0); PINV(p.rd1); };
    auto ak = [&](PA& p) { if constexpr (PACE_ATT > 0) __builtin_amdgcn_s_sleep(PACE_ATT);
                           if constexpr (RAMP > 0) asm volatile("s_cmp_ge_u32 %0, 16\n\ts_cbranch_scc1 .Lr_%=\n\ts_sleep %1\n\ts_cmp_ge_u32 %0, 8\n\ts_cbranch_scc1 .Lr_%=\n\ts_sleep %1\n.Lr_%=:" :: "s"(i4), "i"(RAMP) : "scc");
                           PINS(i4); p.rd0 = kb + (((unsigned)((kv_tab() >> 8) >> i4) & 15u) << 14); PINV(p.rd0); i4 += 4u; };
    auto an = [&](PA&) { i4 += 4u; };
    auto at = [&](PA& p) __attribute__((always_inline)) {
      PINS(i4); p.rd0 = vb + (((unsigned)(kv_tab() >> i4) & 15u) << 14); p.rd1 = p.rd0 ^ 32u; PINV(p.rd0); PINV(p.rd1);
      if constexpr (TOUCH_O) {
        const char* tp;
        if constexpr (TOUCH_B == 4) {
          asm volatile("s_cmp_lt_u32 %1, 24\n\ts_cselect_b64 %0, %2, %3" : "=s"(tp) : "s"(i4), "s"(ta), "s"(tb2 - 32768) : "scc");
          asm volatile("global_load_dword %0, %1, %2 nt\n\tglobal_load_dword %0, %1, %2\n\tv_add_u32_e32 %1, 0x1000, %1" : "+v"(dum), "+v"(to) : "s"(tp) : "memory");
        } else {
          asm volatile("s_bitcmp0_b32 %1, 4\n\ts_cselect_b64 %0, %2, %3" : "=s"(tp) : "s"(i4), "s"(ta), "s"(tb2) : "scc");
          asm volatile("global_load_dword %0, %1, %2 nt\n\tglobal_load_dword %0, %1, %2" : "+v"(dum) : "v"(to), "s"(tp) : "memory");
          to = (to + 8192u) & 32767u; PINV(to);
        }
      }
    };
#define ITS
    while (i4 < 24u) {
      ITS
      phaseA<true, false, KV_NT, 63>(ac, SA, SB, at, sK, 0u, st);
      phaseB<true, true, false, KV_NT, 63>(ac, SB, ak, sK, 0u, st);
      phaseA<true, false, KV_NT, 63>(ac, SB, SA, at, sK, 0u, st);
      phaseB<true, true, false, KV_NT, 63>(ac, SA, ak, sK, 0u, st);
    }
    ITS
    barrier();
    phaseA<true, false, KV_NT, 63, 40>(ac, SA, SB, at, sK, 0u, st);
    phaseB<true, true, false, KV_NT, 63, 46>(ac, SB, ak, sK, 0u, st);
    phaseA<true, false, KV_NT, 63, 52>(ac, SB, SA, at, sK, 0u, st);
    phaseB<true, true, false, KV_NT, 63, 58>(ac, SA, ak, sK, 0u, st);
    wait_vm_lgkm<63, 8>(); barrier();
    while (i4 < 56u) {
      ITS
      phaseA<true, false, KV_NT, 63>(ac, SA, SB, at, sK, 0u, st);
      phaseB<true, true, false, KV_NT, 63>(ac, SB, ak, sK, 0u, st);
      phaseA<true, false, KV_NT, 63>(ac, SB, SA, at, sK, 0u, st);
      phaseB<true, true, false, KV_NT, 63>(ac, SA, ak, sK, 0u, st);
    }
    STAMP(14);
    barrier();
    unsigned v14 = vb + kv_pair(14) * 16384u, x14 = v14 ^ 32u; PINV(v14); PINV(x14);
    phaseA<true, true, W_NT, 63>(ac, SA, SB, [&](PA& p) { p.soff = co * KO::CS; p.fill = pc.r01; p.rd0 = v14; p.rd1 = x14; }, sO, KO::US, st);
    phaseB<true, false, true, W_NT, 63>(ac, SB, [&](PA& p) { p.soff = (co + 1u) * KO::CS; p.fill = pc.r01 + SLOT; }, sO, KO::US, st);
    phaseA<false, false, W_NT, 63>(ac, SB, SA, [&](PA& p) { p.rd0 = v14 + 16384u; p.rd1 = x14 + 16384u; }, sO, 0u, st);
    dum0 = dum;
  } else {
    const unsigned P0 = pc.r01, P1 = pc.r01 + SLOT, P2 = pc.r2, P3 = w * 16384u;
    unsigned v0 = tbV + P0, v2 = tbV + P2, x0 = v0 ^ 32u, x2 = v2 ^ 32u, k1 = rb + P1, k3 = rb + P3;
    PINV(v0); PINV(v2); PINV(x0); PINV(x2); PINV(k1); PINV(k3);
    wait_vm<48>();
    k_read(ac, k1 - SLOT);
    wait_lgkm0();
    GAP();
    qk0<true>(ac, SA, sV, ksoff(1u, rk), P0, make_iseq<32>{});
    wait_vm<48>();
    GAP();
    k_read(ac, k1);
    sm_first(ac, SA);
    STAMP(13);
    BS bs{{P0, P1, P2, P3}, {v0, x0, v2, x2, tbV + P3, (tbV + P3) ^ 32u}, {k1, k3}, rk, co * KO::CS, lane * 16u};
    PINV(bs.v[4]); PINV(bs.v[5]);
#define BT(k)
#define B2(I) b_step<KO, I>(ac, SA, SB, bs, sK, sV, sO, st); b_step<KO, I + 1>(ac, SB, SA, bs, sK, sV, sO, st);
    B2(0) BT(0) B2(2) BT(1) B2(4) BT(2) B2(6) BT(3) B2(8) BT(4) B2(10)
    STAMP(14);
    B2(12) B2(14)
#undef B2
  }
  {
    const unsigned l16 = lane * 16u;
    unsigned ow = w * 16384u + l16;
    PINV(ow);
    f32x4 acc[8][4];
    bf16x8 a0[4], a1[4], b0[4], bf[4];
    float inv[4];
    proj_setup<KO>(pc, tb + 512u * g, l16, (co + 3u) << 12, (co + 3u) * KO::CS);
    unsigned toks;
    asm volatile("buffer_load_dword %0, %1, %2, %3 offen sc1" : "=v"(toks) : "v"(l8), "s"(srdH), "s"(tok_off) : "memory");
    GAP();
    phaseL<KO, (isA || LATEW ? 13 : 21), (!isA && LATEW)>(ac, inv, b0, a0, pc, sO, (co + 2u) * KO::CS, make_iseq<36>{});
    STAMP(3);
    if constexpr (isA) {
      barrier();
      dma_elem<W_NT>(sO, (co + 2u) * KO::CS, KO::US, pc.r2);
    }
    const bool ok = ac.l[0][0] <= LMAX && ac.l[1][0] <= LMAX && ac.l[2][0] <= LMAX && ac.l[3][0] <= LMAX;
    if (__builtin_expect(__builtin_amdgcn_ballot_w64(!ok) != 0ull, 0)) {
      asm volatile("s_waitcnt vmcnt(0)" : "+v"(toks) :: "memory");
      acc_fence_a(ac.o);
      redo_head(ac, K + kvo, V + kvo, lane, ow);
#pragma unroll
      for (int q = 0; q < 4; ++q) {
        inv[q] = 1.0f;
        b0[q] = lds128(ow + q * 1024u);
#pragma unroll
        for (int s = 1; s < 4; ++s) {
          const u32x4 t = __builtin_bit_cast(u32x4, lds128(ow + s * 4096u + q * 1024u));
          ac.o[2 * s][q] = f32x4{__uint_as_float(t[0] << 16), __uint_as_float(t[0] & 0xffff0000u), __uint_as_float(t[1] << 16), __uint_as_float(t[1] & 0xffff0000u)};
          ac.o[2 * s + 1][q] = f32x4{__uint_as_float(t[2] << 16), __uint_as_float(t[2] & 0xffff0000u), __uint_as_float(t[3] << 16), __uint_as_float(t[3] & 0xffff0000u)};
        }
      }
      wait_lgkm0();
      acc_fence_a(ac.o);
    }
#pragma unroll
    for (int q = 0; q < 4; ++q) bf[q] = bf16x8{};
    GAP();
    proj_elem<KO, 0, true, false, true, W_NT, 16, 16>(acc, a0, a1, b0, pc, sO, CvX<0>{ac, inv, b0, bf, ow});
    proj_elem<KO, 1, false, false, true, W_NT, 16, 16>(acc, a0, a1, bf, pc, sO, CvX<1>{ac, inv, bf, b0, ow});
    proj_elem<KO, 2, false, false, true, W_NT, 16, 16>(acc, a0, a1, b0, pc, sO, CvX<2>{ac, inv, b0, bf, ow});
    if constexpr (isA && TOUCH_B >= 3) PINV(dum0);
    proj_elem<KO, 0, false, false, true, W_NT, 16, 16, true>(acc, a0, a1, bf, pc, sO, CvX<3>{ac, inv, bf, b0, ow});
    STAMP(4);
    typedef std::conditional_t<isA && (EASY2 > 0), EasyX<EASY2>, NoX> PX;
    const PX px = [&] { if constexpr (isA && EASY2 > 0) return EasyX<EASY2>{tnow, tlim + EASY2_T - EASY_T}; else return NoX{}; }();
#pragma unroll 1
    for (unsigned e = 0; e < 3; ++e) {
      proj_elem<KO, 1, false, false, true, W_NT, 16, 16>(acc, a0, a1, bf, pc, sO, px);
      proj_elem<KO, 2, false, false, true, W_NT, 16, 16>(acc, a0, a1, bf, pc, sO, px);
      proj_elem<KO, 0, false, false, true, W_NT, 16, 16>(acc, a0, a1, bf, pc, sO, px);
    }
    CYC(6);
    Pub pb;
    pb.srd = mk_srd(part + POFF + (size_t)rr * 524288u, 524288u);
    pb.so = xg * 65536u + w * 16384u;
#pragma unroll
    for (unsigned i = 0; i < 4; ++i) { pb.v[i] = l16 + i * 4096u + (SKIPOWN && 2u * w + (i >> 1) == xg ? 0x80000000u : 0u); PINV(pb.v[i]); }
#pragma unroll
    for (int i = 0; i < 16; ++i) pb.pk[i] = u32x4{};
    unsigned xa[3];
#pragma unroll
    for (unsigned i = 0; i < 3; ++i) { xa[i] = (((co + 13u + i) & 15u) << 12) | l16; PINV(xa[i]); }
    constexpr bool PB = !(isA && PUB_SLEEP > 0);
    tail_unit<KO, 0, PB, 12>(acc, a0, a1, bf, pc, xa, pb, make_iseq<16>{});
    tail_unit<KO, 1, PB, 4>(acc, a1, a0, bf, pc, xa, pb, make_iseq<16>{});
    tail_unit<KO, 2, PB, 63>(acc, a0, a1, bf, pc, xa, pb, make_iseq<16>{});
    tail_unit<KO, 3, PB, 0>(acc, a1, a0, bf, pc, xa, pb, make_iseq<16>{});
    CYC(7);
    tail_unit<KO, 4, PB, 63>(acc, a0, a1, bf, pc, xa, pb, make_iseq<16>{});
    tail_unit<KO, 5, PB, 63>(acc, a1, a0, bf, pc, xa, pb, make_iseq<16>{});
    STAMP(5);
    if constexpr (PB) pub_rest(acc, pb, make_iseq<8>{}); else pub_slow(acc, pb, make_iseq<16>{});
    const unsigned lr = ((lane + xg) & 7u) * 4u;
    unsigned fl;
    asm volatile("buffer_load_dword %0, %1, %2, %3 offen sc1" : "=v"(fl) : "v"(lr), "s"(srdH), "s"(flg_off + xg * 128u) : "memory");
    PINV(toks);
    STAMP(6);
    if (w == (xg >> 1)) {
      const unsigned a = pc.r01 + SLOT + l16;
      if (xg & 1u) own_put<8>(pb, a, make_iseq<8>{}); else own_put<0>(pb, a, make_iseq<8>{});
    }
    const unsigned fo = lane * 128u + xg * 4u, tau = __builtin_amdgcn_readlane(toks, xg);
    unsigned oo = ((b * 64u + 16u * w + r16) * 1024u + (isA ? 0u : 512u) + 64u * xg + 8u * g) * 2u;
    PINV(oo);
    wait_lgkm0();
    barrier();
    const unsigned oa = 65536u + SLOT + (xg >> 1) * 16384u + w * 2048u + l16;
    const u32x4 r0 = __builtin_bit_cast(u32x4, lds128(oa)), r1 = __builtin_bit_cast(u32x4, lds128(oa + 1024u));
    GAP();
    wait_vm<9>();
    if (lane == 2u * w) asm volatile("buffer_store_dword %0, %1, %2, %3 offen " FLPOL :: "v"(toks), "v"(fo), "s"(srdH), "s"(flg_off) : "memory");
    wait_vm<2>();
    if (lane == 2u * w + 1u) asm volatile("buffer_store_dword %0, %1, %2, %3 offen " FLPOL :: "v"(toks), "v"(fo), "s"(srdH), "s"(flg_off) : "memory");
    f32x16 f[8];
#pragma unroll
    for (int j = 0; j < 8; ++j) { const unsigned t = j < 4 ? r0[j & 3] : r1[j & 3]; f[0][2 * j] = __uint_as_float(t << 16); f[0][2 * j + 1] = __uint_as_float(t & 0xffff0000u); }
    PINV(f[0]);
    asm volatile("s_waitcnt vmcnt(2)" : "+v"(fl) : "v"(pb.pk[0]), "v"(pb.pk[1]), "v"(pb.pk[2]), "v"(pb.pk[3]), "v"(pb.pk[4]), "v"(pb.pk[5]), "v"(pb.pk[6]), "v"(pb.pk[7]),
                 "v"(pb.pk[8]), "v"(pb.pk[9]), "v"(pb.pk[10]), "v"(pb.pk[11]), "v"(pb.pk[12]), "v"(pb.pk[13]), "v"(pb.pk[14]), "v"(pb.pk[15]) : "memory");
    STAMP(7);
    unsigned got = 0xffffff01u, spin = 1u << 21, fresh, so, f2;
#define POLL(R) "buffer_load_dword %[" R "], %[lr], %[sh], %[flg] offen " PLPOL "\n\t"
#define SEEN(R) "v_cmp_eq_u32_e32 vcc, %[tau], %[" R "]\n\ts_andn2_b32 %[fr], vcc_lo, %[got]\n\ts_cbranch_scc1 .Lproc_%=\n\t"
    asm volatile(
        SEEN("fl") POLL("fl") "s_sleep 4\n"
        ".Lidle_%=:\n\t" POLL("f2") "s_waitcnt vmcnt(1)\n\t" SEEN("fl") "s_sleep %[slp]\n\t" POLL("fl") "s_waitcnt vmcnt(1)\n\t" SEEN("f2")
        "s_sleep %[slp]\n\ts_sub_u32 %[spin], %[spin], 1\n\ts_cbranch_scc0 .Lidle_%=\n\ts_branch .Ldone_%=\n"
        ".Lproc_%=:\n\ts_or_b32 %[got], %[got], %[fr]\n\t"
        "s_bitcmp1_b32 %[fr], 1\n\ts_cbranch_scc0 .La1_%=\n\ts_add_u32 %[so], %[ro], 65536\n\ts_bitset0_b32 %[so], 19\n\t"
        "buffer_load_dwordx4 v[152:155], %[l16], %[sp], %[so] offen " LDPOL "\n\tbuffer_load_dwordx4 v[156:159], %[l16], %[sp], %[so] offen offset:1024 " LDPOL "\n.La1_%=:\n\t"
        "s_bitcmp1_b32 %[fr], 2\n\ts_cbranch_scc0 .La2_%=\n\ts_add_u32 %[so], %[ro], 131072\n\ts_bitset0_b32 %[so], 19\n\t"
        "buffer_load_dwordx4 v[168:171], %[l16], %[sp], %[so] offen " LDPOL "\n\tbuffer_load_dwordx4 v[172:175], %[l16], %[sp], %[so] offen offset:1024 " LDPOL "\n.La2_%=:\n\t"
        "s_bitcmp1_b32 %[fr], 3\n\ts_cbranch_scc0 .La3_%=\n\ts_add_u32 %[so], %[ro], 196608\n\ts_bitset0_b32 %[so], 19\n\t"
        "buffer_load_dwordx4 v[184:187], %[l16], %[sp], %[so] offen " LDPOL "\n\tbuffer_load_dwordx4 v[188:191], %[l16], %[sp], %[so] offen offset:1024 " LDPOL "\n.La3_%=:\n\t"
        "s_bitcmp1_b32 %[fr], 4\n\ts_cbranch_scc0 .La4_%=\n\ts_add_u32 %[so], %[ro], 262144\n\ts_bitset0_b32 %[so], 19\n\t"
        "buffer_load_dwordx4 v[200:203], %[l16], %[sp], %[so] offen " LDPOL "\n\tbuffer_load_dwordx4 v[204:207], %[l16], %[sp], %[so] offen offset:1024 " LDPOL "\n.La4_%=:\n\t"
        "s_bitcmp1_b32 %[fr], 5\n\ts_cbranch_scc0 .La5_%=\n\ts_add_u32 %[so], %[ro], 327680\n\ts_bitset0_b32 %[so], 19\n\t"
        "buffer_load_dwordx4 v[216:219], %[l16], %[sp], %[so] offen " LDPOL "\n\tbuffer_load_dwordx4 v[220:223], %[l16], %[sp], %[so] offen offset:1024 " LDPOL "\n.La5_%=:\n\t"
        "s_bitcmp1_b32 %[fr], 6\n\ts_cbranch_scc0 .La6_%=\n\ts_add_u32 %[so], %[ro], 393216\n\ts_bitset0_b32 %[so], 19\n\t"
        "buffer_load_dwordx4 v[232:235], %[l16], %[sp], %[so] offen " LDPOL "\n\tbuffer_load_dwordx4 v[236:239], %[l16], %[sp], %[so] offen offset:1024 " LDPOL "\n.La6_%=:\n\t"
        "s_bitcmp1_b32 %[fr], 7\n\ts_cbranch_scc0 .La7_%=\n\ts_add_u32 %[so], %[ro], 458752\n\ts_bitset0_b32 %[so], 19\n\t"
        "buffer_load_dwordx4 v[248:251], %[l16], %[sp], %[so] offen " LDPOL "\n\tbuffer_load_dwordx4 v[252:255], %[l16], %[sp], %[so] offen offset:1024 " LDPOL "\n.La7_%=:\n\t"
        "s_waitcnt vmcnt(0)\n\ts_cmp_eq_u32 %[got], -1\n\ts_cbranch_scc1 .Ltok_%=\n\t" POLL("fl") "s_branch .Lwide_%=\n"
        ".Ltok_%=:\n\tbuffer_load_dword %[fl], %[l8], %[sh], %[tok] offen sc1\n"
        ".Lwide_%=:\n\t"
        "s_bitcmp1_b32 %[fr], 1\n\ts_cbranch_scc0 .Lb1_%=\n\t"
        "v_lshlrev_b32_e32 v144, 16, v152\n\tv_and_b32_e32 v145, %[hi], v152\n\tv_lshlrev_b32_e32 v146, 16, v153\n\tv_and_b32_e32 v147, %[hi], v153\n\t"
        "v_lshlrev_b32_e32 v148, 16, v154\n\tv_and_b32_e32 v149, %[hi], v154\n\tv_lshlrev_b32_e32 v150, 16, v155\n\tv_and_b32_e32 v151, %[hi], v155\n\t"
        "v_lshlrev_b32_e32 v152, 16, v156\n\tv_and_b32_e32 v153, %[hi], v156\n\tv_lshlrev_b32_e32 v154, 16, v157\n\tv_and_b32_e32 v155, %[hi], v157\n\t"
        "v_lshlrev_b32_e32 v156, 16, v158\n\tv_and_b32_e32 v157, %[hi], v158\n\tv_lshlrev_b32_e32 v158, 16, v159\n\tv_and_b32_e32 v159, %[hi], v159\n\t"
        ".Lb1_%=:\n\t"
        "s_bitcmp1_b32 %[fr], 2\n\ts_cbranch_scc0 .Lb2_%=\n\t"
        "v_lshlrev_b32_e32 v160, 16, v168\n\tv_and_b32_e32 v161, %[hi], v168\n\tv_lshlrev_b32_e32 v162, 16, v169\n\tv_and_b32_e32 v163, %[hi], v169\n\t"
        "v_lshlrev_b32_e32 v164, 16, v170\n\tv_and_b32_e32 v165, %[hi], v170\n\tv_lshlrev_b32_e32 v166, 16, v171\n\tv_and_b32_e32 v167, %[hi], v171\n\t"
        "v_lshlrev_b32_e32 v168, 16, v172\n\tv_and_b32_e32 v169, %[hi], v172\n\tv_lshlrev_b32_e32 v170, 16, v173\n\tv_and_b32_e32 v171, %[hi], v173\n\t"
        "v_lshlrev_b32_e32 v172, 16, v174\n\tv_and_b32_e32 v173, %[hi], v174\n\tv_lshlrev_b32_e32 v174, 16, v175\n\tv_and_b32_e32 v175, %[hi], v175\n\t"
        ".Lb2_%=:\n\t"
        "s_bitcmp1_b32 %[fr], 3\n\ts_cbranch_scc0 .Lb3_%=\n\t"
        "v_lshlrev_b32_e32 v176, 16, v184\n\tv_and_b32_e32 v177, %[hi], v184\n\tv_lshlrev_b32_e32 v178, 16, v185\n\tv_and_b32_e32 v179, %[hi], v185\n\t"
        "v_lshlrev_b32_e32 v180, 16, v186\n\tv_and_b32_e32 v181, %[hi], v186\n\tv_lshlrev_b32_e32 v182, 16, v187\n\tv_and_b32_e32 v183, %[hi], v187\n\t"
        "v_lshlrev_b32_e32 v184, 16, v188\n\tv_and_b32_e32 v185, %[hi], v188\n\tv_lshlrev_b32_e32 v186, 16, v189\n\tv_and_b32_e32 v187, %[hi], v189\n\t"
        "v_lshlrev_b32_e32 v188, 16, v190\n\tv_and_b32_e32 v189, %[hi], v190\n\tv_lshlrev_b32_e32 v190, 16, v191\n\tv_and_b32_e32 v191, %[hi], v191\n\t"
        ".Lb3_%=:\n\t"
        "s_bitcmp1_b32 %[fr], 4\n\ts_cbranch_scc0 .Lb4_%=\n\t"
        "v_lshlrev_b32_e32 v192, 16, v200\n\tv_and_b32_e32 v193, %[hi], v200\n\tv_lshlrev_b32_e32 v194, 16, v201\n\tv_and_b32_e32 v195, %[hi], v201\n\t"
        "v_lshlrev_b32_e32 v196, 16, v202\n\tv_and_b32_e32 v197, %[hi], v202\n\tv_lshlrev_b32_e32 v198, 16, v203\n\tv_and_b32_e32 v199, %[hi], v203\n\t"
        "v_lshlrev_b32_e32 v200, 16, v204\n\tv_and_b32_e32 v201, %[hi], v204\n\tv_lshlrev_b32_e32 v202, 16, v205\n\tv_and_b32_e32 v203, %[hi], v205\n\t"
        "v_lshlrev_b32_e32 v204, 16, v206\n\tv_and_b32_e32 v205, %[hi], v206\n\tv_lshlrev_b32_e32 v206, 16, v207\n\tv_and_b32_e32 v207, %[hi], v207\n\t"
        ".Lb4_%=:\n\t"
        "s_bitcmp1_b32 %[fr], 5\n\ts_cbranch_scc0 .Lb5_%=\n\t"
        "v_lshlrev_b32_e32 v208, 16, v216\n\tv_and_b32_e32 v209, %[hi], v216\n\tv_lshlrev_b32_e32 v210, 16, v217\n\tv_and_b32_e32 v211, %[hi], v217\n\t"
        "v_lshlrev_b32_e32 v212, 16, v218\n\tv_and_b32_e32 v213, %[hi], v218\n\tv_lshlrev_b32_e32 v214, 16, v219\n\tv_and_b32_e32 v215, %[hi], v219\n\t"
        "v_lshlrev_b32_e32 v216, 16, v220\n\tv_and_b32_e32 v217, %[hi], v220\n\tv_lshlrev_b32_e32 v218, 16, v221\n\tv_and_b32_e32 v219, %[hi], v221\n\t"
        "v_lshlrev_b32_e32 v220, 16, v222\n\tv_and_b32_e32 v221, %[hi], v222\n\tv_lshlrev_b32_e32 v222, 16, v223\n\tv_and_b32_e32 v223, %[hi], v223\n\t"
        ".Lb5_%=:\n\t"
        "s_bitcmp1_b32 %[fr], 6\n\ts_cbranch_scc0 .Lb6_%=\n\t"
        "v_lshlrev_b32_e32 v224, 16, v232\n\tv_and_b32_e32 v225, %[hi], v232\n\tv_lshlrev_b32_e32 v226, 16, v233\n\tv_and_b32_e32 v227, %[hi], v233\n\t"
        "v_lshlrev_b32_e32 v228, 16, v234\n\tv_and_b32_e32 v229, %[hi], v234\n\tv_lshlrev_b32_e32 v230, 16, v235\n\tv_and_b32_e32 v231, %[hi], v235\n\t"
        "v_lshlrev_b32_e32 v232, 16, v236\n\tv_and_b32_e32 v233, %[hi], v236\n\tv_lshlrev_b32_e32 v234, 16, v237\n\tv_and_b32_e32 v235, %[hi], v237\n\t"
        "v_lshlrev_b32_e32 v236, 16, v238\n\tv_and_b32_e32 v237, %[hi], v238\n\tv_lshlrev_b32_e32 v238, 16, v239\n\tv_and_b32_e32 v239, %[hi], v239\n\t"
        ".Lb6_%=:\n\t"
        "s_bitcmp1_b32 %[fr], 7\n\ts_cbranch_scc0 .Lb7_%=\n\t"
        "v_lshlrev_b32_e32 v240, 16, v248\n\tv_and_b32_e32 v241, %[hi], v248\n\tv_lshlrev_b32_e32 v242, 16, v249\n\tv_and_b32_e32 v243, %[hi], v249\n\t"
        "v_lshlrev_b32_e32 v244, 16, v250\n\tv_and_b32_e32 v245, %[hi], v250\n\tv_lshlrev_b32_e32 v246, 16, v251\n\tv_and_b32_e32 v247, %[hi], v251\n\t"
        "v_lshlrev_b32_e32 v248, 16, v252\n\tv_and_b32_e32 v249, %[hi], v252\n\tv_lshlrev_b32_e32 v250, 16, v253\n\tv_and_b32_e32 v251, %[hi], v253\n\t"
        "v_lshlrev_b32_e32 v252, 16, v254\n\tv_and_b32_e32 v253, %[hi], v254\n\tv_lshlrev_b32_e32 v254, 16, v255\n\tv_and_b32_e32 v255, %[hi], v255\n\t"
        ".Lb7_%=:\n\t"
        "s_cmp_eq_u32 %[got], -1\n\ts_cbranch_scc0 .Lidle_%=\n"
        ".Ldone_%=:"
        : "=&{v[144:159]}"(f[1]), "=&{v[160:175]}"(f[2]), "=&{v[176:191]}"(f[3]), "=&{v[192:207]}"(f[4]), "=&{v[208:223]}"(f[5]), "=&{v[224:239]}"(f[6]), "=&{v[240:255]}"(f[7]),
          [fl] "+v"(fl), [f2] "=&v"(f2), [got] "+s"(got), [spin] "+s"(spin), [fr] "=&s"(fresh), [so] "=&s"(so)
        : [tau] "s"(tau), [l16] "v"(l16), [l8] "v"(l8), [lr] "v"(lr), [sp] "s"(pb.srd), [sh] "s"(srdH), [ro] "s"(xg * 73728u + w * 2048u), [tok] "s"(tok_off),
          [flg] "s"(flg_off + xg * 128u), [hi] "s"(0xffff0000u), [slp] "i"(isA ? SLEEP_A : SLEEP_B)
        : "vcc", "scc", "memory");
#undef POLL
#undef SEEN
    STAMP(15);
    const f32x16 t = ((f[0] + f[1]) + (f[2] + f[3])) + ((f[4] + f[5]) + (f[6] + f[7]));
#pragma unroll
    for (int s = 0; s < 2; ++s)
    {
      const u32x4 ov = natural8(u32x4{cvtpk(t[8 * s], t[8 * s + 1]), cvtpk(t[8 * s + 2], t[8 * s + 3]), cvtpk(t[8 * s + 4], t[8 * s + 5]), cvtpk(t[8 * s + 6], t[8 * s + 7])});
      __builtin_amdgcn_raw_buffer_store_b128(ov, mk_srd(out, 2097152u), oo + s * 64, 0, OUTPOL);
    }
    asm volatile("s_waitcnt vmcnt(2)" : "+v"(fl) :: "memory");
    if ((lane >> 1) == w && fl != toks) asm volatile("buffer_store_dword %0, %1, %2, %3 offen sc1" :: "v"(fl), "v"(fo), "s"(srdH), "s"(flg_off) : "memory");
  }
  asm volatile("s_waitcnt vmcnt(0)" ::: "memory");
  STAMP(12);
  if (PROBE_WHO == 3 || (PROBE_WHO == 0 && bid == 0) || (PROBE_WHO == 1 && !isA) || (PROBE_WHO == 2 && isA) || (PROBE_WHO == 4 && bid == 128)) {
    const unsigned long long target = pr0 + 10000ull + PROBE_MUL * (prk - pr0);
    for (int i = 0; i < 1000000; ++i) { unsigned long long t_; asm volatile("s_memrealtime %0\n\ts_waitcnt lgkmcnt(0)" : "=s"(t_) :: "memory"); if (t_ >= target) break; }
  }
}

__global__ __launch_bounds__(256, 1)
void fused_attn(const char* __restrict__ Ax, const char* __restrict__ Ak, const char* __restrict__ Av, const char* __restrict__ Awq, const char* __restrict__ Awo,
                const char* __restrict__ Bx, const char* __restrict__ Bk, const char* __restrict__ Bv, const char* __restrict__ Bwq, const char* __restrict__ Bwo,
                char* __restrict__ part, char* __restrict__ out) {
  const char* pc; unsigned left;
  asm volatile("s_getpc_b64 %0\n.Lpc_%=:\n\ts_mov_b32 %1, .Lhot_end-.Lpc_%=" : "=s"(pc), "=s"(left));
  const srd_t code = mk_srd(pc, left);
  if (vbid() >= 128) { body<true>(Ax, Ak, Av, Awq, Awo, Bwo, code, part, out); asm volatile(".Lhot_end:"); __builtin_amdgcn_endpgm(); }
  body<false>(Bx, Bk, Bv, Bwq, Bwo, Bwo, code, part, out);
}

extern "C" void kernel_launch(void* const* d_in, const int* in_sizes, int n_in, void* d_out, int out_size, void* d_ws, size_t ws_size, hipStream_t stream) {
  (void)in_sizes; (void)n_in; (void)out_size; (void)ws_size;
  (void)hipFuncSetAttribute((const void*)fused_attn, hipFuncAttributeMaxDynamicSharedMemorySize, (int)LDS_BYTES);
  fused_attn<<<256, 256, LDS_BYTES, stream>>>((const char*)d_in[0], (const char*)d_in[1], (const char*)d_in[2], (const char*)d_in[3], (const char*)d_in[4],
                                              (const char*)d_in[5], (const char*)d_in[6], (const char*)d_in[7], (const char*)d_in[8], (const char*)d_in[9], (char*)d_ws, (char*)d_out);
}
```

```cpp
#include <hip/hip_runtime.h>
#include <hip/hip_bf16.h>
#include <utility>
#include <type_traits>

typedef __bf16 bf16x8 __attribute__((ext_vector_type(8)));
typedef __bf16 bf16x4 __attribute__((ext_vector_type(4)));
typedef __bf16 bf16x2 __attribute__((ext_vector_type(2)));
typedef float f32x4 __attribute__((ext_vector_type(4)));
typedef float f32x2 __attribute__((ext_vector_type(2)));
typedef float f32x16 __attribute__((ext_vector_type(16)));
typedef unsigned u32x4 __attribute__((ext_vector_type(4)));
#define LAS __attribute__((address_space(3)))
#define DI __device__ __forceinline__
typedef __amdgpu_buffer_rsrc_t srd_t;
template <int... Is> using iseq = std::integer_sequence<int, Is...>;
template <int N> using make_iseq = std::make_integer_sequence<int, N>;

constexpr int KV_NT = 1, W_NT = 0, X_NT = 0;
constexpr int SK = 4, SV = 4, SP = 5;
constexpr unsigned EASY_T = 1350;
constexpr unsigned WARM = 40;

constexpr unsigned SLOT = 8192, LDS_BYTES = 20 * SLOT;
constexpr unsigned HOFF = 16u << 20;
constexpr float SC = 0.12751743082f;
constexpr float LMAX = 1048576.0f;

#define GAP() __builtin_amdgcn_sched_barrier(0)
#define PINS(x) asm volatile("" : "+s"(x))
#define PINV(x) asm volatile("" : "+v"(x))
DI unsigned rfl(unsigned x) { return __builtin_amdgcn_readfirstlane(x); }
DI unsigned cvtpk(float lo, float hi) { f32x2 v = {lo, hi}; bf16x2 b = __builtin_convertvector(v, bf16x2); return __builtin_bit_cast(unsigned, b); }
DI u32x4 pack2(const f32x4& a, const f32x4& b) { return u32x4{cvtpk(a[0], a[1]), cvtpk(a[2], a[3]), cvtpk(b[0], b[1]), cvtpk(b[2], b[3])}; }
DI u32x4 natural8(u32x4 w) {
  auto s0 = __builtin_amdgcn_permlane32_swap(w[0], w[2], false, false);
  auto s1 = __builtin_amdgcn_permlane32_swap(w[1], w[3], false, false);
  auto t0 = __builtin_amdgcn_permlane16_swap(s0[0], s0[1], false, false);
  auto t1 = __builtin_amdgcn_permlane16_swap(s1[0], s1[1], false, false);
  return u32x4{t0[0], t1[0], t0[1], t1[1]};
}
#define MF "v_mfma_f32_16x16x32_bf16 "
template <bool Z> DI void mm_vvv(f32x4& c, const bf16x8& a, const bf16x8& b) {
  if constexpr (Z) asm volatile(MF "%0, %1, %2, 0" : "=&v"(c) : "v"(a), "v"(b));
  else             asm volatile(MF "%0, %1, %2, %0" : "+v"(c) : "v"(a), "v"(b));
}
template <bool Z> DI void mm_vva(f32x4& c, const bf16x8& a, const bf16x8& b) {
  if constexpr (Z) asm volatile(MF "%0, %1, %2, 0" : "=&v"(c) : "v"(a), "a"(b));
  else             asm volatile(MF "%0, %1, %2, %0" : "+v"(c) : "v"(a), "a"(b));
}
DI void mm_vca(f32x4& d, const bf16x8& a, const bf16x8& b, const f32x4& c) { asm volatile(MF "%0, %1, %2, %3" : "=&v"(d) : "v"(a), "a"(b), "v"(c)); }
DI void mm_avv(f32x4& c, const bf16x8& a, const bf16x8& b) { asm volatile(MF "%0, %1, %2, %0" : "+a"(c) : "v"(a), "v"(b)); }
DI void set_m0(unsigned v) { asm volatile("s_mov_b32 m0, %0" :: "s"(v) : "memory"); }
template <int NT, int P> DI void dma_p(unsigned voff, srd_t srd, unsigned soff) {
  if constexpr (NT) asm volatile("buffer_load_dwordx4 %0, %1, %2 offen offset:%3 nt lds" :: "v"(voff), "s"(srd), "s"(soff), "i"(1024 * P) : "memory");
  else              asm volatile("buffer_load_dwordx4 %0, %1, %2 offen offset:%3 lds" :: "v"(voff), "s"(srd), "s"(soff), "i"(1024 * P) : "memory");
}
struct Src { unsigned v[4]; srd_t srd; };
template <int NT> DI void dma_unit(const Src& s, unsigned soff, unsigned ldsaddr) {
  asm volatile("s_mov_b32 m0, %0\n\ts_nop 0" :: "s"(ldsaddr) : "memory");
  dma_p<NT, 0>(s.v[0], s.srd, soff); dma_p<NT, 1>(s.v[1], s.srd, soff); dma_p<NT, 2>(s.v[2], s.srd, soff); dma_p<NT, 3>(s.v[3], s.srd, soff);
}
template <int NT> DI void dma_elem(const Src& s, unsigned soff, unsigned ustep, unsigned ldsaddr) { dma_unit<NT>(s, soff, ldsaddr); dma_unit<NT>(s, soff + ustep, ldsaddr + 4096u); }
template <int N> DI void wait_vm() { asm volatile("s_waitcnt vmcnt(%0)" :: "i"(N) : "memory"); }
template <int N, int L> DI void wait_vm_lgkm() { asm volatile("" ::: "memory"); __builtin_amdgcn_s_waitcnt((N & 15) | ((N >> 4) << 14) | 0x70 | (L << 8)); asm volatile("" ::: "memory"); }
DI void wait_lgkm0() { wait_vm_lgkm<63, 0>(); }
DI void barrier() { asm volatile("s_barrier" ::: "memory"); }
DI bf16x8 lds128(unsigned a) { return *(const LAS bf16x8*)(size_t)(a); }
DI bf16x4 lds_tr1(unsigned a) { return __builtin_amdgcn_ds_read_tr16_b64_v4bf16((LAS bf16x4*)(size_t)(a)); }
DI bf16x8 cat(bf16x4 lo, bf16x4 hi) { return __builtin_shufflevector(lo, hi, 0, 1, 2, 3, 4, 5, 6, 7); }
DI bf16x8 lds_tr(unsigned a0, unsigned a1) { return cat(lds_tr1(a0), lds_tr1(a1)); }
DI srd_t mk_srd(const void* base, unsigned bytes) { return __builtin_amdgcn_make_buffer_rsrc((void*)base, (short)0, (int)bytes, 0x00020000); }
template <bool VX> DI void mk_src_rows(Src& s, const char* base, unsigned bytes, unsigned stride, unsigned lane) {
  s.srd = mk_srd(base, bytes);
  const unsigned row = (lane >> 2) & 3u, cb = lane >> 4, sl = lane & 3u;
#pragma unroll
  for (int p = 0; p < 4; ++p) s.v[p] = row * stride + cb * 64u + ((sl ^ (VX ? 2 * (p & 1) : (p & 2))) << 4) + (4u * stride - 1024u) * p;
}
template <bool VX> DI void mk_src_w(Src& s, const char* base, unsigned bytes, unsigned stride, unsigned lane) {
  s.srd = mk_srd(base, bytes);
  const unsigned sub = lane >> 5, cb = (lane >> 4) & 1u, row = (lane >> 2) & 3u, sl = lane & 3u;
#pragma unroll
  for (int p = 0; p < 4; ++p) s.v[p] = (4u * sub + row) * stride + cb * 64u + ((sl ^ (VX ? 2u * sub : 2u * (p & 1))) << 4) + (8u * stride - 1024u) * p;
}
DI void touch(unsigned& d, unsigned& voff, const char* base) {
  asm volatile("global_load_dword %0, %1, %2 nt\n\tglobal_load_dword %0, %1, %2\n\tv_add_u32_e32 %1, 0x1000, %1" : "+v"(d), "+v"(voff) : "s"(base) : "memory");
}
struct Geo { int r0, r1, p0, p1, u, t0, t1, t2, t3; };
constexpr Geo GB = {8, 15, 16, 12, 11, 13, 14, 10, 9}, GA = {15, 8, 16, 9, 10, 14, 11, 12, 13};
constexpr unsigned KUS = 1u << GB.u;
template <bool isA> DI unsigned ksoffg(unsigned m, unsigned rk) {
  constexpr Geo G = isA ? GA : GB;
  const unsigned t = m + rk;
  return ((t & 1u) << G.t0) | (((t >> 1) & 1u) << G.t1) | (((t >> 2) & 1u) << G.t2) | (((t >> 3) & 1u) << G.t3);
}
DI unsigned ksoff(unsigned m, unsigned rk) { return ksoffg<false>(m, rk); }
template <bool isA, bool VX> DI void mk_src_kv(Src& s, const char* base, unsigned lane, unsigned w) {
  constexpr Geo G = isA ? GA : GB;
  s.srd = mk_srd(base, 131072u);
  const unsigned row = (lane >> 2) & 3u, sl = lane & 3u, o = ((row & 1u) << G.r0) + ((row >> 1) << G.r1) + (lane >> 4) * 64u;
#pragma unroll
  for (unsigned p = 0; p < (isA ? 2u : 4u); ++p)
    s.v[p] = o + ((sl ^ (VX ? 2u * (p & 1u) : isA ? 2u * (w & 1u) : (p & 2u))) << 4) + ((p & 1u) << G.p0) + ((p >> 1) << G.p1) - (isA ? 0u : 1024u * p);
}

struct PC {
  unsigned r01, r2;
  unsigned w01[2], w2[2];
  unsigned xb, xmask, xa;
  unsigned z, soff;
};
template <unsigned TS_, unsigned QS_, unsigned ZI_, unsigned ZM_, unsigned ZO_, unsigned US_, unsigned CS_, bool ROT_> struct PK {
  static constexpr unsigned TS = TS_, QS = QS_, ZI = ZI_, ZM = ZM_, ZO = ZO_, US = US_, CS = CS_; static constexpr bool ROT = ROT_; };
DI unsigned q_soff(unsigned z) { return ((z & 0xC300u) * 0x440u) & 0x3C0000u; }
template <int POS> DI unsigned ring_lds(const PC& c) { return POS == 2 ? c.r2 : c.r01 + POS * SLOT; }
template <int POS> DI unsigned ring_rd(const PC& c, int d) { return (POS == 2 ? c.w2[d & 1] : c.w01[d & 1] + POS * SLOT) + (d >> 1) * 256u; }
struct NoX { static constexpr bool ACT = true; template <int G> DI void run() const {} };
struct EasyX {
  static constexpr bool ACT = true;
  unsigned long long& t; unsigned lim;
  template <int G> DI void run() const {
    if constexpr (G == 8) asm volatile("s_memrealtime %0" : "=s"(t));
    if constexpr (G == 16) PINS(t);
    if constexpr (G == 16) asm volatile("s_cmp_lt_i32 %0, 0\n\ts_cbranch_scc1 .Le_%=\n\ts_sleep %1\n.Le_%=:" :: "s"((unsigned)t - lim), "i"(6) : "scc");
  }
};
template <class K, int POS, int S, bool Z, bool RD, bool DM, int NT, bool XT, int M, class X>
DI void proj_gap(f32x4 (&acc)[8][4], const bf16x8 (&cur)[4], bf16x8 (&nxt)[4], bf16x4 (&lo)[4], bf16x8 (&bf)[4], PC& c, unsigned soff, const Src& src, const Src& xs, unsigned xsoff, const X& ex) {
  constexpr int qb = M >> 2, db = M & 3;
  mm_vvv<Z>(acc[4 * S + db][qb], cur[db], bf[qb]);
  if constexpr (RD && M < 8) {
    constexpr int d = M >> 1; const unsigned a = S == 0 ? ring_rd<POS>(c, d) + 4096u : ring_rd<(POS + 1) % 3>(c, d);
    if constexpr (M & 1) nxt[d] = cat(lo[d], lds_tr1(a + K::TS)); else lo[d] = lds_tr1(a);
  }
  if constexpr (X::ACT && RD && S == 1 && db == 3) bf[qb] = lds128(c.xa + qb * K::QS);
  if constexpr (XT) {
    if constexpr (M < 8 && !(M & 1)) dma_p<X_NT, M / 2>(xs.v[M / 2], xs.srd, xsoff);
    if constexpr (M == 7) set_m0(ring_lds<POS>(c) + S * 4096u);
    if constexpr (M >= 8 && !(M & 1)) dma_p<NT, (M - 8) / 2>(src.v[(M - 8) / 2], src.srd, soff);
  } else if constexpr (DM && M >= 8 && !(M & 1)) dma_p<NT, (M - 8) / 2>(src.v[(M - 8) / 2], src.srd, soff);
  if constexpr (X::ACT && S == 0 && M == 9) { PINS(c.z); c.z = (c.z + K::ZI) & K::ZM; PINS(c.z); }
  if constexpr (X::ACT && S == 0 && M == 11) { c.xa = (c.z & c.xmask) | c.xb; PINV(c.xa); }
  if constexpr (S == 1 && DM && !K::ROT && M == 9) { PINS(c.soff); c.soff = (c.soff + K::CS) & (16u * K::CS - 1u); PINS(c.soff); }
  if constexpr (S == 1 && DM && K::ROT && M == 9) { PINS(c.z); c.soff = (c.z + 0x3100u) & 0xC300u; PINS(c.soff); }
  if constexpr (S == 1 && DM && K::ROT && M == 13) { PINS(c.soff); c.soff = (c.soff * 0x440u) & 0x3C0000u; PINS(c.soff); }
  ex.template run<16 * S + M>();
  GAP();
}
template <class K, int POS, int S, bool Z, bool RD, bool DM, int NT, int VM, bool BAR, bool XT, class X, int... Ms>
DI void proj_unit(f32x4 (&acc)[8][4], const bf16x8 (&cur)[4], bf16x8 (&nxt)[4], bf16x8 (&bf)[4], PC& c, const Src& src, const Src& xs, unsigned xsoff, unsigned xdst, const X& ex, iseq<Ms...>) {
  wait_vm_lgkm<(RD ? VM : 63), (S == 0 ? 2 : 0)>();
  if constexpr (BAR) barrier();
  const unsigned soff = c.soff + S * K::US;
  if constexpr (XT) set_m0(xdst); else if constexpr (DM) set_m0(ring_lds<POS>(c) + S * 4096u);
  GAP();
  bf16x4 lo[4];
  (proj_gap<K, POS, S, Z, RD, DM, NT, XT, Ms>(acc, cur, nxt, lo, bf, c, soff, src, xs, xsoff, ex), ...);
}
template <class K, int POS, bool Z, bool LAST, bool DM, int NT, int VM0, int VM1, bool BAR1 = false, bool XT0 = false, bool XT1 = false, class X = NoX>
DI void proj_elem(f32x4 (&acc)[8][4], bf16x8 (&a0)[4], bf16x8 (&a1)[4], bf16x8 (&bf)[4], PC& c, const Src& src, const X& ex = X{},
                  const Src* xs = nullptr, unsigned xso0 = 0, unsigned xd0 = 0, unsigned xso1 = 0, unsigned xd1 = 0) {
  proj_unit<K, POS, 0, Z, true, DM, NT, VM0, false, XT0>(acc, a0, a1, bf, c, src, xs ? *xs : src, xso0, xd0, ex, make_iseq<16>{});
  proj_unit<K, POS, 1, Z, !LAST, DM, NT, VM1, BAR1, XT1>(acc, a1, a0, bf, c, src, xs ? *xs : src, xso1, xd1, ex, make_iseq<16>{});
}
template <class K> DI void proj_setup(PC& c, unsigned tb, unsigned xb, unsigned z0, unsigned s3) {
  c.w01[0] = c.r01 + tb; c.w01[1] = c.w01[0] ^ 32u; c.w2[0] = c.r2 + tb; c.w2[1] = c.w2[0] ^ 32u;
  PINV(c.w01[0]); PINV(c.w01[1]); PINV(c.w2[0]); PINV(c.w2[1]);
  c.xb = xb; c.xmask = K::ZO; PINV(c.xmask);
  c.z = z0; c.soff = s3;
}
template <class K, int POS> DI void proj_start(bf16x8 (&a0)[4], bf16x8 (&bf)[4], PC& c, unsigned tb, unsigned xb, unsigned z0, unsigned s3) {
  proj_setup<K>(c, tb, xb, z0, s3);
  c.xa = (c.z & K::ZO) | xb; PINV(c.xa);
#pragma unroll
  for (int d = 0; d < 4; ++d) { const unsigned a = ring_rd<POS>(c, d); a0[d] = lds_tr(a, a + K::TS); }
  GAP();
#pragma unroll
  for (int q = 0; q < 4; ++q) bf[q] = lds128(c.xa + q * K::QS);
  GAP();
}
#define ACC_ALL(C, a) C(a[0][0]), C(a[0][1]), C(a[0][2]), C(a[0][3]), C(a[1][0]), C(a[1][1]), C(a[1][2]), C(a[1][3]), C(a[2][0]), C(a[2][1]), C(a[2][2]), C(a[2][3]), C(a[3][0]), C(a[3][1]), C(a[3][2]), C(a[3][3])
DI void acc_fence_v(f32x4 (&acc)[8][4]) {
  asm volatile("s_nop 7\n\ts_nop 3" : ACC_ALL("+v", acc) :: "memory");
  f32x4 (&hi)[4][4] = *(f32x4 (*)[4][4])&acc[4];
  asm volatile("" : ACC_ALL("+v", hi) :: "memory");
  GAP();
}
DI void acc_fence_a(f32x4 (&acc)[8][4]) {
  asm volatile("s_nop 7\n\ts_nop 3" : ACC_ALL("+a", acc) :: "memory");
  f32x4 (&hi)[4][4] = *(f32x4 (*)[4][4])&acc[4];
  asm volatile("" : ACC_ALL("+a", hi) :: "memory");
  GAP();
}

struct Stash { u32x4 p[64]; unsigned base[3]; };
template <int I> DI void stash_load1(Stash& st, const Src& sK, const Src& sV, unsigned rk, unsigned wo_) {
  constexpr int m = I >> 2, isV = (I >> 1) & 1, pc_ = I & 1;
  const unsigned so = ksoffg<true>(m, rk) + wo_;
  if constexpr (I < 56) asm volatile("buffer_load_dwordx4 %0, %1, %2, %3 offen nt" : "=a"(st.p[I]) : "v"(isV ? sV.v[pc_] : sK.v[pc_]), "s"(isV ? sV.srd : sK.srd), "s"(so) : "memory");
  else                  asm volatile("buffer_load_dwordx4 %0, %1, %2, %3 offen nt" : "=v"(st.p[I]) : "v"(isV ? sV.v[pc_] : sK.v[pc_]), "s"(isV ? sV.srd : sK.srd), "s"(so) : "memory");
}
constexpr int stash_idx(int j) { return 4 * ((j >> 1) & 15) + 2 * (j >> 5) + (j & 1); }
template <int... Is> DI void stash_load(Stash& st, const Src& sK, const Src& sV, unsigned rk, unsigned wo_, iseq<Is...>) { (stash_load1<stash_idx(Is)>(st, sK, sV, rk, wo_), ...); }
constexpr int kv_pair(int m) { constexpr int t[10] = {0, 1, 2, 3, 8, 9, 4, 5, 6, 7}; return t[m % 10]; }
constexpr unsigned long long kv_tab() { unsigned long long v = 0; for (int i = 0; i < 16; ++i) v |= (unsigned long long)kv_pair(i) << (4 * i); return v; }
template <int I> DI void stash_write1(const Stash& st) {
  constexpr int m = I >> 2, isV = (I >> 1) & 1, pc_ = I & 1, off = kv_pair(m) * 16384 + isV * 8192 + pc_ * 1024;
  if constexpr (I < 56) asm volatile("ds_write_b128 %0, %1 offset:%2" :: "v"(st.base[off >> 16]), "a"(st.p[I]), "i"(off & 65535) : "memory");
  else                  asm volatile("ds_write_b128 %0, %1 offset:%2" :: "v"(st.base[off >> 16]), "v"(st.p[I]), "i"(off & 65535) : "memory");
}

struct AC {
  f32x4 o[8][4];
  bf16x8 q[4][4];
  bf16x8 kf[8], vf[8], pf[4];
  f32x4 negm[4];
  f32x4 l[4];
  bf16x8 ones;
};
typedef f32x4 ST[2][4];
constexpr int NEB = 22;
template <int E> DI float sm_val(ST& S) { return S[(E >> 2) & 1][E >> 3][E & 3]; }
template <int E> DI void sm_exp1(ST& S) { float a; asm volatile("v_exp_f32_e32 %0, %1" : "=v"(a) : "v"(sm_val<E>(S))); S[(E >> 2) & 1][E >> 3][E & 3] = a; }
template <int QB, int P> DI void sm_cvt(ST& S, AC& c) {
  u32x4 w = __builtin_bit_cast(u32x4, c.pf[QB]);
  unsigned r;
  asm volatile("v_cvt_pk_bf16_f32 %0, %1, %2" : "=v"(r) : "v"(sm_val<8 * QB + 2 * P>(S)), "v"(sm_val<8 * QB + 2 * P + 1>(S)));
  w[P] = r;
  c.pf[QB] = __builtin_bit_cast(bf16x8, w);
}
template <bool DM, int VM, int R, int M> DI void mem_pre(unsigned ldsaddr) {
  if constexpr (M == 6) wait_lgkm0();
  if constexpr (DM && (M == 6 || M == 14)) set_m0(ldsaddr + (M == 14 ? 4096u : 0u));
  if constexpr (M == R && VM < 63) wait_vm<VM>();
}
template <bool DM, int NT, int M> DI void mem_post(const Src& src, unsigned soff, unsigned ustep) {
  if constexpr (DM && M >= 6 && M <= 20 && !(M & 1)) dma_p<NT, ((M - 6) / 2) & 3>(src.v[((M - 6) / 2) & 3], src.srd, soff + (M >= 14 ? ustep : 0u));
}
template <int WR, int M> DI void mem_wr(const Stash& st) {
  if constexpr (WR >= 40 && M >= 7 && M < 13) stash_write1<WR + M - 7>(st);
  if constexpr (WR >= 0 && WR < 40 && M >= 8 && M < 16)
    asm volatile("ds_write_b128 %0, %1 offset:%2" :: "v"(st.base[0]), "a"(st.p[WR + M - 8]), "i"((M - 8) * 1024) : "memory");
}
template <int N> using IC = std::integral_constant<int, N>;
struct PA { unsigned rd0, rd1, soff, fill; };
template <bool QK, bool DM, int NT, int VM, int WR, int M, class F>
DI void gapA(AC& c, ST& S, ST& Sn, PA& pa, F& addr, const Src& src, unsigned ustep, const Stash& st, bf16x4& lo) {
  constexpr int R = QK ? 16 : 0;
  if constexpr ((DM || !QK) && M == 0) addr(pa, IC<0>{});
  if constexpr (QK) mem_pre<DM, VM, 16, M>(pa.fill); else if constexpr (M == 0 && VM < 63) wait_vm<VM>();
  if constexpr (QK) {
    constexpr int f = M >> 2, s = f >> 1, kb = f & 1, qb = M & 3;
    if constexpr (s == 0) mm_vca(Sn[kb][qb], c.kf[f], c.q[qb][0], c.negm[qb]); else mm_vva<false>(Sn[kb][qb], c.kf[f], c.q[qb][s]);
  }
  mem_post<DM, NT, M>(src, pa.soff, ustep);
  mem_wr<WR, M>(st);
  if constexpr (QK && !DM && M >= 10 && M < 16) addr(pa, IC<M - 10>{});
  if constexpr (QK && M == 12) asm volatile("" :: "v"(c.negm[0]), "v"(c.negm[1]), "v"(c.negm[2]), "v"(c.negm[3]));
  if constexpr (M >= R && M < R + 16) {
    constexpr int db = (M - R) >> 1; const unsigned a = ((db & 1) ? pa.rd1 : pa.rd0) + (db >> 1) * 256u;
    if constexpr (M & 1) c.vf[db] = cat(lo, lds_tr1(a + 4096u)); else lo = lds_tr1(a);
  }
  if constexpr (NEB + M < 32) sm_exp1<NEB + M>(S);
  if constexpr (M >= 16 && (M & 1)) sm_cvt<2 + ((M - 16) >> 3), ((M - 16) >> 1) & 3>(S, c);
  GAP();
}
template <bool QK, bool DM, int NT, int VM, int WR, class F, int... Ms>
DI void phaseA_(AC& c, ST& S, ST& Sn, F& addr, const Src& src, unsigned ustep, const Stash& st, iseq<Ms...>) {
  PA pa; bf16x4 lo;
  (gapA<QK, DM, NT, VM, WR, Ms>(c, S, Sn, pa, addr, src, ustep, st, lo), ...);
}
template <bool QK, bool DM, int NT, int VM, int WR = -1, class F>
DI void phaseA(AC& c, ST& S, ST& Sn, F addr, const Src& src, unsigned ustep, const Stash& st) {
  phaseA_<QK, DM, NT, VM, WR>(c, S, Sn, addr, src, ustep, st, make_iseq<32>{});
}
template <bool MAX> DI float xlane(float v) {
  auto a = __builtin_amdgcn_permlane32_swap(__float_as_uint(v), __float_as_uint(v), false, false);
  v = MAX ? fmaxf(__uint_as_float(a[0]), __uint_as_float(a[1])) : __uint_as_float(a[0]) + __uint_as_float(a[1]);
  auto b = __builtin_amdgcn_permlane16_swap(__float_as_uint(v), __float_as_uint(v), false, false);
  return MAX ? fmaxf(__uint_as_float(b[0]), __uint_as_float(b[1])) : __uint_as_float(b[0]) + __uint_as_float(b[1]);
}
DI float row_max(const f32x4& x, const f32x4& y) {
  return xlane<true>(fmaxf(fmaxf(fmaxf(x[0], x[1]), fmaxf(x[2], x[3])), fmaxf(fmaxf(y[0], y[1]), fmaxf(y[2], y[3]))));
}
template <bool SM, bool KR, bool DM, int NT, int VM, int WR, int M, class F>
DI void gapB(AC& c, ST& Sn, PA& pa, F& addr, const Src& src, unsigned ustep, const Stash& st) {
  mem_pre<DM, (KR ? VM : 63), 18, M>(pa.fill);
  {
    constexpr int h = M / 18, r = M % 18, qb = 2 * h + (r & 1);
    if constexpr (r < 2) mm_vvv<false>(c.l[qb], c.ones, c.pf[qb]); else mm_avv(c.o[(r - 2) >> 1][qb], c.vf[(r - 2) >> 1], c.pf[qb]);
  }
  mem_post<DM, NT, M>(src, pa.soff, ustep);
  mem_wr<WR, M>(st);
  if constexpr (M < 4) addr(pa, IC<M>{});
  if constexpr (KR && M >= 18 && M < 34 && !(M & 1)) { constexpr int f = (M - 18) >> 1; c.kf[f] = lds128(pa.rd0 + (f & 1) * 4096u + (f >> 1) * 256u); }
  if constexpr (SM) {
    if constexpr (M >= 4 && M < 18) sm_exp1<M - 4>(Sn);
    if constexpr (M >= 19 && M < 35 && (M & 1)) sm_exp1<14 + (M - 19) / 2>(Sn);
    if constexpr (M >= 20 && !(M & 1)) sm_cvt<((M - 20) >> 3), ((M - 20) >> 1) & 3>(Sn, c);
  }
  GAP();
}
template <bool SM, bool KR, bool DM, int NT, int VM, int WR, class F, int... Ms>
DI void phaseB_(AC& c, ST& Sn, F& addr, const Src& src, unsigned ustep, const Stash& st, iseq<Ms...>) {
  PA pa;
  (gapB<SM, KR, DM, NT, VM, WR, Ms>(c, Sn, pa, addr, src, ustep, st), ...);
}
template <bool SM, bool KR, bool DM, int NT, int VM, int WR = -1, class F>
DI void phaseB(AC& c, ST& Sn, F addr, const Src& src, unsigned ustep, const Stash& st) {
  phaseB_<SM, KR, DM, NT, VM, WR>(c, Sn, addr, src, ustep, st, make_iseq<36>{});
}
DI void k_read(AC& ac, unsigned ka) {
#pragma unroll
  for (int f = 0; f < 8; ++f) ac.kf[f] = lds128(ka + (f & 1) * 4096u + (f >> 1) * 256u);
}
template <bool DM, int M> DI void qk0_gap(AC& ac, ST& SA, const Src& src, unsigned soff, unsigned fill) {
  constexpr int f = M >> 2, qb = M & 3;
  if constexpr (DM && (M == 0 || M == 16)) set_m0(fill + M * 256u);
  mm_vva<(f < 2)>(SA[f & 1][qb], ac.kf[f], ac.q[qb][f >> 1]);
  if constexpr (DM && qb == 0) dma_p<KV_NT, f & 3>(src.v[f & 3], src.srd, soff + (f >> 2) * KUS);
  if constexpr (!DM && (M & 1)) { ac.o[4 + (M >> 3)][(M >> 1) & 3] = f32x4{}; asm volatile("" : "+a"(ac.o[4 + (M >> 3)][(M >> 1) & 3])); }
  GAP();
}
template <bool DM, int... Ms> DI void qk0(AC& ac, ST& SA, const Src& src, unsigned soff, unsigned fill, iseq<Ms...>) { (qk0_gap<DM, Ms>(ac, SA, src, soff, fill), ...); }
template <int... Js> DI void exp_first(ST& SA, iseq<Js...>) { (sm_exp1<Js>(SA), ...); }
template <int... Js> DI void cvt_first(ST& SA, AC& c, iseq<Js...>) { (sm_cvt<(Js >> 2), (Js & 3)>(SA, c), ...); }
DI void sm_first(AC& ac, ST& SA) {
  asm volatile("s_nop 7\n\ts_nop 3" : "+v"(SA[0][0]), "+v"(SA[0][1]), "+v"(SA[0][2]), "+v"(SA[0][3]), "+v"(SA[1][0]), "+v"(SA[1][1]), "+v"(SA[1][2]), "+v"(SA[1][3]) :: "memory");
  GAP();
#pragma unroll
  for (int q = 0; q < 4; ++q) {
    const float m = row_max(SA[0][q], SA[1][q]);
    SA[0][q] -= m; SA[1][q] -= m;
    ac.negm[q] = f32x4{-m, -m, -m, -m};
    ac.l[q] = f32x4{};
    ac.pf[q] = bf16x8{};
  }
  ac.ones = __builtin_bit_cast(bf16x8, u32x4{0x3f803f80u, 0x3f803f80u, 0x3f803f80u, 0x3f803f80u});
  asm volatile("" : "+v"(ac.negm[0]), "+v"(ac.negm[1]), "+v"(ac.negm[2]), "+v"(ac.negm[3]), "+v"(ac.l[0]), "+v"(ac.l[1]), "+v"(ac.l[2]), "+v"(ac.l[3]), "+v"(ac.ones));
  exp_first(SA, make_iseq<NEB>{});
  cvt_first(SA, ac, make_iseq<8>{});
  GAP();
}
struct BS { unsigned p[4], v[6], k[2], rk, wo, l16; };
constexpr bool b_dma(int f) { return f != 2 * (SK - 3) && f != 2 * (SV - 2) + 1 && f != 2 * (SP - 2) + 1 && f < 30 && f != 27; }
constexpr int b_vm(int f) {
  const int s = f == 30 ? 26 : f - 3;
  if (!b_dma(s)) return 63;
  int n = b_dma(f) ? 5 + (f & 1) : 0;
  for (int i = s + 1; i < f; ++i) n += b_dma(i) ? 8 : 0;
  return n;
}
template <class KO, int I> DI void b_step(AC& ac, ST& S, ST& Sn, BS& b, const Src& sK, const Src& sV, const Src& sO, Stash& st) {
  constexpr int fa = 2 * I, fb = 2 * I + 1, vi = (I == SP || I == 15) ? 4 : (I & 1) ? 0 : 2;
  const unsigned pa = b.p[(I & 1) ? 3 : 1], pb = b.p[(I & 1) ? 0 : 2];
  if constexpr (I >= 1 && I <= 13) barrier();
  if constexpr (I == SK - 3) st.base[0] = pa + b.l16;
  phaseA<(I < 15), b_dma(fa), (I < 14 ? KV_NT : W_NT), b_vm(fa), (I == SK - 3 ? 0 : -1)>(ac, S, Sn, [&](PA& p, auto k) __attribute__((always_inline)) {
    if (k.value) return;
    PINS(b.rk);
    p.soff = I < 14 ? ksoff(I < 13 ? I + 3 : 15, b.rk) : b.wo; p.fill = I == 14 ? b.p[0] : pa;
    p.rd0 = b.v[vi] + (I == SP ? SLOT : 0u); p.rd1 = b.v[vi + 1] + (I == SP ? SLOT : 0u); }, I < 13 ? sK : I < 14 ? sV : sO, I < 14 ? KUS : KO::US, st);
  if constexpr (I == SV - 2) st.base[0] = pb + b.l16;
  if constexpr (I < 15)
    phaseB<true, (I < 14), b_dma(fb), (I < 13 ? KV_NT : W_NT), b_vm(fb), (I == SV - 2 ? 8 : -1)>(ac, Sn, [&](PA& p, auto k) __attribute__((always_inline)) {
      if (k.value) return;
      PINS(b.rk);
      p.soff = I < 13 ? ksoff(I + 2, b.rk) : b.wo + KO::CS; p.fill = I == 14 ? b.p[1] : pb; p.rd0 = b.k[(I & 1) ? 0 : 1]; }, I < 13 ? sV : sO, I < 13 ? KUS : KO::US, st);
}
template <int CH, int Q, int J> DI void o_word(const AC& c, const float (&inv)[4], bf16x8 (&nb)[4]) {
  u32x4 w = __builtin_bit_cast(u32x4, nb[Q]);
  unsigned r; float t;
  asm volatile("v_accvgpr_read_b32 %0, %2\n\tv_accvgpr_read_b32 %1, %3\n\tv_mul_f32_e32 %0, %0, %4\n\tv_mul_f32_e32 %1, %1, %4\n\tv_cvt_pk_bf16_f32 %0, %0, %1"
               : "=&v"(r), "=&v"(t) : "a"(c.o[2 * CH + (J >> 1)][Q][2 * (J & 1)]), "a"(c.o[2 * CH + (J >> 1)][Q][2 * (J & 1) + 1]), "v"(inv[Q]));
  w[J] = r;
  nb[Q] = __builtin_bit_cast(bf16x8, w);
}
template <int CH, int Q> DI void o_put(const bf16x8 (&nb)[4], unsigned ow) { asm volatile("ds_write_b128 %0, %1 offset:%2" :: "v"(ow), "v"(nb[Q]), "i"(CH * 4096 + Q * 1024) : "memory"); }
template <int E> struct CvX {
  static constexpr bool ACT = E == 3;
  const AC& c; const float (&inv)[4]; const bf16x8 (&cur)[4]; bf16x8 (&nb)[4]; unsigned ow;
  template <int G> DI void run() const {
    if constexpr (E < 3 && (G & 1)) o_word<E + 1, (G >> 3), (G >> 1) & 3>(c, inv, nb);
    if constexpr (E < 3 && ((G & 15) == 8 || (G & 15) == 10)) o_put<E, 2 * (G >> 4) + ((G >> 1) & 1)>(cur, ow);
    if constexpr (E == 2 && (G & 15) == 12) o_put<3, (G >> 4)>(nb, ow);
    if constexpr (E == 3 && (G == 8 || G == 10)) o_put<3, 2 + ((G >> 1) & 1)>(cur, ow);
  }
};
template <class K, int VM, bool DM, int M> DI void gapL(AC& c, float (&inv)[4], bf16x8 (&nb)[4], bf16x8 (&a0)[4], bf16x4& lo, const PC& pc, const Src& src, unsigned soff) {
  if constexpr (M == 6) wait_vm_lgkm<VM, 0>();
  if constexpr (DM && (M == 12 || M == 32)) set_m0(pc.r2 + (M == 32 ? 4096u : 0u));
  if constexpr (M < 4) mm_vvv<false>(c.l[M], c.ones, c.pf[M]); else mm_avv(c.o[(M - 4) >> 2][M & 3], c.vf[(M - 4) >> 2], c.pf[M & 3]);
  if constexpr (M >= 6 && M < 14) {
    constexpr int d = (M - 6) >> 1; const unsigned a = ring_rd<0>(pc, d);
    if constexpr (M & 1) a0[d] = cat(lo, lds_tr1(a + K::TS)); else lo = lds_tr1(a);
  }
  if constexpr (M >= 4 && M < 12 && !(M & 1)) asm volatile("v_rcp_f32_e32 %0, %1" : "=v"(inv[(M - 4) >> 1]) : "v"(c.l[(M - 4) >> 1][0]));
  if constexpr (M >= 16 && M < 32) o_word<0, ((M - 16) >> 2), (M & 3)>(c, inv, nb);
  if constexpr (DM && ((M >= 12 && M < 16) || M >= 32)) dma_p<W_NT, M & 3>(src.v[M & 3], src.srd, soff + (M >= 32 ? K::US : 0u));
  GAP();
}
template <class K, int VM, bool DM, int... Ms> DI void phaseL(AC& c, float (&inv)[4], bf16x8 (&nb)[4], bf16x8 (&a0)[4], const PC& pc, const Src& src, unsigned soff, iseq<Ms...>) {
  bf16x4 lo;
#pragma unroll
  for (int q = 0; q < 4; ++q) nb[q] = bf16x8{};
  (gapL<K, VM, DM, Ms>(c, inv, nb, a0, lo, pc, src, soff), ...);
}
struct Pub { u32x4 pk[16]; srd_t srd; unsigned v[4], so; };
template <int F, int H> DI void pub_half(const f32x4 (&acc)[8][4], Pub& p) {
  const f32x4& a = acc[4 * (F >> 3) + 2 * (F & 1) + H][(F >> 1) & 3];
  unsigned r0, r1;
  asm volatile("v_cvt_pk_bf16_f32 %0, %2, %3\n\tv_cvt_pk_bf16_f32 %1, %4, %5" : "=&v"(r0), "=v"(r1) : "v"(a[0]), "v"(a[1]), "v"(a[2]), "v"(a[3]));
  p.pk[F][2 * H] = r0; p.pk[F][2 * H + 1] = r1;
  if constexpr (H) asm volatile("buffer_store_dwordx4 %0, %1, %2, %3 offen offset:%4 sc0 sc1" :: "v"(p.pk[F]), "v"(p.v[F >> 2]), "s"(p.srd), "s"(p.so), "i"((F & 3) * 1024) : "memory");
}
template <class K, int U, int M>
DI void tail_gap(f32x4 (&acc)[8][4], const bf16x8 (&cur)[4], bf16x8 (&nxt)[4], bf16x4 (&lo)[4], bf16x8 (&bf)[4], const PC& c, const unsigned (&xa)[3], Pub& p) {
  constexpr int qb = M >> 2, db = M & 3, S = U / 3, ne = (U + 1) % 3;
  mm_vvv<false>(acc[4 * S + db][qb], cur[db], bf[qb]);
  if constexpr (U < 5 && M < 8) {
    constexpr int d = M >> 1; const unsigned a = ring_rd<(ne + 1) % 3>(c, d) + ((U + 1) / 3) * 4096u;
    if constexpr (M & 1) nxt[d] = cat(lo[d], lds_tr1(a + K::TS)); else lo[d] = lds_tr1(a);
  }
  if constexpr (U < 5 && db == 3) bf[qb] = lds128(xa[ne] + qb * K::QS);
  if constexpr ((U == 3 || U == 4) && M >= 8) pub_half<4 * (U - 3) + ((M - 8) >> 1), (M & 1)>(acc, p);
  if constexpr (U == 5 && M >= 8) pub_half<8 + ((M - 8) >> 1), (M & 1)>(acc, p);
  GAP();
}
template <class K, int U, int VM, int... Ms>
DI void tail_unit(f32x4 (&acc)[8][4], const bf16x8 (&cur)[4], bf16x8 (&nxt)[4], bf16x8 (&bf)[4], const PC& c, const unsigned (&xa)[3], Pub& p, iseq<Ms...>) {
  wait_vm_lgkm<VM, 2>();
  GAP();
  bf16x4 lo[4];
  (tail_gap<K, U, Ms>(acc, cur, nxt, lo, bf, c, xa, p), ...);
}
template <int I> DI void own_put1(const u32x4& v, unsigned a) { asm volatile("ds_write_b128 %0, %1 offset:%2 ; %3" :: "v"(a), "v"(v), "i"((I & 7) * 1024), "i"(I) : "memory"); }
template <int B, int... Is> DI void own_put(const Pub& p, unsigned a, iseq<Is...>) { (own_put1<B + Is>(p.pk[B + Is], a), ...); }
template <int... Is> DI void pub_rest(const f32x4 (&acc)[8][4], Pub& p, iseq<Is...>) { (pub_half<12 + (Is >> 1), (Is & 1)>(acc, p), ...); }
DI void redo_head(const AC& ac, const char* __restrict__ Kp, const char* __restrict__ Vp, unsigned lane, unsigned ow) {
  const unsigned r16 = lane & 15u, g = lane >> 4;
#pragma unroll 1
  for (unsigned qb = 0; qb < 4; ++qb) {
    bf16x8 qq[4];
#pragma unroll
    for (int s = 0; s < 4; ++s) qq[s] = qb == 0 ? ac.q[0][s] : qb == 1 ? ac.q[1][s] : qb == 2 ? ac.q[2][s] : ac.q[3][s];
    f32x4 o[8];
#pragma unroll
    for (int d = 0; d < 8; ++d) o[d] = f32x4{};
    float m = -__builtin_inff(), l = 0.f;
#pragma unroll 1
    for (unsigned t = 0; t < 16; ++t) {
      f32x4 s0 = {}, s1 = {};
      const char* kp = Kp + (t * 32u + r16) * 256u + g * 16u;
#pragma unroll
      for (int s = 0; s < 4; ++s) {
        s0 = __builtin_amdgcn_mfma_f32_16x16x32_bf16(*(const bf16x8*)(kp + s * 64), qq[s], s0, 0, 0, 0);
        s1 = __builtin_amdgcn_mfma_f32_16x16x32_bf16(*(const bf16x8*)(kp + s * 64 + 4096), qq[s], s1, 0, 0, 0);
      }
      const float mn = fmaxf(m, row_max(s0, s1)), al = __builtin_amdgcn_exp2f(m - mn);
      m = mn;
#pragma unroll
      for (int j = 0; j < 4; ++j) { s0[j] = __builtin_amdgcn_exp2f(s0[j] - mn); s1[j] = __builtin_amdgcn_exp2f(s1[j] - mn); }
      l = l * al + ((s0[0] + s0[1]) + (s0[2] + s0[3])) + ((s1[0] + s1[1]) + (s1[2] + s1[3]));
      const bf16x8 pf = __builtin_bit_cast(bf16x8, pack2(s0, s1));
      const __bf16* vp = (const __bf16*)(Vp + (t * 32u + g * 4u) * 256u) + r16;
#pragma unroll
      for (int d = 0; d < 8; ++d) {
        bf16x8 vf;
#pragma unroll
        for (int j = 0; j < 8; ++j) vf[j] = vp[((j & 3) + (j >> 2) * 16) * 128 + d * 16];
        o[d] = __builtin_amdgcn_mfma_f32_16x16x32_bf16(vf, pf, o[d] * al, 0, 0, 0);
      }
    }
    const float inv = 1.0f / xlane<false>(l);
#pragma unroll
    for (int s = 0; s < 4; ++s) *(LAS u32x4*)(size_t)(ow + s * 4096u + qb * 1024u) = pack2(o[2 * s] * inv, o[2 * s + 1] * inv);
  }
}
template <bool isA, int J> DI void q_conv1(AC& ac, f32x4 (&acc)[8][4], Stash& st, const Src& sK, const Src& sV, unsigned rk, unsigned p3) {
  constexpr int s = J >> 2, qb = J & 3;
  if constexpr (isA) { stash_write1<(5 * J) / 2>(st); stash_write1<(5 * J) / 2 + 1>(st); if constexpr (J & 1) stash_write1<(5 * J) / 2 + 2>(st); }
  else {
    constexpr int p = 2 * (J & 1);
    const Src& src = s < 2 ? sK : sV;
    const unsigned so = ksoff(s == 0 ? 2 : s == 1 ? SK : s == 2 ? SV : SP, rk) + ((J >> 1) & 1) * KUS;
    if constexpr (s == 0 || s == 3) {
      if constexpr (p == 0) asm volatile("s_mov_b32 m0, %0\n\ts_nop 0" :: "s"(p3 + (s == 3 ? SLOT : 0u) + (J & 2) * 2048u) : "memory");
      dma_p<KV_NT, p>(src.v[p], src.srd, so); dma_p<KV_NT, p + 1>(src.v[p + 1], src.srd, so);
    } else {
      asm volatile("buffer_load_dwordx4 %0, %1, %2, %3 offen offset:%4 nt" : "=a"(st.p[2 * J - 8]) : "v"(src.v[p]), "s"(src.srd), "s"(so), "i"(1024 * p) : "memory");
      asm volatile("buffer_load_dwordx4 %0, %1, %2, %3 offen offset:%4 nt" : "=a"(st.p[2 * J - 7]) : "v"(src.v[p + 1]), "s"(src.srd), "s"(so), "i"(1024 * p + 1024) : "memory");
    }
  }
  ac.q[qb][s] = __builtin_bit_cast(bf16x8, natural8(pack2(acc[2 * s][qb] * SC, acc[2 * s + 1][qb] * SC)));
  asm volatile("" : "+a"(ac.q[qb][s]));
  GAP();
}
template <bool isA, int... Js> DI void q_conv(AC& ac, f32x4 (&acc)[8][4], Stash& st, const Src& sK, const Src& sV, unsigned rk, unsigned p3, iseq<Js...>) { (q_conv1<isA, Js>(ac, acc, st, sK, sV, rk, p3), ...); }
template <int N> DI void zero_o(AC& ac) {
#pragma unroll
  for (int i = 0; i < N; ++i)
#pragma unroll
    for (int q = 0; q < 4; ++q) { ac.o[i][q] = f32x4{}; asm volatile("" : "+a"(ac.o[i][q])); }
}

template <bool isA>
DI void body(const char* __restrict__ X, const char* __restrict__ K, const char* __restrict__ V, const char* __restrict__ Wq, const char* __restrict__ Wo,
             const char* __restrict__ Wo2, srd_t code, char* __restrict__ part, char* __restrict__ out) {
  extern __shared__ __attribute__((aligned(16))) char lds[];
  unsigned long long tnow;
  asm volatile("s_memrealtime %0\n\ts_waitcnt lgkmcnt(0)" : "=s"(tnow));
  const unsigned tlim = (unsigned)tnow + EASY_T;
  unsigned tid = threadIdx.x;
  PINV(tid);
  const unsigned lane = tid & 63u, w = rfl(tid >> 6);
  const unsigned bid = blockIdx.x, xg = bid & 7u, rr = bid >> 3, b = rr & 15u, h = xg * 4u + w;
  const unsigned r16 = lane & 15u, g = lane >> 4;
  const char* xp  = X + (size_t)b * 65536u + w * 16384u;
  const char* wqp = Wq + h * 256u;
  const char* wop = Wo + (size_t)xg * 524288u + w * 256u;
  const size_t kvo = isA ? (size_t)(b * 8u + xg) * 131072u : (size_t)(b * 32u + h) * 131072u;
  const unsigned rk = isA ? 7u * b : 5u * b + 8u * w;
  typedef PK<512u, 4096u, 0x1300u, 0xF300u, 0xC300u, 128u, 32u * 8192u, true> KQ;
  typedef PK<512u, 4096u, 0x1300u, 0xF300u, 0xC300u, KUS, 8192u, false> KT;
  typedef PK<2048u, 1024u, 0x1000u, 0xF000u, 0xF000u, 128u, 32u * 1024u, false> KO;
  asm volatile("" :: "v"(lds));
  PC pc;
  pc.r01 = 65536u + w * 16384u; pc.r2 = 131072u + w * 8192u;
  const unsigned zq = ((b & 3u) << 14) | ((b >> 2) << 8), co = 4u * w;
  const srd_t srdH = mk_srd(part + HOFF, 4096u + 256u * 128u);
  const unsigned tok_off = rr * 128u, flg_off = 4096u + rr * 1024u;
  const unsigned l8 = (lane & 7u) * 4u;
  unsigned entry_flags;
  asm volatile("buffer_load_dword %0, %1, %2, %3 offen sc1" : "=v"(entry_flags) : "v"(l8), "s"(srdH), "s"(flg_off + xg * 128u) : "memory");
  if constexpr (isA) { if (w == 3 && lane < WARM) asm volatile("buffer_load_dword %0, %1, %2, 0 offen" : "+v"(entry_flags) : "v"((b * WARM + lane) * 128u), "s"(code) : "memory"); }
  Src sQ, sX, sK, sV, sO;
  mk_src_w<false>(sQ, wqp, 511u * 8192u + 256u, 8192u, lane);
  mk_src_rows<false>(sX, xp, 16384u, 1024u, lane);
  const unsigned p0 = b & 3u, p1 = (p0 + 1u) & 3u, p2 = (p0 + 2u) & 3u, p3 = (p0 + 3u) & 3u, xw = w * 4096u;
  Stash st;
  unsigned dum0 = lane;
  dma_unit<1>(sQ, q_soff(zq), pc.r2);
  dma_unit<W_NT>(sQ, q_soff(zq + 0x1300u), pc.r01);
  if constexpr (!isA) dma_unit<X_NT>(sX, p0 * 256u, xw + p0 * 16384u);
  dma_unit<1>(sQ, q_soff(zq) + KQ::US, pc.r2 + 4096u);
  dma_unit<W_NT>(sQ, q_soff(zq + 0x1300u) + KQ::US, pc.r01 + 4096u);
  mk_src_kv<isA, false>(sK, K + kvo, lane, w);
  mk_src_kv<isA, true>(sV, V + kvo, lane, w);
  mk_src_w<true>(sO, wop, 511u * 1024u + 256u, 1024u, lane);
  if constexpr (isA) {
    stash_load(st, sK, sV, rk, ((w & 1u) << GA.p1) + ((w >> 1) << GA.u), make_iseq<64>{});
    dma_unit<X_NT>(sX, p0 * 256u, xw + p0 * 16384u);
    dma_elem<W_NT>(sQ, q_soff(zq + 0x2200u), KQ::US, pc.r01 + SLOT);
    dma_unit<X_NT>(sX, p1 * 256u, xw + p1 * 16384u);
    dma_unit<X_NT>(sX, p2 * 256u, xw + p2 * 16384u);
    dma_unit<X_NT>(sX, p3 * 256u, xw + p3 * 16384u);
  }
  const unsigned rb = 1024u * (r16 >> 2) + 64u * (r16 & 3u) + 16u * (g ^ ((r16 >> 2) & 2u));
  const unsigned tb = 64u * ((lane >> 2) & 3u) + 32u * (g & 1u) + 8u * (lane & 3u);
  const unsigned tbV = tb + 1024u * g;
  const unsigned sb0 = w * 2048u + lane * 16u;
  st.base[0] = sb0; st.base[1] = sb0 + 65536u; st.base[2] = sb0 + 131072u;
  AC ac;
  if constexpr (!isA) zero_o<8>(ac);
  {
    f32x4 acc[8][4];
    bf16x8 a0[4], a1[4], bf[4];
    asm volatile("s_waitcnt vmcnt(%2)" : "+v"(entry_flags), "+v"(dum0) : "i"(isA ? 20 : 8) : "memory");
    if (w == 0) {
      const unsigned long long mk = __builtin_amdgcn_ballot_w64(((entry_flags >> (lane & 7u)) & 1u) != 0u);
      const unsigned tok = 0x5a5a5a00u | (~(unsigned)mk & 0xffu);
      if (lane == 0) asm volatile("buffer_store_dword %0, off, %1, %2 sc1" :: "v"(tok), "s"(srdH), "s"(tok_off + xg * 4u) : "memory");
    }
    barrier();
    proj_start<KQ, 2>(a0, bf, pc, tbV, rb, zq, q_soff(zq + 0x3100u));
    if constexpr (isA) {
      proj_elem<KQ, 2, true,  false, true, W_NT, 63, 63>(acc, a0, a1, bf, pc, sQ);
      proj_elem<KQ, 0, false, false, true, W_NT, 63, 28>(acc, a0, a1, bf, pc, sQ);
      proj_elem<KQ, 1, false, false, true, W_NT, 28, 16>(acc, a0, a1, bf, pc, sQ);
      proj_elem<KQ, 2, false, false, true, W_NT, 16, 16, true>(acc, a0, a1, bf, pc, sQ);
    } else {
      dma_elem<W_NT>(sQ, q_soff(zq + 0x2200u), KQ::US, pc.r01 + SLOT);
      proj_elem<KQ, 2, true,  false, true, W_NT, 12, 20, false, true, true>(acc, a0, a1, bf, pc, sQ, NoX{}, &sX, p1 * 256u, xw + p1 * 16384u, p2 * 256u, xw + p2 * 16384u);
      proj_elem<KQ, 0, false, false, true, W_NT, 24, 28, false, true, false>(acc, a0, a1, bf, pc, sQ, NoX{}, &sX, p3 * 256u, xw + p3 * 16384u);
      proj_elem<KQ, 1, false, false, true, W_NT, 28, 24>(acc, a0, a1, bf, pc, sQ);
      proj_elem<KQ, 2, false, false, true, W_NT, 20, 16, true>(acc, a0, a1, bf, pc, sQ);
    }
    typedef std::conditional_t<isA, EasyX, NoX> PX;
    const PX px = [&] { if constexpr (isA) return EasyX{tnow, tlim}; else return NoX{}; }();
#pragma unroll 1
    for (unsigned e = 0; e < 3; ++e) {
      proj_elem<KQ, 0, false, false, true, W_NT, 16, 16>(acc, a0, a1, bf, pc, sQ, px);
      proj_elem<KQ, 1, false, false, true, W_NT, 16, 16>(acc, a0, a1, bf, pc, sQ, px);
      proj_elem<KQ, 2, false, false, true, W_NT, 16, 16>(acc, a0, a1, bf, pc, sQ, px);
    }
    if constexpr (isA) {
      proj_elem<KQ, 0, false, false, false, W_NT, 16, 12>(acc, a0, a1, bf, pc, sQ, px);
      proj_elem<KQ, 1, false, false, false, W_NT, 8, 4>(acc, a0, a1, bf, pc, sQ, px);
      proj_elem<KQ, 2, false, true, false, W_NT, 0, 0>(acc, a0, a1, bf, pc, sQ, px);
    } else {
      pc.soff = ksoff(0u, rk);
      proj_elem<KT, 0, false, false, true, KV_NT, 16, 16>(acc, a0, a1, bf, pc, sK);
      pc.soff = ksoff(1u, rk);
      proj_elem<KT, 1, false, false, true, KV_NT, 16, 16>(acc, a0, a1, bf, pc, sK);
      pc.soff = ksoff(0u, rk);
      proj_elem<KT, 2, false, true, true, KV_NT, 16, 16>(acc, a0, a1, bf, pc, sV);
    }
    barrier();
    acc_fence_v(acc);
    q_conv<isA>(ac, acc, st, sK, sV, rk, w * 16384u, make_iseq<16>{});
  }
  ST SA, SB;
  if constexpr (isA) {
    zero_o<4>(ac);
    const unsigned kb = rb, vb = SLOT + tbV;
    unsigned dum = lane, to = ((lane & 8u) * 16u + (lane & 7u) * 4u + (lane >> 4) * 32768u) | (((lane >> 4) ^ w) << 8);
    const unsigned tbo = (b & 3u) * 1024u + (b >> 2) * 131072u;
    PINV(to);
    const char* ta = Wo + (size_t)xg * 524288u + tbo;
    const char* tb2 = Wo2 + (size_t)xg * 524288u + tbo;
    touch(dum, to, ta);
    asm volatile("s_waitcnt lgkmcnt(0)" ::: "memory");
    barrier();
    k_read(ac, kb + kv_pair(0) * 16384u);
    wait_lgkm0();
    GAP();
    qk0<false>(ac, SA, sK, 0u, 0u, make_iseq<32>{});
    k_read(ac, kb + kv_pair(1) * 16384u);
    touch(dum, to, ta);
    sm_first(ac, SA);
    unsigned i4 = 0;
    unsigned blk; const char* tp;
    auto ak = [&](PA& p, auto k) __attribute__((always_inline)) {
      if constexpr (k.value == 0) { __builtin_amdgcn_s_sleep(1); PINS(i4); blk = (unsigned)((kv_tab() >> 8) >> i4) << 14; i4 += 4u; PINS(blk); PINS(i4); }
      if constexpr (k.value == 1) { p.rd0 = kb + (blk & 0x3c000u); PINV(p.rd0); }
    };
    auto at = [&](PA& p, auto k) __attribute__((always_inline)) {
      if constexpr (k.value == 0) { PINS(i4); blk = (unsigned)(kv_tab() >> i4) << 14; PINS(blk); }
      if constexpr (k.value == 1) { p.rd0 = vb + (blk & 0x3c000u); PINV(p.rd0); }
      if constexpr (k.value == 2) asm volatile("s_cmp_lt_u32 %1, 24\n\ts_cselect_b64 %0, %2, %3" : "=s"(tp) : "s"(i4), "s"(ta), "s"(tb2 - 32768) : "scc");
      if constexpr (k.value == 3) { p.rd1 = p.rd0 ^ 32u; PINV(p.rd1); asm volatile("global_load_dword %0, %1, %2 nt" : "+v"(dum) : "v"(to), "s"(tp) : "memory"); }
      if constexpr (k.value == 4) asm volatile("global_load_dword %0, %1, %2\n\tv_add_u32_e32 %1, %3, %1" : "+v"(dum), "+v"(to) : "s"(tp), "i"(4096) : "memory");
    };
    while (i4 < 24u) {
      phaseA<true, false, KV_NT, 63>(ac, SA, SB, at, sK, 0u, st);
      phaseB<true, true, false, KV_NT, 63>(ac, SB, ak, sK, 0u, st);
      phaseA<true, false, KV_NT, 63>(ac, SB, SA, at, sK, 0u, st);
      phaseB<true, true, false, KV_NT, 63>(ac, SA, ak, sK, 0u, st);
    }
    barrier();
    phaseA<true, false, KV_NT, 63, 40>(ac, SA, SB, at, sK, 0u, st);
    phaseB<true, true, false, KV_NT, 63, 46>(ac, SB, ak, sK, 0u, st);
    phaseA<true, false, KV_NT, 63, 52>(ac, SB, SA, at, sK, 0u, st);
    phaseB<true, true, false, KV_NT, 63, 58>(ac, SA, ak, sK, 0u, st);
    wait_vm_lgkm<63, 8>(); barrier();
    while (i4 < 56u) {
      phaseA<true, false, KV_NT, 63>(ac, SA, SB, at, sK, 0u, st);
      phaseB<true, true, false, KV_NT, 63>(ac, SB, ak, sK, 0u, st);
      phaseA<true, false, KV_NT, 63>(ac, SB, SA, at, sK, 0u, st);
      phaseB<true, true, false, KV_NT, 63>(ac, SA, ak, sK, 0u, st);
    }
    barrier();
    unsigned v14 = vb + kv_pair(14) * 16384u, x14 = v14 ^ 32u; PINV(v14); PINV(x14);
    phaseA<true, true, W_NT, 63>(ac, SA, SB, [&](PA& p, auto) { p.soff = co * KO::CS; p.fill = pc.r01; p.rd0 = v14; p.rd1 = x14; }, sO, KO::US, st);
    phaseB<true, false, true, W_NT, 63>(ac, SB, [&](PA& p, auto) { p.soff = (co + 1u) * KO::CS; p.fill = pc.r01 + SLOT; }, sO, KO::US, st);
    phaseA<false, false, W_NT, 63>(ac, SB, SA, [&](PA& p, auto) { p.rd0 = v14 + 16384u; p.rd1 = x14 + 16384u; }, sO, 0u, st);
    dum0 = dum;
  } else {
    const unsigned P0 = pc.r01, P1 = pc.r01 + SLOT, P2 = pc.r2, P3 = w * 16384u;
    unsigned v0 = tbV + P0, v2 = tbV + P2, x0 = v0 ^ 32u, x2 = v2 ^ 32u, k1 = rb + P1, k3 = rb + P3;
    PINV(v0); PINV(v2); PINV(x0); PINV(x2); PINV(k1); PINV(k3);
    wait_vm<48>();
    k_read(ac, k1 - SLOT);
    wait_lgkm0();
    GAP();
    qk0<true>(ac, SA, sV, ksoff(1u, rk), P0, make_iseq<32>{});
    wait_vm<48>();
    GAP();
    k_read(ac, k1);
    sm_first(ac, SA);
    BS bs{{P0, P1, P2, P3}, {v0, x0, v2, x2, tbV + P3, (tbV + P3) ^ 32u}, {k1, k3}, rk, co * KO::CS, lane * 16u};
    PINV(bs.v[4]); PINV(bs.v[5]);
#define B2(I) b_step<KO, I>(ac, SA, SB, bs, sK, sV, sO, st); b_step<KO, I + 1>(ac, SB, SA, bs, sK, sV, sO, st);
    B2(0) B2(2) B2(4) B2(6) B2(8) B2(10)
    B2(12) B2(14)
#undef B2
  }
  {
    const unsigned l16 = lane * 16u;
    unsigned ow = w * 16384u + l16;
    PINV(ow);
    f32x4 acc[8][4];
    bf16x8 a0[4], a1[4], b0[4], bf[4];
    float inv[4];
    proj_setup<KO>(pc, tb + 512u * g, l16, (co + 3u) << 12, (co + 3u) * KO::CS);
    unsigned toks;
    asm volatile("buffer_load_dword %0, %1, %2, %3 offen sc1" : "=v"(toks) : "v"(l8), "s"(srdH), "s"(tok_off) : "memory");
    GAP();
    phaseL<KO, 13, !isA>(ac, inv, b0, a0, pc, sO, (co + 2u) * KO::CS, make_iseq<36>{});
    const bool ok = ac.l[0][0] <= LMAX && ac.l[1][0] <= LMAX && ac.l[2][0] <= LMAX && ac.l[3][0] <= LMAX;
    if (__builtin_expect(__builtin_amdgcn_ballot_w64(!ok) != 0ull, 0)) {
      asm volatile("s_waitcnt vmcnt(0)" : "+v"(toks) :: "memory");
      acc_fence_a(ac.o);
      redo_head(ac, K + kvo, V + kvo, lane, ow);
#pragma unroll
      for (int q = 0; q < 4; ++q) {
        inv[q] = 1.0f;
        b0[q] = lds128(ow + q * 1024u);
#pragma unroll
        for (int s = 1; s < 4; ++s) {
          const u32x4 t = __builtin_bit_cast(u32x4, lds128(ow + s * 4096u + q * 1024u));
          ac.o[2 * s][q] = f32x4{__uint_as_float(t[0] << 16), __uint_as_float(t[0] & 0xffff0000u), __uint_as_float(t[1] << 16), __uint_as_float(t[1] & 0xffff0000u)};
          ac.o[2 * s + 1][q] = f32x4{__uint_as_float(t[2] << 16), __uint_as_float(t[2] & 0xffff0000u), __uint_as_float(t[3] << 16), __uint_as_float(t[3] & 0xffff0000u)};
        }
      }
      wait_lgkm0();
      acc_fence_a(ac.o);
    }
#pragma unroll
    for (int q = 0; q < 4; ++q) bf[q] = bf16x8{};
    GAP();
    if constexpr (isA) {
      proj_elem<KO, 0, true, false, true, W_NT, 9, 9, true, false, true>(acc, a0, a1, b0, pc, sO, CvX<0>{ac, inv, b0, bf, ow}, &sO, 0u, 0u, (co + 2u) * KO::CS, pc.r2);
      proj_elem<KO, 1, false, false, true, W_NT, 13, 12, false, true, false>(acc, a0, a1, bf, pc, sO, CvX<1>{ac, inv, bf, b0, ow}, &sO, (co + 2u) * KO::CS + KO::US, pc.r2 + 4096u);
      proj_elem<KO, 2, false, false, true, W_NT, 8, 16>(acc, a0, a1, b0, pc, sO, CvX<2>{ac, inv, b0, bf, ow});
    } else {
      proj_elem<KO, 0, true, false, true, W_NT, 16, 16>(acc, a0, a1, b0, pc, sO, CvX<0>{ac, inv, b0, bf, ow});
      proj_elem<KO, 1, false, false, true, W_NT, 16, 16>(acc, a0, a1, bf, pc, sO, CvX<1>{ac, inv, bf, b0, ow});
      proj_elem<KO, 2, false, false, true, W_NT, 16, 16>(acc, a0, a1, b0, pc, sO, CvX<2>{ac, inv, b0, bf, ow});
    }
    if constexpr (isA) PINV(dum0);
    proj_elem<KO, 0, false, false, true, W_NT, 16, 16, true>(acc, a0, a1, bf, pc, sO, CvX<3>{ac, inv, bf, b0, ow});
#pragma unroll 1
    for (unsigned e = 0; e < 3; ++e) {
      proj_elem<KO, 1, false, false, true, W_NT, 16, 16>(acc, a0, a1, bf, pc, sO);
      proj_elem<KO, 2, false, false, true, W_NT, 16, 16>(acc, a0, a1, bf, pc, sO);
      proj_elem<KO, 0, false, false, true, W_NT, 16, 16>(acc, a0, a1, bf, pc, sO);
    }
    Pub pb;
    pb.srd = mk_srd(part + (size_t)rr * 524288u, 524288u);
    pb.so = xg * 65536u + w * 16384u;
#pragma unroll
    for (unsigned i = 0; i < 4; ++i) { pb.v[i] = l16 + i * 4096u + (2u * w + (i >> 1) == xg ? 0x80000000u : 0u); PINV(pb.v[i]); }
#pragma unroll
    for (int i = 0; i < 16; ++i) pb.pk[i] = u32x4{};
    unsigned xa[3];
#pragma unroll
    for (unsigned i = 0; i < 3; ++i) { xa[i] = (((co + 13u + i) & 15u) << 12) | l16; PINV(xa[i]); }
    tail_unit<KO, 0, 12>(acc, a0, a1, bf, pc, xa, pb, make_iseq<16>{});
    tail_unit<KO, 1, 4>(acc, a1, a0, bf, pc, xa, pb, make_iseq<16>{});
    tail_unit<KO, 2, 63>(acc, a0, a1, bf, pc, xa, pb, make_iseq<16>{});
    tail_unit<KO, 3, 0>(acc, a1, a0, bf, pc, xa, pb, make_iseq<16>{});
    tail_unit<KO, 4, 63>(acc, a0, a1, bf, pc, xa, pb, make_iseq<16>{});
    tail_unit<KO, 5, 63>(acc, a1, a0, bf, pc, xa, pb, make_iseq<16>{});
    pub_rest(acc, pb, make_iseq<8>{});
    const unsigned lr = ((lane + xg) & 7u) * 4u;
    unsigned fl;
    asm volatile("buffer_load_dword %0, %1, %2, %3 offen sc1" : "=v"(fl) : "v"(lr), "s"(srdH), "s"(flg_off + xg * 128u) : "memory");
    PINV(toks);
    if (w == (xg >> 1)) {
      const unsigned a = pc.r01 + SLOT + l16;
      if (xg & 1u) own_put<8>(pb, a, make_iseq<8>{}); else own_put<0>(pb, a, make_iseq<8>{});
    }
    const unsigned fo = lane * 128u + xg * 4u, tau = __builtin_amdgcn_readlane(toks, xg);
    unsigned oo = ((b * 64u + 16u * w + r16) * 1024u + (isA ? 0u : 512u) + 64u * xg + 8u * g) * 2u;
    PINV(oo);
    wait_lgkm0();
    barrier();
    const unsigned oa = 65536u + SLOT + (xg >> 1) * 16384u + w * 2048u + l16;
    const u32x4 r0 = __builtin_bit_cast(u32x4, lds128(oa)), r1 = __builtin_bit_cast(u32x4, lds128(oa + 1024u));
    GAP();
    wait_vm<9>();
    if (lane == 2u * w) asm volatile("buffer_store_dword %0, %1, %2, %3 offen sc1" :: "v"(toks), "v"(fo), "s"(srdH), "s"(flg_off) : "memory");
    wait_vm<2>();
    if (lane == 2u * w + 1u) asm volatile("buffer_store_dword %0, %1, %2, %3 offen sc1" :: "v"(toks), "v"(fo), "s"(srdH), "s"(flg_off) : "memory");
    f32x16 f[8];
#pragma unroll
    for (int j = 0; j < 8; ++j) { const unsigned t = j < 4 ? r0[j & 3] : r1[j & 3]; f[0][2 * j] = __uint_as_float(t << 16); f[0][2 * j + 1] = __uint_as_float(t & 0xffff0000u); }
    PINV(f[0]);
    asm volatile("s_waitcnt vmcnt(2)" : "+v"(fl) : "v"(pb.pk[0]), "v"(pb.pk[1]), "v"(pb.pk[2]), "v"(pb.pk[3]), "v"(pb.pk[4]), "v"(pb.pk[5]), "v"(pb.pk[6]), "v"(pb.pk[7]),
                 "v"(pb.pk[8]), "v"(pb.pk[9]), "v"(pb.pk[10]), "v"(pb.pk[11]), "v"(pb.pk[12]), "v"(pb.pk[13]), "v"(pb.pk[14]), "v"(pb.pk[15]) : "memory");
    unsigned got = 0xffffff01u, spin = 1u << 21, fresh, so, f2;
#define POLL(R) "buffer_load_dword %[" R "], %[lr], %[sh], %[flg] offen sc1\n\t"
#define SEEN(R) "v_cmp_eq_u32_e32 vcc, %[tau], %[" R "]\n\ts_andn2_b32 %[fr], vcc_lo, %[got]\n\ts_cbranch_scc1 .Lproc_%=\n\t"
    asm volatile(
        SEEN("fl") POLL("fl") "s_sleep 4\n"
        ".Lidle_%=:\n\t" POLL("f2") "s_waitcnt vmcnt(1)\n\t" SEEN("fl") "s_sleep %[slp]\n\t" POLL("fl") "s_waitcnt vmcnt(1)\n\t" SEEN("f2")
        "s_sleep %[slp]\n\ts_sub_u32 %[spin], %[spin], 1\n\ts_cbranch_scc0 .Lidle_%=\n\ts_branch .Ldone_%=\n"
        ".Lproc_%=:\n\ts_or_b32 %[got], %[got], %[fr]\n\t"
        "s_bitcmp1_b32 %[fr], 1\n\ts_cbranch_scc0 .La1_%=\n\ts_add_u32 %[so], %[ro], 65536\n\ts_bitset0_b32 %[so], 19\n\t"
        "buffer_load_dwordx4 v[152:155], %[l16], %[sp], %[so] offen sc1\n\tbuffer_load_dwordx4 v[156:159], %[l16], %[sp], %[so] offen offset:1024 sc1\n.La1_%=:\n\t"
        "s_bitcmp1_b32 %[fr], 2\n\ts_cbranch_scc0 .La2_%=\n\ts_add_u32 %[so], %[ro], 131072\n\ts_bitset0_b32 %[so], 19\n\t"
        "buffer_load_dwordx4 v[168:171], %[l16], %[sp], %[so] offen sc1\n\tbuffer_load_dwordx4 v[172:175], %[l16], %[sp], %[so] offen offset:1024 sc1\n.La2_%=:\n\t"
        "s_bitcmp1_b32 %[fr], 3\n\ts_cbranch_scc0 .La3_%=\n\ts_add_u32 %[so], %[ro], 196608\n\ts_bitset0_b32 %[so], 19\n\t"
        "buffer_load_dwordx4 v[184:187], %[l16], %[sp], %[so] offen sc1\n\tbuffer_load_dwordx4 v[188:191], %[l16], %[sp], %[so] offen offset:1024 sc1\n.La3_%=:\n\t"
        "s_bitcmp1_b32 %[fr], 4\n\ts_cbranch_scc0 .La4_%=\n\ts_add_u32 %[so], %[ro], 262144\n\ts_bitset0_b32 %[so], 19\n\t"
        "buffer_load_dwordx4 v[200:203], %[l16], %[sp], %[so] offen sc1\n\tbuffer_load_dwordx4 v[204:207], %[l16], %[sp], %[so] offen offset:1024 sc1\n.La4_%=:\n\t"
        "s_bitcmp1_b32 %[fr], 5\n\ts_cbranch_scc0 .La5_%=\n\ts_add_u32 %[so], %[ro], 327680\n\ts_bitset0_b32 %[so], 19\n\t"
        "buffer_load_dwordx4 v[216:219], %[l16], %[sp], %[so] offen sc1\n\tbuffer_load_dwordx4 v[220:223], %[l16], %[sp], %[so] offen offset:1024 sc1\n.La5_%=:\n\t"
        "s_bitcmp1_b32 %[fr], 6\n\ts_cbranch_scc0 .La6_%=\n\ts_add_u32 %[so], %[ro], 393216\n\ts_bitset0_b32 %[so], 19\n\t"
        "buffer_load_dwordx4 v[232:235], %[l16], %[sp], %[so] offen sc1\n\tbuffer_load_dwordx4 v[236:239], %[l16], %[sp], %[so] offen offset:1024 sc1\n.La6_%=:\n\t"
        "s_bitcmp1_b32 %[fr], 7\n\ts_cbranch_scc0 .La7_%=\n\ts_add_u32 %[so], %[ro], 458752\n\ts_bitset0_b32 %[so], 19\n\t"
        "buffer_load_dwordx4 v[248:251], %[l16], %[sp], %[so] offen sc1\n\tbuffer_load_dwordx4 v[252:255], %[l16], %[sp], %[so] offen offset:1024 sc1\n.La7_%=:\n\t"
        "s_waitcnt vmcnt(0)\n\ts_cmp_eq_u32 %[got], -1\n\ts_cbranch_scc1 .Ltok_%=\n\t" POLL("fl") "s_branch .Lwide_%=\n"
        ".Ltok_%=:\n\tbuffer_load_dword %[fl], %[l8], %[sh], %[tok] offen sc1\n"
        ".Lwide_%=:\n\t"
        "s_bitcmp1_b32 %[fr], 1\n\ts_cbranch_scc0 .Lb1_%=\n\t"
        "v_lshlrev_b32_e32 v144, 16, v152\n\tv_and_b32_e32 v145, %[hi], v152\n\tv_lshlrev_b32_e32 v146, 16, v153\n\tv_and_b32_e32 v147, %[hi], v153\n\t"
        "v_lshlrev_b32_e32 v148, 16, v154\n\tv_and_b32_e32 v149, %[hi], v154\n\tv_lshlrev_b32_e32 v150, 16, v155\n\tv_and_b32_e32 v151, %[hi], v155\n\t"
        "v_lshlrev_b32_e32 v152, 16, v156\n\tv_and_b32_e32 v153, %[hi], v156\n\tv_lshlrev_b32_e32 v154, 16, v157\n\tv_and_b32_e32 v155, %[hi], v157\n\t"
        "v_lshlrev_b32_e32 v156, 16, v158\n\tv_and_b32_e32 v157, %[hi], v158\n\tv_lshlrev_b32_e32 v158, 16, v159\n\tv_and_b32_e32 v159, %[hi], v159\n\t"
        ".Lb1_%=:\n\t"
        "s_bitcmp1_b32 %[fr], 2\n\ts_cbranch_scc0 .Lb2_%=\n\t"
        "v_lshlrev_b32_e32 v160, 16, v168\n\tv_and_b32_e32 v161, %[hi], v168\n\tv_lshlrev_b32_e32 v162, 16, v169\n\tv_and_b32_e32 v163, %[hi], v169\n\t"
        "v_lshlrev_b32_e32 v164, 16, v170\n\tv_and_b32_e32 v165, %[hi], v170\n\tv_lshlrev_b32_e32 v166, 16, v171\n\tv_and_b32_e32 v167, %[hi], v171\n\t"
        "v_lshlrev_b32_e32 v168, 16, v172\n\tv_and_b32_e32 v169, %[hi], v172\n\tv_lshlrev_b32_e32 v170, 16, v173\n\tv_and_b32_e32 v171, %[hi], v173\n\t"
        "v_lshlrev_b32_e32 v172, 16, v174\n\tv_and_b32_e32 v173, %[hi], v174\n\tv_lshlrev_b32_e32 v174, 16, v175\n\tv_and_b32_e32 v175, %[hi], v175\n\t"
        ".Lb2_%=:\n\t"
        "s_bitcmp1_b32 %[fr], 3\n\ts_cbranch_scc0 .Lb3_%=\n\t"
        "v_lshlrev_b32_e32 v176, 16, v184\n\tv_and_b32_e32 v177, %[hi], v184\n\tv_lshlrev_b32_e32 v178, 16, v185\n\tv_and_b32_e32 v179, %[hi], v185\n\t"
        "v_lshlrev_b32_e32 v180, 16, v186\n\tv_and_b32_e32 v181, %[hi], v186\n\tv_lshlrev_b32_e32 v182, 16, v187\n\tv_and_b32_e32 v183, %[hi], v187\n\t"
        "v_lshlrev_b32_e32 v184, 16, v188\n\tv_and_b32_e32 v185, %[hi], v188\n\tv_lshlrev_b32_e32 v186, 16, v189\n\tv_and_b32_e32 v187, %[hi], v189\n\t"
        "v_lshlrev_b32_e32 v188, 16, v190\n\tv_and_b32_e32 v189, %[hi], v190\n\tv_lshlrev_b32_e32 v190, 16, v191\n\tv_and_b32_e32 v191, %[hi], v191\n\t"
        ".Lb3_%=:\n\t"
        "s_bitcmp1_b32 %[fr], 4\n\ts_cbranch_scc0 .Lb4_%=\n\t"
        "v_lshlrev_b32_e32 v192, 16, v200\n\tv_and_b32_e32 v193, %[hi], v200\n\tv_lshlrev_b32_e32 v194, 16, v201\n\tv_and_b32_e32 v195, %[hi], v201\n\t"
        "v_lshlrev_b32_e32 v196, 16, v202\n\tv_and_b32_e32 v197, %[hi], v202\n\tv_lshlrev_b32_e32 v198, 16, v203\n\tv_and_b32_e32 v199, %[hi], v203\n\t"
        "v_lshlrev_b32_e32 v200, 16, v204\n\tv_and_b32_e32 v201, %[hi], v204\n\tv_lshlrev_b32_e32 v202, 16, v205\n\tv_and_b32_e32 v203, %[hi], v205\n\t"
        "v_lshlrev_b32_e32 v204, 16, v206\n\tv_and_b32_e32 v205, %[hi], v206\n\tv_lshlrev_b32_e32 v206, 16, v207\n\tv_and_b32_e32 v207, %[hi], v207\n\t"
        ".Lb4_%=:\n\t"
        "s_bitcmp1_b32 %[fr], 5\n\ts_cbranch_scc0 .Lb5_%=\n\t"
        "v_lshlrev_b32_e32 v208, 16, v216\n\tv_and_b32_e32 v209, %[hi], v216\n\tv_lshlrev_b32_e32 v210, 16, v217\n\tv_and_b32_e32 v211, %[hi], v217\n\t"
        "v_lshlrev_b32_e32 v212, 16, v218\n\tv_and_b32_e32 v213, %[hi], v218\n\tv_lshlrev_b32_e32 v214, 16, v219\n\tv_and_b32_e32 v215, %[hi], v219\n\t"
        "v_lshlrev_b32_e32 v216, 16, v220\n\tv_and_b32_e32 v217, %[hi], v220\n\tv_lshlrev_b32_e32 v218, 16, v221\n\tv_and_b32_e32 v219, %[hi], v221\n\t"
        "v_lshlrev_b32_e32 v220, 16, v222\n\tv_and_b32_e32 v221, %[hi], v222\n\tv_lshlrev_b32_e32 v222, 16, v223\n\tv_and_b32_e32 v223, %[hi], v223\n\t"
        ".Lb5_%=:\n\t"
        "s_bitcmp1_b32 %[fr], 6\n\ts_cbranch_scc0 .Lb6_%=\n\t"
        "v_lshlrev_b32_e32 v224, 16, v232\n\tv_and_b32_e32 v225, %[hi], v232\n\tv_lshlrev_b32_e32 v226, 16, v233\n\tv_and_b32_e32 v227, %[hi], v233\n\t"
        "v_lshlrev_b32_e32 v228, 16, v234\n\tv_and_b32_e32 v229, %[hi], v234\n\tv_lshlrev_b32_e32 v230, 16, v235\n\tv_and_b32_e32 v231, %[hi], v235\n\t"
        "v_lshlrev_b32_e32 v232, 16, v236\n\tv_and_b32_e32 v233, %[hi], v236\n\tv_lshlrev_b32_e32 v234, 16, v237\n\tv_and_b32_e32 v235, %[hi], v237\n\t"
        "v_lshlrev_b32_e32 v236, 16, v238\n\tv_and_b32_e32 v237, %[hi], v238\n\tv_lshlrev_b32_e32 v238, 16, v239\n\tv_and_b32_e32 v239, %[hi], v239\n\t"
        ".Lb6_%=:\n\t"
        "s_bitcmp1_b32 %[fr], 7\n\ts_cbranch_scc0 .Lb7_%=\n\t"
        "v_lshlrev_b32_e32 v240, 16, v248\n\tv_and_b32_e32 v241, %[hi], v248\n\tv_lshlrev_b32_e32 v242, 16, v249\n\tv_and_b32_e32 v243, %[hi], v249\n\t"
        "v_lshlrev_b32_e32 v244, 16, v250\n\tv_and_b32_e32 v245, %[hi], v250\n\tv_lshlrev_b32_e32 v246, 16, v251\n\tv_and_b32_e32 v247, %[hi], v251\n\t"
        "v_lshlrev_b32_e32 v248, 16, v252\n\tv_and_b32_e32 v249, %[hi], v252\n\tv_lshlrev_b32_e32 v250, 16, v253\n\tv_and_b32_e32 v251, %[hi], v253\n\t"
        "v_lshlrev_b32_e32 v252, 16, v254\n\tv_and_b32_e32 v253, %[hi], v254\n\tv_lshlrev_b32_e32 v254, 16, v255\n\tv_and_b32_e32 v255, %[hi], v255\n\t"
        ".Lb7_%=:\n\t"
        "s_cmp_eq_u32 %[got], -1\n\ts_cbranch_scc0 .Lidle_%=\n"
        ".Ldone_%=:"
        : "=&{v[144:159]}"(f[1]), "=&{v[160:175]}"(f[2]), "=&{v[176:191]}"(f[3]), "=&{v[192:207]}"(f[4]), "=&{v[208:223]}"(f[5]), "=&{v[224:239]}"(f[6]), "=&{v[240:255]}"(f[7]),
          [fl] "+v"(fl), [f2] "=&v"(f2), [got] "+s"(got), [spin] "+s"(spin), [fr] "=&s"(fresh), [so] "=&s"(so)
        : [tau] "s"(tau), [l16] "v"(l16), [l8] "v"(l8), [lr] "v"(lr), [sp] "s"(pb.srd), [sh] "s"(srdH), [ro] "s"(xg * 73728u + w * 2048u), [tok] "s"(tok_off),
          [flg] "s"(flg_off + xg * 128u), [hi] "s"(0xffff0000u), [slp] "i"(isA ? 8 : 0)
        : "vcc", "scc", "memory");
#undef POLL
#undef SEEN
    const f32x16 t = ((f[0] + f[1]) + (f[2] + f[3])) + ((f[4] + f[5]) + (f[6] + f[7]));
#pragma unroll
    for (int s = 0; s < 2; ++s)
    {
      const u32x4 ov = natural8(u32x4{cvtpk(t[8 * s], t[8 * s + 1]), cvtpk(t[8 * s + 2], t[8 * s + 3]), cvtpk(t[8 * s + 4], t[8 * s + 5]), cvtpk(t[8 * s + 6], t[8 * s + 7])});
      __builtin_amdgcn_raw_buffer_store_b128(ov, mk_srd(out, 2097152u), oo + s * 64, 0, 2);
    }
    asm volatile("s_waitcnt vmcnt(2)" : "+v"(fl) :: "memory");
    if ((lane >> 1) == w && fl != toks) asm volatile("buffer_store_dword %0, %1, %2, %3 offen sc1" :: "v"(fl), "v"(fo), "s"(srdH), "s"(flg_off) : "memory");
  }
}

__global__ __launch_bounds__(256, 1)
void fused_attn(const char* __restrict__ Ax, const char* __restrict__ Ak, const char* __restrict__ Av, const char* __restrict__ Awq, const char* __restrict__ Awo,
                const char* __restrict__ Bx, const char* __restrict__ Bk, const char* __restrict__ Bv, const char* __restrict__ Bwq, const char* __restrict__ Bwo,
                char* __restrict__ part, char* __restrict__ out) {
  const char* pc; unsigned left;
  asm volatile("s_getpc_b64 %0\n.Lpc_%=:\n\ts_mov_b32 %1, .Lhot_end-.Lpc_%=" : "=s"(pc), "=s"(left));
  const srd_t code = mk_srd(pc, left);
  if (blockIdx.x >= 128) { body<true>(Ax, Ak, Av, Awq, Awo, Bwo, code, part, out); asm volatile(".Lhot_end:"); __builtin_amdgcn_endpgm(); }
  body<false>(Bx, Bk, Bv, Bwq, Bwo, Bwo, code, part, out);
}

extern "C" void kernel_launch(void* const* d_in, const int* in_sizes, int n_in, void* d_out, int out_size, void* d_ws, size_t ws_size, hipStream_t stream) {
  (void)in_sizes; (void)n_in; (void)out_size; (void)ws_size;
  (void)hipFuncSetAttribute((const void*)fused_attn, hipFuncAttributeMaxDynamicSharedMemorySize, (int)LDS_BYTES);
  fused_attn<<<256, 256, LDS_BYTES, stream>>>((const char*)d_in[0], (const char*)d_in[1], (const char*)d_in[2], (const char*)d_in[3], (const char*)d_in[4],
                                              (const char*)d_in[5], (const char*)d_in[6], (const char*)d_in[7], (const char*)d_in[8], (const char*)d_in[9], (char*)d_ws, (char*)d_out);
}
```

```cpp
#include <hip/hip_runtime.h>
#include <hip/hip_bf16.h>
#include <utility>
#include <type_traits>

typedef __bf16 bf16x8 __attribute__((ext_vector_type(8)));
typedef __bf16 bf16x4 __attribute__((ext_vector_type(4)));
typedef __bf16 bf16x2 __attribute__((ext_vector_type(2)));
typedef float f32x4 __attribute__((ext_vector_type(4)));
typedef float f32x2 __attribute__((ext_vector_type(2)));
typedef float f32x16 __attribute__((ext_vector_type(16)));
typedef unsigned u32x4 __attribute__((ext_vector_type(4)));
#define LAS __attribute__((address_space(3)))
#define DI __device__ __forceinline__
typedef __amdgpu_buffer_rsrc_t srd_t;
template <int... Is> using iseq = std::integer_sequence<int, Is...>;
template <int N> using make_iseq = std::make_integer_sequence<int, N>;

#define KV_NT 1
#define W_NT 0
#define X_NT 0
#define SLEEP_A 8
#define SLEEP_B 0
#define PACE_A 0
#define PACE_ATT 0
#define TOUCH_Q 1
#define TOUCH_O 3
#define TO_SKIP 16u
#define TO_B 8u
#define WARM_CODE 40
#define ALT 0
#define ORD 2
#define EARLY_O 0
#define TOUCH_B 4
#define PUB_SLEEP 0
#define KVPOL "nt"
#define CVV 0
#define DELAY_A 0
#define SK 4
#define SV 4
#define SP 5
#define OUTPOL 2
#define STAG 0
#define RAMP 0
#define PUBPOL "sc0 sc1"
#define ILV_A 1
#define ILV_B 1
#define LDPOL "sc1"
#define PLPOL "sc1"
#define FLPOL "sc1"
#define BARM 0x3ffe
#define LATEW 1
#define SKIPOWN 1
#define EASY 6
#define EASY_T 1350
#define EASY2 0
#define EASY2_T 3000
#define RKA b
#define RKB b + 4u * w
#define WS_END 0

constexpr unsigned SLOT = 8192, LDS_BYTES = 20 * SLOT;
constexpr unsigned POFF = (unsigned)WS_END << 20, HOFF = POFF + (16u << 20);
constexpr float SC = 0.12751743082f;
constexpr float LMAX = 1048576.0f;

#define NO_REDUCE_STAMPS
#define GAP() __builtin_amdgcn_sched_barrier(0)
#define STAMP(i) do {} while (0)
#define CYC(i) do {} while (0)
#define PINS(x) asm volatile("" : "+s"(x))
#define PINV(x) asm volatile("" : "+v"(x))
DI unsigned rfl(unsigned x) { return __builtin_amdgcn_readfirstlane(x); }
DI unsigned cvtpk(float lo, float hi) { f32x2 v = {lo, hi}; bf16x2 b = __builtin_convertvector(v, bf16x2); return __builtin_bit_cast(unsigned, b); }
DI u32x4 pack2(const f32x4& a, const f32x4& b) { return u32x4{cvtpk(a[0], a[1]), cvtpk(a[2], a[3]), cvtpk(b[0], b[1]), cvtpk(b[2], b[3])}; }
DI u32x4 natural8(u32x4 w) {
  auto s0 = __builtin_amdgcn_permlane32_swap(w[0], w[2], false, false);
  auto s1 = __builtin_amdgcn_permlane32_swap(w[1], w[3], false, false);
  auto t0 = __builtin_amdgcn_permlane16_swap(s0[0], s0[1], false, false);
  auto t1 = __builtin_amdgcn_permlane16_swap(s1[0], s1[1], false, false);
  return u32x4{t0[0], t1[0], t0[1], t1[1]};
}
#define MF "v_mfma_f32_16x16x32_bf16 "
template <bool Z> DI void mm_vvv(f32x4& c, const bf16x8& a, const bf16x8& b) {
  if constexpr (Z) asm volatile(MF "%0, %1, %2, 0" : "=&v"(c) : "v"(a), "v"(b));
  else             asm volatile(MF "%0, %1, %2, %0" : "+v"(c) : "v"(a), "v"(b));
}
template <bool Z> DI void mm_vva(f32x4& c, const bf16x8& a, const bf16x8& b) {
  if constexpr (Z) asm volatile(MF "%0, %1, %2, 0" : "=&v"(c) : "v"(a), "a"(b));
  else             asm volatile(MF "%0, %1, %2, %0" : "+v"(c) : "v"(a), "a"(b));
}
DI void mm_vca(f32x4& d, const bf16x8& a, const bf16x8& b, const f32x4& c) { asm volatile(MF "%0, %1, %2, %3" : "=&v"(d) : "v"(a), "a"(b), "v"(c)); }
DI void mm_avv(f32x4& c, const bf16x8& a, const bf16x8& b) { asm volatile(MF "%0, %1, %2, %0" : "+a"(c) : "v"(a), "v"(b)); }
DI void set_m0(unsigned v) { asm volatile("s_mov_b32 m0, %0" :: "s"(v) : "memory"); }
template <int NT, int P> DI void dma_p(unsigned voff, srd_t srd, unsigned soff) {
  if constexpr (NT) asm volatile("buffer_load_dwordx4 %0, %1, %2 offen offset:%3 " KVPOL " lds" :: "v"(voff), "s"(srd), "s"(soff), "i"(1024 * P) : "memory");
  else              asm volatile("buffer_load_dwordx4 %0, %1, %2 offen offset:%3 lds" :: "v"(voff), "s"(srd), "s"(soff), "i"(1024 * P) : "memory");
}
struct Src { unsigned v[4]; srd_t srd; };
template <int NT> DI void dma_unit(const Src& s, unsigned soff, unsigned ldsaddr) {
  asm volatile("s_mov_b32 m0, %0\n\ts_nop 0" :: "s"(ldsaddr) : "memory");
  dma_p<NT, 0>(s.v[0], s.srd, soff); dma_p<NT, 1>(s.v[1], s.srd, soff); dma_p<NT, 2>(s.v[2], s.srd, soff); dma_p<NT, 3>(s.v[3], s.srd, soff);
}
template <int NT> DI void dma_elem(const Src& s, unsigned soff, unsigned ustep, unsigned ldsaddr) { dma_unit<NT>(s, soff, ldsaddr); dma_unit<NT>(s, soff + ustep, ldsaddr + 4096u); }
template <int N> DI void wait_vm() { asm volatile("s_waitcnt vmcnt(%0)" :: "i"(N) : "memory"); }
template <int N, int L> DI void wait_vm_lgkm() { asm volatile("" ::: "memory"); __builtin_amdgcn_s_waitcnt((N & 15) | ((N >> 4) << 14) | 0x70 | (L << 8)); asm volatile("" ::: "memory"); }
DI void wait_lgkm0() { wait_vm_lgkm<63, 0>(); }
DI void barrier() { asm volatile("s_barrier" ::: "memory"); }
DI bf16x8 lds128(unsigned a) { return *(const LAS bf16x8*)(size_t)(a); }
DI bf16x4 lds_tr1(unsigned a) { return __builtin_amdgcn_ds_read_tr16_b64_v4bf16((LAS bf16x4*)(size_t)(a)); }
DI bf16x8 cat(bf16x4 lo, bf16x4 hi) { return __builtin_shufflevector(lo, hi, 0, 1, 2, 3, 4, 5, 6, 7); }
DI bf16x8 lds_tr(unsigned a0, unsigned a1) { return cat(lds_tr1(a0), lds_tr1(a1)); }
DI srd_t mk_srd(const void* base, unsigned bytes) { return __builtin_amdgcn_make_buffer_rsrc((void*)base, (short)0, (int)bytes, 0x00020000); }
template <bool VX> DI void mk_src_rows(Src& s, const char* base, unsigned bytes, unsigned stride, unsigned lane) {
  s.srd = mk_srd(base, bytes);
  const unsigned row = (lane >> 2) & 3u, cb = lane >> 4, sl = lane & 3u;
#pragma unroll
  for (int p = 0; p < 4; ++p) s.v[p] = row * stride + cb * 64u + ((sl ^ (VX ? 2 * (p & 1) : (p & 2))) << 4) + (4u * stride - 1024u) * p;
}
template <bool VX> DI void mk_src_w(Src& s, const char* base, unsigned bytes, unsigned stride, unsigned lane) {
  s.srd = mk_srd(base, bytes);
  const unsigned sub = lane >> 5, cb = (lane >> 4) & 1u, row = (lane >> 2) & 3u, sl = lane & 3u;
#pragma unroll
  for (int p = 0; p < 4; ++p) s.v[p] = (4u * sub + row) * stride + cb * 64u + ((sl ^ (VX ? 2u * sub : 2u * (p & 1))) << 4) + (8u * stride - 1024u) * p;
}
template <int NT> DI void touch(unsigned& d, unsigned voff, srd_t srd, unsigned soff) {
  if constexpr (NT) asm volatile("buffer_load_dword %0, %1, %2, %3 offen nt" : "+v"(d) : "v"(voff), "s"(srd), "s"(soff) : "memory");
  else              asm volatile("buffer_load_dword %0, %1, %2, %3 offen" : "+v"(d) : "v"(voff), "s"(srd), "s"(soff) : "memory");
}
DI unsigned ksoff(unsigned m, unsigned rk) { return ((m + rk) & 15u) * 8192u; }

struct PC {
  unsigned r01, r2;
  unsigned w01[2], w2[2];
  unsigned xb, xmask, xa;
  unsigned z, soff;
};
template <unsigned TS_, unsigned QS_, unsigned ZI_, unsigned ZM_, unsigned ZO_, unsigned US_, unsigned CS_, bool ROT_> struct PK {
  static constexpr unsigned TS = TS_, QS = QS_, ZI = ZI_, ZM = ZM_, ZO = ZO_, US = US_, CS = CS_; static constexpr bool ROT = ROT_; };
DI unsigned q_soff(unsigned z) { return ((z & 0xC300u) * 0x440u) & 0x3C0000u; }
template <int POS> DI unsigned ring_lds(const PC& c) { return POS == 2 ? c.r2 : c.r01 + POS * SLOT; }
template <int POS> DI unsigned ring_rd(const PC& c, int d) { return (POS == 2 ? c.w2[d & 1] : c.w01[d & 1] + POS * SLOT) + (d >> 1) * 256u; }
struct NoX { static constexpr bool ACT = true; template <int G> DI void run() const {} };
template <int SL> struct EasyX {
  static constexpr bool ACT = true;
  unsigned long long& t; unsigned lim;
  template <int G> DI void run() const {
    if constexpr (G == 8) asm volatile("s_memrealtime %0" : "=s"(t));
    if constexpr (G == 16) PINS(t);
    if constexpr (G == 16) asm volatile("s_cmp_lt_i32 %0, 0\n\ts_cbranch_scc1 .Le_%=\n\ts_sleep %1\n.Le_%=:" :: "s"((unsigned)t - lim), "i"(SL) : "scc");
  }
};
template <class K, int POS, int S, bool Z, bool RD, bool DM, int NT, bool XT, int M, class X>
DI void proj_gap(f32x4 (&acc)[8][4], const bf16x8 (&cur)[4], bf16x8 (&nxt)[4], bf16x4 (&lo)[4], bf16x8 (&bf)[4], PC& c, unsigned soff, const Src& src, const Src& xs, unsigned xsoff, const X& ex) {
  constexpr int qb = M >> 2, db = M & 3;
  mm_vvv<Z>(acc[4 * S + db][qb], cur[db], bf[qb]);
  if constexpr (RD && M < 8) {
    constexpr int d = M >> 1; const unsigned a = S == 0 ? ring_rd<POS>(c, d) + 4096u : ring_rd<(POS + 1) % 3>(c, d);
    if constexpr (M & 1) nxt[d] = cat(lo[d], lds_tr1(a + K::TS)); else lo[d] = lds_tr1(a);
  }
  if constexpr (X::ACT && RD && S == 1 && db == 3) bf[qb] = lds128(c.xa + qb * K::QS);
  if constexpr (XT) {
    if constexpr (M < 8 && !(M & 1)) dma_p<X_NT, M / 2>(xs.v[M / 2], xs.srd, xsoff);
    if constexpr (M == 7) set_m0(ring_lds<POS>(c) + S * 4096u);
    if constexpr (M >= 8 && !(M & 1)) dma_p<NT, (M - 8) / 2>(src.v[(M - 8) / 2], src.srd, soff);
  } else if constexpr (DM && M >= 8 && !(M & 1)) dma_p<NT, (M - 8) / 2>(src.v[(M - 8) / 2], src.srd, soff);
  if constexpr (X::ACT && S == 0 && M == 9) { PINS(c.z); c.z = (c.z + K::ZI) & K::ZM; PINS(c.z); }
  if constexpr (X::ACT && S == 0 && M == 11) { c.xa = (c.z & c.xmask) | c.xb; PINV(c.xa); }
  if constexpr (S == 1 && DM && !K::ROT && M == 9) { PINS(c.soff); c.soff = (c.soff + K::CS) & (16u * K::CS - 1u); PINS(c.soff); }
  if constexpr (S == 1 && DM && K::ROT && M == 9) { PINS(c.z); c.soff = (c.z + 0x3100u) & 0xC300u; PINS(c.soff); }
  if constexpr (S == 1 && DM && K::ROT && M == 13) { PINS(c.soff); c.soff = (c.soff * 0x440u) & 0x3C0000u; PINS(c.soff); }
  ex.template run<16 * S + M>();
  GAP();
}
template <class K, int POS, int S, bool Z, bool RD, bool DM, int NT, int VM, bool BAR, bool XT, class X, int... Ms>
DI void proj_unit(f32x4 (&acc)[8][4], const bf16x8 (&cur)[4], bf16x8 (&nxt)[4], bf16x8 (&bf)[4], PC& c, const Src& src, const Src& xs, unsigned xsoff, unsigned xdst, const X& ex, iseq<Ms...>) {
  wait_vm_lgkm<(RD ? VM : 63), (S == 0 ? 2 : 0)>();
  if constexpr (BAR) barrier();
  const unsigned soff = c.soff + S * K::US;
  if constexpr (XT) set_m0(xdst); else if constexpr (DM) set_m0(ring_lds<POS>(c) + S * 4096u);
  GAP();
  bf16x4 lo[4];
  (proj_gap<K, POS, S, Z, RD, DM, NT, XT, Ms>(acc, cur, nxt, lo, bf, c, soff, src, xs, xsoff, ex), ...);
}
template <class K, int POS, bool Z, bool LAST, bool DM, int NT, int VM0, int VM1, bool BAR1 = false, bool XT0 = false, bool XT1 = false, class X = NoX>
DI void proj_elem(f32x4 (&acc)[8][4], bf16x8 (&a0)[4], bf16x8 (&a1)[4], bf16x8 (&bf)[4], PC& c, const Src& src, const X& ex = X{},
                  const Src* xs = nullptr, unsigned xso0 = 0, unsigned xd0 = 0, unsigned xso1 = 0, unsigned xd1 = 0) {
  proj_unit<K, POS, 0, Z, true, DM, NT, VM0, false, XT0>(acc, a0, a1, bf, c, src, xs ? *xs : src, xso0, xd0, ex, make_iseq<16>{});
  proj_unit<K, POS, 1, Z, !LAST, DM, NT, VM1, BAR1, XT1>(acc, a1, a0, bf, c, src, xs ? *xs : src, xso1, xd1, ex, make_iseq<16>{});
}
template <class K> DI void proj_setup(PC& c, unsigned tb, unsigned xb, unsigned z0, unsigned s3) {
  c.w01[0] = c.r01 + tb; c.w01[1] = c.w01[0] ^ 32u; c.w2[0] = c.r2 + tb; c.w2[1] = c.w2[0] ^ 32u;
  PINV(c.w01[0]); PINV(c.w01[1]); PINV(c.w2[0]); PINV(c.w2[1]);
  c.xb = xb; c.xmask = K::ZO; PINV(c.xmask);
  c.z = z0; c.soff = s3;
}
template <class K, int POS> DI void proj_start(bf16x8 (&a0)[4], bf16x8 (&bf)[4], PC& c, unsigned tb, unsigned xb, unsigned z0, unsigned s3) {
  proj_setup<K>(c, tb, xb, z0, s3);
  c.xa = (c.z & K::ZO) | xb; PINV(c.xa);
#pragma unroll
  for (int d = 0; d < 4; ++d) { const unsigned a = ring_rd<POS>(c, d); a0[d] = lds_tr(a, a + K::TS); }
  GAP();
#pragma unroll
  for (int q = 0; q < 4; ++q) bf[q] = lds128(c.xa + q * K::QS);
  GAP();
}
#define ACC_ALL(C, a) C(a[0][0]), C(a[0][1]), C(a[0][2]), C(a[0][3]), C(a[1][0]), C(a[1][1]), C(a[1][2]), C(a[1][3]), C(a[2][0]), C(a[2][1]), C(a[2][2]), C(a[2][3]), C(a[3][0]), C(a[3][1]), C(a[3][2]), C(a[3][3])
DI void acc_fence_v(f32x4 (&acc)[8][4]) {
  asm volatile("s_nop 7\n\ts_nop 3" : ACC_ALL("+v", acc) :: "memory");
  f32x4 (&hi)[4][4] = *(f32x4 (*)[4][4])&acc[4];
  asm volatile("" : ACC_ALL("+v", hi) :: "memory");
  GAP();
}
DI void acc_fence_a(f32x4 (&acc)[8][4]) {
  asm volatile("s_nop 7\n\ts_nop 3" : ACC_ALL("+a", acc) :: "memory");
  f32x4 (&hi)[4][4] = *(f32x4 (*)[4][4])&acc[4];
  asm volatile("" : ACC_ALL("+a", hi) :: "memory");
  GAP();
}

struct Stash { u32x4 p[64]; unsigned base[3]; };
template <int I> DI void stash_load1(Stash& st, unsigned vk, const Src& sK, const Src& sV, unsigned rk, unsigned wo_) {
  constexpr int m = I >> 2, isV = (I >> 1) & 1, pc_ = I & 1;
  const unsigned so = ksoff(m, rk) + wo_;
  if constexpr (I < 56) asm volatile("buffer_load_dwordx4 %0, %1, %2, %3 offen offset:%4 " KVPOL : "=a"(st.p[I]) : "v"(isV ? sV.v[pc_] : vk), "s"(isV ? sV.srd : sK.srd), "s"(so), "i"(1024 * pc_) : "memory");
  else                  asm volatile("buffer_load_dwordx4 %0, %1, %2, %3 offen offset:%4 " KVPOL : "=v"(st.p[I]) : "v"(isV ? sV.v[pc_] : vk), "s"(isV ? sV.srd : sK.srd), "s"(so), "i"(1024 * pc_) : "memory");
}
template <int... Is> DI void stash_load(Stash& st, unsigned vk, const Src& sK, const Src& sV, unsigned rk, unsigned wo_, iseq<Is...>) { (stash_load1<Is>(st, vk, sK, sV, rk, wo_), ...); }
constexpr int kv_pair(int m) { constexpr int t[10] = {0, 1, 2, 3, 8, 9, 4, 5, 6, 7}; return t[m % 10]; }
constexpr unsigned long long kv_tab() { unsigned long long v = 0; for (int i = 0; i < 16; ++i) v |= (unsigned long long)kv_pair(i) << (4 * i); return v; }
template <int I> DI void stash_write1(const Stash& st) {
  constexpr int m = I >> 2, isV = (I >> 1) & 1, pc_ = I & 1, off = kv_pair(m) * 16384 + isV * 8192 + pc_ * 1024;
  if constexpr (I < 56) asm volatile("ds_write_b128 %0, %1 offset:%2" :: "v"(st.base[off >> 16]), "a"(st.p[I]), "i"(off & 65535) : "memory");
  else                  asm volatile("ds_write_b128 %0, %1 offset:%2" :: "v"(st.base[off >> 16]), "v"(st.p[I]), "i"(off & 65535) : "memory");
}
template <int... Is> DI void stash_late(const Stash& st, iseq<Is...>) { (stash_write1<40 + Is>(st), ...); }

struct AC {
  f32x4 o[8][4];
  bf16x8 q[4][4];
  bf16x8 kf[8], vf[8], pf[4];
  f32x4 negm[4];
  f32x4 l[4];
  bf16x8 ones;
};
typedef f32x4 ST[2][4];
constexpr int NEB = 22;
template <int E> DI float sm_val(ST& S) { return S[(E >> 2) & 1][E >> 3][E & 3]; }
template <int E> DI void sm_exp1(ST& S) { float a; asm volatile("v_exp_f32_e32 %0, %1" : "=v"(a) : "v"(sm_val<E>(S))); S[(E >> 2) & 1][E >> 3][E & 3] = a; }
template <int QB, int P> DI void sm_cvt(ST& S, AC& c) {
  u32x4 w = __builtin_bit_cast(u32x4, c.pf[QB]);
  unsigned r;
  asm volatile("v_cvt_pk_bf16_f32 %0, %1, %2" : "=v"(r) : "v"(sm_val<8 * QB + 2 * P>(S)), "v"(sm_val<8 * QB + 2 * P + 1>(S)));
  w[P] = r;
  c.pf[QB] = __builtin_bit_cast(bf16x8, w);
}
template <bool DM, int VM, int R, int M> DI void mem_pre(unsigned ldsaddr) {
  if constexpr (M == 6) wait_lgkm0();
  if constexpr (DM && (M == 6 || M == 14)) set_m0(ldsaddr + (M == 14 ? 4096u : 0u));
  if constexpr (M == R && VM < 63) wait_vm<VM>();
}
template <bool DM, int NT, int M> DI void mem_post(const Src& src, unsigned soff, unsigned ustep) {
  if constexpr (DM && M >= 6 && M <= 20 && !(M & 1)) dma_p<NT, ((M - 6) / 2) & 3>(src.v[((M - 6) / 2) & 3], src.srd, soff + (M >= 14 ? ustep : 0u));
}
template <int WR, int M> DI void mem_wr(const Stash& st) {
  if constexpr (WR >= 40 && M >= 7 && M < 13) stash_write1<WR + M - 7>(st);
  if constexpr (WR >= 0 && WR < 40 && M >= 8 && M < 16)
    asm volatile("ds_write_b128 %0, %1 offset:%2" :: "v"(st.base[0]), "a"(st.p[WR + M - 8]), "i"((M - 8) * 1024) : "memory");
}
struct PA { unsigned rd0, rd1, soff, fill; };
template <bool QK, bool DM, int NT, int VM, int WR, int M, class F>
DI void gapA(AC& c, ST& S, ST& Sn, PA& pa, F& addr, const Src& src, unsigned ustep, const Stash& st, bf16x4& lo) {
  constexpr int R = QK ? 16 : 0;
  if constexpr ((DM || !QK) && M == 0) addr(pa);
  if constexpr (QK) mem_pre<DM, VM, 16, M>(pa.fill); else if constexpr (M == 0 && VM < 63) wait_vm<VM>();
  if constexpr (QK) {
    constexpr int f = M >> 2, s = f >> 1, kb = f & 1, qb = M & 3;
    if constexpr (s == 0) mm_vca(Sn[kb][qb], c.kf[f], c.q[qb][0], c.negm[qb]); else mm_vva<false>(Sn[kb][qb], c.kf[f], c.q[qb][s]);
  }
  mem_post<DM, NT, M>(src, pa.soff, ustep);
  mem_wr<WR, M>(st);
  if constexpr (QK && !DM && M == 10) addr(pa);
  if constexpr (QK && M == 12) asm volatile("" :: "v"(c.negm[0]), "v"(c.negm[1]), "v"(c.negm[2]), "v"(c.negm[3]));
  if constexpr (M >= R && M < R + 16) {
    constexpr int db = (M - R) >> 1; const unsigned a = ((db & 1) ? pa.rd1 : pa.rd0) + (db >> 1) * 256u;
    if constexpr (M & 1) c.vf[db] = cat(lo, lds_tr1(a + 4096u)); else lo = lds_tr1(a);
  }
  if constexpr (NEB + M < 32) sm_exp1<NEB + M>(S);
  if constexpr (M >= 16 && (M & 1)) sm_cvt<2 + ((M - 16) >> 3), ((M - 16) >> 1) & 3>(S, c);
  GAP();
}
template <bool QK, bool DM, int NT, int VM, int WR, class F, int... Ms>
DI void phaseA_(AC& c, ST& S, ST& Sn, F& addr, const Src& src, unsigned ustep, const Stash& st, iseq<Ms...>) {
  PA pa; bf16x4 lo;
  (gapA<QK, DM, NT, VM, WR, Ms>(c, S, Sn, pa, addr, src, ustep, st, lo), ...);
}
template <bool QK, bool DM, int NT, int VM, int WR = -1, class F>
DI void phaseA(AC& c, ST& S, ST& Sn, F addr, const Src& src, unsigned ustep, const Stash& st) {
  phaseA_<QK, DM, NT, VM, WR>(c, S, Sn, addr, src, ustep, st, make_iseq<32>{});
}
template <bool MAX> DI float xlane(float v) {
  auto a = __builtin_amdgcn_permlane32_swap(__float_as_uint(v), __float_as_uint(v), false, false);
  v = MAX ? fmaxf(__uint_as_float(a[0]), __uint_as_float(a[1])) : __uint_as_float(a[0]) + __uint_as_float(a[1]);
  auto b = __builtin_amdgcn_permlane16_swap(__float_as_uint(v), __float_as_uint(v), false, false);
  return MAX ? fmaxf(__uint_as_float(b[0]), __uint_as_float(b[1])) : __uint_as_float(b[0]) + __uint_as_float(b[1]);
}
DI float row_max(const f32x4& x, const f32x4& y) {
  return xlane<true>(fmaxf(fmaxf(fmaxf(x[0], x[1]), fmaxf(x[2], x[3])), fmaxf(fmaxf(y[0], y[1]), fmaxf(y[2], y[3]))));
}
template <bool SM, bool KR, bool DM, int NT, int VM, int WR, int M, class F>
DI void gapB(AC& c, ST& Sn, PA& pa, F& addr, const Src& src, unsigned ustep, const Stash& st) {
  mem_pre<DM, (KR ? VM : 63), 18, M>(pa.fill);
  {
    constexpr int h = M / 18, r = M % 18, qb = 2 * h + (r & 1);
    if constexpr (r < 2) mm_vvv<false>(c.l[qb], c.ones, c.pf[qb]); else mm_avv(c.o[(r - 2) >> 1][qb], c.vf[(r - 2) >> 1], c.pf[qb]);
  }
  mem_post<DM, NT, M>(src, pa.soff, ustep);
  mem_wr<WR, M>(st);
  if constexpr (M == 0) addr(pa);
  if constexpr (KR && M >= 18 && M < 34 && !(M & 1)) { constexpr int f = (M - 18) >> 1; c.kf[f] = lds128(pa.rd0 + (f & 1) * 4096u + (f >> 1) * 256u); }
  if constexpr (SM) {
    if constexpr (M >= 4 && M < 18) sm_exp1<M - 4>(Sn);
    if constexpr (M >= 19 && M < 35 && (M & 1)) sm_exp1<14 + (M - 19) / 2>(Sn);
    if constexpr (M >= 20 && !(M & 1)) sm_cvt<((M - 20) >> 3), ((M - 20) >> 1) & 3>(Sn, c);
  }
  GAP();
}
template <bool SM, bool KR, bool DM, int NT, int VM, int WR, class F, int... Ms>
DI void phaseB_(AC& c, ST& Sn, F& addr, const Src& src, unsigned ustep, const Stash& st, iseq<Ms...>) {
  PA pa;
  (gapB<SM, KR, DM, NT, VM, WR, Ms>(c, Sn, pa, addr, src, ustep, st), ...);
}
template <bool SM, bool KR, bool DM, int NT, int VM, int WR = -1, class F>
DI void phaseB(AC& c, ST& Sn, F addr, const Src& src, unsigned ustep, const Stash& st) {
  phaseB_<SM, KR, DM, NT, VM, WR>(c, Sn, addr, src, ustep, st, make_iseq<36>{});
}
DI void k_read(AC& ac, unsigned ka) {
#pragma unroll
  for (int f = 0; f < 8; ++f) ac.kf[f] = lds128(ka + (f & 1) * 4096u + (f >> 1) * 256u);
}
template <bool DM, int M> DI void qk0_gap(AC& ac, ST& SA, const Src& src, unsigned soff, unsigned fill) {
  constexpr int f = M >> 2, qb = M & 3;
  if constexpr (DM && (M == 0 || M == 16)) set_m0(fill + M * 256u);
  mm_vva<(f < 2)>(SA[f & 1][qb], ac.kf[f], ac.q[qb][f >> 1]);
  if constexpr (DM && qb == 0) dma_p<KV_NT, f & 3>(src.v[f & 3], src.srd, soff + (f >> 2) * 4096u);
  GAP();
}
template <bool DM, int... Ms> DI void qk0(AC& ac, ST& SA, const Src& src, unsigned soff, unsigned fill, iseq<Ms...>) { (qk0_gap<DM, Ms>(ac, SA, src, soff, fill), ...); }
template <int... Js> DI void exp_first(ST& SA, iseq<Js...>) { (sm_exp1<Js>(SA), ...); }
template <int... Js> DI void cvt_first(ST& SA, AC& c, iseq<Js...>) { (sm_cvt<(Js >> 2), (Js & 3)>(SA, c), ...); }
DI void sm_first(AC& ac, ST& SA) {
  asm volatile("s_nop 7\n\ts_nop 3" : "+v"(SA[0][0]), "+v"(SA[0][1]), "+v"(SA[0][2]), "+v"(SA[0][3]), "+v"(SA[1][0]), "+v"(SA[1][1]), "+v"(SA[1][2]), "+v"(SA[1][3]) :: "memory");
  GAP();
#pragma unroll
  for (int q = 0; q < 4; ++q) {
    const float m = row_max(SA[0][q], SA[1][q]);
    SA[0][q] -= m; SA[1][q] -= m;
    ac.negm[q] = f32x4{-m, -m, -m, -m};
    ac.l[q] = f32x4{};
    ac.pf[q] = bf16x8{};
  }
  ac.ones = __builtin_bit_cast(bf16x8, u32x4{0x3f803f80u, 0x3f803f80u, 0x3f803f80u, 0x3f803f80u});
  asm volatile("" : "+v"(ac.negm[0]), "+v"(ac.negm[1]), "+v"(ac.negm[2]), "+v"(ac.negm[3]), "+v"(ac.l[0]), "+v"(ac.l[1]), "+v"(ac.l[2]), "+v"(ac.l[3]), "+v"(ac.ones));
  exp_first(SA, make_iseq<NEB>{});
  cvt_first(SA, ac, make_iseq<8>{});
  GAP();
}
struct BS { unsigned p[4], v[6], k[2], rk, wo, l16; };
constexpr bool b_dma(int f) { return f != 2 * (SK - 3) && f != 2 * (SV - 2) + 1 && f != 2 * (SP - 2) + 1 && f < 30 && !(LATEW && f == 27); }
constexpr int b_vm(int f) {
  const int s = f == 30 ? 26 : f - 3;
  if (!b_dma(s)) return 63;
  int n = b_dma(f) ? 5 + (f & 1) : 0;
  for (int i = s + 1; i < f; ++i) n += b_dma(i) ? 8 : 0;
  return n;
}
template <class KO, int I> DI void b_step(AC& ac, ST& S, ST& Sn, BS& b, const Src& sK, const Src& sV, const Src& sO, Stash& st) {
  constexpr int fa = 2 * I, fb = 2 * I + 1, vi = (I == SP || I == 15) ? 4 : (I & 1) ? 0 : 2;
  const unsigned pa = b.p[(I & 1) ? 3 : 1], pb = b.p[(I & 1) ? 0 : 2];
  if constexpr ((BARM >> I) & 1) barrier();
  if constexpr (I == SK - 3) st.base[0] = pa + b.l16;
  phaseA<(I < 15), b_dma(fa), (I < 14 ? KV_NT : W_NT), b_vm(fa), (I == SK - 3 ? 0 : -1)>(ac, S, Sn, [&](PA& p) __attribute__((always_inline)) {
    PINS(b.rk);
    p.soff = I < 14 ? ksoff(I < 13 ? I + 3 : 15, b.rk) : b.wo + (LATEW ? 0u : KO::CS); p.fill = (LATEW && I == 14) ? b.p[0] : pa;
    p.rd0 = b.v[vi] + (I == SP ? SLOT : 0u); p.rd1 = b.v[vi + 1] + (I == SP ? SLOT : 0u); }, I < 13 ? sK : I < 14 ? sV : sO, I < 14 ? 4096u : KO::US, st);
  if constexpr (I == SV - 2) st.base[0] = pb + b.l16;
  if constexpr (I < 15)
    phaseB<true, (I < 14), b_dma(fb), (I < 13 ? KV_NT : W_NT), b_vm(fb), (I == SV - 2 ? 8 : -1)>(ac, Sn, [&](PA& p) __attribute__((always_inline)) {
      PINS(b.rk);
      p.soff = I < 13 ? ksoff(I + 2, b.rk) : b.wo + (I - 13) * (LATEW ? 1u : 2u) * KO::CS; p.fill = (LATEW && I == 14) ? b.p[1] : pb; p.rd0 = b.k[(I & 1) ? 0 : 1]; }, I < 13 ? sV : sO, I < 13 ? 4096u : KO::US, st);
}
template <int CH, int Q, int J> DI void o_word(const AC& c, const float (&inv)[4], bf16x8 (&nb)[4]) {
  u32x4 w = __builtin_bit_cast(u32x4, nb[Q]);
  unsigned r; float t;
  asm volatile("v_accvgpr_read_b32 %0, %2\n\tv_accvgpr_read_b32 %1, %3\n\tv_mul_f32_e32 %0, %0, %4\n\tv_mul_f32_e32 %1, %1, %4\n\tv_cvt_pk_bf16_f32 %0, %0, %1"
               : "=&v"(r), "=&v"(t) : "a"(c.o[2 * CH + (J >> 1)][Q][2 * (J & 1)]), "a"(c.o[2 * CH + (J >> 1)][Q][2 * (J & 1) + 1]), "v"(inv[Q]));
  w[J] = r;
  nb[Q] = __builtin_bit_cast(bf16x8, w);
}
template <int CH, int Q> DI void o_put(const bf16x8 (&nb)[4], unsigned ow) { asm volatile("ds_write_b128 %0, %1 offset:%2" :: "v"(ow), "v"(nb[Q]), "i"(CH * 4096 + Q * 1024) : "memory"); }
template <int E> struct CvX {
  static constexpr bool ACT = E == 3;
  const AC& c; const float (&inv)[4]; const bf16x8 (&cur)[4]; bf16x8 (&nb)[4]; unsigned ow;
  template <int G> DI void run() const {
    if constexpr (E < 3 && (G & 1)) o_word<E + 1, (G >> 3), (G >> 1) & 3>(c, inv, nb);
    if constexpr (E < 3 && ((G & 15) == 8 || (G & 15) == 10)) o_put<E, 2 * (G >> 4) + ((G >> 1) & 1)>(cur, ow);
    if constexpr (E == 2 && (G & 15) == 12) o_put<3, (G >> 4)>(nb, ow);
    if constexpr (E == 3 && (G == 8 || G == 10)) o_put<3, 2 + ((G >> 1) & 1)>(cur, ow);
  }
};
template <class K, int VM, bool DM, int M> DI void gapL(AC& c, float (&inv)[4], bf16x8 (&nb)[4], bf16x8 (&a0)[4], bf16x4& lo, const PC& pc, const Src& src, unsigned soff) {
  if constexpr (M == 6) wait_vm_lgkm<VM, 0>();
  if constexpr (DM && (M == 12 || M == 32)) set_m0(pc.r2 + (M == 32 ? 4096u : 0u));
  if constexpr (M < 4) mm_vvv<false>(c.l[M], c.ones, c.pf[M]); else mm_avv(c.o[(M - 4) >> 2][M & 3], c.vf[(M - 4) >> 2], c.pf[M & 3]);
  if constexpr (M >= 6 && M < 14) {
    constexpr int d = (M - 6) >> 1; const unsigned a = ring_rd<0>(pc, d);
    if constexpr (M & 1) a0[d] = cat(lo, lds_tr1(a + K::TS)); else lo = lds_tr1(a);
  }
  if constexpr (M >= 4 && M < 12 && !(M & 1)) asm volatile("v_rcp_f32_e32 %0, %1" : "=v"(inv[(M - 4) >> 1]) : "v"(c.l[(M - 4) >> 1][0]));
  if constexpr (M >= 16 && M < 32) o_word<0, ((M - 16) >> 2), (M & 3)>(c, inv, nb);
  if constexpr (DM && ((M >= 12 && M < 16) || M >= 32)) dma_p<W_NT, M & 3>(src.v[M & 3], src.srd, soff + (M >= 32 ? K::US : 0u));
  GAP();
}
template <class K, int VM, bool DM, int... Ms> DI void phaseL(AC& c, float (&inv)[4], bf16x8 (&nb)[4], bf16x8 (&a0)[4], const PC& pc, const Src& src, unsigned soff, iseq<Ms...>) {
  bf16x4 lo;
#pragma unroll
  for (int q = 0; q < 4; ++q) nb[q] = bf16x8{};
  (gapL<K, VM, DM, Ms>(c, inv, nb, a0, lo, pc, src, soff), ...);
}
struct Pub { u32x4 pk[16]; srd_t srd; unsigned v[4], so; };
template <int F, int H> DI void pub_half(const f32x4 (&acc)[8][4], Pub& p) {
  const f32x4& a = acc[4 * (F >> 3) + 2 * (F & 1) + H][(F >> 1) & 3];
  unsigned r0, r1;
  asm volatile("v_cvt_pk_bf16_f32 %0, %2, %3\n\tv_cvt_pk_bf16_f32 %1, %4, %5" : "=&v"(r0), "=v"(r1) : "v"(a[0]), "v"(a[1]), "v"(a[2]), "v"(a[3]));
  p.pk[F][2 * H] = r0; p.pk[F][2 * H + 1] = r1;
  if constexpr (H) asm volatile("buffer_store_dwordx4 %0, %1, %2, %3 offen offset:%4 " PUBPOL :: "v"(p.pk[F]), "v"(p.v[F >> 2]), "s"(p.srd), "s"(p.so), "i"((F & 3) * 1024) : "memory");
}
struct PubX {
  static constexpr bool ACT = false;
  const f32x4 (&acc)[8][4]; Pub& p;
  template <int G> DI void run() const { if constexpr (G >= 16) pub_half<((G - 16) >> 1), (G & 1)>(acc, p); }
};
template <class K, int U, bool PB, int M>
DI void tail_gap(f32x4 (&acc)[8][4], const bf16x8 (&cur)[4], bf16x8 (&nxt)[4], bf16x4 (&lo)[4], bf16x8 (&bf)[4], const PC& c, const unsigned (&xa)[3], Pub& p) {
  constexpr int qb = M >> 2, db = M & 3, S = U / 3, ne = (U + 1) % 3;
  mm_vvv<false>(acc[4 * S + db][qb], cur[db], bf[qb]);
  if constexpr (U < 5 && M < 8) {
    constexpr int d = M >> 1; const unsigned a = ring_rd<(ne + 1) % 3>(c, d) + ((U + 1) / 3) * 4096u;
    if constexpr (M & 1) nxt[d] = cat(lo[d], lds_tr1(a + K::TS)); else lo[d] = lds_tr1(a);
  }
  if constexpr (U < 5 && db == 3) bf[qb] = lds128(xa[ne] + qb * K::QS);
  if constexpr (PB && (U == 3 || U == 4) && M >= 8) pub_half<4 * (U - 3) + ((M - 8) >> 1), (M & 1)>(acc, p);
  if constexpr (PB && U == 5 && M >= 8) pub_half<8 + ((M - 8) >> 1), (M & 1)>(acc, p);
  GAP();
}
template <class K, int U, bool PB, int VM, int... Ms>
DI void tail_unit(f32x4 (&acc)[8][4], const bf16x8 (&cur)[4], bf16x8 (&nxt)[4], bf16x8 (&bf)[4], const PC& c, const unsigned (&xa)[3], Pub& p, iseq<Ms...>) {
  wait_vm_lgkm<VM, 2>();
  GAP();
  bf16x4 lo[4];
  (tail_gap<K, U, PB, Ms>(acc, cur, nxt, lo, bf, c, xa, p), ...);
}
template <int I> DI void own_put1(const u32x4& v, unsigned a) { asm volatile("ds_write_b128 %0, %1 offset:%2 ; %3" :: "v"(a), "v"(v), "i"((I & 7) * 1024), "i"(I) : "memory"); }
template <int B, int... Is> DI void own_put(const Pub& p, unsigned a, iseq<Is...>) { (own_put1<B + Is>(p.pk[B + Is], a), ...); }
template <int... Is> DI void pub_rest(const f32x4 (&acc)[8][4], Pub& p, iseq<Is...>) { (pub_half<12 + (Is >> 1), (Is & 1)>(acc, p), ...); }
template <int... Is> DI void pub_slow(const f32x4 (&acc)[8][4], Pub& p, iseq<Is...>) { ((pub_half<Is, 0>(acc, p), pub_half<Is, 1>(acc, p), __builtin_amdgcn_s_sleep(PUB_SLEEP)), ...); }
DI void redo_head(const AC& ac, const char* __restrict__ Kp, const char* __restrict__ Vp, unsigned lane, unsigned ow) {
  const unsigned r16 = lane & 15u, g = lane >> 4;
#pragma unroll 1
  for (unsigned qb = 0; qb < 4; ++qb) {
    bf16x8 qq[4];
#pragma unroll
    for (int s = 0; s < 4; ++s) qq[s] = qb == 0 ? ac.q[0][s] : qb == 1 ? ac.q[1][s] : qb == 2 ? ac.q[2][s] : ac.q[3][s];
    f32x4 o[8];
#pragma unroll
    for (int d = 0; d < 8; ++d) o[d] = f32x4{};
    float m = -__builtin_inff(), l = 0.f;
#pragma unroll 1
    for (unsigned t = 0; t < 16; ++t) {
      f32x4 s0 = {}, s1 = {};
      const char* kp = Kp + (t * 32u + r16) * 256u + g * 16u;
#pragma unroll
      for (int s = 0; s < 4; ++s) {
        s0 = __builtin_amdgcn_mfma_f32_16x16x32_bf16(*(const bf16x8*)(kp + s * 64), qq[s], s0, 0, 0, 0);
        s1 = __builtin_amdgcn_mfma_f32_16x16x32_bf16(*(const bf16x8*)(kp + s * 64 + 4096), qq[s], s1, 0, 0, 0);
      }
      const float mn = fmaxf(m, row_max(s0, s1)), al = __builtin_amdgcn_exp2f(m - mn);
      m = mn;
#pragma unroll
      for (int j = 0; j < 4; ++j) { s0[j] = __builtin_amdgcn_exp2f(s0[j] - mn); s1[j] = __builtin_amdgcn_exp2f(s1[j] - mn); }
      l = l * al + ((s0[0] + s0[1]) + (s0[2] + s0[3])) + ((s1[0] + s1[1]) + (s1[2] + s1[3]));
      const bf16x8 pf = __builtin_bit_cast(bf16x8, pack2(s0, s1));
      const __bf16* vp = (const __bf16*)(Vp + (t * 32u + g * 4u) * 256u) + r16;
#pragma unroll
      for (int d = 0; d < 8; ++d) {
        bf16x8 vf;
#pragma unroll
        for (int j = 0; j < 8; ++j) vf[j] = vp[((j & 3) + (j >> 2) * 16) * 128 + d * 16];
        o[d] = __builtin_amdgcn_mfma_f32_16x16x32_bf16(vf, pf, o[d] * al, 0, 0, 0);
      }
    }
    const float inv = 1.0f / xlane<false>(l);
#pragma unroll
    for (int s = 0; s < 4; ++s) *(LAS u32x4*)(size_t)(ow + s * 4096u + qb * 1024u) = pack2(o[2 * s] * inv, o[2 * s + 1] * inv);
  }
}
template <bool isA, int J> DI void q_conv1(AC& ac, f32x4 (&acc)[8][4], Stash& st, const Src& sK, const Src& sV, unsigned rk, unsigned p3) {
  constexpr int s = J >> 2, qb = J & 3;
  if constexpr (isA) { stash_write1<(5 * J) / 2>(st); stash_write1<(5 * J) / 2 + 1>(st); if constexpr (J & 1) stash_write1<(5 * J) / 2 + 2>(st); }
  else {
    constexpr int p = 2 * (J & 1);
    const Src& src = s < 2 ? sK : sV;
    const unsigned so = ksoff(s == 0 ? 2 : s == 1 ? SK : s == 2 ? SV : SP, rk) + (J & 2) * 2048u;
    if constexpr (s == 0 || s == 3) {
      if constexpr (p == 0) asm volatile("s_mov_b32 m0, %0\n\ts_nop 0" :: "s"(p3 + (s == 3 ? SLOT : 0u) + (J & 2) * 2048u) : "memory");
      dma_p<KV_NT, p>(src.v[p], src.srd, so); dma_p<KV_NT, p + 1>(src.v[p + 1], src.srd, so);
    } else {
      asm volatile("buffer_load_dwordx4 %0, %1, %2, %3 offen offset:%4 " KVPOL : "=a"(st.p[2 * J - 8]) : "v"(src.v[p]), "s"(src.srd), "s"(so), "i"(1024 * p) : "memory");
      asm volatile("buffer_load_dwordx4 %0, %1, %2, %3 offen offset:%4 " KVPOL : "=a"(st.p[2 * J - 7]) : "v"(src.v[p + 1]), "s"(src.srd), "s"(so), "i"(1024 * p + 1024) : "memory");
    }
  }
  ac.q[qb][s] = __builtin_bit_cast(bf16x8, natural8(pack2(acc[2 * s][qb] * SC, acc[2 * s + 1][qb] * SC)));
  asm volatile("" : "+a"(ac.q[qb][s]));
  GAP();
}
template <bool isA, int... Js> DI void q_conv(AC& ac, f32x4 (&acc)[8][4], Stash& st, const Src& sK, const Src& sV, unsigned rk, unsigned p3, iseq<Js...>) { (q_conv1<isA, Js>(ac, acc, st, sK, sV, rk, p3), ...); }
DI void zero_o(AC& ac) {
#pragma unroll
  for (int i = 0; i < 8; ++i)
#pragma unroll
    for (int q = 0; q < 4; ++q) { ac.o[i][q] = f32x4{}; asm volatile("" : "+a"(ac.o[i][q])); }
}

DI unsigned vbid() {
  const unsigned i = blockIdx.x;
  return i;
}
template <bool isA>
DI void body(const char* __restrict__ X, const char* __restrict__ K, const char* __restrict__ V, const char* __restrict__ Wq, const char* __restrict__ Wo,
             const char* __restrict__ Wo2, srd_t code, char* __restrict__ part, char* __restrict__ out) {
  extern __shared__ __attribute__((aligned(16))) char lds[];
  STAMP(0);
  unsigned long long tnow;
  asm volatile("s_memrealtime %0\n\ts_waitcnt lgkmcnt(0)" : "=s"(tnow));
  const unsigned tlim = (unsigned)tnow + EASY_T;
  unsigned tid = threadIdx.x;
  PINV(tid);
  const unsigned lane = tid & 63u, w = rfl(tid >> 6);
  const unsigned bid = vbid(), xg = bid & 7u, rr = bid >> 3, h = xg * 4u + w;
  const unsigned b = rr & 15u;
  const unsigned r16 = lane & 15u, g = lane >> 4;
  const char* xp  = X + (size_t)b * 65536u + w * 16384u;
  const char* wqp = Wq + h * 256u;
  const char* wop = Wo + (size_t)xg * 524288u + w * 256u;
  const size_t kvo = isA ? (size_t)(b * 8u + xg) * 131072u : (size_t)(b * 32u + h) * 131072u;
  const unsigned rk = isA ? (RKA) : (RKB);
  typedef PK<512u, 4096u, 0x1300u, 0xF300u, 0xC300u, 128u, 32u * 8192u, true> KQ;
  typedef PK<512u, 4096u, 0x1300u, 0xF300u, 0xC300u, 4096u, 8192u, false> KT;
  typedef PK<2048u, 1024u, 0x1000u, 0xF000u, 0xF000u, 128u, 32u * 1024u, false> KO;
  asm volatile("" :: "v"(lds));
  PC pc;
  pc.r01 = 65536u + w * 16384u; pc.r2 = 131072u + w * 8192u;
  const unsigned zq = ((b & 3u) << 14) | ((b >> 2) << 8), co = 4u * w;
  const srd_t srdH = mk_srd(part + HOFF, 4096u + 256u * 128u);
  const unsigned tok_off = rr * 128u, flg_off = 4096u + rr * 1024u;
  const unsigned l8 = (lane & 7u) * 4u;
  unsigned entry_flags;
  asm volatile("buffer_load_dword %0, %1, %2, %3 offen sc1" : "=v"(entry_flags) : "v"(l8), "s"(srdH), "s"(flg_off + xg * 128u) : "memory");
  if constexpr (WARM_CODE && isA) { if (w == 3 && lane < WARM_CODE) asm volatile("buffer_load_dword %0, %1, %2, 0 offen" : "+v"(entry_flags) : "v"((b * WARM_CODE + lane) * 128u), "s"(code) : "memory"); }
  Src sQ, sX, sK, sV, sO;
  mk_src_w<false>(sQ, wqp, 511u * 8192u + 256u, 8192u, lane);
  mk_src_rows<false>(sX, xp, 16384u, 1024u, lane);
  const unsigned p0 = b & 3u, p1 = (p0 + 1u) & 3u, p2 = (p0 + 2u) & 3u, p3 = (p0 + 3u) & 3u, xw = w * 4096u;
  Stash st;
  unsigned dum0 = lane;
  if constexpr (isA && ORD == 0) {
    mk_src_rows<false>(sK, K + kvo, 131072u, 256u, lane);
    mk_src_rows<true>(sV, V + kvo, 131072u, 256u, lane);
    const unsigned to = (lane >> 1) * 1024u + (lane & 1u) * 128u + b * KO::CS;
    const char* wb = Wo2 + (size_t)xg * 524288u + w * 256u;
    if constexpr (EARLY_O == 1) asm volatile("global_load_dword %0, %1, %2\n\tglobal_load_dword %0, %1, %3" : "=&v"(dum0) : "v"(to), "s"(wop), "s"(wb) : "memory");
    stash_load(st, (w & 1u) ? sK.v[2] : sK.v[0], sK, sV, rk, w * 2048u, make_iseq<64>{});
    if constexpr (EARLY_O == 2) asm volatile("global_load_dword %0, %1, %2\n\tglobal_load_dword %0, %1, %3" : "=&v"(dum0) : "v"(to), "s"(wop), "s"(wb) : "memory");
  }
  if constexpr ((isA ? ILV_A : ILV_B) == 1) {
    dma_unit<TOUCH_Q>(sQ, q_soff(zq), pc.r2);
    dma_unit<W_NT>(sQ, q_soff(zq + 0x1300u), pc.r01);
    if constexpr (!(isA && ORD == 2)) dma_unit<X_NT>(sX, p0 * 256u, xw + p0 * 16384u);
    dma_unit<TOUCH_Q>(sQ, q_soff(zq) + KQ::US, pc.r2 + 4096u);
    dma_unit<W_NT>(sQ, q_soff(zq + 0x1300u) + KQ::US, pc.r01 + 4096u);
  } else {
    dma_elem<(isA ? ILV_A : ILV_B) == 2 ? 0 : TOUCH_Q>(sQ, q_soff(zq), KQ::US, pc.r2);
    if constexpr (!(isA && ORD == 2)) dma_unit<X_NT>(sX, p0 * 256u, xw + p0 * 16384u);
    dma_elem<W_NT>(sQ, q_soff(zq + 0x1300u), KQ::US, pc.r01);
  }
  if constexpr (!(isA && ORD == 0)) {
    mk_src_rows<false>(sK, K + kvo, 131072u, 256u, lane);
    mk_src_rows<true>(sV, V + kvo, 131072u, 256u, lane);
  }
  mk_src_w<true>(sO, wop, 511u * 1024u + 256u, 1024u, lane);
  if constexpr (isA) {
    if constexpr (ORD == 2) {
      if constexpr (DELAY_A > 0) __builtin_amdgcn_s_sleep(DELAY_A);
      stash_load(st, (w & 1u) ? sK.v[2] : sK.v[0], sK, sV, rk, w * 2048u, make_iseq<64>{});
      dma_unit<X_NT>(sX, p0 * 256u, xw + p0 * 16384u);
    }
    dma_elem<W_NT>(sQ, q_soff(zq + 0x2200u), KQ::US, pc.r01 + SLOT);
    dma_unit<X_NT>(sX, p1 * 256u, xw + p1 * 16384u);
    dma_unit<X_NT>(sX, p2 * 256u, xw + p2 * 16384u);
    dma_unit<X_NT>(sX, p3 * 256u, xw + p3 * 16384u);
    if constexpr (ORD == 1) stash_load(st, (w & 1u) ? sK.v[2] : sK.v[0], sK, sV, rk, w * 2048u, make_iseq<64>{});
  }
  const unsigned rb = 1024u * (r16 >> 2) + 64u * (r16 & 3u) + 16u * (g ^ ((r16 >> 2) & 2u));
  const unsigned tb = 64u * ((lane >> 2) & 3u) + 32u * (g & 1u) + 8u * (lane & 3u);
  const unsigned tbV = tb + 1024u * g;
  const unsigned sb0 = w * 2048u + lane * 16u;
  st.base[0] = sb0; st.base[1] = sb0 + 65536u; st.base[2] = sb0 + 131072u;
  AC ac;
  if constexpr (!isA) zero_o(ac);
  {
    f32x4 acc[8][4];
    bf16x8 a0[4], a1[4], bf[4];
    asm volatile("s_waitcnt vmcnt(%2)" : "+v"(entry_flags), "+v"(dum0) : "i"(isA ? (ORD == 1 ? 63 : ORD == 0 ? 28 : 20) : 8) : "memory");
    if (w == 0) {
      const unsigned long long mk = __builtin_amdgcn_ballot_w64(((entry_flags >> (lane & 7u)) & 1u) != 0u);
      const unsigned tok = 0x5a5a5a00u | (~(unsigned)mk & 0xffu);
      if (lane == 0) asm volatile("buffer_store_dword %0, off, %1, %2 sc1" :: "v"(tok), "s"(srdH), "s"(tok_off + xg * 4u) : "memory");
    }
    barrier();
    STAMP(1);
    proj_start<KQ, 2>(a0, bf, pc, tbV, rb, zq, q_soff(zq + 0x3100u));
    if constexpr (isA) {
      constexpr int V1 = ORD == 0 ? 28 : 63, V2 = ORD == 1 ? 63 : 28;
      proj_elem<KQ, 2, true,  false, true, W_NT, 63, V1>(acc, a0, a1, bf, pc, sQ);
      STAMP(10);
      proj_elem<KQ, 0, false, false, true, W_NT, V1, V2>(acc, a0, a1, bf, pc, sQ);
      STAMP(11);
      proj_elem<KQ, 1, false, false, true, W_NT, V2, 16>(acc, a0, a1, bf, pc, sQ);
      proj_elem<KQ, 2, false, false, true, W_NT, 16, 16, ORD != 1>(acc, a0, a1, bf, pc, sQ);
    } else {
      dma_elem<W_NT>(sQ, q_soff(zq + 0x2200u), KQ::US, pc.r01 + SLOT);
      proj_elem<KQ, 2, true,  false, true, W_NT, (ILV_B == 1 ? 12 : 16), 20, false, true, true>(acc, a0, a1, bf, pc, sQ, NoX{}, &sX, p1 * 256u, xw + p1 * 16384u, p2 * 256u, xw + p2 * 16384u);
      STAMP(10);
      proj_elem<KQ, 0, false, false, true, W_NT, 24, 28, false, true, false>(acc, a0, a1, bf, pc, sQ, NoX{}, &sX, p3 * 256u, xw + p3 * 16384u);
      STAMP(11);
      proj_elem<KQ, 1, false, false, true, W_NT, 28, 24>(acc, a0, a1, bf, pc, sQ);
      proj_elem<KQ, 2, false, false, true, W_NT, 20, 16, true>(acc, a0, a1, bf, pc, sQ);
    }
    STAMP(8);
    typedef std::conditional_t<isA && (EASY > 0), EasyX<EASY>, NoX> PX;
    const PX px = [&] { if constexpr (isA && EASY > 0) return EasyX<EASY>{tnow, tlim}; else return NoX{}; }();
#pragma unroll 1
    for (unsigned e = 0; e < 3; ++e) {
      proj_elem<KQ, 0, false, false, true, W_NT, 16, 16>(acc, a0, a1, bf, pc, sQ, px);
      proj_elem<KQ, 1, false, false, true, W_NT, 16, 16>(acc, a0, a1, bf, pc, sQ, px);
      proj_elem<KQ, 2, false, false, true, W_NT, 16, 16>(acc, a0, a1, bf, pc, sQ, px);
    }
    STAMP(9);
    if constexpr (isA) {
      proj_elem<KQ, 0, false, false, false, W_NT, 16, 12>(acc, a0, a1, bf, pc, sQ, px);
      proj_elem<KQ, 1, false, false, false, W_NT, 8, 4>(acc, a0, a1, bf, pc, sQ, px);
      proj_elem<KQ, 2, false, true, false, W_NT, 0, 0>(acc, a0, a1, bf, pc, sQ, px);
    } else {
      pc.soff = ksoff(0u, rk);
      proj_elem<KT, 0, false, false, true, KV_NT, 16, 16>(acc, a0, a1, bf, pc, sK);
      proj_elem<KT, 1, false, false, true, KV_NT, 16, 16>(acc, a0, a1, bf, pc, sK);
      pc.soff = ksoff(0u, rk);
      proj_elem<KT, 2, false, true, true, KV_NT, 16, 16>(acc, a0, a1, bf, pc, sV);
    }
    barrier();
    STAMP(2);
    acc_fence_v(acc);
    q_conv<isA>(ac, acc, st, sK, sV, rk, w * 16384u, make_iseq<16>{});
  }
  ST SA, SB;
  if constexpr (isA) {
    zero_o(ac);
    const unsigned kb = rb, vb = SLOT + tbV;
    unsigned dum = lane, to = TOUCH_B == 4 ? (lane >> 4) * 1024u + (lane & 8u) * 16u + (lane & 7u) * 4u : (lane >> 3) * 1024u + (lane & 4u) * 32u + (lane & 3u) * 4u;
    PINV(to);
    const char* ta = wop + b * KO::CS;
    const char* tb2 = Wo2 + (size_t)xg * 524288u + w * 256u + b * KO::CS;
    if constexpr (TOUCH_B == 4) asm volatile("global_load_dword %0, %1, %2 nt\n\tglobal_load_dword %0, %1, %2\n\tv_add_u32_e32 %1, 0x1000, %1" : "+v"(dum), "+v"(to) : "s"(ta) : "memory");
    asm volatile("s_waitcnt lgkmcnt(0)" ::: "memory");
    barrier();
    k_read(ac, kb + kv_pair(0) * 16384u);
    wait_lgkm0();
    GAP();
    qk0<false>(ac, SA, sK, 0u, 0u, make_iseq<32>{});
    k_read(ac, kb + kv_pair(1) * 16384u);
    if constexpr (TOUCH_B == 4) asm volatile("global_load_dword %0, %1, %2 nt\n\tglobal_load_dword %0, %1, %2\n\tv_add_u32_e32 %1, 0x1000, %1" : "+v"(dum), "+v"(to) : "s"(ta) : "memory");
    sm_first(ac, SA);
    if constexpr (STAG > 0) { for (unsigned k = 0; k < (b & 7u); ++k) __builtin_amdgcn_s_sleep(STAG); }
    STAMP(13);
    unsigned i4 = 0;
    auto av = [&](PA& p) { if constexpr (PACE_ATT > 0) __builtin_amdgcn_s_sleep(PACE_ATT);
                           PINS(i4); p.rd0 = vb + (((unsigned)(kv_tab() >> i4) & 15u) << 14); p.rd1 = p.rd0 ^ 32u; PINV(p.rd0); PINV(p.rd1); };
    auto ak = [&](PA& p) { if constexpr (PACE_ATT > 0) __builtin_amdgcn_s_sleep(PACE_ATT);
                           if constexpr (RAMP > 0) asm volatile("s_cmp_ge_u32 %0, 16\n\ts_cbranch_scc1 .Lr_%=\n\ts_sleep %1\n\ts_cmp_ge_u32 %0, 8\n\ts_cbranch_scc1 .Lr_%=\n\ts_sleep %1\n.Lr_%=:" :: "s"(i4), "i"(RAMP) : "scc");
                           PINS(i4); p.rd0 = kb + (((unsigned)((kv_tab() >> 8) >> i4) & 15u) << 14); PINV(p.rd0); i4 += 4u; };
    auto an = [&](PA&) { i4 += 4u; };
    auto at = [&](PA& p) __attribute__((always_inline)) {
      PINS(i4); p.rd0 = vb + (((unsigned)(kv_tab() >> i4) & 15u) << 14); p.rd1 = p.rd0 ^ 32u; PINV(p.rd0); PINV(p.rd1);
      if constexpr (TOUCH_O) {
        const char* tp;
        if constexpr (TOUCH_B == 4) {
          asm volatile("s_cmp_lt_u32 %1, 24\n\ts_cselect_b64 %0, %2, %3" : "=s"(tp) : "s"(i4), "s"(ta), "s"(tb2 - 32768) : "scc");
          asm volatile("global_load_dword %0, %1, %2 nt\n\tglobal_load_dword %0, %1, %2\n\tv_add_u32_e32 %1, 0x1000, %1" : "+v"(dum), "+v"(to) : "s"(tp) : "memory");
        } else {
          asm volatile("s_bitcmp0_b32 %1, 4\n\ts_cselect_b64 %0, %2, %3" : "=s"(tp) : "s"(i4), "s"(ta), "s"(tb2) : "scc");
          asm volatile("global_load_dword %0, %1, %2 nt\n\tglobal_load_dword %0, %1, %2" : "+v"(dum) : "v"(to), "s"(tp) : "memory");
          to = (to + 8192u) & 32767u; PINV(to);
        }
      }
    };
#define ITS
    while (i4 < 24u) {
      ITS
      phaseA<true, false, KV_NT, 63>(ac, SA, SB, at, sK, 0u, st);
      phaseB<true, true, false, KV_NT, 63>(ac, SB, ak, sK, 0u, st);
      phaseA<true, false, KV_NT, 63>(ac, SB, SA, at, sK, 0u, st);
      phaseB<true, true, false, KV_NT, 63>(ac, SA, ak, sK, 0u, st);
    }
    ITS
    barrier();
    phaseA<true, false, KV_NT, 63, 40>(ac, SA, SB, at, sK, 0u, st);
    phaseB<true, true, false, KV_NT, 63, 46>(ac, SB, ak, sK, 0u, st);
    phaseA<true, false, KV_NT, 63, 52>(ac, SB, SA, at, sK, 0u, st);
    phaseB<true, true, false, KV_NT, 63, 58>(ac, SA, ak, sK, 0u, st);
    wait_vm_lgkm<63, 8>(); barrier();
    while (i4 < 56u) {
      ITS
      phaseA<true, false, KV_NT, 63>(ac, SA, SB, at, sK, 0u, st);
      phaseB<true, true, false, KV_NT, 63>(ac, SB, ak, sK, 0u, st);
      phaseA<true, false, KV_NT, 63>(ac, SB, SA, at, sK, 0u, st);
      phaseB<true, true, false, KV_NT, 63>(ac, SA, ak, sK, 0u, st);
    }
    STAMP(14);
    barrier();
    unsigned v14 = vb + kv_pair(14) * 16384u, x14 = v14 ^ 32u; PINV(v14); PINV(x14);
    phaseA<true, true, W_NT, 63>(ac, SA, SB, [&](PA& p) { p.soff = co * KO::CS; p.fill = pc.r01; p.rd0 = v14; p.rd1 = x14; }, sO, KO::US, st);
    phaseB<true, false, true, W_NT, 63>(ac, SB, [&](PA& p) { p.soff = (co + 1u) * KO::CS; p.fill = pc.r01 + SLOT; }, sO, KO::US, st);
    phaseA<false, false, W_NT, 63>(ac, SB, SA, [&](PA& p) { p.rd0 = v14 + 16384u; p.rd1 = x14 + 16384u; }, sO, 0u, st);
    dum0 = dum;
  } else {
    const unsigned P0 = pc.r01, P1 = pc.r01 + SLOT, P2 = pc.r2, P3 = w * 16384u;
    unsigned v0 = tbV + P0, v2 = tbV + P2, x0 = v0 ^ 32u, x2 = v2 ^ 32u, k1 = rb + P1, k3 = rb + P3;
    PINV(v0); PINV(v2); PINV(x0); PINV(x2); PINV(k1); PINV(k3);
    wait_vm<48>();
    k_read(ac, k1 - SLOT);
    wait_lgkm0();
    GAP();
    qk0<true>(ac, SA, sV, ksoff(1u, rk), P0, make_iseq<32>{});
    wait_vm<48>();
    GAP();
    k_read(ac, k1);
    sm_first(ac, SA);
    STAMP(13);
    BS bs{{P0, P1, P2, P3}, {v0, x0, v2, x2, tbV + P3, (tbV + P3) ^ 32u}, {k1, k3}, rk, co * KO::CS, lane * 16u};
    PINV(bs.v[4]); PINV(bs.v[5]);
#define BT(k)
#define B2(I) b_step<KO, I>(ac, SA, SB, bs, sK, sV, sO, st); b_step<KO, I + 1>(ac, SB, SA, bs, sK, sV, sO, st);
    B2(0) BT(0) B2(2) BT(1) B2(4) BT(2) B2(6) BT(3) B2(8) BT(4) B2(10)
    STAMP(14);
    B2(12) B2(14)
#undef B2
  }
  {
    const unsigned l16 = lane * 16u;
    unsigned ow = w * 16384u + l16;
    PINV(ow);
    f32x4 acc[8][4];
    bf16x8 a0[4], a1[4], b0[4], bf[4];
    float inv[4];
    proj_setup<KO>(pc, tb + 512u * g, l16, (co + 3u) << 12, (co + 3u) * KO::CS);
    unsigned toks;
    asm volatile("buffer_load_dword %0, %1, %2, %3 offen sc1" : "=v"(toks) : "v"(l8), "s"(srdH), "s"(tok_off) : "memory");
    GAP();
    phaseL<KO, (isA || LATEW ? 13 : 21), (!isA && LATEW)>(ac, inv, b0, a0, pc, sO, (co + 2u) * KO::CS, make_iseq<36>{});
    STAMP(3);
    if constexpr (isA) {
      barrier();
      dma_elem<W_NT>(sO, (co + 2u) * KO::CS, KO::US, pc.r2);
    }
    const bool ok = ac.l[0][0] <= LMAX && ac.l[1][0] <= LMAX && ac.l[2][0] <= LMAX && ac.l[3][0] <= LMAX;
    if (__builtin_expect(__builtin_amdgcn_ballot_w64(!ok) != 0ull, 0)) {
      asm volatile("s_waitcnt vmcnt(0)" : "+v"(toks) :: "memory");
      acc_fence_a(ac.o);
      redo_head(ac, K + kvo, V + kvo, lane, ow);
#pragma unroll
      for (int q = 0; q < 4; ++q) {
        inv[q] = 1.0f;
        b0[q] = lds128(ow + q * 1024u);
#pragma unroll
        for (int s = 1; s < 4; ++s) {
          const u32x4 t = __builtin_bit_cast(u32x4, lds128(ow + s * 4096u + q * 1024u));
          ac.o[2 * s][q] = f32x4{__uint_as_float(t[0] << 16), __uint_as_float(t[0] & 0xffff0000u), __uint_as_float(t[1] << 16), __uint_as_float(t[1] & 0xffff0000u)};
          ac.o[2 * s + 1][q] = f32x4{__uint_as_float(t[2] << 16), __uint_as_float(t[2] & 0xffff0000u), __uint_as_float(t[3] << 16), __uint_as_float(t[3] & 0xffff0000u)};
        }
      }
      wait_lgkm0();
      acc_fence_a(ac.o);
    }
#pragma unroll
    for (int q = 0; q < 4; ++q) bf[q] = bf16x8{};
    GAP();
    proj_elem<KO, 0, true, false, true, W_NT, 16, 16>(acc, a0, a1, b0, pc, sO, CvX<0>{ac, inv, b0, bf, ow});
    proj_elem<KO, 1, false, false, true, W_NT, 16, 16>(acc, a0, a1, bf, pc, sO, CvX<1>{ac, inv, bf, b0, ow});
    proj_elem<KO, 2, false, false, true, W_NT, 16, 16>(acc, a0, a1, b0, pc, sO, CvX<2>{ac, inv, b0, bf, ow});
    if constexpr (isA && TOUCH_B >= 3) PINV(dum0);
    proj_elem<KO, 0, false, false, true, W_NT, 16, 16, true>(acc, a0, a1, bf, pc, sO, CvX<3>{ac, inv, bf, b0, ow});
    STAMP(4);
    typedef std::conditional_t<isA && (EASY2 > 0), EasyX<EASY2>, NoX> PX;
    const PX px = [&] { if constexpr (isA && EASY2 > 0) return EasyX<EASY2>{tnow, tlim + EASY2_T - EASY_T}; else return NoX{}; }();
#pragma unroll 1
    for (unsigned e = 0; e < 3; ++e) {
      proj_elem<KO, 1, false, false, true, W_NT, 16, 16>(acc, a0, a1, bf, pc, sO, px);
      proj_elem<KO, 2, false, false, true, W_NT, 16, 16>(acc, a0, a1, bf, pc, sO, px);
      proj_elem<KO, 0, false, false, true, W_NT, 16, 16>(acc, a0, a1, bf, pc, sO, px);
    }
    CYC(6);
    Pub pb;
    pb.srd = mk_srd(part + POFF + (size_t)rr * 524288u, 524288u);
    pb.so = xg * 65536u + w * 16384u;
#pragma unroll
    for (unsigned i = 0; i < 4; ++i) { pb.v[i] = l16 + i * 4096u + (SKIPOWN && 2u * w + (i >> 1) == xg ? 0x80000000u : 0u); PINV(pb.v[i]); }
#pragma unroll
    for (int i = 0; i < 16; ++i) pb.pk[i] = u32x4{};
    unsigned xa[3];
#pragma unroll
    for (unsigned i = 0; i < 3; ++i) { xa[i] = (((co + 13u + i) & 15u) << 12) | l16; PINV(xa[i]); }
    constexpr bool PB = !(isA && PUB_SLEEP > 0);
    tail_unit<KO, 0, PB, 12>(acc, a0, a1, bf, pc, xa, pb, make_iseq<16>{});
    tail_unit<KO, 1, PB, 4>(acc, a1, a0, bf, pc, xa, pb, make_iseq<16>{});
    tail_unit<KO, 2, PB, 63>(acc, a0, a1, bf, pc, xa, pb, make_iseq<16>{});
    tail_unit<KO, 3, PB, 0>(acc, a1, a0, bf, pc, xa, pb, make_iseq<16>{});
    CYC(7);
    tail_unit<KO, 4, PB, 63>(acc, a0, a1, bf, pc, xa, pb, make_iseq<16>{});
    tail_unit<KO, 5, PB, 63>(acc, a1, a0, bf, pc, xa, pb, make_iseq<16>{});
    STAMP(5);
    if constexpr (PB) pub_rest(acc, pb, make_iseq<8>{}); else pub_slow(acc, pb, make_iseq<16>{});
    const unsigned lr = ((lane + xg) & 7u) * 4u;
    unsigned fl;
    asm volatile("buffer_load_dword %0, %1, %2, %3 offen sc1" : "=v"(fl) : "v"(lr), "s"(srdH), "s"(flg_off + xg * 128u) : "memory");
    PINV(toks);
    STAMP(6);
    if (w == (xg >> 1)) {
      const unsigned a = pc.r01 + SLOT + l16;
      if (xg & 1u) own_put<8>(pb, a, make_iseq<8>{}); else own_put<0>(pb, a, make_iseq<8>{});
    }
    const unsigned fo = lane * 128u + xg * 4u, tau = __builtin_amdgcn_readlane(toks, xg);
    unsigned oo = ((b * 64u + 16u * w + r16) * 1024u + (isA ? 0u : 512u) + 64u * xg + 8u * g) * 2u;
    PINV(oo);
    wait_lgkm0();
    barrier();
    const unsigned oa = 65536u + SLOT + (xg >> 1) * 16384u + w * 2048u + l16;
    const u32x4 r0 = __builtin_bit_cast(u32x4, lds128(oa)), r1 = __builtin_bit_cast(u32x4, lds128(oa + 1024u));
    GAP();
    wait_vm<9>();
    if (lane == 2u * w) asm volatile("buffer_store_dword %0, %1, %2, %3 offen " FLPOL :: "v"(toks), "v"(fo), "s"(srdH), "s"(flg_off) : "memory");
    wait_vm<2>();
    if (lane == 2u * w + 1u) asm volatile("buffer_store_dword %0, %1, %2, %3 offen " FLPOL :: "v"(toks), "v"(fo), "s"(srdH), "s"(flg_off) : "memory");
    f32x16 f[8];
#pragma unroll
    for (int j = 0; j < 8; ++j) { const unsigned t = j < 4 ? r0[j & 3] : r1[j & 3]; f[0][2 * j] = __uint_as_float(t << 16); f[0][2 * j + 1] = __uint_as_float(t & 0xffff0000u); }
    PINV(f[0]);
    asm volatile("s_waitcnt vmcnt(2)" : "+v"(fl) : "v"(pb.pk[0]), "v"(pb.pk[1]), "v"(pb.pk[2]), "v"(pb.pk[3]), "v"(pb.pk[4]), "v"(pb.pk[5]), "v"(pb.pk[6]), "v"(pb.pk[7]),
                 "v"(pb.pk[8]), "v"(pb.pk[9]), "v"(pb.pk[10]), "v"(pb.pk[11]), "v"(pb.pk[12]), "v"(pb.pk[13]), "v"(pb.pk[14]), "v"(pb.pk[15]) : "memory");
    STAMP(7);
    unsigned got = 0xffffff01u, spin = 1u << 21, fresh, so, f2;
#define POLL(R) "buffer_load_dword %[" R "], %[lr], %[sh], %[flg] offen " PLPOL "\n\t"
#define SEEN(R) "v_cmp_eq_u32_e32 vcc, %[tau], %[" R "]\n\ts_andn2_b32 %[fr], vcc_lo, %[got]\n\ts_cbranch_scc1 .Lproc_%=\n\t"
    asm volatile(
        SEEN("fl") POLL("fl") "s_sleep 4\n"
        ".Lidle_%=:\n\t" POLL("f2") "s_waitcnt vmcnt(1)\n\t" SEEN("fl") "s_sleep %[slp]\n\t" POLL("fl") "s_waitcnt vmcnt(1)\n\t" SEEN("f2")
        "s_sleep %[slp]\n\ts_sub_u32 %[spin], %[spin], 1\n\ts_cbranch_scc0 .Lidle_%=\n\ts_branch .Ldone_%=\n"
        ".Lproc_%=:\n\ts_or_b32 %[got], %[got], %[fr]\n\t"
        "s_bitcmp1_b32 %[fr], 1\n\ts_cbranch_scc0 .La1_%=\n\ts_add_u32 %[so], %[ro], 65536\n\ts_bitset0_b32 %[so], 19\n\t"
        "buffer_load_dwordx4 v[152:155], %[l16], %[sp], %[so] offen " LDPOL "\n\tbuffer_load_dwordx4 v[156:159], %[l16], %[sp], %[so] offen offset:1024 " LDPOL "\n.La1_%=:\n\t"
        "s_bitcmp1_b32 %[fr], 2\n\ts_cbranch_scc0 .La2_%=\n\ts_add_u32 %[so], %[ro], 131072\n\ts_bitset0_b32 %[so], 19\n\t"
        "buffer_load_dwordx4 v[168:171], %[l16], %[sp], %[so] offen " LDPOL "\n\tbuffer_load_dwordx4 v[172:175], %[l16], %[sp], %[so] offen offset:1024 " LDPOL "\n.La2_%=:\n\t"
        "s_bitcmp1_b32 %[fr], 3\n\ts_cbranch_scc0 .La3_%=\n\ts_add_u32 %[so], %[ro], 196608\n\ts_bitset0_b32 %[so], 19\n\t"
        "buffer_load_dwordx4 v[184:187], %[l16], %[sp], %[so] offen " LDPOL "\n\tbuffer_load_dwordx4 v[188:191], %[l16], %[sp], %[so] offen offset:1024 " LDPOL "\n.La3_%=:\n\t"
        "s_bitcmp1_b32 %[fr], 4\n\ts_cbranch_scc0 .La4_%=\n\ts_add_u32 %[so], %[ro], 262144\n\ts_bitset0_b32 %[so], 19\n\t"
        "buffer_load_dwordx4 v[200:203], %[l16], %[sp], %[so] offen " LDPOL "\n\tbuffer_load_dwordx4 v[204:207], %[l16], %[sp], %[so] offen offset:1024 " LDPOL "\n.La4_%=:\n\t"
        "s_bitcmp1_b32 %[fr], 5\n\ts_cbranch_scc0 .La5_%=\n\ts_add_u32 %[so], %[ro], 327680\n\ts_bitset0_b32 %[so], 19\n\t"
        "buffer_load_dwordx4 v[216:219], %[l16], %[sp], %[so] offen " LDPOL "\n\tbuffer_load_dwordx4 v[220:223], %[l16], %[sp], %[so] offen offset:1024 " LDPOL "\n.La5_%=:\n\t"
        "s_bitcmp1_b32 %[fr], 6\n\ts_cbranch_scc0 .La6_%=\n\ts_add_u32 %[so], %[ro], 393216\n\ts_bitset0_b32 %[so], 19\n\t"
        "buffer_load_dwordx4 v[232:235], %[l16], %[sp], %[so] offen " LDPOL "\n\tbuffer_load_dwordx4 v[236:239], %[l16], %[sp], %[so] offen offset:1024 " LDPOL "\n.La6_%=:\n\t"
        "s_bitcmp1_b32 %[fr], 7\n\ts_cbranch_scc0 .La7_%=\n\ts_add_u32 %[so], %[ro], 458752\n\ts_bitset0_b32 %[so], 19\n\t"
        "buffer_load_dwordx4 v[248:251], %[l16], %[sp], %[so] offen " LDPOL "\n\tbuffer_load_dwordx4 v[252:255], %[l16], %[sp], %[so] offen offset:1024 " LDPOL "\n.La7_%=:\n\t"
        "s_waitcnt vmcnt(0)\n\ts_cmp_eq_u32 %[got], -1\n\ts_cbranch_scc1 .Ltok_%=\n\t" POLL("fl") "s_branch .Lwide_%=\n"
        ".Ltok_%=:\n\tbuffer_load_dword %[fl], %[l8], %[sh], %[tok] offen sc1\n"
        ".Lwide_%=:\n\t"
        "s_bitcmp1_b32 %[fr], 1\n\ts_cbranch_scc0 .Lb1_%=\n\t"
        "v_lshlrev_b32_e32 v144, 16, v152\n\tv_and_b32_e32 v145, %[hi], v152\n\tv_lshlrev_b32_e32 v146, 16, v153\n\tv_and_b32_e32 v147, %[hi], v153\n\t"
        "v_lshlrev_b32_e32 v148, 16, v154\n\tv_and_b32_e32 v149, %[hi], v154\n\tv_lshlrev_b32_e32 v150, 16, v155\n\tv_and_b32_e32 v151, %[hi], v155\n\t"
        "v_lshlrev_b32_e32 v152, 16, v156\n\tv_and_b32_e32 v153, %[hi], v156\n\tv_lshlrev_b32_e32 v154, 16, v157\n\tv_and_b32_e32 v155, %[hi], v157\n\t"
        "v_lshlrev_b32_e32 v156, 16, v158\n\tv_and_b32_e32 v157, %[hi], v158\n\tv_lshlrev_b32_e32 v158, 16, v159\n\tv_and_b32_e32 v159, %[hi], v159\n\t"
        ".Lb1_%=:\n\t"
        "s_bitcmp1_b32 %[fr], 2\n\ts_cbranch_scc0 .Lb2_%=\n\t"
        "v_lshlrev_b32_e32 v160, 16, v168\n\tv_and_b32_e32 v161, %[hi], v168\n\tv_lshlrev_b32_e32 v162, 16, v169\n\tv_and_b32_e32 v163, %[hi], v169\n\t"
        "v_lshlrev_b32_e32 v164, 16, v170\n\tv_and_b32_e32 v165, %[hi], v170\n\tv_lshlrev_b32_e32 v166, 16, v171\n\tv_and_b32_e32 v167, %[hi], v171\n\t"
        "v_lshlrev_b32_e32 v168, 16, v172\n\tv_and_b32_e32 v169, %[hi], v172\n\tv_lshlrev_b32_e32 v170, 16, v173\n\tv_and_b32_e32 v171, %[hi], v173\n\t"
        "v_lshlrev_b32_e32 v172, 16, v174\n\tv_and_b32_e32 v173, %[hi], v174\n\tv_lshlrev_b32_e32 v174, 16, v175\n\tv_and_b32_e32 v175, %[hi], v175\n\t"
        ".Lb2_%=:\n\t"
        "s_bitcmp1_b32 %[fr], 3\n\ts_cbranch_scc0 .Lb3_%=\n\t"
        "v_lshlrev_b32_e32 v176, 16, v184\n\tv_and_b32_e32 v177, %[hi], v184\n\tv_lshlrev_b32_e32 v178, 16, v185\n\tv_and_b32_e32 v179, %[hi], v185\n\t"
        "v_lshlrev_b32_e32 v180, 16, v186\n\tv_and_b32_e32 v181, %[hi], v186\n\tv_lshlrev_b32_e32 v182, 16, v187\n\tv_and_b32_e32 v183, %[hi], v187\n\t"
        "v_lshlrev_b32_e32 v184, 16, v188\n\tv_and_b32_e32 v185, %[hi], v188\n\tv_lshlrev_b32_e32 v186, 16, v189\n\tv_and_b32_e32 v187, %[hi], v189\n\t"
        "v_lshlrev_b32_e32 v188, 16, v190\n\tv_and_b32_e32 v189, %[hi], v190\n\tv_lshlrev_b32_e32 v190, 16, v191\n\tv_and_b32_e32 v191, %[hi], v191\n\t"
        ".Lb3_%=:\n\t"
        "s_bitcmp1_b32 %[fr], 4\n\ts_cbranch_scc0 .Lb4_%=\n\t"
        "v_lshlrev_b32_e32 v192, 16, v200\n\tv_and_b32_e32 v193, %[hi], v200\n\tv_lshlrev_b32_e32 v194, 16, v201\n\tv_and_b32_e32 v195, %[hi], v201\n\t"
        "v_lshlrev_b32_e32 v196, 16, v202\n\tv_and_b32_e32 v197, %[hi], v202\n\tv_lshlrev_b32_e32 v198, 16, v203\n\tv_and_b32_e32 v199, %[hi], v203\n\t"
        "v_lshlrev_b32_e32 v200, 16, v204\n\tv_and_b32_e32 v201, %[hi], v204\n\tv_lshlrev_b32_e32 v202, 16, v205\n\tv_and_b32_e32 v203, %[hi], v205\n\t"
        "v_lshlrev_b32_e32 v204, 16, v206\n\tv_and_b32_e32 v205, %[hi], v206\n\tv_lshlrev_b32_e32 v206, 16, v207\n\tv_and_b32_e32 v207, %[hi], v207\n\t"
        ".Lb4_%=:\n\t"
        "s_bitcmp1_b32 %[fr], 5\n\ts_cbranch_scc0 .Lb5_%=\n\t"
        "v_lshlrev_b32_e32 v208, 16, v216\n\tv_and_b32_e32 v209, %[hi], v216\n\tv_lshlrev_b32_e32 v210, 16, v217\n\tv_and_b32_e32 v211, %[hi], v217\n\t"
        "v_lshlrev_b32_e32 v212, 16, v218\n\tv_and_b32_e32 v213, %[hi], v218\n\tv_lshlrev_b32_e32 v214, 16, v219\n\tv_and_b32_e32 v215, %[hi], v219\n\t"
        "v_lshlrev_b32_e32 v216, 16, v220\n\tv_and_b32_e32 v217, %[hi], v220\n\tv_lshlrev_b32_e32 v218, 16, v221\n\tv_and_b32_e32 v219, %[hi], v221\n\t"
        "v_lshlrev_b32_e32 v220, 16, v222\n\tv_and_b32_e32 v221, %[hi], v222\n\tv_lshlrev_b32_e32 v222, 16, v223\n\tv_and_b32_e32 v223, %[hi], v223\n\t"
        ".Lb5_%=:\n\t"
        "s_bitcmp1_b32 %[fr], 6\n\ts_cbranch_scc0 .Lb6_%=\n\t"
        "v_lshlrev_b32_e32 v224, 16, v232\n\tv_and_b32_e32 v225, %[hi], v232\n\tv_lshlrev_b32_e32 v226, 16, v233\n\tv_and_b32_e32 v227, %[hi], v233\n\t"
        "v_lshlrev_b32_e32 v228, 16, v234\n\tv_and_b32_e32 v229, %[hi], v234\n\tv_lshlrev_b32_e32 v230, 16, v235\n\tv_and_b32_e32 v231, %[hi], v235\n\t"
        "v_lshlrev_b32_e32 v232, 16, v236\n\tv_and_b32_e32 v233, %[hi], v236\n\tv_lshlrev_b32_e32 v234, 16, v237\n\tv_and_b32_e32 v235, %[hi], v237\n\t"
        "v_lshlrev_b32_e32 v236, 16, v238\n\tv_and_b32_e32 v237, %[hi], v238\n\tv_lshlrev_b32_e32 v238, 16, v239\n\tv_and_b32_e32 v239, %[hi], v239\n\t"
        ".Lb6_%=:\n\t"
        "s_bitcmp1_b32 %[fr], 7\n\ts_cbranch_scc0 .Lb7_%=\n\t"
        "v_lshlrev_b32_e32 v240, 16, v248\n\tv_and_b32_e32 v241, %[hi], v248\n\tv_lshlrev_b32_e32 v242, 16, v249\n\tv_and_b32_e32 v243, %[hi], v249\n\t"
        "v_lshlrev_b32_e32 v244, 16, v250\n\tv_and_b32_e32 v245, %[hi], v250\n\tv_lshlrev_b32_e32 v246, 16, v251\n\tv_and_b32_e32 v247, %[hi], v251\n\t"
        "v_lshlrev_b32_e32 v248, 16, v252\n\tv_and_b32_e32 v249, %[hi], v252\n\tv_lshlrev_b32_e32 v250, 16, v253\n\tv_and_b32_e32 v251, %[hi], v253\n\t"
        "v_lshlrev_b32_e32 v252, 16, v254\n\tv_and_b32_e32 v253, %[hi], v254\n\tv_lshlrev_b32_e32 v254, 16, v255\n\tv_and_b32_e32 v255, %[hi], v255\n\t"
        ".Lb7_%=:\n\t"
        "s_cmp_eq_u32 %[got], -1\n\ts_cbranch_scc0 .Lidle_%=\n"
        ".Ldone_%=:"
        : "=&{v[144:159]}"(f[1]), "=&{v[160:175]}"(f[2]), "=&{v[176:191]}"(f[3]), "=&{v[192:207]}"(f[4]), "=&{v[208:223]}"(f[5]), "=&{v[224:239]}"(f[6]), "=&{v[240:255]}"(f[7]),
          [fl] "+v"(fl), [f2] "=&v"(f2), [got] "+s"(got), [spin] "+s"(spin), [fr] "=&s"(fresh), [so] "=&s"(so)
        : [tau] "s"(tau), [l16] "v"(l16), [l8] "v"(l8), [lr] "v"(lr), [sp] "s"(pb.srd), [sh] "s"(srdH), [ro] "s"(xg * 73728u + w * 2048u), [tok] "s"(tok_off),
          [flg] "s"(flg_off + xg * 128u), [hi] "s"(0xffff0000u), [slp] "i"(isA ? SLEEP_A : SLEEP_B)
        : "vcc", "scc", "memory");
#undef POLL
#undef SEEN
    STAMP(15);
    const f32x16 t = ((f[0] + f[1]) + (f[2] + f[3])) + ((f[4] + f[5]) + (f[6] + f[7]));
#pragma unroll
    for (int s = 0; s < 2; ++s)
    {
      const u32x4 ov = natural8(u32x4{cvtpk(t[8 * s], t[8 * s + 1]), cvtpk(t[8 * s + 2], t[8 * s + 3]), cvtpk(t[8 * s + 4], t[8 * s + 5]), cvtpk(t[8 * s + 6], t[8 * s + 7])});
      __builtin_amdgcn_raw_buffer_store_b128(ov, mk_srd(out, 2097152u), oo + s * 64, 0, OUTPOL);
    }
    asm volatile("s_waitcnt vmcnt(2)" : "+v"(fl) :: "memory");
    if ((lane >> 1) == w && fl != toks) asm volatile("buffer_store_dword %0, %1, %2, %3 offen sc1" :: "v"(fl), "v"(fo), "s"(srdH), "s"(flg_off) : "memory");
  }
}

__global__ __launch_bounds__(256, 1)
void fused_attn(const char* __restrict__ Ax, const char* __restrict__ Ak, const char* __restrict__ Av, const char* __restrict__ Awq, const char* __restrict__ Awo,
                const char* __restrict__ Bx, const char* __restrict__ Bk, const char* __restrict__ Bv, const char* __restrict__ Bwq, const char* __restrict__ Bwo,
                char* __restrict__ part, char* __restrict__ out) {
  const char* pc; unsigned left;
  asm volatile("s_getpc_b64 %0\n.Lpc_%=:\n\ts_mov_b32 %1, .Lhot_end-.Lpc_%=" : "=s"(pc), "=s"(left));
  const srd_t code = mk_srd(pc, left);
  if (vbid() >= 128) { body<true>(Ax, Ak, Av, Awq, Awo, Bwo, code, part, out); asm volatile(".Lhot_end:"); __builtin_amdgcn_endpgm(); }
  body<false>(Bx, Bk, Bv, Bwq, Bwo, Bwo, code, part, out);
}

extern "C" void kernel_launch(void* const* d_in, const int* in_sizes, int n_in, void* d_out, int out_size, void* d_ws, size_t ws_size, hipStream_t stream) {
  (void)in_sizes; (void)n_in; (void)out_size; (void)ws_size;
  (void)hipFuncSetAttribute((const void*)fused_attn, hipFuncAttributeMaxDynamicSharedMemorySize, (int)LDS_BYTES);
  fused_attn<<<256, 256, LDS_BYTES, stream>>>((const char*)d_in[0], (const char*)d_in[1], (const char*)d_in[2], (const char*)d_in[3], (const char*)d_in[4],
                                              (const char*)d_in[5], (const char*)d_in[6], (const char*)d_in[7], (const char*)d_in[8], (const char*)d_in[9], (char*)d_ws, (char*)d_out);
}
```

```cpp
#include <hip/hip_runtime.h>
#include <hip/hip_bf16.h>
#include <utility>
#include <type_traits>

typedef __bf16 bf16x8 __attribute__((ext_vector_type(8)));
typedef __bf16 bf16x4 __attribute__((ext_vector_type(4)));
typedef __bf16 bf16x2 __attribute__((ext_vector_type(2)));
typedef float f32x4 __attribute__((ext_vector_type(4)));
typedef float f32x2 __attribute__((ext_vector_type(2)));
typedef float f32x16 __attribute__((ext_vector_type(16)));
typedef unsigned u32x4 __attribute__((ext_vector_type(4)));
#define LAS __attribute__((address_space(3)))
#define DI __device__ __forceinline__
typedef __amdgpu_buffer_rsrc_t srd_t;
template <int... Is> using iseq = std::integer_sequence<int, Is...>;
template <int N> using make_iseq = std::make_integer_sequence<int, N>;

constexpr int KV_NT = 1, W_NT = 0, X_NT = 0;
constexpr int SK = 4, SV = 4, SP = 5;
constexpr unsigned EASY_T = 1350, EASY2_T = 3100;
constexpr int EASY = 6, PACE_ATT = 1, EASY2 = 4;
constexpr unsigned WARM = 40;
constexpr int PGB = 9, AWSH = 100, BSHIFT = 0, ASHIFT = 0;

constexpr unsigned SLOT = 8192, LDS_BYTES = 20 * SLOT;
constexpr unsigned HOFF = 16u << 20;
constexpr float SC = 0.12751743082f;
constexpr float LMAX = 1048576.0f;

#define GAP() __builtin_amdgcn_sched_barrier(0)
#define PINS(x) asm volatile("" : "+s"(x))
#define PINV(x) asm volatile("" : "+v"(x))
DI unsigned rfl(unsigned x) { return __builtin_amdgcn_readfirstlane(x); }
DI unsigned cvtpk(float lo, float hi) { f32x2 v = {lo, hi}; bf16x2 b = __builtin_convertvector(v, bf16x2); return __builtin_bit_cast(unsigned, b); }
DI u32x4 pack2(const f32x4& a, const f32x4& b) { return u32x4{cvtpk(a[0], a[1]), cvtpk(a[2], a[3]), cvtpk(b[0], b[1]), cvtpk(b[2], b[3])}; }
DI u32x4 natural8(u32x4 w) {
  auto s0 = __builtin_amdgcn_permlane32_swap(w[0], w[2], false, false);
  auto s1 = __builtin_amdgcn_permlane32_swap(w[1], w[3], false, false);
  auto t0 = __builtin_amdgcn_permlane16_swap(s0[0], s0[1], false, false);
  auto t1 = __builtin_amdgcn_permlane16_swap(s1[0], s1[1], false, false);
  return u32x4{t0[0], t1[0], t0[1], t1[1]};
}
#define KSYM "_Z10fused_attnPKcS0_S0_S0_S0_S0_S0_S0_S0_S0_PcS1_"
#define PGL(c) ".if (((. - " KSYM ") >> 12) & 3) == " #c "\n777" #c ": s_cbranch_execz 777" #c "f\n.endif\n"
#define PG_DEFS ".set pgnext, 4096\n.macro pgstop\n.if (. - " KSYM ") >= pgnext\n.set pgnext, ((. - " KSYM ") | 4095) + 1\n" PGL(0) PGL(1) PGL(2) PGL(3) ".endif\n.endm\n" \
                ".macro mfp a:vararg\nv_mfma_f32_16x16x32_bf16 \\a\npgstop\n.endm\n"
#define PG_END "s_endpgm\n7770:\n7771:\n7772:\n7773:\n\ts_setpc_b64 vcc\n"
#define MF "mfp "
#define PGS "pgstop\n\t"
template <int SH = 0> DI void pg_walk(unsigned w) {
  asm volatile("s_mov_b64 exec, 0\n\ts_call_b64 vcc, .Lw1_%=\n\ts_branch .Lw2_%=\n.Lw1_%=:\n\ts_cmp_eq_u32 %0, 0\n\ts_cbranch_scc1 7770f\n\ts_cmp_eq_u32 %0, 1\n\ts_cbranch_scc1 7771f\n\t"
               "s_cmp_eq_u32 %0, 2\n\ts_cbranch_scc1 7772f\n\ts_branch 7773f\n.rept %1\n\ts_nop 0\n.endr\n.Lw2_%=:\n\ts_mov_b64 exec, -1" :: "s"(w), "i"(SH) : "vcc", "scc", "memory");
}
template <bool Z> DI void mm_vvv(f32x4& c, const bf16x8& a, const bf16x8& b) {
  if constexpr (Z) asm volatile(MF "%0, %1, %2, 0" : "=&v"(c) : "v"(a), "v"(b));
  else             asm volatile(MF "%0, %1, %2, %0" : "+v"(c) : "v"(a), "v"(b));
}
template <bool Z> DI void mm_vva(f32x4& c, const bf16x8& a, const bf16x8& b) {
  if constexpr (Z) asm volatile(MF "%0, %1, %2, 0" : "=&v"(c) : "v"(a), "a"(b));
  else             asm volatile(MF "%0, %1, %2, %0" : "+v"(c) : "v"(a), "a"(b));
}
DI void mm_vca(f32x4& d, const bf16x8& a, const bf16x8& b, const f32x4& c) { asm volatile(MF "%0, %1, %2, %3" : "=&v"(d) : "v"(a), "a"(b), "v"(c)); }
DI void mm_avv(f32x4& c, const bf16x8& a, const bf16x8& b) { asm volatile(MF "%0, %1, %2, %0" : "+a"(c) : "v"(a), "v"(b)); }
DI void set_m0(unsigned v) { asm volatile("s_mov_b32 m0, %0" :: "s"(v) : "memory"); }
template <int NT, int P> DI void dma_p(unsigned voff, srd_t srd, unsigned soff) {
  if constexpr (NT) asm volatile("buffer_load_dwordx4 %0, %1, %2 offen offset:%3 nt lds" :: "v"(voff), "s"(srd), "s"(soff), "i"(1024 * P) : "memory");
  else              asm volatile("buffer_load_dwordx4 %0, %1, %2 offen offset:%3 lds" :: "v"(voff), "s"(srd), "s"(soff), "i"(1024 * P) : "memory");
}
struct Src { unsigned v[4]; srd_t srd; };
template <int NT> DI void dma_unit(const Src& s, unsigned soff, unsigned ldsaddr) {
  asm volatile("s_mov_b32 m0, %0\n\ts_nop 0" :: "s"(ldsaddr) : "memory");
  dma_p<NT, 0>(s.v[0], s.srd, soff); dma_p<NT, 1>(s.v[1], s.srd, soff); dma_p<NT, 2>(s.v[2], s.srd, soff); dma_p<NT, 3>(s.v[3], s.srd, soff);
}
template <int NT> DI void dma_elem(const Src& s, unsigned soff, unsigned ustep, unsigned ldsaddr) { dma_unit<NT>(s, soff, ldsaddr); dma_unit<NT>(s, soff + ustep, ldsaddr + 4096u); }
template <int N> DI void wait_vm() { asm volatile("s_waitcnt vmcnt(%0)" :: "i"(N) : "memory"); }
template <int N, int L> DI void wait_vm_lgkm() { asm volatile("" ::: "memory"); __builtin_amdgcn_s_waitcnt((N & 15) | ((N >> 4) << 14) | 0x70 | (L << 8)); asm volatile("" ::: "memory"); }
DI void wait_lgkm0() { wait_vm_lgkm<63, 0>(); }
DI void barrier() { asm volatile("s_barrier" ::: "memory"); }
DI bf16x8 lds128(unsigned a) { return *(const LAS bf16x8*)(size_t)(a); }
DI bf16x4 lds_tr1(unsigned a) { return __builtin_amdgcn_ds_read_tr16_b64_v4bf16((LAS bf16x4*)(size_t)(a)); }
DI bf16x8 cat(bf16x4 lo, bf16x4 hi) { return __builtin_shufflevector(lo, hi, 0, 1, 2, 3, 4, 5, 6, 7); }
DI bf16x8 lds_tr(unsigned a0, unsigned a1) { return cat(lds_tr1(a0), lds_tr1(a1)); }
DI srd_t mk_srd(const void* base, unsigned bytes) { return __builtin_amdgcn_make_buffer_rsrc((void*)base, (short)0, (int)bytes, 0x00020000); }
template <bool VX> DI void mk_rows(Src& s, unsigned base, unsigned stride, unsigned lane) {
  const unsigned row = (lane >> 2) & 3u, cb = lane >> 4, sl = lane & 3u;
#pragma unroll
  for (int p = 0; p < 4; ++p) s.v[p] = base + row * stride + cb * 64u + ((sl ^ (VX ? 2 * (p & 1) : (p & 2))) << 4) + (4u * stride - 1024u) * p;
}
template <bool VX> DI void mk_w(Src& s, unsigned base, unsigned stride, unsigned lane) {
  const unsigned sub = lane >> 5, cb = (lane >> 4) & 1u, row = (lane >> 2) & 3u, sl = lane & 3u;
#pragma unroll
  for (int p = 0; p < 4; ++p) s.v[p] = base + (4u * sub + row) * stride + cb * 64u + ((sl ^ (VX ? 2u * sub : 2u * (p & 1))) << 4) + (8u * stride - 1024u) * p;
}
DI void touch(unsigned& d, unsigned& voff, const char* base) {
  asm volatile("global_load_dword %0, %1, %2 nt\n\tglobal_load_dword %0, %1, %2\n\tv_add_u32_e32 %1, 0x1000, %1" : "+v"(d), "+v"(voff) : "s"(base) : "memory");
}
struct Geo { int r0, r1, p0, p1, u, t0, t1, t2, t3; };
constexpr Geo GB = {8, 15, 16, 12, 11, 13, 14, 10, 9}, GA = {15, 8, 16, 9, 10, 14, 11, 12, 13};
constexpr unsigned KUS = 1u << GB.u;
template <bool isA> DI unsigned ksoffg(unsigned m, unsigned rk) {
  constexpr Geo G = isA ? GA : GB;
  const unsigned t = m + rk;
  return ((t & 1u) << G.t0) | (((t >> 1) & 1u) << G.t1) | (((t >> 2) & 1u) << G.t2) | (((t >> 3) & 1u) << G.t3);
}
DI unsigned ksoff(unsigned m, unsigned rk) { return ksoffg<false>(m, rk); }
template <bool isA, bool VX> DI void mk_src_kv(Src& s, const char* base, unsigned lane, unsigned w) {
  constexpr Geo G = isA ? GA : GB;
  s.srd = mk_srd(base, 131072u);
  const unsigned row = (lane >> 2) & 3u, sl = lane & 3u, o = ((row & 1u) << G.r0) + ((row >> 1) << G.r1) + (lane >> 4) * 64u;
#pragma unroll
  for (unsigned p = 0; p < (isA ? 2u : 4u); ++p)
    s.v[p] = o + ((sl ^ (VX ? 2u * (p & 1u) : isA ? 2u * (w & 1u) : (p & 2u))) << 4) + ((p & 1u) << G.p0) + ((p >> 1) << G.p1) - (isA ? 0u : 1024u * p);
}

struct PC {
  unsigned r01, r2;
  unsigned w01[2], w2[2];
  unsigned xb, xmask, xa;
  unsigned z, soff;
};
template <unsigned TS_, unsigned QS_, unsigned ZI_, unsigned ZM_, unsigned ZO_, unsigned US_, unsigned CS_, bool ROT_> struct PK {
  static constexpr unsigned TS = TS_, QS = QS_, ZI = ZI_, ZM = ZM_, ZO = ZO_, US = US_, CS = CS_; static constexpr bool ROT = ROT_; };
DI unsigned q_soff(unsigned z) { return ((z & 0xC300u) * 0x440u) & 0x3C0000u; }
template <int POS> DI unsigned ring_lds(const PC& c) { return POS == 2 ? c.r2 : c.r01 + POS * SLOT; }
template <int POS> DI unsigned ring_rd(const PC& c, int d) { return (POS == 2 ? c.w2[d & 1] : c.w01[d & 1] + POS * SLOT) + (d >> 1) * 256u; }
struct NoX { static constexpr bool ACT = true; template <int G> DI void run() const {} };
template <int SL, bool REV = false> struct EasyX {
  static constexpr bool ACT = true;
  unsigned long long& t; unsigned lim;
  template <int G> DI void run() const {
    if constexpr (G == 8) asm volatile("s_memrealtime %0" : "=s"(t));
    if constexpr (G == 16) PINS(t);
    if constexpr (G == 16) asm volatile("s_cmp_lt_i32 %0, 0\n\ts_cbranch_scc1 .Le_%=\n\ts_sleep %1\n.Le_%=:" :: "s"(REV ? lim - (unsigned)t : (unsigned)t - lim), "i"(SL) : "scc");
  }
};
template <class K, int POS, int S, bool Z, bool RD, bool DM, int NT, bool XT, int M, class X>
DI void proj_gap(f32x4 (&acc)[8][4], const bf16x8 (&cur)[4], bf16x8 (&nxt)[4], bf16x4 (&lo)[4], bf16x8 (&bf)[4], PC& c, unsigned soff, const Src& src, const Src& xs, unsigned xsoff, const X& ex) {
  constexpr int qb = M >> 2, db = M & 3;
  mm_vvv<Z>(acc[4 * S + db][qb], cur[db], bf[qb]);
  if constexpr (RD && M < 8) {
    constexpr int d = M >> 1; const unsigned a = S == 0 ? ring_rd<POS>(c, d) + 4096u : ring_rd<(POS + 1) % 3>(c, d);
    if constexpr (M & 1) nxt[d] = cat(lo[d], lds_tr1(a + K::TS)); else lo[d] = lds_tr1(a);
  }
  if constexpr (X::ACT && RD && S == 1 && db == 3) bf[qb] = lds128(c.xa + qb * K::QS);
  if constexpr (XT) {
    if constexpr (M < 8 && !(M & 1)) dma_p<X_NT, M / 2>(xs.v[M / 2], xs.srd, xsoff);
    if constexpr (M == 7) set_m0(ring_lds<POS>(c) + S * 4096u);
    if constexpr (M >= 8 && !(M & 1)) dma_p<NT, (M - 8) / 2>(src.v[(M - 8) / 2], src.srd, soff);
  } else if constexpr (DM && M >= 8 && !(M & 1)) dma_p<NT, (M - 8) / 2>(src.v[(M - 8) / 2], src.srd, soff);
  if constexpr (X::ACT && S == 0 && M == 9) { PINS(c.z); c.z = (c.z + K::ZI) & K::ZM; PINS(c.z); }
  if constexpr (X::ACT && S == 0 && M == 11) { c.xa = (c.z & c.xmask) | c.xb; PINV(c.xa); }
  if constexpr (S == 1 && DM && !K::ROT && M == 9) { PINS(c.soff); c.soff = (c.soff + K::CS) & (16u * K::CS - 1u); PINS(c.soff); }
  if constexpr (S == 1 && DM && K::ROT && M == 9) { PINS(c.z); c.soff = (c.z + 0x3100u) & 0xC300u; PINS(c.soff); }
  if constexpr (S == 1 && DM && K::ROT && M == 13) { PINS(c.soff); c.soff = (c.soff * 0x440u) & 0x3C0000u; PINS(c.soff); }
  ex.template run<16 * S + M>();
  GAP();
}
template <class K, int POS, int S, bool Z, bool RD, bool DM, int NT, int VM, bool BAR, bool XT, class X, int... Ms>
DI void proj_unit(f32x4 (&acc)[8][4], const bf16x8 (&cur)[4], bf16x8 (&nxt)[4], bf16x8 (&bf)[4], PC& c, const Src& src, const Src& xs, unsigned xsoff, unsigned xdst, const X& ex, iseq<Ms...>) {
  wait_vm_lgkm<(RD ? VM : 63), (S == 0 ? 2 : 0)>();
  if constexpr (BAR) barrier();
  const unsigned soff = c.soff + S * K::US;
  if constexpr (XT) set_m0(xdst); else if constexpr (DM) set_m0(ring_lds<POS>(c) + S * 4096u);
  GAP();
  bf16x4 lo[4];
  (proj_gap<K, POS, S, Z, RD, DM, NT, XT, Ms>(acc, cur, nxt, lo, bf, c, soff, src, xs, xsoff, ex), ...);
}
template <class K, int POS, bool Z, bool LAST, bool DM, int NT, int VM0, int VM1, bool BAR1 = false, bool XT0 = false, bool XT1 = false, class X = NoX>
DI void proj_elem(f32x4 (&acc)[8][4], bf16x8 (&a0)[4], bf16x8 (&a1)[4], bf16x8 (&bf)[4], PC& c, const Src& src, const X& ex = X{},
                  const Src* xs = nullptr, unsigned xso0 = 0, unsigned xd0 = 0, unsigned xso1 = 0, unsigned xd1 = 0) {
  proj_unit<K, POS, 0, Z, true, DM, NT, VM0, false, XT0>(acc, a0, a1, bf, c, src, xs ? *xs : src, xso0, xd0, ex, make_iseq<16>{});
  proj_unit<K, POS, 1, Z, !LAST, DM, NT, VM1, BAR1, XT1>(acc, a1, a0, bf, c, src, xs ? *xs : src, xso1, xd1, ex, make_iseq<16>{});
}
template <class K> DI void proj_setup(PC& c, unsigned tb, unsigned xb, unsigned z0, unsigned s3) {
  c.w01[0] = c.r01 + tb; c.w01[1] = c.w01[0] ^ 32u; c.w2[0] = c.r2 + tb; c.w2[1] = c.w2[0] ^ 32u;
  PINV(c.w01[0]); PINV(c.w01[1]); PINV(c.w2[0]); PINV(c.w2[1]);
  c.xb = xb; c.xmask = K::ZO; PINV(c.xmask);
  c.z = z0; c.soff = s3;
}
template <class K, int POS> DI void proj_start(bf16x8 (&a0)[4], bf16x8 (&bf)[4], PC& c, unsigned tb, unsigned xb, unsigned z0, unsigned s3) {
  proj_setup<K>(c, tb, xb, z0, s3);
  c.xa = (c.z & K::ZO) | xb; PINV(c.xa);
#pragma unroll
  for (int d = 0; d < 4; ++d) { const unsigned a = ring_rd<POS>(c, d); a0[d] = lds_tr(a, a + K::TS); }
  GAP();
#pragma unroll
  for (int q = 0; q < 4; ++q) bf[q] = lds128(c.xa + q * K::QS);
  GAP();
}
#define ACC_ALL(C, a) C(a[0][0]), C(a[0][1]), C(a[0][2]), C(a[0][3]), C(a[1][0]), C(a[1][1]), C(a[1][2]), C(a[1][3]), C(a[2][0]), C(a[2][1]), C(a[2][2]), C(a[2][3]), C(a[3][0]), C(a[3][1]), C(a[3][2]), C(a[3][3])
DI void acc_fence_v(f32x4 (&acc)[8][4]) {
  asm volatile("s_nop 7\n\ts_nop 3" : ACC_ALL("+v", acc) :: "memory");
  f32x4 (&hi)[4][4] = *(f32x4 (*)[4][4])&acc[4];
  asm volatile("" : ACC_ALL("+v", hi) :: "memory");
  GAP();
}
DI void acc_fence_a(f32x4 (&acc)[8][4]) {
  asm volatile("s_nop 7\n\ts_nop 3" : ACC_ALL("+a", acc) :: "memory");
  f32x4 (&hi)[4][4] = *(f32x4 (*)[4][4])&acc[4];
  asm volatile("" : ACC_ALL("+a", hi) :: "memory");
  GAP();
}

struct Stash { u32x4 p[64]; unsigned base[3]; };
template <int I> DI void stash_load1(Stash& st, const Src& sK, const Src& sV, unsigned& rk, unsigned wo_, unsigned (&sm)[16]) {
  constexpr int m = I >> 2, isV = (I >> 1) & 1, pc_ = I & 1;
  if constexpr (!isV && !pc_) { PINS(rk); sm[m] = ksoffg<true>(m, rk) + wo_; }
  const unsigned so = sm[m];
  if constexpr (I < 56) asm volatile("buffer_load_dwordx4 %0, %1, %2, %3 offen nt" : "=a"(st.p[I]) : "v"(isV ? sV.v[pc_] : sK.v[pc_]), "s"(isV ? sV.srd : sK.srd), "s"(so) : "memory");
  else                  asm volatile("buffer_load_dwordx4 %0, %1, %2, %3 offen nt" : "=v"(st.p[I]) : "v"(isV ? sV.v[pc_] : sK.v[pc_]), "s"(isV ? sV.srd : sK.srd), "s"(so) : "memory");
}
constexpr int stash_idx(int j) { return 4 * ((j >> 1) & 15) + 2 * (j >> 5) + (j & 1); }
template <int... Is> DI void stash_load(Stash& st, const Src& sK, const Src& sV, unsigned rk, unsigned wo_, iseq<Is...>) { unsigned sm[16]; (stash_load1<stash_idx(Is)>(st, sK, sV, rk, wo_, sm), ...); }
constexpr int kv_pair(int m) { constexpr int t[10] = {0, 1, 2, 3, 8, 9, 4, 5, 6, 7}; return t[m % 10]; }
constexpr unsigned long long kv_tab() { unsigned long long v = 0; for (int i = 0; i < 16; ++i) v |= (unsigned long long)kv_pair(i) << (4 * i); return v; }
template <int I> DI void stash_write1(const Stash& st) {
  constexpr int m = I >> 2, isV = (I >> 1) & 1, pc_ = I & 1, off = kv_pair(m) * 16384 + isV * 8192 + pc_ * 1024;
  if constexpr (I < 56) asm volatile("ds_write_b128 %0, %1 offset:%2" :: "v"(st.base[off >> 16]), "a"(st.p[I]), "i"(off & 65535) : "memory");
  else                  asm volatile("ds_write_b128 %0, %1 offset:%2" :: "v"(st.base[off >> 16]), "v"(st.p[I]), "i"(off & 65535) : "memory");
}

struct AC {
  f32x4 o[8][4];
  bf16x8 q[4][4];
  bf16x8 kf[8], vf[8], pf[4];
  f32x4 negm[4];
  f32x4 l[4];
  bf16x8 ones;
};
typedef f32x4 ST[2][4];
constexpr int NEB = 22;
template <int E> DI float sm_val(ST& S) { return S[(E >> 2) & 1][E >> 3][E & 3]; }
template <int E> DI void sm_exp1(ST& S) { float a; asm volatile("v_exp_f32_e32 %0, %1" : "=v"(a) : "v"(sm_val<E>(S))); S[(E >> 2) & 1][E >> 3][E & 3] = a; }
template <int QB, int P> DI void sm_cvt(ST& S, AC& c) {
  u32x4 w = __builtin_bit_cast(u32x4, c.pf[QB]);
  unsigned r;
  asm volatile("v_cvt_pk_bf16_f32 %0, %1, %2" : "=v"(r) : "v"(sm_val<8 * QB + 2 * P>(S)), "v"(sm_val<8 * QB + 2 * P + 1>(S)));
  w[P] = r;
  c.pf[QB] = __builtin_bit_cast(bf16x8, w);
}
template <bool DM, int VM, int R, int M> DI void mem_pre(unsigned ldsaddr) {
  if constexpr (M == 6) wait_lgkm0();
  if constexpr (DM && (M == 6 || M == 14)) set_m0(ldsaddr + (M == 14 ? 4096u : 0u));
  if constexpr (M == R && VM < 63) wait_vm<VM>();
}
template <bool DM, int NT, int M> DI void mem_post(const Src& src, unsigned soff, unsigned ustep) {
  if constexpr (DM && M >= 6 && M <= 20 && !(M & 1)) dma_p<NT, ((M - 6) / 2) & 3>(src.v[((M - 6) / 2) & 3], src.srd, soff + (M >= 14 ? ustep : 0u));
}
template <int WR, int M> DI void mem_wr(const Stash& st) {
  if constexpr (WR >= 40 && M >= 7 && M < 13) stash_write1<WR + M - 7>(st);
  if constexpr (WR >= 0 && WR < 40 && M >= 8 && M < 16)
    asm volatile("ds_write_b128 %0, %1 offset:%2" :: "v"(st.base[0]), "a"(st.p[WR + M - 8]), "i"((M - 8) * 1024) : "memory");
}
template <int N> using IC = std::integral_constant<int, N>;
struct PA { unsigned rd0, rd1, soff, fill; };
template <bool QK, bool DM, int NT, int VM, int WR, int M, class F>
DI void gapA(AC& c, ST& S, ST& Sn, PA& pa, F& addr, const Src& src, unsigned ustep, const Stash& st, bf16x4& lo) {
  constexpr int R = QK ? 16 : 0;
  if constexpr ((DM || !QK) && M == 0) addr(pa, IC<0>{});
  if constexpr (QK) mem_pre<DM, VM, 16, M>(pa.fill); else if constexpr (M == 0 && VM < 63) wait_vm<VM>();
  if constexpr (QK) {
    constexpr int f = M >> 2, s = f >> 1, kb = f & 1, qb = M & 3;
    if constexpr (s == 0) mm_vca(Sn[kb][qb], c.kf[f], c.q[qb][0], c.negm[qb]); else mm_vva<false>(Sn[kb][qb], c.kf[f], c.q[qb][s]);
  }
  mem_post<DM, NT, M>(src, pa.soff, ustep);
  mem_wr<WR, M>(st);
  if constexpr (QK && !DM && M >= 10 && M < 16) addr(pa, IC<M - 10>{});
  if constexpr (QK && M == 12) asm volatile("" :: "v"(c.negm[0]), "v"(c.negm[1]), "v"(c.negm[2]), "v"(c.negm[3]));
  if constexpr (M >= R && M < R + 16) {
    constexpr int db = (M - R) >> 1; const unsigned a = ((db & 1) ? pa.rd1 : pa.rd0) + (db >> 1) * 256u;
    if constexpr (M & 1) c.vf[db] = cat(lo, lds_tr1(a + 4096u)); else lo = lds_tr1(a);
  }
  if constexpr (NEB + M < 32) sm_exp1<NEB + M>(S);
  if constexpr (M >= 16 && (M & 1)) sm_cvt<2 + ((M - 16) >> 3), ((M - 16) >> 1) & 3>(S, c);
  GAP();
}
template <bool QK, bool DM, int NT, int VM, int WR, class F, int... Ms>
DI void phaseA_(AC& c, ST& S, ST& Sn, F& addr, const Src& src, unsigned ustep, const Stash& st, iseq<Ms...>) {
  PA pa; bf16x4 lo;
  (gapA<QK, DM, NT, VM, WR, Ms>(c, S, Sn, pa, addr, src, ustep, st, lo), ...);
}
template <bool QK, bool DM, int NT, int VM, int WR = -1, class F>
DI void phaseA(AC& c, ST& S, ST& Sn, F addr, const Src& src, unsigned ustep, const Stash& st) {
  phaseA_<QK, DM, NT, VM, WR>(c, S, Sn, addr, src, ustep, st, make_iseq<32>{});
}
template <bool MAX> DI float xlane(float v) {
  auto a = __builtin_amdgcn_permlane32_swap(__float_as_uint(v), __float_as_uint(v), false, false);
  v = MAX ? fmaxf(__uint_as_float(a[0]), __uint_as_float(a[1])) : __uint_as_float(a[0]) + __uint_as_float(a[1]);
  auto b = __builtin_amdgcn_permlane16_swap(__float_as_uint(v), __float_as_uint(v), false, false);
  return MAX ? fmaxf(__uint_as_float(b[0]), __uint_as_float(b[1])) : __uint_as_float(b[0]) + __uint_as_float(b[1]);
}
DI float row_max(const f32x4& x, const f32x4& y) {
  return xlane<true>(fmaxf(fmaxf(fmaxf(x[0], x[1]), fmaxf(x[2], x[3])), fmaxf(fmaxf(y[0], y[1]), fmaxf(y[2], y[3]))));
}
template <bool SM, bool KR, bool DM, int NT, int VM, int WR, int M, class F>
DI void gapB(AC& c, ST& Sn, PA& pa, F& addr, const Src& src, unsigned ustep, const Stash& st, bf16x4& lo) {
  mem_pre<DM, VM, (KR ? 18 : 20), M>(pa.fill);
  {
    constexpr int h = M / 18, r = M % 18, qb = 2 * h + (r & 1);
    if constexpr (r < 2) mm_vvv<false>(c.l[qb], c.ones, c.pf[qb]); else mm_avv(c.o[(r - 2) >> 1][qb], c.vf[(r - 2) >> 1], c.pf[qb]);
  }
  mem_post<DM, NT, M>(src, pa.soff, ustep);
  mem_wr<WR, M>(st);
  if constexpr (M < 4) addr(pa, IC<M>{});
  if constexpr (KR && M >= 18 && M < 34 && !(M & 1)) { constexpr int f = (M - 18) >> 1; c.kf[f] = lds128(pa.rd0 + (f & 1) * 4096u + (f >> 1) * 256u); }
  if constexpr (!KR && M >= 20) {
    constexpr int db = (M - 20) >> 1; const unsigned a = ((db & 1) ? pa.rd1 : pa.rd0) + (db >> 1) * 256u;
    if constexpr (M & 1) c.kf[db] = cat(lo, lds_tr1(a + 4096u)); else lo = lds_tr1(a);
  }
  if constexpr (SM) {
    if constexpr (M >= 4 && M < 18) sm_exp1<M - 4>(Sn);
    if constexpr (M >= 19 && M < 35 && (M & 1)) sm_exp1<14 + (M - 19) / 2>(Sn);
    if constexpr (M >= 20 && !(M & 1)) sm_cvt<((M - 20) >> 3), ((M - 20) >> 1) & 3>(Sn, c);
  }
  GAP();
}
template <bool SM, bool KR, bool DM, int NT, int VM, int WR, class F, int... Ms>
DI void phaseB_(AC& c, ST& Sn, F& addr, const Src& src, unsigned ustep, const Stash& st, iseq<Ms...>) {
  PA pa; bf16x4 lo;
  (gapB<SM, KR, DM, NT, VM, WR, Ms>(c, Sn, pa, addr, src, ustep, st, lo), ...);
}
template <bool SM, bool KR, bool DM, int NT, int VM, int WR = -1, class F>
DI void phaseB(AC& c, ST& Sn, F addr, const Src& src, unsigned ustep, const Stash& st) {
  phaseB_<SM, KR, DM, NT, VM, WR>(c, Sn, addr, src, ustep, st, make_iseq<36>{});
}
DI void k_read(AC& ac, unsigned ka) {
#pragma unroll
  for (int f = 0; f < 8; ++f) ac.kf[f] = lds128(ka + (f & 1) * 4096u + (f >> 1) * 256u);
}
template <bool DM, int M> DI void qk0_gap(AC& ac, ST& SA, const Src& src, unsigned soff, unsigned fill) {
  constexpr int f = M >> 2, qb = M & 3;
  if constexpr (DM && (M == 0 || M == 16)) set_m0(fill + M * 256u);
  mm_vva<(f < 2)>(SA[f & 1][qb], ac.kf[f], ac.q[qb][f >> 1]);
  if constexpr (DM && qb == 0) dma_p<KV_NT, f & 3>(src.v[f & 3], src.srd, soff + (f >> 2) * KUS);
  if constexpr (!DM && (M & 1)) { ac.o[4 + (M >> 3)][(M >> 1) & 3] = f32x4{}; asm volatile("" : "+a"(ac.o[4 + (M >> 3)][(M >> 1) & 3])); }
  GAP();
}
template <bool DM, int... Ms> DI void qk0(AC& ac, ST& SA, const Src& src, unsigned soff, unsigned fill, iseq<Ms...>) { (qk0_gap<DM, Ms>(ac, SA, src, soff, fill), ...); }
template <int... Js> DI void exp_first(ST& SA, iseq<Js...>) { (sm_exp1<Js>(SA), ...); }
template <int... Js> DI void cvt_first(ST& SA, AC& c, iseq<Js...>) { (sm_cvt<(Js >> 2), (Js & 3)>(SA, c), ...); }
DI void sm_first(AC& ac, ST& SA) {
  asm volatile("s_nop 7\n\ts_nop 3" : "+v"(SA[0][0]), "+v"(SA[0][1]), "+v"(SA[0][2]), "+v"(SA[0][3]), "+v"(SA[1][0]), "+v"(SA[1][1]), "+v"(SA[1][2]), "+v"(SA[1][3]) :: "memory");
  GAP();
#pragma unroll
  for (int q = 0; q < 4; ++q) {
    const float m = row_max(SA[0][q], SA[1][q]);
    SA[0][q] -= m; SA[1][q] -= m;
    ac.negm[q] = f32x4{-m, -m, -m, -m};
    ac.l[q] = f32x4{};
    ac.pf[q] = bf16x8{};
  }
  ac.ones = __builtin_bit_cast(bf16x8, u32x4{0x3f803f80u, 0x3f803f80u, 0x3f803f80u, 0x3f803f80u});
  asm volatile("" : "+v"(ac.negm[0]), "+v"(ac.negm[1]), "+v"(ac.negm[2]), "+v"(ac.negm[3]), "+v"(ac.l[0]), "+v"(ac.l[1]), "+v"(ac.l[2]), "+v"(ac.l[3]), "+v"(ac.ones));
  exp_first(SA, make_iseq<NEB>{});
  cvt_first(SA, ac, make_iseq<8>{});
  GAP();
}
struct BS { unsigned p[4], v[6], k[2], rk, wo, l16, w; };
constexpr bool b_dma(int f) { return f != 2 * (SK - 3) && f != 2 * (SV - 2) + 1 && f != 2 * (SP - 2) + 1 && f < 30 && f != 27; }
constexpr int b_vm(int f) {
  const int s = f - 3;
  if (!b_dma(s)) return 63;
  int n = b_dma(f) ? 5 + (f & 1) + (f == 29) : 0;
  for (int i = s + 1; i < f; ++i) n += b_dma(i) ? 8 : 0;
  return n;
}
template <class KO, int I> DI void b_step(AC& ac, ST& S, ST& Sn, BS& b, const Src& sK, const Src& sV, const Src& sO, Stash& st) {
  constexpr int fa = 2 * I, fb = 2 * I + 1, vi = I == SP ? 4 : (I & 1) ? 0 : 2;
  const unsigned pa = b.p[(I & 1) ? 3 : 1], pb = b.p[(I & 1) ? 0 : 2];
  if constexpr (I == PGB) pg_walk<BSHIFT>(b.w);
  if constexpr (I >= 1 && I <= 13) barrier();
  if constexpr (I == SK - 3) st.base[0] = pa + b.l16;
  phaseA<true, b_dma(fa), (I < 14 ? KV_NT : W_NT), b_vm(fa), (I == SK - 3 ? 0 : -1)>(ac, S, Sn, [&](PA& p, auto k) __attribute__((always_inline)) {
    if (k.value) return;
    PINS(b.rk);
    p.soff = I < 14 ? ksoff(I < 13 ? I + 3 : 15, b.rk) : b.wo; p.fill = I == 14 ? b.p[0] : pa;
    p.rd0 = b.v[vi] + (I == SP ? SLOT : 0u); p.rd1 = b.v[vi + 1] + (I == SP ? SLOT : 0u); }, I < 13 ? sK : I < 14 ? sV : sO, I < 14 ? KUS : KO::US, st);
  if constexpr (I == SV - 2) st.base[0] = pb + b.l16;
  phaseB<true, (I < 14), b_dma(fb), (I < 13 ? KV_NT : W_NT), b_vm(fb), (I == SV - 2 ? 8 : -1)>(ac, Sn, [&](PA& p, auto k) __attribute__((always_inline)) {
      if (k.value) return;
      PINS(b.rk);
      p.soff = I < 13 ? ksoff(I + 2, b.rk) : b.wo + KO::CS; p.fill = I == 14 ? b.p[1] : pb; p.rd0 = I == 14 ? b.v[4] : b.k[(I & 1) ? 0 : 1]; p.rd1 = b.v[5]; }, I < 13 ? sV : sO, I < 13 ? KUS : KO::US, st);
}
template <int CH, int Q, int J> DI void o_word(const AC& c, const float (&inv)[5], bf16x8 (&nb)[4]) {
  u32x4 w = __builtin_bit_cast(u32x4, nb[Q]);
  unsigned r;
  asm volatile("v_accvgpr_read_b32 %0, %1\n\tv_accvgpr_read_b32 %4, %2\n\tv_mul_f32_e32 %0, %0, %3\n\tv_mul_f32_e32 %4, %4, %3\n\tv_cvt_pk_bf16_f32 %0, %0, %4"
               : "=&v"(r) : "a"(c.o[2 * CH + (J >> 1)][Q][2 * (J & 1)]), "a"(c.o[2 * CH + (J >> 1)][Q][2 * (J & 1) + 1]), "v"(inv[Q]), "v"(inv[4]));
  w[J] = r;
  nb[Q] = __builtin_bit_cast(bf16x8, w);
}
template <int CH, int Q> DI void o_put(const bf16x8 (&nb)[4], unsigned ow) { asm volatile("ds_write_b128 %0, %1 offset:%2" :: "v"(ow), "v"(nb[Q]), "i"(CH * 4096 + Q * 1024) : "memory"); }
template <int E> struct CvX {
  static constexpr bool ACT = E == 3;
  const AC& c; const float (&inv)[5]; const bf16x8 (&cur)[4]; bf16x8 (&nb)[4]; unsigned ow;
  template <int G> DI void run() const {
    if constexpr (E < 3 && (G & 1)) o_word<E + 1, (G >> 3), (G >> 1) & 3>(c, inv, nb);
    if constexpr (G == 8 || G == 10) o_put<E, 2 + ((G >> 1) & 1)>(cur, ow);
    if constexpr (E < 3 && (G == 24 || G == 26)) o_put<E + 1, ((G >> 1) & 1)>(nb, ow);
  }
};
template <class K, int VM, int DM, int M> DI void gapE(AC& c, ST& S, float (&inv)[5], bf16x8 (&nb)[4], bf16x8 (&a0)[4], bf16x4& lo, const PC& pc, const Src& src, unsigned soff, unsigned long long& bad, unsigned ow) {
  constexpr int h = M / 18, r = M % 18, qb = 2 * h + (r & 1);
  if constexpr (M == 24) wait_vm<VM>();
  constexpr int D = M - (DM == 2 ? 4 : 0);
  if constexpr (M == 4) wait_lgkm0();
  if constexpr (M == 6 && DM == 2) barrier();
  if constexpr (DM && (D == 3 || D == 19)) set_m0(pc.r2 + (D == 19 ? 4096u : 0u));
  if constexpr (r < 2) mm_vvv<false>(c.l[qb], c.ones, c.pf[qb]); else mm_avv(c.o[(r - 2) >> 1][qb], c.kf[(r - 2) >> 1], c.pf[qb]);
  if constexpr (DM && D > 0 && D < 32 && (D & 3) == 3) dma_p<W_NT, (D >> 2) & 3>(src.v[(D >> 2) & 3], src.srd, soff + (D >= 16 ? K::US : 0u));
  if constexpr (M >= 24 && M < 32) {
    constexpr int d = (M - 24) >> 1; const unsigned a = ring_rd<0>(pc, d);
    if constexpr (M & 1) a0[d] = cat(lo, lds_tr1(a + K::TS)); else lo = lds_tr1(a);
  }
  if constexpr (NEB + M < 32) sm_exp1<NEB + M>(S);
  if constexpr (M >= 10 && M < 14) { sm_cvt<2 + ((M - 10) >> 1), 2 * (M & 1)>(S, c); sm_cvt<2 + ((M - 10) >> 1), 2 * (M & 1) + 1>(S, c); }
  if constexpr (M == 14 || M == 15 || M == 22 || M == 23) asm volatile("v_rcp_f32_e32 %0, %1" : "=v"(inv[qb]) : "v"(c.l[qb][0]));
  if constexpr (M >= 16 && M < 22) o_word<0, ((M - 16) >> 2), (M & 3)>(c, inv, nb);
  if constexpr (M >= 24 && M < 34) o_word<0, ((M - 18) >> 2), ((M - 18) & 3)>(c, inv, nb);
  if constexpr (M == 32) o_put<0, 0>(nb, ow);
  if constexpr (M == 33) o_put<0, 1>(nb, ow);
  if constexpr (r == 4) asm volatile("" :: "v"(c.l[2 * h]), "v"(c.l[2 * h + 1]));
  if constexpr (M == 34)
    asm volatile("v_add_f32_e32 %1, %2, %3\n\tv_add_f32_e32 %1, %1, %4\n\tv_add_f32_e32 %1, %1, %5\n\tv_cmp_nle_f32_e64 %0, %1, %6"
                 : "=s"(bad) : "v"(inv[4]), "v"(c.l[0][0]), "v"(c.l[1][0]), "v"(c.l[2][0]), "v"(c.l[3][0]), "s"(LMAX));
  GAP();
}
template <class K, int VM, int DM, int... Ms> DI void phaseE(AC& c, ST& S, float (&inv)[5], bf16x8 (&nb)[4], bf16x8 (&a0)[4], const PC& pc, const Src& src, unsigned soff, unsigned long long& bad, unsigned ow, iseq<Ms...>) {
  bf16x4 lo;
#pragma unroll
  for (int q = 0; q < 4; ++q) nb[q] = bf16x8{};
  (gapE<K, VM, DM, Ms>(c, S, inv, nb, a0, lo, pc, src, soff, bad, ow), ...);
}
struct Pub { u32x4 pk[16]; srd_t srd; unsigned v[4], so; };
struct TL { bf16x8 w[2][3][4], x[2][3][4]; bf16x4 lo[2]; unsigned xa[3], xs[3], drop[2], l16; };
template <int S, int QB, int J> DI void pub_word(const f32x4 (&acc)[8][4], Pub& p) {
  const f32x4& a = acc[4 * S + (J >> 1)][QB];
  unsigned r;
  asm volatile("v_cvt_pk_bf16_f32 %0, %1, %2" : "=v"(r) : "v"(a[2 * (J & 1)]), "v"(a[2 * (J & 1) + 1]));
  p.pk[8 * S + 2 * QB + (J >> 2)][J & 3] = r;
}
template <int F> DI void pub_store(const Pub& p) {
  asm volatile("buffer_store_dwordx4 %0, %1, %2, %3 offen offset:%4 sc0 sc1" :: "v"(p.pk[F]), "v"(p.v[F >> 2]), "s"(p.srd), "s"(p.so), "i"((F & 3) * 1024) : "memory");
}
template <class K, int S, int E, int R> DI void tl_w(TL& t, const PC& c) {
  const unsigned a = ring_rd<(E + 1) % 3>(c, R >> 1) + S * 4096u;
  if constexpr (R & 1) t.w[S][E][R >> 1] = cat(t.lo[S], lds_tr1(a + K::TS)); else t.lo[S] = lds_tr1(a);
}
template <class K, int G> DI void tl_gap(f32x4 (&acc)[8][4], TL& t, const PC& c, Pub& p) {
  constexpr int S = G >= 48, qb = G < 16 ? G >> 2 : G < 48 ? (G - 16) >> 3 : (G - 48) / 12, e = G < 16 ? 0 : G < 48 ? 1 + ((G >> 2) & 1) : G % 3, db = G < 48 ? G & 3 : (G % 12) / 3;
  if constexpr (G == 10) wait_vm<4>();
  if constexpr (G == 27) wait_vm<0>();
  mm_vvv<false>(acc[4 * S + db][qb], t.w[S][e][db], t.x[S][e][qb]);
  if constexpr (G >= 28 && G <= 56 && !(G & 3)) pub_store<(G - 28) / 4>(p);
  if constexpr (G >= 59 && G % 6 == 5) pub_store<8 + (G - 59) / 6>(p);
  if constexpr (G < 8) tl_w<K, 0, 1, G>(t, c);
  if constexpr (G >= 10 && G < 18) tl_w<K, 0, 2, G - 10>(t, c);
  if constexpr (G >= 16 && G < 24) tl_w<K, 1, 0, G - 16>(t, c);
  if constexpr (G >= 25 && G < 56 && (G & 1)) tl_w<K, 1, 1 + (((G - 25) >> 2) & 1), 2 * ((G - 25) >> 3) + (((G - 25) >> 1) & 1)>(t, c);
  if constexpr (G < 34 && (G & 3) == 2) t.x[0][1 + ((G >> 2) & 1)][G >> 3] = lds128(t.xa[1 + ((G >> 2) & 1)] + (G >> 3) * K::QS);
  if constexpr (G >= 34 && G < 82 && (G & 3) == 2) t.x[1][((G - 34) % 12) >> 2][(G - 34) / 12] = lds128(t.xa[((G - 34) % 12) >> 2] + ((G - 34) / 12) * K::QS);
  if constexpr (G >= 24 && G < 56) pub_word<0, ((G - 24) >> 3), (G & 7)>(acc, p);
  if constexpr (G >= 54 && G % 3 < 2) pub_word<1, (G - 54) / 12, 2 * (((G - 54) % 12) / 3) + G % 3>(acc, p);
  if constexpr (G == 0 || G == 1 || G == 3) { t.xa[(G + 1) & 3] = t.xs[(G + 1) & 3] | t.l16; PINV(t.xa[(G + 1) & 3]); }
  if constexpr (G == 4 || G == 5) { p.v[2 * (G & 1)] = t.drop[G & 1] | t.l16; PINV(p.v[2 * (G & 1)]); }
  if constexpr (G == 7 || G == 8) { p.v[2 * (G & 1) + 1] = p.v[2 * (G & 1)] | 4096u; PINV(p.v[2 * (G & 1) + 1]); }
  GAP();
}
template <class K, int... Gs> DI void tl_run(f32x4 (&acc)[8][4], TL& t, const PC& c, Pub& p, iseq<Gs...>) {
  wait_vm_lgkm<12, 2>();
  GAP();
  (tl_gap<K, Gs>(acc, t, c, p), ...);
  pub_word<1, 3, 4>(acc, p); pub_word<1, 3, 5>(acc, p);
  asm volatile("s_nop 4");
  pub_word<1, 3, 6>(acc, p); pub_word<1, 3, 7>(acc, p); pub_store<15>(p);
}
template <int I> DI void own_put1(const u32x4& v, unsigned a) { asm volatile("ds_write_b128 %0, %1 offset:%2 ; %3" :: "v"(a), "v"(v), "i"((I & 7) * 1024), "i"(I) : "memory"); }
template <int B, int... Is> DI void own_put(const Pub& p, unsigned a, iseq<Is...>) { (own_put1<B + Is>(p.pk[B + Is], a), ...); }
DI void redo_head(const AC& ac, const char* __restrict__ Kp, const char* __restrict__ Vp, unsigned lane, unsigned ow) {
  const unsigned r16 = lane & 15u, g = lane >> 4;
#pragma unroll 1
  for (unsigned qb = 0; qb < 4; ++qb) {
    bf16x8 qq[4];
#pragma unroll
    for (int s = 0; s < 4; ++s) qq[s] = qb == 0 ? ac.q[0][s] : qb == 1 ? ac.q[1][s] : qb == 2 ? ac.q[2][s] : ac.q[3][s];
    f32x4 o[8];
#pragma unroll
    for (int d = 0; d < 8; ++d) o[d] = f32x4{};
    float m = -__builtin_inff(), l = 0.f;
#pragma unroll 1
    for (unsigned t = 0; t < 16; ++t) {
      f32x4 s0 = {}, s1 = {};
      const char* kp = Kp + (t * 32u + r16) * 256u + g * 16u;
#pragma unroll
      for (int s = 0; s < 4; ++s) {
        s0 = __builtin_amdgcn_mfma_f32_16x16x32_bf16(*(const bf16x8*)(kp + s * 64), qq[s], s0, 0, 0, 0);
        s1 = __builtin_amdgcn_mfma_f32_16x16x32_bf16(*(const bf16x8*)(kp + s * 64 + 4096), qq[s], s1, 0, 0, 0);
      }
      const float mn = fmaxf(m, row_max(s0, s1)), al = __builtin_amdgcn_exp2f(m - mn);
      m = mn;
#pragma unroll
      for (int j = 0; j < 4; ++j) { s0[j] = __builtin_amdgcn_exp2f(s0[j] - mn); s1[j] = __builtin_amdgcn_exp2f(s1[j] - mn); }
      l = l * al + ((s0[0] + s0[1]) + (s0[2] + s0[3])) + ((s1[0] + s1[1]) + (s1[2] + s1[3]));
      const bf16x8 pf = __builtin_bit_cast(bf16x8, pack2(s0, s1));
      const __bf16* vp = (const __bf16*)(Vp + (t * 32u + g * 4u) * 256u) + r16;
#pragma unroll
      for (int d = 0; d < 8; ++d) {
        bf16x8 vf;
#pragma unroll
        for (int j = 0; j < 8; ++j) vf[j] = vp[((j & 3) + (j >> 2) * 16) * 128 + d * 16];
        o[d] = __builtin_amdgcn_mfma_f32_16x16x32_bf16(vf, pf, o[d] * al, 0, 0, 0);
      }
    }
    const float inv = 1.0f / xlane<false>(l);
#pragma unroll
    for (int s = 0; s < 4; ++s) *(LAS u32x4*)(size_t)(ow + s * 4096u + qb * 1024u) = pack2(o[2 * s] * inv, o[2 * s + 1] * inv);
  }
}
template <bool isA, int J> DI void q_conv1(AC& ac, f32x4 (&acc)[8][4], Stash& st, const Src& sK, const Src& sV, unsigned rk, unsigned p3) {
  constexpr int s = J >> 2, qb = J & 3;
  if constexpr (isA) { stash_write1<(5 * J) / 2>(st); stash_write1<(5 * J) / 2 + 1>(st); if constexpr (J & 1) stash_write1<(5 * J) / 2 + 2>(st); }
  else {
    constexpr int p = 2 * (J & 1);
    const Src& src = s < 2 ? sK : sV;
    const unsigned so = ksoff(s == 0 ? 2 : s == 1 ? SK : s == 2 ? SV : SP, rk) + ((J >> 1) & 1) * KUS;
    if constexpr (s == 0 || s == 3) {
      if constexpr (p == 0) asm volatile("s_mov_b32 m0, %0\n\ts_nop 0" :: "s"(p3 + (s == 3 ? SLOT : 0u) + (J & 2) * 2048u) : "memory");
      dma_p<KV_NT, p>(src.v[p], src.srd, so); dma_p<KV_NT, p + 1>(src.v[p + 1], src.srd, so);
    } else {
      asm volatile("buffer_load_dwordx4 %0, %1, %2, %3 offen offset:%4 nt" : "=a"(st.p[2 * J - 8]) : "v"(src.v[p]), "s"(src.srd), "s"(so), "i"(1024 * p) : "memory");
      asm volatile("buffer_load_dwordx4 %0, %1, %2, %3 offen offset:%4 nt" : "=a"(st.p[2 * J - 7]) : "v"(src.v[p + 1]), "s"(src.srd), "s"(so), "i"(1024 * p + 1024) : "memory");
    }
  }
  ac.q[qb][s] = __builtin_bit_cast(bf16x8, natural8(pack2(acc[2 * s][qb] * SC, acc[2 * s + 1][qb] * SC)));
  asm volatile("" : "+a"(ac.q[qb][s]));
  GAP();
}
template <bool isA, int... Js> DI void q_conv(AC& ac, f32x4 (&acc)[8][4], Stash& st, const Src& sK, const Src& sV, unsigned rk, unsigned p3, iseq<Js...>) { (q_conv1<isA, Js>(ac, acc, st, sK, sV, rk, p3), ...); }
template <int N> DI void zero_o(AC& ac) {
#pragma unroll
  for (int i = 0; i < N; ++i)
#pragma unroll
    for (int q = 0; q < 4; ++q) { ac.o[i][q] = f32x4{}; asm volatile("" : "+a"(ac.o[i][q])); }
}

template <bool isA>
DI void body(const char* __restrict__ X, const char* __restrict__ K, const char* __restrict__ V, const char* __restrict__ Wq, const char* __restrict__ Wo,
             const char* __restrict__ Wo2, unsigned long long tnow, char* __restrict__ part, char* __restrict__ out) {
  const unsigned tlim = (unsigned)tnow + EASY_T;
  unsigned tid = threadIdx.x;
  PINV(tid);
  const unsigned lane = tid & 63u, w = rfl(tid >> 6);
  const unsigned bid = blockIdx.x, xg = bid & 7u, rr = bid >> 3, b = rr & 15u, h = xg * 4u + w;
  const unsigned r16 = lane & 15u, g = lane >> 4;
  const size_t kvo = isA ? (size_t)(b * 8u + xg) * 131072u : (size_t)(b * 32u + h) * 131072u;
  const unsigned rk = isA ? 7u * b : 5u * b + 8u * w;
  typedef PK<512u, 4096u, 0x1300u, 0xF300u, 0xC300u, 128u, 32u * 8192u, true> KQ;
  typedef PK<512u, 4096u, 0x1300u, 0xF300u, 0xC300u, KUS, 8192u, false> KT;
  typedef PK<2048u, 1024u, 0x1000u, 0xF000u, 0xF000u, 128u, 32u * 1024u, false> KO;
  PC pc;
  pc.r01 = 65536u + w * 16384u; pc.r2 = 131072u + w * 8192u;
  const unsigned zq = ((b & 3u) << 14) | ((b >> 2) << 8), co = 4u * w;
  const srd_t srdH = mk_srd(part + HOFF, 4096u + 256u * 128u);
  const unsigned tok_off = rr * 128u, flg_off = 4096u + rr * 1024u;
  const unsigned l8 = (lane & 7u) * 4u;
  unsigned entry_flags, dum0 = lane;
  Src sQ, sX, sK, sV, sO;
  const unsigned p0 = b & 3u, p1 = (p0 + 1u) & 3u, p2 = (p0 + 2u) & 3u, p3 = (p0 + 3u) & 3u, xw = w * 4096u;
  Stash st;
  mk_src_kv<isA, false>(sK, K + kvo, lane, w);
  mk_src_kv<isA, true>(sV, V + kvo, lane, w);
  if constexpr (isA) { stash_load(st, sK, sV, rk, ((w & 1u) << GA.p1) + ((w >> 1) << GA.u), make_iseq<64>{}); GAP(); }
  asm volatile("buffer_load_dword %0, %1, %2, %3 offen sc1" : "=v"(entry_flags) : "v"(l8), "s"(srdH), "s"(flg_off + xg * 128u) : "memory");
  sQ.srd = mk_srd(Wq, 4194304u); sX.srd = mk_srd(X, 1048576u);
  mk_w<false>(sQ, h * 256u, 8192u, lane);
  mk_rows<false>(sX, b * 65536u + w * 16384u, 1024u, lane);
  sO.srd = mk_srd(Wo + (size_t)xg * 524288u, 524288u);
  mk_w<true>(sO, w * 256u, 1024u, lane);
  if constexpr (isA) {
    dma_unit<X_NT>(sX, p0 * 256u, xw + p0 * 16384u);
    dma_elem<W_NT>(sQ, q_soff(zq + 0x2200u), KQ::US, pc.r01 + SLOT);
    dma_unit<X_NT>(sX, p1 * 256u, xw + p1 * 16384u);
    dma_unit<X_NT>(sX, p2 * 256u, xw + p2 * 16384u);
    dma_unit<X_NT>(sX, p3 * 256u, xw + p3 * 16384u);
  }
  srd_t pb_srd = mk_srd(part + (size_t)rr * 524288u, 524288u);
  unsigned pb_so = xg * 65536u + w * 16384u, drop0 = 2u * w == xg ? 0x80000000u : 0u, drop1 = 2u * w + 1u == xg ? 0x80002000u : 0x2000u;
  unsigned xs0 = ((co + 13u) & 15u) << 12, xs1 = ((co + 14u) & 15u) << 12, xs2 = ((co + 15u) & 15u) << 12, oz = (co + 3u) << 12, os3 = (co + 3u) * KO::CS;
  unsigned ks0 = ksoff(0u, rk), ks1 = ksoff(1u, rk);
  {
    PINS(pb_srd); PINS(pb_so); PINS(drop0); PINS(drop1); PINS(xs0); PINS(xs1); PINS(xs2); PINS(oz); PINS(os3); PINS(sO.srd);
    if constexpr (!isA) {
      PINS(sK.srd); PINS(sV.srd); PINS(ks0); PINS(ks1);
#pragma unroll
      for (int i = 0; i < 4; ++i) { PINV(sK.v[i]); PINV(sV.v[i]); PINV(sO.v[i]); }
    }
  }
  const unsigned rb = 1024u * (r16 >> 2) + 64u * (r16 & 3u) + 16u * (g ^ ((r16 >> 2) & 2u));
  const unsigned tb = 64u * ((lane >> 2) & 3u) + 32u * (g & 1u) + 8u * (lane & 3u);
  const unsigned tbV = tb + 1024u * g;
  const unsigned sb0 = w * 2048u + lane * 16u;
  st.base[0] = sb0; st.base[1] = sb0 + 65536u; st.base[2] = sb0 + 131072u;
  AC ac;
  if constexpr (!isA) zero_o<8>(ac);
  {
    f32x4 acc[8][4];
    bf16x8 a0[4], a1[4], bf[4];
    if constexpr (isA) pg_walk<AWSH>(w);
    wait_vm<(isA ? 20 : 9)>();
    barrier();
    proj_start<KQ, 2>(a0, bf, pc, tbV, rb, zq, q_soff(zq + 0x3100u));
    if constexpr (isA) {
      proj_elem<KQ, 2, true,  false, true, W_NT, 63, 63>(acc, a0, a1, bf, pc, sQ);
      proj_elem<KQ, 0, false, false, true, W_NT, 63, 28>(acc, a0, a1, bf, pc, sQ);
      proj_elem<KQ, 1, false, false, true, W_NT, 28, 16>(acc, a0, a1, bf, pc, sQ);
      proj_elem<KQ, 2, false, false, true, W_NT, 16, 16, true>(acc, a0, a1, bf, pc, sQ);
    } else {
      dma_elem<W_NT>(sQ, q_soff(zq + 0x2200u), KQ::US, pc.r01 + SLOT);
      proj_elem<KQ, 2, true,  false, true, W_NT, 13, 20, false, true, true>(acc, a0, a1, bf, pc, sQ, NoX{}, &sX, p1 * 256u, xw + p1 * 16384u, p2 * 256u, xw + p2 * 16384u);
      proj_elem<KQ, 0, false, false, true, W_NT, 24, 28, false, true, false>(acc, a0, a1, bf, pc, sQ, NoX{}, &sX, p3 * 256u, xw + p3 * 16384u);
      proj_elem<KQ, 1, false, false, true, W_NT, 28, 24>(acc, a0, a1, bf, pc, sQ);
      proj_elem<KQ, 2, false, false, true, W_NT, 20, 16, true>(acc, a0, a1, bf, pc, sQ);
    }
    typedef std::conditional_t<isA, EasyX<EASY>, NoX> PX;
    const PX px = [&] { if constexpr (isA) return EasyX<EASY>{tnow, tlim}; else return NoX{}; }();
#pragma unroll 1
    for (unsigned e = 0; e < 3; ++e) {
      proj_elem<KQ, 0, false, false, true, W_NT, 16, 16>(acc, a0, a1, bf, pc, sQ, px);
      proj_elem<KQ, 1, false, false, true, W_NT, 16, 16>(acc, a0, a1, bf, pc, sQ, px);
      proj_elem<KQ, 2, false, false, true, W_NT, 16, 16>(acc, a0, a1, bf, pc, sQ, px);
    }
    if constexpr (isA) {
      proj_elem<KQ, 0, false, false, false, W_NT, 16, 12>(acc, a0, a1, bf, pc, sQ, px);
      proj_elem<KQ, 1, false, false, false, W_NT, 8, 4>(acc, a0, a1, bf, pc, sQ, px);
      proj_elem<KQ, 2, false, true, false, W_NT, 0, 0>(acc, a0, a1, bf, pc, sQ, px);
    } else {
      pc.soff = ks0;
      proj_elem<KT, 0, false, false, true, KV_NT, 16, 16>(acc, a0, a1, bf, pc, sK);
      pc.soff = ks1;
      proj_elem<KT, 1, false, false, true, KV_NT, 16, 16>(acc, a0, a1, bf, pc, sK);
      pc.soff = ks0;
      proj_elem<KT, 2, false, true, true, KV_NT, 16, 16>(acc, a0, a1, bf, pc, sV);
    }
    PINV(entry_flags);
    if (w == 0) {
      const unsigned long long mk = __builtin_amdgcn_ballot_w64(((entry_flags >> (lane & 7u)) & 1u) != 0u);
      const unsigned tok = 0x5a5a5a00u | (~(unsigned)mk & 0xffu);
      if (lane == 0) asm volatile("buffer_store_dword %0, off, %1, %2 sc1" :: "v"(tok), "s"(srdH), "s"(tok_off + xg * 4u) : "memory");
    }
    barrier();
    acc_fence_v(acc);
    q_conv<isA>(ac, acc, st, sK, sV, rk, w * 16384u, make_iseq<16>{});
  }
  ST SA, SB;
  if constexpr (isA) {
    zero_o<4>(ac);
    const unsigned kb = rb, vb = SLOT + tbV;
    unsigned dum = lane, to = ((lane & 8u) * 16u + (lane & 7u) * 4u + (lane >> 4) * 32768u) | (((lane >> 4) ^ w) << 8);
    const unsigned tbo = (b & 3u) * 1024u + (b >> 2) * 131072u;
    PINV(to);
    const char* ta = Wo + (size_t)xg * 524288u + tbo;
    const char* tb2 = Wo2 + (size_t)xg * 524288u + tbo;
    touch(dum, to, ta);
    asm volatile("s_waitcnt lgkmcnt(0)" ::: "memory");
    barrier();
    k_read(ac, kb + kv_pair(0) * 16384u);
    wait_lgkm0();
    GAP();
    qk0<false>(ac, SA, sK, 0u, 0u, make_iseq<32>{});
    k_read(ac, kb + kv_pair(1) * 16384u);
    touch(dum, to, ta);
    sm_first(ac, SA);
    unsigned i4 = 0;
    unsigned blk; const char* tp;
    auto ak = [&](PA& p, auto k) __attribute__((always_inline)) {
      if constexpr (k.value == 0) { __builtin_amdgcn_s_sleep(PACE_ATT); PINS(i4); blk = (unsigned)((kv_tab() >> 8) >> i4) << 14; i4 += 4u; PINS(blk); PINS(i4); }
      if constexpr (k.value == 1) { p.rd0 = kb + (blk & 0x3c000u); PINV(p.rd0); }
    };
    auto at = [&](PA& p, auto k) __attribute__((always_inline)) {
      if constexpr (k.value == 0) { PINS(i4); blk = (unsigned)(kv_tab() >> i4) << 14; PINS(blk); }
      if constexpr (k.value == 1) { p.rd0 = vb + (blk & 0x3c000u); PINV(p.rd0); }
      if constexpr (k.value == 2) asm volatile("s_cmp_lt_u32 %1, 24\n\ts_cselect_b64 %0, %2, %3" : "=s"(tp) : "s"(i4), "s"(ta), "s"(tb2 - 32768) : "scc");
      if constexpr (k.value == 3) { p.rd1 = p.rd0 ^ 32u; PINV(p.rd1); asm volatile("global_load_dword %0, %1, %2 nt" : "+v"(dum) : "v"(to), "s"(tp) : "memory"); }
      if constexpr (k.value == 4) asm volatile("global_load_dword %0, %1, %2\n\tv_add_u32_e32 %1, %3, %1" : "+v"(dum), "+v"(to) : "s"(tp), "i"(4096) : "memory");
    };
    while (i4 < 24u) {
      phaseA<true, false, KV_NT, 63>(ac, SA, SB, at, sK, 0u, st);
      phaseB<true, true, false, KV_NT, 63>(ac, SB, ak, sK, 0u, st);
      phaseA<true, false, KV_NT, 63>(ac, SB, SA, at, sK, 0u, st);
      phaseB<true, true, false, KV_NT, 63>(ac, SA, ak, sK, 0u, st);
    }
    barrier();
    phaseA<true, false, KV_NT, 63, 40>(ac, SA, SB, at, sK, 0u, st);
    phaseB<true, true, false, KV_NT, 63, 46>(ac, SB, ak, sK, 0u, st);
    phaseA<true, false, KV_NT, 63, 52>(ac, SB, SA, at, sK, 0u, st);
    phaseB<true, true, false, KV_NT, 63, 58>(ac, SA, ak, sK, 0u, st);
    wait_vm_lgkm<63, 8>(); barrier();
    while (i4 < 56u) {
      phaseA<true, false, KV_NT, 63>(ac, SA, SB, at, sK, 0u, st);
      phaseB<true, true, false, KV_NT, 63>(ac, SB, ak, sK, 0u, st);
      phaseA<true, false, KV_NT, 63>(ac, SB, SA, at, sK, 0u, st);
      phaseB<true, true, false, KV_NT, 63>(ac, SA, ak, sK, 0u, st);
    }
    barrier();
    unsigned v14 = vb + kv_pair(14) * 16384u, x14 = v14 ^ 32u; PINV(v14); PINV(x14);
    phaseA<true, true, W_NT, 63>(ac, SA, SB, [&](PA& p, auto) { p.soff = co * KO::CS; p.fill = pc.r01; p.rd0 = v14; p.rd1 = x14; }, sO, KO::US, st);
    phaseB<true, false, true, W_NT, 63>(ac, SB, [&](PA& p, auto k) __attribute__((always_inline)) {
      p.soff = (co + 1u) * KO::CS; p.fill = pc.r01 + SLOT; p.rd0 = v14 + 16384u; p.rd1 = x14 + 16384u;
      if constexpr (k.value == 1) proj_setup<KO>(pc, tb + 512u * g, lane * 16u, oz, os3); }, sO, KO::US, st);
    dum0 = dum;
  } else {
    const unsigned P0 = pc.r01, P1 = pc.r01 + SLOT, P2 = pc.r2, P3 = w * 16384u;
    unsigned v0 = tbV + P0, v2 = tbV + P2, x0 = v0 ^ 32u, x2 = v2 ^ 32u, k1 = rb + P1, k3 = rb + P3;
    PINV(v0); PINV(v2); PINV(x0); PINV(x2); PINV(k1); PINV(k3);
    wait_vm<48>();
    k_read(ac, k1 - SLOT);
    wait_lgkm0();
    GAP();
    qk0<true>(ac, SA, sV, ks1, P0, make_iseq<32>{});
    wait_vm<48>();
    GAP();
    k_read(ac, k1);
    sm_first(ac, SA);
    BS bs{{P0, P1, P2, P3}, {v0, x0, v2, x2, tbV + P3, (tbV + P3) ^ 32u}, {k1, k3}, rk, co * KO::CS, lane * 16u, w};
    PINV(bs.v[4]); PINV(bs.v[5]);
    proj_setup<KO>(pc, tb + 512u * g, lane * 16u, oz, os3);
#define B2(I) b_step<KO, I>(ac, SA, SB, bs, sK, sV, sO, st); b_step<KO, I + 1>(ac, SB, SA, bs, sK, sV, sO, st);
    B2(0) B2(2) B2(4) B2(6) B2(8) B2(10)
    B2(12) b_step<KO, 14>(ac, SA, SB, bs, sK, sV, sO, st);
#undef B2
  }
  {
    const unsigned l16 = lane * 16u;
    unsigned ow = w * 16384u + l16;
    PINV(ow);
    f32x4 acc[8][4];
    bf16x8 a0[4], a1[4], b0[4], bf[4];
    float inv[5];
    asm volatile("" : "=v"(inv[4]));
    unsigned toks;
    asm volatile("buffer_load_dword %0, %1, %2, %3 offen sc1" : "=v"(toks) : "v"(l8), "s"(srdH), "s"(tok_off) : "memory");
    GAP();
    unsigned long long bad;
    phaseE<KO, (isA ? 18 : 19), (isA ? 2 : 1)>(ac, SB, inv, b0, a0, pc, sO, os3 - KO::CS, bad, ow, make_iseq<36>{});
    if (__builtin_expect(bad != 0ull, 0)) {
      asm volatile("s_waitcnt vmcnt(0)" : "+v"(toks) :: "memory");
      acc_fence_a(ac.o);
      redo_head(ac, K + kvo, V + kvo, lane, ow);
#pragma unroll
      for (int q = 0; q < 4; ++q) {
        inv[q] = 1.0f;
        b0[q] = lds128(ow + q * 1024u);
#pragma unroll
        for (int s = 1; s < 4; ++s) {
          const u32x4 t = __builtin_bit_cast(u32x4, lds128(ow + s * 4096u + q * 1024u));
          ac.o[2 * s][q] = f32x4{__uint_as_float(t[0] << 16), __uint_as_float(t[0] & 0xffff0000u), __uint_as_float(t[1] << 16), __uint_as_float(t[1] & 0xffff0000u)};
          ac.o[2 * s + 1][q] = f32x4{__uint_as_float(t[2] << 16), __uint_as_float(t[2] & 0xffff0000u), __uint_as_float(t[3] << 16), __uint_as_float(t[3] & 0xffff0000u)};
        }
      }
      wait_lgkm0();
      acc_fence_a(ac.o);
    }
#pragma unroll
    for (int q = 0; q < 4; ++q) bf[q] = bf16x8{};
    GAP();
    proj_elem<KO, 0, true, false, true, W_NT, 16, 16>(acc, a0, a1, b0, pc, sO, CvX<0>{ac, inv, b0, bf, ow});
    proj_elem<KO, 1, false, false, true, W_NT, 16, 16>(acc, a0, a1, bf, pc, sO, CvX<1>{ac, inv, bf, b0, ow});
    proj_elem<KO, 2, false, false, true, W_NT, 16, 16>(acc, a0, a1, b0, pc, sO, CvX<2>{ac, inv, b0, bf, ow});
    if constexpr (isA) PINV(dum0);
    proj_elem<KO, 0, false, false, true, W_NT, 16, 16, true>(acc, a0, a1, bf, pc, sO, CvX<3>{ac, inv, bf, b0, ow});
    typedef std::conditional_t<isA, EasyX<EASY2, true>, NoX> PX;
    const PX px = [&] { if constexpr (isA) return EasyX<EASY2, true>{tnow, tlim + (EASY2_T - EASY_T)}; else return NoX{}; }();
#pragma unroll 1
    for (unsigned e = 0; e < 3; ++e) {
      proj_elem<KO, 1, false, false, true, W_NT, 16, 16>(acc, a0, a1, bf, pc, sO, px);
      proj_elem<KO, 2, false, false, true, W_NT, 16, 16>(acc, a0, a1, bf, pc, sO, px);
      proj_elem<KO, 0, false, false, true, W_NT, 16, 16>(acc, a0, a1, bf, pc, sO, px);
    }
    Pub pb;
    pb.srd = pb_srd; pb.so = pb_so;
#pragma unroll
    for (int i = 0; i < 16; ++i) pb.pk[i] = u32x4{};
    TL tl;
#pragma unroll
    for (int i = 0; i < 4; ++i) { tl.w[0][0][i] = a0[i]; tl.x[0][0][i] = bf[i]; }
    tl.xs[0] = xs0; tl.xs[1] = xs1; tl.xs[2] = xs2; tl.drop[0] = drop0; tl.drop[1] = drop1; tl.l16 = l16;
    tl_run<KO>(acc, tl, pc, pb, make_iseq<96>{});
    const unsigned lr = ((lane + xg) & 7u) * 4u;
    unsigned fl;
    asm volatile("buffer_load_dword %0, %1, %2, %3 offen sc1" : "=v"(fl) : "v"(lr), "s"(srdH), "s"(flg_off + xg * 128u) : "memory");
    PINV(toks);
    if (w == (xg >> 1)) {
      const unsigned a = pc.r01 + SLOT + l16;
      if (xg & 1u) own_put<8>(pb, a, make_iseq<8>{}); else own_put<0>(pb, a, make_iseq<8>{});
    }
    const unsigned fo = lane * 128u + xg * 4u, tau = __builtin_amdgcn_readlane(toks, xg);
    unsigned oo = ((b * 64u + 16u * w + r16) * 1024u + (isA ? 0u : 512u) + 64u * xg + 8u * g) * 2u;
    PINV(oo);
    wait_lgkm0();
    barrier();
    const unsigned oa = 65536u + SLOT + (xg >> 1) * 16384u + w * 2048u + l16;
    const u32x4 r0 = __builtin_bit_cast(u32x4, lds128(oa)), r1 = __builtin_bit_cast(u32x4, lds128(oa + 1024u));
    GAP();
    wait_vm<9>();
    if (lane == 2u * w) asm volatile("buffer_store_dword %0, %1, %2, %3 offen sc1" :: "v"(toks), "v"(fo), "s"(srdH), "s"(flg_off) : "memory");
    wait_vm<2>();
    if (lane == 2u * w + 1u) asm volatile("buffer_store_dword %0, %1, %2, %3 offen sc1" :: "v"(toks), "v"(fo), "s"(srdH), "s"(flg_off) : "memory");
    f32x16 f[8];
#pragma unroll
    for (int j = 0; j < 8; ++j) { const unsigned t = j < 4 ? r0[j & 3] : r1[j & 3]; f[0][2 * j] = __uint_as_float(t << 16); f[0][2 * j + 1] = __uint_as_float(t & 0xffff0000u); }
    PINV(f[0]);
    asm volatile("s_waitcnt vmcnt(2)" : "+v"(fl) : "v"(pb.pk[0]), "v"(pb.pk[1]), "v"(pb.pk[2]), "v"(pb.pk[3]), "v"(pb.pk[4]), "v"(pb.pk[5]), "v"(pb.pk[6]), "v"(pb.pk[7]),
                 "v"(pb.pk[8]), "v"(pb.pk[9]), "v"(pb.pk[10]), "v"(pb.pk[11]), "v"(pb.pk[12]), "v"(pb.pk[13]), "v"(pb.pk[14]), "v"(pb.pk[15]) : "memory");
    unsigned got = 0xffffff01u, spin = 1u << 21, fresh, so, f2;
#define POLL(R) "buffer_load_dword %[" R "], %[lr], %[sh], %[flg] offen sc1\n\t"
#define SEEN(R) "v_cmp_eq_u32_e32 vcc, %[tau], %[" R "]\n\ts_andn2_b32 %[fr], vcc_lo, %[got]\n\ts_cbranch_scc1 .Lproc_%=\n\t"
    asm volatile(
        SEEN("fl") POLL("fl") "s_sleep 4\n"
        ".Lidle_%=:\n\t" POLL("f2") "s_waitcnt vmcnt(1)\n\t" SEEN("fl") "s_sleep %[slp]\n\t" POLL("fl") "s_waitcnt vmcnt(1)\n\t" SEEN("f2")
        "s_sleep %[slp]\n\ts_sub_u32 %[spin], %[spin], 1\n\ts_cbranch_scc0 .Lidle_%=\n\ts_branch .Ldone_%=\n"
        ".Lproc_%=:\n\ts_or_b32 %[got], %[got], %[fr]\n\t"
        PGS "s_bitcmp1_b32 %[fr], 1\n\ts_cbranch_scc0 .La1_%=\n\ts_add_u32 %[so], %[ro], 65536\n\ts_bitset0_b32 %[so], 19\n\t"
        "buffer_load_dwordx4 v[152:155], %[l16], %[sp], %[so] offen sc1\n\tbuffer_load_dwordx4 v[156:159], %[l16], %[sp], %[so] offen offset:1024 sc1\n.La1_%=:\n\t"
        PGS "s_bitcmp1_b32 %[fr], 2\n\ts_cbranch_scc0 .La2_%=\n\ts_add_u32 %[so], %[ro], 131072\n\ts_bitset0_b32 %[so], 19\n\t"
        "buffer_load_dwordx4 v[168:171], %[l16], %[sp], %[so] offen sc1\n\tbuffer_load_dwordx4 v[172:175], %[l16], %[sp], %[so] offen offset:1024 sc1\n.La2_%=:\n\t"
        PGS "s_bitcmp1_b32 %[fr], 3\n\ts_cbranch_scc0 .La3_%=\n\ts_add_u32 %[so], %[ro], 196608\n\ts_bitset0_b32 %[so], 19\n\t"
        "buffer_load_dwordx4 v[184:187], %[l16], %[sp], %[so] offen sc1\n\tbuffer_load_dwordx4 v[188:191], %[l16], %[sp], %[so] offen offset:1024 sc1\n.La3_%=:\n\t"
        PGS "s_bitcmp1_b32 %[fr], 4\n\ts_cbranch_scc0 .La4_%=\n\ts_add_u32 %[so], %[ro], 262144\n\ts_bitset0_b32 %[so], 19\n\t"
        "buffer_load_dwordx4 v[200:203], %[l16], %[sp], %[so] offen sc1\n\tbuffer_load_dwordx4 v[204:207], %[l16], %[sp], %[so] offen offset:1024 sc1\n.La4_%=:\n\t"
        PGS "s_bitcmp1_b32 %[fr], 5\n\ts_cbranch_scc0 .La5_%=\n\ts_add_u32 %[so], %[ro], 327680\n\ts_bitset0_b32 %[so], 19\n\t"
        "buffer_load_dwordx4 v[216:219], %[l16], %[sp], %[so] offen sc1\n\tbuffer_load_dwordx4 v[220:223], %[l16], %[sp], %[so] offen offset:1024 sc1\n.La5_%=:\n\t"
        PGS "s_bitcmp1_b32 %[fr], 6\n\ts_cbranch_scc0 .La6_%=\n\ts_add_u32 %[so], %[ro], 393216\n\ts_bitset0_b32 %[so], 19\n\t"
        "buffer_load_dwordx4 v[232:235], %[l16], %[sp], %[so] offen sc1\n\tbuffer_load_dwordx4 v[236:239], %[l16], %[sp], %[so] offen offset:1024 sc1\n.La6_%=:\n\t"
        PGS "s_bitcmp1_b32 %[fr], 7\n\ts_cbranch_scc0 .La7_%=\n\ts_add_u32 %[so], %[ro], 458752\n\ts_bitset0_b32 %[so], 19\n\t"
        "buffer_load_dwordx4 v[248:251], %[l16], %[sp], %[so] offen sc1\n\tbuffer_load_dwordx4 v[252:255], %[l16], %[sp], %[so] offen offset:1024 sc1\n.La7_%=:\n\t"
        "s_waitcnt vmcnt(0)\n\ts_cmp_eq_u32 %[got], -1\n\ts_cbranch_scc1 .Ltok_%=\n\t" POLL("fl") "s_branch .Lwide_%=\n"
        ".Ltok_%=:\n\tbuffer_load_dword %[fl], %[l8], %[sh], %[tok] offen sc1\n"
        ".Lwide_%=:\n\t"
        "s_bitcmp1_b32 %[fr], 1\n\ts_cbranch_scc0 .Lb1_%=\n\t"
        PGS "v_lshlrev_b32_e32 v144, 16, v152\n\tv_and_b32_e32 v145, %[hi], v152\n\tv_lshlrev_b32_e32 v146, 16, v153\n\tv_and_b32_e32 v147, %[hi], v153\n\t"
        PGS "v_lshlrev_b32_e32 v148, 16, v154\n\tv_and_b32_e32 v149, %[hi], v154\n\tv_lshlrev_b32_e32 v150, 16, v155\n\tv_and_b32_e32 v151, %[hi], v155\n\t"
        PGS "v_lshlrev_b32_e32 v152, 16, v156\n\tv_and_b32_e32 v153, %[hi], v156\n\tv_lshlrev_b32_e32 v154, 16, v157\n\tv_and_b32_e32 v155, %[hi], v157\n\t"
        PGS "v_lshlrev_b32_e32 v156, 16, v158\n\tv_and_b32_e32 v157, %[hi], v158\n\tv_lshlrev_b32_e32 v158, 16, v159\n\tv_and_b32_e32 v159, %[hi], v159\n\t"
        ".Lb1_%=:\n\t"
        "s_bitcmp1_b32 %[fr], 2\n\ts_cbranch_scc0 .Lb2_%=\n\t"
        PGS "v_lshlrev_b32_e32 v160, 16, v168\n\tv_and_b32_e32 v161, %[hi], v168\n\tv_lshlrev_b32_e32 v162, 16, v169\n\tv_and_b32_e32 v163, %[hi], v169\n\t"
        PGS "v_lshlrev_b32_e32 v164, 16, v170\n\tv_and_b32_e32 v165, %[hi], v170\n\tv_lshlrev_b32_e32 v166, 16, v171\n\tv_and_b32_e32 v167, %[hi], v171\n\t"
        PGS "v_lshlrev_b32_e32 v168, 16, v172\n\tv_and_b32_e32 v169, %[hi], v172\n\tv_lshlrev_b32_e32 v170, 16, v173\n\tv_and_b32_e32 v171, %[hi], v173\n\t"
        PGS "v_lshlrev_b32_e32 v172, 16, v174\n\tv_and_b32_e32 v173, %[hi], v174\n\tv_lshlrev_b32_e32 v174, 16, v175\n\tv_and_b32_e32 v175, %[hi], v175\n\t"
        ".Lb2_%=:\n\t"
        "s_bitcmp1_b32 %[fr], 3\n\ts_cbranch_scc0 .Lb3_%=\n\t"
        PGS "v_lshlrev_b32_e32 v176, 16, v184\n\tv_and_b32_e32 v177, %[hi], v184\n\tv_lshlrev_b32_e32 v178, 16, v185\n\tv_and_b32_e32 v179, %[hi], v185\n\t"
        PGS "v_lshlrev_b32_e32 v180, 16, v186\n\tv_and_b32_e32 v181, %[hi], v186\n\tv_lshlrev_b32_e32 v182, 16, v187\n\tv_and_b32_e32 v183, %[hi], v187\n\t"
        PGS "v_lshlrev_b32_e32 v184, 16, v188\n\tv_and_b32_e32 v185, %[hi], v188\n\tv_lshlrev_b32_e32 v186, 16, v189\n\tv_and_b32_e32 v187, %[hi], v189\n\t"
        PGS "v_lshlrev_b32_e32 v188, 16, v190\n\tv_and_b32_e32 v189, %[hi], v190\n\tv_lshlrev_b32_e32 v190, 16, v191\n\tv_and_b32_e32 v191, %[hi], v191\n\t"
        ".Lb3_%=:\n\t"
        "s_bitcmp1_b32 %[fr], 4\n\ts_cbranch_scc0 .Lb4_%=\n\t"
        PGS "v_lshlrev_b32_e32 v192, 16, v200\n\tv_and_b32_e32 v193, %[hi], v200\n\tv_lshlrev_b32_e32 v194, 16, v201\n\tv_and_b32_e32 v195, %[hi], v201\n\t"
        PGS "v_lshlrev_b32_e32 v196, 16, v202\n\tv_and_b32_e32 v197, %[hi], v202\n\tv_lshlrev_b32_e32 v198, 16, v203\n\tv_and_b32_e32 v199, %[hi], v203\n\t"
        PGS "v_lshlrev_b32_e32 v200, 16, v204\n\tv_and_b32_e32 v201, %[hi], v204\n\tv_lshlrev_b32_e32 v202, 16, v205\n\tv_and_b32_e32 v203, %[hi], v205\n\t"
        PGS "v_lshlrev_b32_e32 v204, 16, v206\n\tv_and_b32_e32 v205, %[hi], v206\n\tv_lshlrev_b32_e32 v206, 16, v207\n\tv_and_b32_e32 v207, %[hi], v207\n\t"
        ".Lb4_%=:\n\t"
        "s_bitcmp1_b32 %[fr], 5\n\ts_cbranch_scc0 .Lb5_%=\n\t"
        PGS "v_lshlrev_b32_e32 v208, 16, v216\n\tv_and_b32_e32 v209, %[hi], v216\n\tv_lshlrev_b32_e32 v210, 16, v217\n\tv_and_b32_e32 v211, %[hi], v217\n\t"
        PGS "v_lshlrev_b32_e32 v212, 16, v218\n\tv_and_b32_e32 v213, %[hi], v218\n\tv_lshlrev_b32_e32 v214, 16, v219\n\tv_and_b32_e32 v215, %[hi], v219\n\t"
        PGS "v_lshlrev_b32_e32 v216, 16, v220\n\tv_and_b32_e32 v217, %[hi], v220\n\tv_lshlrev_b32_e32 v218, 16, v221\n\tv_and_b32_e32 v219, %[hi], v221\n\t"
        PGS "v_lshlrev_b32_e32 v220, 16, v222\n\tv_and_b32_e32 v221, %[hi], v222\n\tv_lshlrev_b32_e32 v222, 16, v223\n\tv_and_b32_e32 v223, %[hi], v223\n\t"
        ".Lb5_%=:\n\t"
        "s_bitcmp1_b32 %[fr], 6\n\ts_cbranch_scc0 .Lb6_%=\n\t"
        PGS "v_lshlrev_b32_e32 v224, 16, v232\n\tv_and_b32_e32 v225, %[hi], v232\n\tv_lshlrev_b32_e32 v226, 16, v233\n\tv_and_b32_e32 v227, %[hi], v233\n\t"
        PGS "v_lshlrev_b32_e32 v228, 16, v234\n\tv_and_b32_e32 v229, %[hi], v234\n\tv_lshlrev_b32_e32 v230, 16, v235\n\tv_and_b32_e32 v231, %[hi], v235\n\t"
        PGS "v_lshlrev_b32_e32 v232, 16, v236\n\tv_and_b32_e32 v233, %[hi], v236\n\tv_lshlrev_b32_e32 v234, 16, v237\n\tv_and_b32_e32 v235, %[hi], v237\n\t"
        PGS "v_lshlrev_b32_e32 v236, 16, v238\n\tv_and_b32_e32 v237, %[hi], v238\n\tv_lshlrev_b32_e32 v238, 16, v239\n\tv_and_b32_e32 v239, %[hi], v239\n\t"
        ".Lb6_%=:\n\t"
        "s_bitcmp1_b32 %[fr], 7\n\ts_cbranch_scc0 .Lb7_%=\n\t"
        PGS "v_lshlrev_b32_e32 v240, 16, v248\n\tv_and_b32_e32 v241, %[hi], v248\n\tv_lshlrev_b32_e32 v242, 16, v249\n\tv_and_b32_e32 v243, %[hi], v249\n\t"
        PGS "v_lshlrev_b32_e32 v244, 16, v250\n\tv_and_b32_e32 v245, %[hi], v250\n\tv_lshlrev_b32_e32 v246, 16, v251\n\tv_and_b32_e32 v247, %[hi], v251\n\t"
        PGS "v_lshlrev_b32_e32 v248, 16, v252\n\tv_and_b32_e32 v249, %[hi], v252\n\tv_lshlrev_b32_e32 v250, 16, v253\n\tv_and_b32_e32 v251, %[hi], v253\n\t"
        PGS "v_lshlrev_b32_e32 v252, 16, v254\n\tv_and_b32_e32 v253, %[hi], v254\n\tv_lshlrev_b32_e32 v254, 16, v255\n\tv_and_b32_e32 v255, %[hi], v255\n\t"
        ".Lb7_%=:\n\t"
        "s_cmp_eq_u32 %[got], -1\n\ts_cbranch_scc0 .Lidle_%=\n"
        ".Ldone_%=:"
        : "=&{v[144:159]}"(f[1]), "=&{v[160:175]}"(f[2]), "=&{v[176:191]}"(f[3]), "=&{v[192:207]}"(f[4]), "=&{v[208:223]}"(f[5]), "=&{v[224:239]}"(f[6]), "=&{v[240:255]}"(f[7]),
          [fl] "+v"(fl), [f2] "=&v"(f2), [got] "+s"(got), [spin] "+s"(spin), [fr] "=&s"(fresh), [so] "=&s"(so)
        : [tau] "s"(tau), [l16] "v"(l16), [l8] "v"(l8), [lr] "v"(lr), [sp] "s"(pb.srd), [sh] "s"(srdH), [ro] "s"(xg * 73728u + w * 2048u), [tok] "s"(tok_off),
          [flg] "s"(flg_off + xg * 128u), [hi] "s"(0xffff0000u), [slp] "i"(isA ? 8 : 0)
        : "vcc", "scc", "memory");
#undef POLL
#undef SEEN
    const f32x16 t = ((f[0] + f[1]) + (f[2] + f[3])) + ((f[4] + f[5]) + (f[6] + f[7]));
#pragma unroll
    for (int s = 0; s < 2; ++s)
    {
      const u32x4 ov = natural8(u32x4{cvtpk(t[8 * s], t[8 * s + 1]), cvtpk(t[8 * s + 2], t[8 * s + 3]), cvtpk(t[8 * s + 4], t[8 * s + 5]), cvtpk(t[8 * s + 6], t[8 * s + 7])});
      __builtin_amdgcn_raw_buffer_store_b128(ov, mk_srd(out, 2097152u), oo + s * 64, 0, 2);
    }
    asm volatile("s_waitcnt vmcnt(2)" : "+v"(fl) :: "memory");
    if ((lane >> 1) == w && fl != toks) asm volatile("buffer_store_dword %0, %1, %2, %3 offen sc1" :: "v"(fl), "v"(fo), "s"(srdH), "s"(flg_off) : "memory");
  }
}

__global__ __launch_bounds__(256, 1) __attribute__((aligned(4096)))
void fused_attn(const char* __restrict__ Ax, const char* __restrict__ Ak, const char* __restrict__ Av, const char* __restrict__ Awq, const char* __restrict__ Awo,
                const char* __restrict__ Bx, const char* __restrict__ Bk, const char* __restrict__ Bv, const char* __restrict__ Bwq, const char* __restrict__ Bwo,
                char* __restrict__ part, char* __restrict__ out) {
  extern __shared__ __attribute__((aligned(16))) char lds[];
  const unsigned bid = blockIdx.x;
  unsigned long long tnow;
  {
    const unsigned xg = bid & 7u, b = (bid >> 3) & 15u;
    const char *px, *pq, *pc; unsigned left;
    asm volatile(PG_DEFS "s_load_dwordx2 %0, %5, %6\n\ts_load_dwordx2 %1, %5, 0x28\n\ts_memrealtime %2\n\ts_getpc_b64 %3\n.Lpc_%=:\n\ts_mov_b32 %4, .Lhot_end-.Lpc_%="
                 : "=&s"(pq), "=&s"(px), "=&s"(tnow), "=&s"(pc), "=&s"(left) : "s"(__builtin_amdgcn_kernarg_segment_ptr()), "s"(bid >= 128u ? 0x18u : 0x40u));
    unsigned tid = threadIdx.x;
    PINV(tid);
    const unsigned lane = tid & 63u, w = rfl(tid >> 6), zq = ((b & 3u) << 14) | ((b >> 2) << 8);
    unsigned q0 = q_soff(zq), q1 = q_soff(zq + 0x1300u), r01 = 65536u + w * 16384u, r2 = 131072u + w * 8192u, xs = (b & 3u) * 256u, xd = w * 4096u + (b & 3u) * 16384u;
    asm volatile("s_mov_b32 m0, %2\n\ts_nop 0\n\tbuffer_load_dword %0, %1, 0 offen lds"
                 :: "v"(bid >= 128u && tid - 192u < WARM ? (b * WARM + tid - 192u) * 128u : 0x80000000u), "s"(mk_srd(pc, left)), "s"(r01 + SLOT) : "memory");
    Src sQ, sX;
    mk_w<false>(sQ, (xg * 4u + w) * 256u, 8192u, lane);
    mk_rows<false>(sX, b * 65536u + w * 16384u, 1024u, lane);
#pragma unroll
    for (int i = 0; i < 4; ++i) PINV(sX.v[i]);
    PINS(xs); PINS(xd);
#pragma unroll
    for (int i = 0; i < 4; ++i) PINV(sQ.v[i]);
    PINS(q0); PINS(q1); PINS(r01); PINS(r2);
    GAP();
    asm volatile("s_waitcnt lgkmcnt(0)" : "+s"(pq), "+s"(px), "+s"(tnow));
    GAP();
    sQ.srd = mk_srd(pq, 4194304u); sX.srd = mk_srd(px, 1048576u);
    dma_unit<1>(sQ, q0, r2);
    dma_unit<W_NT>(sQ, q1, r01);
    asm volatile("s_cmp_ge_u32 %0, 0x80\n\ts_cbranch_scc1 .Lx_%=\n\ts_mov_b32 m0, %1\n\ts_nop 0\n\tbuffer_load_dwordx4 %2, %6, %7 offen lds\n\tbuffer_load_dwordx4 %3, %6, %7 offen offset:1024 lds\n\t"
                 "buffer_load_dwordx4 %4, %6, %7 offen offset:2048 lds\n\tbuffer_load_dwordx4 %5, %6, %7 offen offset:3072 lds\n.Lx_%=:"
                 :: "s"(bid), "s"(xd), "v"(sX.v[0]), "v"(sX.v[1]), "v"(sX.v[2]), "v"(sX.v[3]), "s"(sX.srd), "s"(xs) : "scc", "memory");
    dma_unit<1>(sQ, q0 + 128u, r2 + 4096u);
    dma_unit<W_NT>(sQ, q1 + 128u, r01 + 4096u);
    asm volatile("" :: "v"(lds));
  }
  if (bid < 128u) { body<false>(Bx, Bk, Bv, Bwq, Bwo, Bwo, tnow, part, out); asm volatile(PG_END ".Lhot_end:"); __builtin_amdgcn_endpgm(); }
  body<true>(Ax, Ak, Av, Awq, Awo, Bwo, tnow, part, out);
  asm volatile(PG_END ".rept %0\n\ts_nop 0\n.endr" :: "i"(ASHIFT));
}

extern "C" void kernel_launch(void* const* d_in, const int* in_sizes, int n_in, void* d_out, int out_size, void* d_ws, size_t ws_size, hipStream_t stream) {
  (void)in_sizes; (void)n_in; (void)out_size; (void)ws_size;
  (void)hipFuncSetAttribute((const void*)fused_attn, hipFuncAttributeMaxDynamicSharedMemorySize, (int)LDS_BYTES);
  fused_attn<<<256, 256, LDS_BYTES, stream>>>((const char*)d_in[0], (const char*)d_in[1], (const char*)d_in[2], (const char*)d_in[3], (const char*)d_in[4],
                                              (const char*)d_in[5], (const char*)d_in[6], (const char*)d_in[7], (const char*)d_in[8], (const char*)d_in[9], (char*)d_ws, (char*)d_out);
}
```

```cpp
#include <hip/hip_runtime.h>
#include <hip/hip_bf16.h>
#include <utility>
#include <type_traits>

typedef __bf16 bf16x8 __attribute__((ext_vector_type(8)));
typedef __bf16 bf16x4 __attribute__((ext_vector_type(4)));
typedef __bf16 bf16x2 __attribute__((ext_vector_type(2)));
typedef float f32x4 __attribute__((ext_vector_type(4)));
typedef float f32x2 __attribute__((ext_vector_type(2)));
typedef float f32x16 __attribute__((ext_vector_type(16)));
typedef unsigned u32x4 __attribute__((ext_vector_type(4)));
#define LAS __attribute__((address_space(3)))
#define DI __device__ __forceinline__
typedef __amdgpu_buffer_rsrc_t srd_t;
template <int... Is> using iseq = std::integer_sequence<int, Is...>;
template <int N> using make_iseq = std::make_integer_sequence<int, N>;

constexpr int KV_NT = 1, W_NT = 0, X_NT = 0;
constexpr int SK = 4, SV = 4, SP = 5;
constexpr unsigned EASY_T = 1450, EASY2_T = 3100;
constexpr int EASY = 6, PACE_ATT = 0, EASY2 = 4;
constexpr unsigned WARM = 40;
constexpr int PGB = 9, AWSH = 100, BSHIFT = 0, ASHIFT = 0;

constexpr unsigned SLOT = 8192, LDS_BYTES = 20 * SLOT;
constexpr unsigned HOFF = 16u << 20;
constexpr float SC = 0.12751743082f;
constexpr float LMAX = 1048576.0f;

#define GAP() __builtin_amdgcn_sched_barrier(0)
#define PINS(x) asm volatile("" : "+s"(x))
#define PINV(x) asm volatile("" : "+v"(x))
DI unsigned rfl(unsigned x) { return __builtin_amdgcn_readfirstlane(x); }
DI unsigned cvtpk(float lo, float hi) { f32x2 v = {lo, hi}; bf16x2 b = __builtin_convertvector(v, bf16x2); return __builtin_bit_cast(unsigned, b); }
DI u32x4 pack2(const f32x4& a, const f32x4& b) { return u32x4{cvtpk(a[0], a[1]), cvtpk(a[2], a[3]), cvtpk(b[0], b[1]), cvtpk(b[2], b[3])}; }
DI u32x4 natural8(u32x4 w) {
  auto s0 = __builtin_amdgcn_permlane32_swap(w[0], w[2], false, false);
  auto s1 = __builtin_amdgcn_permlane32_swap(w[1], w[3], false, false);
  auto t0 = __builtin_amdgcn_permlane16_swap(s0[0], s0[1], false, false);
  auto t1 = __builtin_amdgcn_permlane16_swap(s1[0], s1[1], false, false);
  return u32x4{t0[0], t1[0], t0[1], t1[1]};
}
#define KSYM "_Z10fused_attnPKcS0_S0_S0_S0_S0_S0_S0_S0_S0_PcS1_"
#define PGL(c) ".if (((. - " KSYM ") >> 12) & 3) == " #c "\n777" #c ": s_cbranch_execz 777" #c "f\n.endif\n"
#define PG_DEFS ".set pgnext, 4096\n.macro pgstop\n.if (. - " KSYM ") >= pgnext\n.set pgnext, ((. - " KSYM ") | 4095) + 1\n" PGL(0) PGL(1) PGL(2) PGL(3) ".endif\n.endm\n" \
                ".macro mfp a:vararg\nv_mfma_f32_16x16x32_bf16 \\a\npgstop\n.endm\n"
#define PG_END "s_endpgm\n7770:\n7771:\n7772:\n7773:\n\ts_setpc_b64 vcc\n"
#define MF "mfp "
#define PGS "pgstop\n\t"
template <int SH = 0> DI void pg_walk(unsigned w) {
  asm volatile("s_mov_b64 exec, 0\n\ts_call_b64 vcc, .Lw1_%=\n\ts_branch .Lw2_%=\n.Lw1_%=:\n\ts_cmp_eq_u32 %0, 0\n\ts_cbranch_scc1 7770f\n\ts_cmp_eq_u32 %0, 1\n\ts_cbranch_scc1 7771f\n\t"
               "s_cmp_eq_u32 %0, 2\n\ts_cbranch_scc1 7772f\n\ts_branch 7773f\n.rept %1\n\ts_nop 0\n.endr\n.Lw2_%=:\n\ts_mov_b64 exec, -1" :: "s"(w), "i"(SH) : "vcc", "scc", "memory");
}
template <bool Z> DI void mm_vvv(f32x4& c, const bf16x8& a, const bf16x8& b) {
  if constexpr (Z) asm volatile(MF "%0, %1, %2, 0" : "=&v"(c) : "v"(a), "v"(b));
  else             asm volatile(MF "%0, %1, %2, %0" : "+v"(c) : "v"(a), "v"(b));
}
template <bool Z> DI void mm_vva(f32x4& c, const bf16x8& a, const bf16x8& b) {
  if constexpr (Z) asm volatile(MF "%0, %1, %2, 0" : "=&v"(c) : "v"(a), "a"(b));
  else             asm volatile(MF "%0, %1, %2, %0" : "+v"(c) : "v"(a), "a"(b));
}
DI void mm_vca(f32x4& d, const bf16x8& a, const bf16x8& b, const f32x4& c) { asm volatile(MF "%0, %1, %2, %3" : "=&v"(d) : "v"(a), "a"(b), "v"(c)); }
DI void mm_avv(f32x4& c, const bf16x8& a, const bf16x8& b) { asm volatile(MF "%0, %1, %2, %0" : "+a"(c) : "v"(a), "v"(b)); }
DI void set_m0(unsigned v) { asm volatile("s_mov_b32 m0, %0" :: "s"(v) : "memory"); }
template <int NT, int P> DI void dma_p(unsigned voff, srd_t srd, unsigned soff) {
  if constexpr (NT) asm volatile("buffer_load_dwordx4 %0, %1, %2 offen offset:%3 nt lds" :: "v"(voff), "s"(srd), "s"(soff), "i"(1024 * P) : "memory");
  else              asm volatile("buffer_load_dwordx4 %0, %1, %2 offen offset:%3 lds" :: "v"(voff), "s"(srd), "s"(soff), "i"(1024 * P) : "memory");
}
struct Src { unsigned v[4]; srd_t srd; };
template <int NT> DI void dma_unit(const Src& s, unsigned soff, unsigned ldsaddr) {
  asm volatile("s_mov_b32 m0, %0\n\ts_nop 0" :: "s"(ldsaddr) : "memory");
  dma_p<NT, 0>(s.v[0], s.srd, soff); dma_p<NT, 1>(s.v[1], s.srd, soff); dma_p<NT, 2>(s.v[2], s.srd, soff); dma_p<NT, 3>(s.v[3], s.srd, soff);
}
template <int NT> DI void dma_elem(const Src& s, unsigned soff, unsigned ustep, unsigned ldsaddr) { dma_unit<NT>(s, soff, ldsaddr); dma_unit<NT>(s, soff + ustep, ldsaddr + 4096u); }
template <int N> DI void wait_vm() { asm volatile("s_waitcnt vmcnt(%0)" :: "i"(N) : "memory"); }
template <int N, int L> DI void wait_vm_lgkm() { asm volatile("" ::: "memory"); __builtin_amdgcn_s_waitcnt((N & 15) | ((N >> 4) << 14) | 0x70 | (L << 8)); asm volatile("" ::: "memory"); }
DI void wait_lgkm0() { wait_vm_lgkm<63, 0>(); }
DI void barrier() { asm volatile("s_barrier" ::: "memory"); }
DI bf16x8 lds128(unsigned a) { return *(const LAS bf16x8*)(size_t)(a); }
DI bf16x4 lds_tr1(unsigned a) { return __builtin_amdgcn_ds_read_tr16_b64_v4bf16((LAS bf16x4*)(size_t)(a)); }
DI bf16x8 cat(bf16x4 lo, bf16x4 hi) { return __builtin_shufflevector(lo, hi, 0, 1, 2, 3, 4, 5, 6, 7); }
DI bf16x8 lds_tr(unsigned a0, unsigned a1) { return cat(lds_tr1(a0), lds_tr1(a1)); }
DI srd_t mk_srd(const void* base, unsigned bytes) { return __builtin_amdgcn_make_buffer_rsrc((void*)base, (short)0, (int)bytes, 0x00020000); }
template <bool VX> DI void mk_rows(Src& s, unsigned base, unsigned stride, unsigned lane) {
  const unsigned row = (lane >> 2) & 3u, cb = lane >> 4, sl = lane & 3u;
#pragma unroll
  for (int p = 0; p < 4; ++p) s.v[p] = base + row * stride + cb * 64u + ((sl ^ (VX ? 2 * (p & 1) : (p & 2))) << 4) + (4u * stride - 1024u) * p;
}
template <bool VX> DI void mk_w(Src& s, unsigned base, unsigned stride, unsigned lane) {
  const unsigned sub = lane >> 5, cb = (lane >> 4) & 1u, row = (lane >> 2) & 3u, sl = lane & 3u;
#pragma unroll
  for (int p = 0; p < 4; ++p) s.v[p] = base + (4u * sub + row) * stride + cb * 64u + ((sl ^ (VX ? 2u * sub : 2u * (p & 1))) << 4) + (8u * stride - 1024u) * p;
}
DI void touch(unsigned& d, unsigned& voff, const char* base) {
  asm volatile("global_load_dword %0, %1, %2 nt\n\tglobal_load_dword %0, %1, %2\n\tv_add_u32_e32 %1, 0x1000, %1" : "+v"(d), "+v"(voff) : "s"(base) : "memory");
}
struct Geo { int r0, r1, p0, p1, u, t0, t1, t2, t3; };
constexpr Geo GB = {8, 15, 16, 12, 11, 13, 14, 10, 9}, GA = {15, 8, 16, 9, 10, 14, 11, 12, 13};
constexpr unsigned KUS = 1u << GB.u;
template <bool isA> DI unsigned ksoffg(unsigned m, unsigned rk) {
  constexpr Geo G = isA ? GA : GB;
  const unsigned t = m + rk;
  return ((t & 1u) << G.t0) | (((t >> 1) & 1u) << G.t1) | (((t >> 2) & 1u) << G.t2) | (((t >> 3) & 1u) << G.t3);
}
DI unsigned ksoff(unsigned m, unsigned rk) { return ksoffg<false>(m, rk); }
template <bool isA, bool VX> DI void mk_src_kv(Src& s, const char* base, unsigned lane, unsigned w) {
  constexpr Geo G = isA ? GA : GB;
  s.srd = mk_srd(base, 131072u);
  const unsigned row = (lane >> 2) & 3u, sl = lane & 3u, o = ((row & 1u) << G.r0) + ((row >> 1) << G.r1) + (lane >> 4) * 64u;
#pragma unroll
  for (unsigned p = 0; p < (isA ? 2u : 4u); ++p)
    s.v[p] = o + ((sl ^ (VX ? 2u * (p & 1u) : isA ? 2u * (w & 1u) : (p & 2u))) << 4) + ((p & 1u) << G.p0) + ((p >> 1) << G.p1) - (isA ? 0u : 1024u * p);
}

struct PC {
  unsigned r01, r2;
  unsigned w01[2], w2[2];
  unsigned xb, xmask, xa;
  unsigned z, soff;
};
template <unsigned TS_, unsigned QS_, unsigned ZI_, unsigned ZM_, unsigned ZO_, unsigned US_, unsigned CS_, bool ROT_> struct PK {
  static constexpr unsigned TS = TS_, QS = QS_, ZI = ZI_, ZM = ZM_, ZO = ZO_, US = US_, CS = CS_; static constexpr bool ROT = ROT_; };
DI unsigned q_soff(unsigned z) { return ((z & 0xC300u) * 0x440u) & 0x3C0000u; }
template <int POS> DI unsigned ring_lds(const PC& c) { return POS == 2 ? c.r2 : c.r01 + POS * SLOT; }
template <int POS> DI unsigned ring_rd(const PC& c, int d) { return (POS == 2 ? c.w2[d & 1] : c.w01[d & 1] + POS * SLOT) + (d >> 1) * 256u; }
struct NoX { static constexpr bool ACT = true; template <int G> DI void run() const {} };
template <int SL, bool REV = false> struct EasyX {
  static constexpr bool ACT = true;
  unsigned long long& t; unsigned lim;
  template <int G> DI void run() const {
    if constexpr (G == 8) asm volatile("s_memrealtime %0" : "=s"(t));
    if constexpr (G == 16) PINS(t);
    if constexpr (G == 16) asm volatile("s_cmp_lt_i32 %0, 0\n\ts_cbranch_scc1 .Le_%=\n\ts_sleep %1\n.Le_%=:" :: "s"(REV ? lim - (unsigned)t : (unsigned)t - lim), "i"(SL) : "scc");
  }
};
template <class K, int POS, int S, bool Z, bool RD, bool DM, int NT, bool XT, int M, class X>
DI void proj_gap(f32x4 (&acc)[8][4], const bf16x8 (&cur)[4], bf16x8 (&nxt)[4], bf16x4 (&lo)[4], bf16x8 (&bf)[4], PC& c, unsigned soff, const Src& src, const Src& xs, unsigned xsoff, const X& ex) {
  constexpr int qb = M >> 2, db = M & 3;
  mm_vvv<Z>(acc[4 * S + db][qb], cur[db], bf[qb]);
  if constexpr (RD && M < 8) {
    constexpr int d = M >> 1; const unsigned a = S == 0 ? ring_rd<POS>(c, d) + 4096u : ring_rd<(POS + 1) % 3>(c, d);
    if constexpr (M & 1) nxt[d] = cat(lo[d], lds_tr1(a + K::TS)); else lo[d] = lds_tr1(a);
  }
  if constexpr (X::ACT && RD && S == 1 && db == 3) bf[qb] = lds128(c.xa + qb * K::QS);
  if constexpr (XT) {
    if constexpr (M < 8 && !(M & 1)) dma_p<X_NT, M / 2>(xs.v[M / 2], xs.srd, xsoff);
    if constexpr (M == 7) set_m0(ring_lds<POS>(c) + S * 4096u);
    if constexpr (M >= 8 && !(M & 1)) dma_p<NT, (M - 8) / 2>(src.v[(M - 8) / 2], src.srd, soff);
  } else if constexpr (DM && M >= 8 && !(M & 1)) dma_p<NT, (M - 8) / 2>(src.v[(M - 8) / 2], src.srd, soff);
  if constexpr (X::ACT && S == 0 && M == 9) { PINS(c.z); c.z = (c.z + K::ZI) & K::ZM; PINS(c.z); }
  if constexpr (X::ACT && S == 0 && M == 11) { c.xa = (c.z & c.xmask) | c.xb; PINV(c.xa); }
  if constexpr (S == 1 && DM && !K::ROT && M == 9) { PINS(c.soff); c.soff = (c.soff + K::CS) & (16u * K::CS - 1u); PINS(c.soff); }
  if constexpr (S == 1 && DM && K::ROT && M == 9) { PINS(c.z); c.soff = (c.z + 0x3100u) & 0xC300u; PINS(c.soff); }
  if constexpr (S == 1 && DM && K::ROT && M == 13) { PINS(c.soff); c.soff = (c.soff * 0x440u) & 0x3C0000u; PINS(c.soff); }
  ex.template run<16 * S + M>();
  GAP();
}
template <class K, int POS, int S, bool Z, bool RD, bool DM, int NT, int VM, bool BAR, bool XT, class X, int... Ms>
DI void proj_unit(f32x4 (&acc)[8][4], const bf16x8 (&cur)[4], bf16x8 (&nxt)[4], bf16x8 (&bf)[4], PC& c, const Src& src, const Src& xs, unsigned xsoff, unsigned xdst, const X& ex, iseq<Ms...>) {
  wait_vm_lgkm<(RD ? VM : 63), (S == 0 ? 2 : 0)>();
  if constexpr (BAR) barrier();
  const unsigned soff = c.soff + S * K::US;
  if constexpr (XT) set_m0(xdst); else if constexpr (DM) set_m0(ring_lds<POS>(c) + S * 4096u);
  GAP();
  bf16x4 lo[4];
  (proj_gap<K, POS, S, Z, RD, DM, NT, XT, Ms>(acc, cur, nxt, lo, bf, c, soff, src, xs, xsoff, ex), ...);
}
template <class K, int POS, bool Z, bool LAST, bool DM, int NT, int VM0, int VM1, bool BAR1 = false, bool XT0 = false, bool XT1 = false, class X = NoX>
DI void proj_elem(f32x4 (&acc)[8][4], bf16x8 (&a0)[4], bf16x8 (&a1)[4], bf16x8 (&bf)[4], PC& c, const Src& src, const X& ex = X{},
                  const Src* xs = nullptr, unsigned xso0 = 0, unsigned xd0 = 0, unsigned xso1 = 0, unsigned xd1 = 0) {
  proj_unit<K, POS, 0, Z, true, DM, NT, VM0, false, XT0>(acc, a0, a1, bf, c, src, xs ? *xs : src, xso0, xd0, ex, make_iseq<16>{});
  proj_unit<K, POS, 1, Z, !LAST, DM, NT, VM1, BAR1, XT1>(acc, a1, a0, bf, c, src, xs ? *xs : src, xso1, xd1, ex, make_iseq<16>{});
}
template <class K> DI void proj_setup(PC& c, unsigned tb, unsigned xb, unsigned z0, unsigned s3) {
  c.w01[0] = c.r01 + tb; c.w01[1] = c.w01[0] ^ 32u; c.w2[0] = c.r2 + tb; c.w2[1] = c.w2[0] ^ 32u;
  PINV(c.w01[0]); PINV(c.w01[1]); PINV(c.w2[0]); PINV(c.w2[1]);
  c.xb = xb; c.xmask = K::ZO; PINV(c.xmask);
  c.z = z0; c.soff = s3;
}
template <class K, int POS> DI void proj_start(bf16x8 (&a0)[4], bf16x8 (&bf)[4], PC& c, unsigned tb, unsigned xb, unsigned z0, unsigned s3) {
  proj_setup<K>(c, tb, xb, z0, s3);
  c.xa = (c.z & K::ZO) | xb; PINV(c.xa);
#pragma unroll
  for (int d = 0; d < 4; ++d) { const unsigned a = ring_rd<POS>(c, d); a0[d] = lds_tr(a, a + K::TS); }
  GAP();
#pragma unroll
  for (int q = 0; q < 4; ++q) bf[q] = lds128(c.xa + q * K::QS);
  GAP();
}
#define ACC_ALL(C, a) C(a[0][0]), C(a[0][1]), C(a[0][2]), C(a[0][3]), C(a[1][0]), C(a[1][1]), C(a[1][2]), C(a[1][3]), C(a[2][0]), C(a[2][1]), C(a[2][2]), C(a[2][3]), C(a[3][0]), C(a[3][1]), C(a[3][2]), C(a[3][3])
DI void acc_fence_v(f32x4 (&acc)[8][4]) {
  asm volatile("s_nop 7\n\ts_nop 3" : ACC_ALL("+v", acc) :: "memory");
  f32x4 (&hi)[4][4] = *(f32x4 (*)[4][4])&acc[4];
  asm volatile("" : ACC_ALL("+v", hi) :: "memory");
  GAP();
}
DI void acc_fence_a(f32x4 (&acc)[8][4]) {
  asm volatile("s_nop 7\n\ts_nop 3" : ACC_ALL("+a", acc) :: "memory");
  f32x4 (&hi)[4][4] = *(f32x4 (*)[4][4])&acc[4];
  asm volatile("" : ACC_ALL("+a", hi) :: "memory");
  GAP();
}

struct Stash { u32x4 p[64]; unsigned base[3]; };
template <int I> DI void stash_load1(Stash& st, const Src& sK, const Src& sV, unsigned& rk, unsigned wo_, unsigned (&sm)[16]) {
  constexpr int m = I >> 2, isV = (I >> 1) & 1, pc_ = I & 1;
  if constexpr (!isV && !pc_) { PINS(rk); sm[m] = ksoffg<true>(m, rk) + wo_; }
  const unsigned so = sm[m];
  if constexpr (I < 56) asm volatile("buffer_load_dwordx4 %0, %1, %2, %3 offen nt" : "=a"(st.p[I]) : "v"(isV ? sV.v[pc_] : sK.v[pc_]), "s"(isV ? sV.srd : sK.srd), "s"(so) : "memory");
  else                  asm volatile("buffer_load_dwordx4 %0, %1, %2, %3 offen nt" : "=v"(st.p[I]) : "v"(isV ? sV.v[pc_] : sK.v[pc_]), "s"(isV ? sV.srd : sK.srd), "s"(so) : "memory");
}
constexpr int stash_idx(int j) { return 4 * ((j >> 1) & 15) + 2 * (j >> 5) + (j & 1); }
template <int... Is> DI void stash_load(Stash& st, const Src& sK, const Src& sV, unsigned rk, unsigned wo_, iseq<Is...>) { unsigned sm[16]; (stash_load1<stash_idx(Is)>(st, sK, sV, rk, wo_, sm), ...); }
constexpr int kv_pair(int m) { constexpr int t[10] = {0, 1, 2, 3, 8, 9, 4, 5, 6, 7}; return t[m % 10]; }
constexpr unsigned long long kv_tab() { unsigned long long v = 0; for (int i = 0; i < 16; ++i) v |= (unsigned long long)kv_pair(i) << (4 * i); return v; }
template <int I> DI void stash_write1(const Stash& st) {
  constexpr int m = I >> 2, isV = (I >> 1) & 1, pc_ = I & 1, off = kv_pair(m) * 16384 + isV * 8192 + pc_ * 1024;
  if constexpr (I < 56) asm volatile("ds_write_b128 %0, %1 offset:%2" :: "v"(st.base[off >> 16]), "a"(st.p[I]), "i"(off & 65535) : "memory");
  else                  asm volatile("ds_write_b128 %0, %1 offset:%2" :: "v"(st.base[off >> 16]), "v"(st.p[I]), "i"(off & 65535) : "memory");
}

struct AC {
  f32x4 o[8][4];
  bf16x8 q[4][4];
  bf16x8 kf[8], vf[8], pf[4];
  f32x4 negm[4];
  f32x4 l[4];
  bf16x8 ones;
  unsigned ra[2];
};
typedef f32x4 ST[2][4];
constexpr int VR = 32;
constexpr int NEB = 22;
template <int E> DI float sm_val(ST& S) { return S[(E >> 2) & 1][E >> 3][E & 3]; }
template <int E> DI void sm_exp1(ST& S) { float a; asm volatile("v_exp_f32_e32 %0, %1" : "=v"(a) : "v"(sm_val<E>(S))); S[(E >> 2) & 1][E >> 3][E & 3] = a; }
template <int QB, int P> DI void sm_cvt(ST& S, AC& c) {
  u32x4 w = __builtin_bit_cast(u32x4, c.pf[QB]);
  unsigned r;
  asm volatile("v_cvt_pk_bf16_f32 %0, %1, %2" : "=v"(r) : "v"(sm_val<8 * QB + 2 * P>(S)), "v"(sm_val<8 * QB + 2 * P + 1>(S)));
  w[P] = r;
  c.pf[QB] = __builtin_bit_cast(bf16x8, w);
}
constexpr bool wide(int nt) { return nt == W_NT; }
template <bool DM, int NT, int VM, int R, int M> DI void mem_pre(unsigned ldsaddr) {
  if constexpr (M == 6) wait_lgkm0();
  constexpr int F = wide(NT) ? 2 : 6, H = wide(NT) ? 18 : 14;
  if constexpr (DM && (M == F || M == H)) set_m0(ldsaddr + (M == H ? 4096u : 0u));
  if constexpr (M == R && VM < 63) wait_vm<VM>();
}
template <bool DM, int NT, int M> DI void mem_post(const Src& src, unsigned soff, unsigned ustep) {
  if constexpr (wide(NT)) { if constexpr (DM && M < 32 && (M & 3) == 2) dma_p<NT, (M >> 2) & 3>(src.v[(M >> 2) & 3], src.srd, soff + (M >= 16 ? ustep : 0u)); }
  else if constexpr (DM && M >= 6 && M <= 20 && !(M & 1)) dma_p<NT, ((M - 6) / 2) & 3>(src.v[((M - 6) / 2) & 3], src.srd, soff + (M >= 14 ? ustep : 0u));
}
template <int WR, int M> DI void mem_wr(const Stash& st) {
  if constexpr (WR >= 40 && M >= 7 && M < 13) stash_write1<WR + M - 7>(st);
  if constexpr (WR >= 0 && WR < 40 && M >= 8 && M < 16)
    asm volatile("ds_write_b128 %0, %1 offset:%2" :: "v"(st.base[0]), "a"(st.p[WR + M - 8]), "i"((M - 8) * 1024) : "memory");
}
template <int N> using IC = std::integral_constant<int, N>;
struct PA { unsigned rd0, rd1, soff, fill; };
template <bool QK, bool DM, int NT, int VM, int WR, int M, class F>
DI void gapA(AC& c, ST& S, ST& Sn, PA& pa, F& addr, const Src& src, unsigned ustep, const Stash& st, bf16x4& lo) {
  constexpr int R = QK ? 16 : 0;
  if constexpr ((DM || !QK) && M == 0) addr(pa, IC<0>{});
  if constexpr (QK) mem_pre<DM, NT, VM, 16, M>(pa.fill); else if constexpr (M == 0 && VM < 63) wait_vm<VM>();
  if constexpr (QK) {
    constexpr int f = M >> 2, s = f >> 1, kb = f & 1, qb = M & 3;
    if constexpr (s == 0) mm_vca(Sn[kb][qb], c.kf[f], c.q[qb][0], c.negm[qb]); else mm_vva<false>(Sn[kb][qb], c.kf[f], c.q[qb][s]);
  }
  mem_post<DM, NT, M>(src, pa.soff, ustep);
  mem_wr<WR, M>(st);
  if constexpr (QK && !DM && M >= 10 && M < 16) addr(pa, IC<M - 10>{});
  if constexpr (QK && M == 12) asm volatile("" :: "v"(c.negm[0]), "v"(c.negm[1]), "v"(c.negm[2]), "v"(c.negm[3]));
  if constexpr (M >= R && M < R + 16) {
    constexpr int db = (M - R) >> 1; const unsigned a = ((db & 1) ? pa.rd1 : pa.rd0) + (db >> 1) * 256u;
    if constexpr (M & 1) c.vf[db] = cat(lo, lds_tr1(a + 4096u)); else lo = lds_tr1(a);
  }
  if constexpr (NEB + M < 32) sm_exp1<NEB + M>(S);
  if constexpr (M >= 16 && (M & 1)) sm_cvt<2 + ((M - 16) >> 3), ((M - 16) >> 1) & 3>(S, c);
  GAP();
}
template <bool QK, bool DM, int NT, int VM, int WR, class F, int... Ms>
DI void phaseA_(AC& c, ST& S, ST& Sn, F& addr, const Src& src, unsigned ustep, const Stash& st, iseq<Ms...>) {
  PA pa; bf16x4 lo;
  (gapA<QK, DM, NT, VM, WR, Ms>(c, S, Sn, pa, addr, src, ustep, st, lo), ...);
}
template <bool QK, bool DM, int NT, int VM, int WR = -1, class F>
DI void phaseA(AC& c, ST& S, ST& Sn, F addr, const Src& src, unsigned ustep, const Stash& st) {
  phaseA_<QK, DM, NT, VM, WR>(c, S, Sn, addr, src, ustep, st, make_iseq<32>{});
}
template <bool MAX> DI float xlane(float v) {
  auto a = __builtin_amdgcn_permlane32_swap(__float_as_uint(v), __float_as_uint(v), false, false);
  v = MAX ? fmaxf(__uint_as_float(a[0]), __uint_as_float(a[1])) : __uint_as_float(a[0]) + __uint_as_float(a[1]);
  auto b = __builtin_amdgcn_permlane16_swap(__float_as_uint(v), __float_as_uint(v), false, false);
  return MAX ? fmaxf(__uint_as_float(b[0]), __uint_as_float(b[1])) : __uint_as_float(b[0]) + __uint_as_float(b[1]);
}
DI float row_max(const f32x4& x, const f32x4& y) {
  return xlane<true>(fmaxf(fmaxf(fmaxf(x[0], x[1]), fmaxf(x[2], x[3])), fmaxf(fmaxf(y[0], y[1]), fmaxf(y[2], y[3]))));
}
template <int SM, bool KR, bool DM, int NT, int VM, int WR, int M, class F>
DI void gapB(AC& c, ST& Sn, PA& pa, F& addr, const Src& src, unsigned ustep, const Stash& st, bf16x4& lo) {
  mem_pre<DM, NT, VM, (KR ? 18 : VR), M>(pa.fill);
  {
    constexpr int h = M / 18, r = M % 18, qb = 2 * h + (r & 1);
    if constexpr (r < 2) mm_vvv<false>(c.l[qb], c.ones, c.pf[qb]);
    else if constexpr (SM == 2 && r < 6) mm_vvv<false>(c.o[(r - 2) >> 1][qb], c.vf[(r - 2) >> 1], c.pf[qb]);
    else mm_avv(c.o[(r - 2) >> 1][qb], c.vf[(r - 2) >> 1], c.pf[qb]);
  }
  mem_post<DM, NT, M>(src, pa.soff, ustep);
  mem_wr<WR, M>(st);
  if constexpr (M < 4) addr(pa, IC<M>{});
  if constexpr (KR && M >= 18 && M < 34 && !(M & 1)) { constexpr int f = (M - 18) >> 1; c.kf[f] = lds128(pa.rd0 + (f & 1) * 4096u + (f >> 1) * 256u); }
  constexpr int R = VM < 63 ? VR : 20;
  if constexpr (!KR && M >= R) {
    c.ra[0] = pa.rd0; c.ra[1] = pa.rd1;
    constexpr int db = (M - R) >> 1; const unsigned a = ((db & 1) ? pa.rd1 : pa.rd0) + (db >> 1) * 256u;
    if constexpr (M & 1) c.kf[db] = cat(lo, lds_tr1(a + 4096u)); else lo = lds_tr1(a);
  }
  if constexpr (SM) {
    if constexpr (M >= 4 && M < 18) sm_exp1<M - 4>(Sn);
    if constexpr (M >= 19 && M < 35 && (M & 1)) sm_exp1<14 + (M - 19) / 2>(Sn);
    if constexpr (M >= 20 && !(M & 1)) sm_cvt<((M - 20) >> 3), ((M - 20) >> 1) & 3>(Sn, c);
  }
  GAP();
}
template <int SM, bool KR, bool DM, int NT, int VM, int WR, class F, int... Ms>
DI void phaseB_(AC& c, ST& Sn, F& addr, const Src& src, unsigned ustep, const Stash& st, iseq<Ms...>) {
  PA pa; bf16x4 lo;
  (gapB<SM, KR, DM, NT, VM, WR, Ms>(c, Sn, pa, addr, src, ustep, st, lo), ...);
}
template <int SM, bool KR, bool DM, int NT, int VM, int WR = -1, class F>
DI void phaseB(AC& c, ST& Sn, F addr, const Src& src, unsigned ustep, const Stash& st) {
  phaseB_<SM, KR, DM, NT, VM, WR>(c, Sn, addr, src, ustep, st, make_iseq<36>{});
}
DI void k_read(AC& ac, unsigned ka) {
#pragma unroll
  for (int f = 0; f < 8; ++f) ac.kf[f] = lds128(ka + (f & 1) * 4096u + (f >> 1) * 256u);
}
template <bool DM, int M> DI void qk0_gap(AC& ac, ST& SA, const Src& src, unsigned soff, unsigned fill) {
  constexpr int f = M >> 2, qb = M & 3;
  if constexpr (DM && (M == 0 || M == 16)) set_m0(fill + M * 256u);
  mm_vva<(f < 2)>(SA[f & 1][qb], ac.kf[f], ac.q[qb][f >> 1]);
  if constexpr (DM && qb == 0) dma_p<KV_NT, f & 3>(src.v[f & 3], src.srd, soff + (f >> 2) * KUS);
  if constexpr (!DM && (M & 1)) { ac.o[4 + (M >> 3)][(M >> 1) & 3] = f32x4{}; asm volatile("" : "+a"(ac.o[4 + (M >> 3)][(M >> 1) & 3])); }
  GAP();
}
template <bool DM, int... Ms> DI void qk0(AC& ac, ST& SA, const Src& src, unsigned soff, unsigned fill, iseq<Ms...>) { (qk0_gap<DM, Ms>(ac, SA, src, soff, fill), ...); }
template <int... Js> DI void exp_first(ST& SA, iseq<Js...>) { (sm_exp1<Js>(SA), ...); }
template <int... Js> DI void cvt_first(ST& SA, AC& c, iseq<Js...>) { (sm_cvt<(Js >> 2), (Js & 3)>(SA, c), ...); }
DI void sm_first(AC& ac, ST& SA) {
  asm volatile("s_nop 7\n\ts_nop 3" : "+v"(SA[0][0]), "+v"(SA[0][1]), "+v"(SA[0][2]), "+v"(SA[0][3]), "+v"(SA[1][0]), "+v"(SA[1][1]), "+v"(SA[1][2]), "+v"(SA[1][3]) :: "memory");
  GAP();
#pragma unroll
  for (int q = 0; q < 4; ++q) {
    const float m = row_max(SA[0][q], SA[1][q]);
    SA[0][q] -= m; SA[1][q] -= m;
    ac.negm[q] = f32x4{-m, -m, -m, -m};
    ac.l[q] = f32x4{};
    ac.pf[q] = bf16x8{};
  }
  ac.ones = __builtin_bit_cast(bf16x8, u32x4{0x3f803f80u, 0x3f803f80u, 0x3f803f80u, 0x3f803f80u});
  asm volatile("" : "+v"(ac.negm[0]), "+v"(ac.negm[1]), "+v"(ac.negm[2]), "+v"(ac.negm[3]), "+v"(ac.l[0]), "+v"(ac.l[1]), "+v"(ac.l[2]), "+v"(ac.l[3]), "+v"(ac.ones));
  exp_first(SA, make_iseq<NEB>{});
  cvt_first(SA, ac, make_iseq<8>{});
  GAP();
}
struct BS { unsigned p[4], v[6], k[2], rk, wo, l16, w; };
constexpr bool b_dma(int f) { return f != 2 * (SK - 3) && f != 2 * (SV - 2) + 1 && f != 2 * (SP - 2) + 1 && f < 30 && f != 26; }
constexpr int b_vm(int f) {
  const int s = f == 29 ? 27 : f - 3;
  if (!b_dma(s)) return 63;
  int n = !b_dma(f) ? 0 : f == 28 ? 4 : f == 29 ? 8 : 5 + (f & 1);
  for (int i = s + 1; i < f; ++i) n += b_dma(i) ? 8 : 0;
  return n;
}
template <class KO, int I> DI void b_step(AC& ac, ST& S, ST& Sn, BS& b, const Src& sK, const Src& sV, const Src& sO, Stash& st) {
  constexpr int fa = 2 * I, fb = 2 * I + 1, vi = I == SP ? 4 : (I & 1) ? 0 : 2;
  const unsigned pa = b.p[(I & 1) ? 3 : 1], pb = b.p[(I & 1) ? 0 : 2];
  if constexpr (I == PGB) pg_walk<BSHIFT>(b.w);
  if constexpr (I >= 1 && I <= 13) barrier();
  if constexpr (I == SK - 3) st.base[0] = pa + b.l16;
  phaseA<true, b_dma(fa), (I < 14 ? KV_NT : W_NT), b_vm(fa), (I == SK - 3 ? 0 : -1)>(ac, S, Sn, [&](PA& p, auto k) __attribute__((always_inline)) {
    if (k.value) return;
    PINS(b.rk);
    p.soff = I < 14 ? ksoff(I < 13 ? I + 3 : 15, b.rk) : b.wo; p.fill = I == 14 ? b.p[0] : pa;
    p.rd0 = b.v[vi] + (I == SP ? SLOT : 0u); p.rd1 = b.v[vi + 1] + (I == SP ? SLOT : 0u); }, I < 13 ? sK : I < 14 ? sV : sO, I < 14 ? KUS : KO::US, st);
  if constexpr (I == SV - 2) st.base[0] = pb + b.l16;
  phaseB<2, (I < 14), b_dma(fb), (I < 14 ? KV_NT : W_NT), b_vm(fb), (I == SV - 2 ? 8 : -1)>(ac, Sn, [&](PA& p, auto k) __attribute__((always_inline)) {
      if (k.value) return;
      PINS(b.rk);
      p.soff = I < 14 ? ksoff(I + 2, b.rk) : b.wo + KO::CS; p.fill = I == 14 ? b.p[1] : I == 13 ? pa : pb; p.rd0 = I == 14 ? b.v[4] : b.k[(I & 1) ? 0 : 1]; p.rd1 = b.v[5]; }, I < 14 ? sV : sO, I < 14 ? KUS : KO::US, st);
}
template <int CH, int Q, int J, bool V = false> DI void o_word(const AC& c, const float (&inv)[5], bf16x8 (&nb)[4]) {
  u32x4 w = __builtin_bit_cast(u32x4, nb[Q]);
  unsigned r;
  if constexpr (V)
    asm volatile("v_mul_f32_e32 %0, %1, %3\n\tv_mul_f32_e32 %4, %2, %3\n\tv_cvt_pk_bf16_f32 %0, %0, %4"
                 : "=&v"(r) : "v"(c.o[2 * CH + (J >> 1)][Q][2 * (J & 1)]), "v"(c.o[2 * CH + (J >> 1)][Q][2 * (J & 1) + 1]), "v"(inv[Q]), "v"(inv[4]));
  else
  asm volatile("v_accvgpr_read_b32 %0, %1\n\tv_accvgpr_read_b32 %4, %2\n\tv_mul_f32_e32 %0, %0, %3\n\tv_mul_f32_e32 %4, %4, %3\n\tv_cvt_pk_bf16_f32 %0, %0, %4"
               : "=&v"(r) : "a"(c.o[2 * CH + (J >> 1)][Q][2 * (J & 1)]), "a"(c.o[2 * CH + (J >> 1)][Q][2 * (J & 1) + 1]), "v"(inv[Q]), "v"(inv[4]));
  w[J] = r;
  nb[Q] = __builtin_bit_cast(bf16x8, w);
}
template <int CH, int Q> DI void o_put(const bf16x8 (&nb)[4], unsigned ow) { asm volatile("ds_write_b128 %0, %1 offset:%2" :: "v"(ow), "v"(nb[Q]), "i"(CH * 4096 + Q * 1024) : "memory"); }
template <int E> struct CvX {
  static constexpr bool ACT = E == 3;
  const AC& c; const float (&inv)[5]; const bf16x8 (&cur)[4]; bf16x8 (&nb)[4]; unsigned ow;
  template <int G> DI void run() const {
    if constexpr (E < 3 && (G & 1)) o_word<E + 1, (G >> 3), (G >> 1) & 3>(c, inv, nb);
    if constexpr (G == 8 || G == 10) o_put<E, 2 + ((G >> 1) & 1)>(cur, ow);
    if constexpr (E < 3 && (G == 24 || G == 26)) o_put<E + 1, ((G >> 1) & 1)>(nb, ow);
  }
};
template <class K, int VM, int DM, int M> DI void gapE(AC& c, ST& S, float (&inv)[5], bf16x8 (&nb)[4], bf16x8 (&a0)[4], bf16x4& lo, const PC& pc, const Src& src, unsigned soff, unsigned long long& bad, unsigned ow) {
  constexpr int h = M / 18, r = M % 18, qb = 2 * h + (r & 1);
  if constexpr (M == 24) wait_vm<VM>();
  constexpr int D = M - (DM == 2 ? 4 : 0);
  constexpr int NR = DM == 2 ? 0 : VR - 20;
  if constexpr (M == NR + 4) wait_lgkm0();
  if constexpr (M == 6 && DM == 2) barrier();
  if constexpr (DM && (D == 3 || D == 19)) set_m0(pc.r2 + (D == 19 ? 4096u : 0u));
  if constexpr (r < 2) mm_vvv<false>(c.l[qb], c.ones, c.pf[qb]);
  else if constexpr (DM == 1 && r < 6) mm_vvv<false>(c.o[(r - 2) >> 1][qb], c.kf[(r - 2) >> 1], c.pf[qb]);
  else mm_avv(c.o[(r - 2) >> 1][qb], c.kf[(r - 2) >> 1], c.pf[qb]);
  if constexpr (DM && D > 0 && D < 32 && (D & 3) == 3) dma_p<W_NT, (D >> 2) & 3>(src.v[(D >> 2) & 3], src.srd, soff + (D >= 16 ? K::US : 0u));
  if constexpr (M < NR) {
    constexpr int db = (M + 16 - NR) >> 1; const unsigned a = c.ra[db & 1] + (db >> 1) * 256u;
    if constexpr (M & 1) c.kf[db] = cat(lo, lds_tr1(a + 4096u)); else lo = lds_tr1(a);
  }
  if constexpr (M >= 24 && M < 32) {
    constexpr int d = (M - 24) >> 1; const unsigned a = ring_rd<0>(pc, d);
    if constexpr (M & 1) a0[d] = cat(lo, lds_tr1(a + K::TS)); else lo = lds_tr1(a);
  }
  if constexpr (NEB + M < 32) sm_exp1<NEB + M>(S);
  if constexpr (M >= 10 && M < 14) { sm_cvt<2 + ((M - 10) >> 1), 2 * (M & 1)>(S, c); sm_cvt<2 + ((M - 10) >> 1), 2 * (M & 1) + 1>(S, c); }
  if constexpr (M == 14 || M == 15 || M == 22 || M == 23) asm volatile("v_rcp_f32_e32 %0, %1" : "=v"(inv[qb]) : "v"(c.l[qb][0]));
  if constexpr (M >= 16 && M < 22) o_word<0, ((M - 16) >> 2), (M & 3), (DM == 1)>(c, inv, nb);
  if constexpr (M >= 24 && M < 34) o_word<0, ((M - 18) >> 2), ((M - 18) & 3), (DM == 1)>(c, inv, nb);
  if constexpr (M == 32) o_put<0, 0>(nb, ow);
  if constexpr (M == 33) o_put<0, 1>(nb, ow);
  if constexpr (r == 4) asm volatile("" :: "v"(c.l[2 * h]), "v"(c.l[2 * h + 1]));
  if constexpr (M == 34)
    asm volatile("v_add_f32_e32 %1, %2, %3\n\tv_add_f32_e32 %1, %1, %4\n\tv_add_f32_e32 %1, %1, %5\n\tv_cmp_nle_f32_e64 %0, %1, %6"
                 : "=s"(bad) : "v"(inv[4]), "v"(c.l[0][0]), "v"(c.l[1][0]), "v"(c.l[2][0]), "v"(c.l[3][0]), "s"(LMAX));
  GAP();
}
template <class K, int VM, int DM, int... Ms> DI void phaseE(AC& c, ST& S, float (&inv)[5], bf16x8 (&nb)[4], bf16x8 (&a0)[4], const PC& pc, const Src& src, unsigned soff, unsigned long long& bad, unsigned ow, iseq<Ms...>) {
  bf16x4 lo;
#pragma unroll
  for (int q = 0; q < 4; ++q) nb[q] = bf16x8{};
  (gapE<K, VM, DM, Ms>(c, S, inv, nb, a0, lo, pc, src, soff, bad, ow), ...);
}
struct Pub { u32x4 pk[16]; srd_t srd; unsigned v[4], so; };
struct TL { bf16x8 w[2][3][4], x[2][3][4]; bf16x4 lo[2]; unsigned xa[3], xs[3], drop[2], l16; };
template <int S, int QB, int J> DI void pub_word(const f32x4 (&acc)[8][4], Pub& p) {
  const f32x4& a = acc[4 * S + (J >> 1)][QB];
  unsigned r;
  asm volatile("v_cvt_pk_bf16_f32 %0, %1, %2" : "=v"(r) : "v"(a[2 * (J & 1)]), "v"(a[2 * (J & 1) + 1]));
  p.pk[8 * S + 2 * QB + (J >> 2)][J & 3] = r;
}
template <int F> DI void pub_store(const Pub& p) {
  asm volatile("buffer_store_dwordx4 %0, %1, %2, %3 offen offset:%4 sc0 sc1" :: "v"(p.pk[F]), "v"(p.v[F >> 2]), "s"(p.srd), "s"(p.so), "i"((F & 3) * 1024) : "memory");
}
template <class K, int S, int E, int R> DI void tl_w(TL& t, const PC& c) {
  const unsigned a = ring_rd<(E + 1) % 3>(c, R >> 1) + S * 4096u;
  if constexpr (R & 1) t.w[S][E][R >> 1] = cat(t.lo[S], lds_tr1(a + K::TS)); else t.lo[S] = lds_tr1(a);
}
template <class K, int G> DI void tl_gap(f32x4 (&acc)[8][4], TL& t, const PC& c, Pub& p) {
  constexpr int S = G >= 48, qb = G < 16 ? G >> 2 : G < 48 ? (G - 16) >> 3 : (G - 48) / 12, e = G < 16 ? 0 : G < 48 ? 1 + ((G >> 2) & 1) : G % 3, db = G < 48 ? G & 3 : (G % 12) / 3;
  if constexpr (G == 10) wait_vm<4>();
  if constexpr (G == 27) wait_vm<0>();
  mm_vvv<false>(acc[4 * S + db][qb], t.w[S][e][db], t.x[S][e][qb]);
  if constexpr (G >= 28 && G <= 56 && !(G & 3)) pub_store<(G - 28) / 4>(p);
  if constexpr (G >= 59 && G % 6 == 5) pub_store<8 + (G - 59) / 6>(p);
  if constexpr (G < 8) tl_w<K, 0, 1, G>(t, c);
  if constexpr (G >= 10 && G < 18) tl_w<K, 0, 2, G - 10>(t, c);
  if constexpr (G >= 16 && G < 24) tl_w<K, 1, 0, G - 16>(t, c);
  if constexpr (G >= 25 && G < 56 && (G & 1)) tl_w<K, 1, 1 + (((G - 25) >> 2) & 1), 2 * ((G - 25) >> 3) + (((G - 25) >> 1) & 1)>(t, c);
  if constexpr (G < 34 && (G & 3) == 2) t.x[0][1 + ((G >> 2) & 1)][G >> 3] = lds128(t.xa[1 + ((G >> 2) & 1)] + (G >> 3) * K::QS);
  if constexpr (G >= 34 && G < 82 && (G & 3) == 2) t.x[1][((G - 34) % 12) >> 2][(G - 34) / 12] = lds128(t.xa[((G - 34) % 12) >> 2] + ((G - 34) / 12) * K::QS);
  if constexpr (G >= 24 && G < 56) pub_word<0, ((G - 24) >> 3), (G & 7)>(acc, p);
  if constexpr (G >= 54 && G % 3 < 2) pub_word<1, (G - 54) / 12, 2 * (((G - 54) % 12) / 3) + G % 3>(acc, p);
  if constexpr (G == 0 || G == 1 || G == 3) { t.xa[(G + 1) & 3] = t.xs[(G + 1) & 3] | t.l16; PINV(t.xa[(G + 1) & 3]); }
  if constexpr (G == 4 || G == 5) { p.v[2 * (G & 1)] = t.drop[G & 1] | t.l16; PINV(p.v[2 * (G & 1)]); }
  if constexpr (G == 7 || G == 8) { p.v[2 * (G & 1) + 1] = p.v[2 * (G & 1)] | 4096u; PINV(p.v[2 * (G & 1) + 1]); }
  GAP();
}
template <class K, int... Gs> DI void tl_run(f32x4 (&acc)[8][4], TL& t, const PC& c, Pub& p, iseq<Gs...>) {
  wait_vm_lgkm<12, 2>();
  GAP();
  (tl_gap<K, Gs>(acc, t, c, p), ...);
  pub_word<1, 3, 4>(acc, p); pub_word<1, 3, 5>(acc, p);
  asm volatile("s_nop 4");
  pub_word<1, 3, 6>(acc, p); pub_word<1, 3, 7>(acc, p); pub_store<15>(p);
}
template <int I> DI void own_put1(const u32x4& v, unsigned a) { asm volatile("ds_write_b128 %0, %1 offset:%2 ; %3" :: "v"(a), "v"(v), "i"((I & 7) * 1024), "i"(I) : "memory"); }
template <int B, int... Is> DI void own_put(const Pub& p, unsigned a, iseq<Is...>) { (own_put1<B + Is>(p.pk[B + Is], a), ...); }
DI void redo_head(const AC& ac, const char* __restrict__ Kp, const char* __restrict__ Vp, unsigned lane, unsigned ow) {
  const unsigned r16 = lane & 15u, g = lane >> 4;
#pragma unroll 1
  for (unsigned qb = 0; qb < 4; ++qb) {
    bf16x8 qq[4];
#pragma unroll
    for (int s = 0; s < 4; ++s) qq[s] = qb == 0 ? ac.q[0][s] : qb == 1 ? ac.q[1][s] : qb == 2 ? ac.q[2][s] : ac.q[3][s];
    f32x4 o[8];
#pragma unroll
    for (int d = 0; d < 8; ++d) o[d] = f32x4{};
    float m = -__builtin_inff(), l = 0.f;
#pragma unroll 1
    for (unsigned t = 0; t < 16; ++t) {
      f32x4 s0 = {}, s1 = {};
      const char* kp = Kp + (t * 32u + r16) * 256u + g * 16u;
#pragma unroll
      for (int s = 0; s < 4; ++s) {
        s0 = __builtin_amdgcn_mfma_f32_16x16x32_bf16(*(const bf16x8*)(kp + s * 64), qq[s], s0, 0, 0, 0);
        s1 = __builtin_amdgcn_mfma_f32_16x16x32_bf16(*(const bf16x8*)(kp + s * 64 + 4096), qq[s], s1, 0, 0, 0);
      }
      const float mn = fmaxf(m, row_max(s0, s1)), al = __builtin_amdgcn_exp2f(m - mn);
      m = mn;
#pragma unroll
      for (int j = 0; j < 4; ++j) { s0[j] = __builtin_amdgcn_exp2f(s0[j] - mn); s1[j] = __builtin_amdgcn_exp2f(s1[j] - mn); }
      l = l * al + ((s0[0] + s0[1]) + (s0[2] + s0[3])) + ((s1[0] + s1[1]) + (s1[2] + s1[3]));
      const bf16x8 pf = __builtin_bit_cast(bf16x8, pack2(s0, s1));
      const __bf16* vp = (const __bf16*)(Vp + (t * 32u + g * 4u) * 256u) + r16;
#pragma unroll
      for (int d = 0; d < 8; ++d) {
        bf16x8 vf;
#pragma unroll
        for (int j = 0; j < 8; ++j) vf[j] = vp[((j & 3) + (j >> 2) * 16) * 128 + d * 16];
        o[d] = __builtin_amdgcn_mfma_f32_16x16x32_bf16(vf, pf, o[d] * al, 0, 0, 0);
      }
    }
    const float inv = 1.0f / xlane<false>(l);
#pragma unroll
    for (int s = 0; s < 4; ++s) *(LAS u32x4*)(size_t)(ow + s * 4096u + qb * 1024u) = pack2(o[2 * s] * inv, o[2 * s + 1] * inv);
  }
}
template <bool isA, int J> DI void q_conv1(AC& ac, f32x4 (&acc)[8][4], Stash& st, const Src& sK, const Src& sV, unsigned rk, unsigned p3) {
  constexpr int s = J >> 2, qb = J & 3;
  if constexpr (isA) { stash_write1<(5 * J) / 2>(st); stash_write1<(5 * J) / 2 + 1>(st); if constexpr (J & 1) stash_write1<(5 * J) / 2 + 2>(st); }
  else {
    constexpr int p = 2 * (J & 1);
    const Src& src = s < 2 ? sK : sV;
    const unsigned so = ksoff(s == 0 ? 2 : s == 1 ? SK : s == 2 ? SV : SP, rk) + ((J >> 1) & 1) * KUS;
    if constexpr (s == 0 || s == 3) {
      if constexpr (p == 0) asm volatile("s_mov_b32 m0, %0\n\ts_nop 0" :: "s"(p3 + (s == 3 ? SLOT : 0u) + (J & 2) * 2048u) : "memory");
      dma_p<KV_NT, p>(src.v[p], src.srd, so); dma_p<KV_NT, p + 1>(src.v[p + 1], src.srd, so);
    } else {
      asm volatile("buffer_load_dwordx4 %0, %1, %2, %3 offen offset:%4 nt" : "=a"(st.p[2 * J - 8]) : "v"(src.v[p]), "s"(src.srd), "s"(so), "i"(1024 * p) : "memory");
      asm volatile("buffer_load_dwordx4 %0, %1, %2, %3 offen offset:%4 nt" : "=a"(st.p[2 * J - 7]) : "v"(src.v[p + 1]), "s"(src.srd), "s"(so), "i"(1024 * p + 1024) : "memory");
    }
  }
  ac.q[qb][s] = __builtin_bit_cast(bf16x8, natural8(pack2(acc[2 * s][qb] * SC, acc[2 * s + 1][qb] * SC)));
  asm volatile("" : "+a"(ac.q[qb][s]));
  GAP();
}
template <bool isA, int... Js> DI void q_conv(AC& ac, f32x4 (&acc)[8][4], Stash& st, const Src& sK, const Src& sV, unsigned rk, unsigned p3, iseq<Js...>) { (q_conv1<isA, Js>(ac, acc, st, sK, sV, rk, p3), ...); }
template <int N, int N0 = 0> DI void zero_o(AC& ac) {
#pragma unroll
  for (int i = N0; i < N; ++i)
#pragma unroll
    for (int q = 0; q < 4; ++q) { ac.o[i][q] = f32x4{}; asm volatile("" : "+a"(ac.o[i][q])); }
}

template <bool isA>
DI void body(const char* __restrict__ X, const char* __restrict__ K, const char* __restrict__ V, const char* __restrict__ Wq, const char* __restrict__ Wo,
             const char* __restrict__ Wo2, unsigned long long tnow, char* __restrict__ part, char* __restrict__ out) {
  const unsigned tlim = (unsigned)tnow + EASY_T;
  unsigned tid = threadIdx.x;
  PINV(tid);
  const unsigned lane = tid & 63u, w = rfl(tid >> 6);
  const unsigned bid = blockIdx.x, xg = bid & 7u, rr = bid >> 3, b = rr & 15u, h = xg * 4u + w;
  const unsigned r16 = lane & 15u, g = lane >> 4;
  const size_t kvo = isA ? (size_t)(b * 8u + xg) * 131072u : (size_t)(b * 32u + h) * 131072u;
  const unsigned rk = isA ? 7u * b : 5u * b + 8u * w;
  typedef PK<512u, 4096u, 0x1300u, 0xF300u, 0xC300u, 128u, 32u * 8192u, true> KQ;
  typedef PK<512u, 4096u, 0x1300u, 0xF300u, 0xC300u, KUS, 8192u, false> KT;
  typedef PK<2048u, 1024u, 0x1000u, 0xF000u, 0xF000u, 128u, 32u * 1024u, false> KO;
  PC pc;
  pc.r01 = 65536u + w * 16384u; pc.r2 = 131072u + w * 8192u;
  const unsigned zq = ((b & 3u) << 14) | ((b >> 2) << 8), co = 4u * w;
  const srd_t srdH = mk_srd(part + HOFF, 4096u + 256u * 128u);
  const unsigned tok_off = rr * 128u, flg_off = 4096u + rr * 1024u;
  const unsigned l8 = (lane & 7u) * 4u;
  unsigned entry_flags, dum0 = lane;
  Src sQ, sX, sK, sV, sO;
  const unsigned p0 = b & 3u, p1 = (p0 + 1u) & 3u, p2 = (p0 + 2u) & 3u, p3 = (p0 + 3u) & 3u, xw = w * 4096u;
  Stash st;
  mk_src_kv<isA, false>(sK, K + kvo, lane, w);
  mk_src_kv<isA, true>(sV, V + kvo, lane, w);
  if constexpr (isA) { stash_load(st, sK, sV, rk, ((w & 1u) << GA.p1) + ((w >> 1) << GA.u), make_iseq<64>{}); GAP(); }
  asm volatile("buffer_load_dword %0, %1, %2, %3 offen sc1" : "=v"(entry_flags) : "v"(l8), "s"(srdH), "s"(flg_off + xg * 128u) : "memory");
  sQ.srd = mk_srd(Wq, 4194304u); sX.srd = mk_srd(X, 1048576u);
  mk_w<false>(sQ, h * 256u, 8192u, lane);
  mk_rows<false>(sX, b * 65536u + w * 16384u, 1024u, lane);
  sO.srd = mk_srd(Wo + (size_t)xg * 524288u, 524288u);
  mk_w<true>(sO, w * 256u, 1024u, lane);
  if constexpr (isA) {
    dma_unit<X_NT>(sX, p0 * 256u, xw + p0 * 16384u);
    dma_elem<W_NT>(sQ, q_soff(zq + 0x2200u), KQ::US, pc.r01 + SLOT);
    dma_unit<X_NT>(sX, p1 * 256u, xw + p1 * 16384u);
    dma_unit<X_NT>(sX, p2 * 256u, xw + p2 * 16384u);
    dma_unit<X_NT>(sX, p3 * 256u, xw + p3 * 16384u);
  }
  srd_t pb_srd = mk_srd(part + (size_t)rr * 524288u, 524288u);
  unsigned pb_so = xg * 65536u + w * 16384u, drop0 = 2u * w == xg ? 0x80000000u : 0u, drop1 = 2u * w + 1u == xg ? 0x80002000u : 0x2000u;
  unsigned xs0 = ((co + 13u) & 15u) << 12, xs1 = ((co + 14u) & 15u) << 12, xs2 = ((co + 15u) & 15u) << 12, oz = (co + 3u) << 12, os3 = (co + 3u) * KO::CS;
  unsigned ks0 = ksoff(0u, rk), ks1 = ksoff(1u, rk);
  {
    PINS(pb_srd); PINS(pb_so); PINS(drop0); PINS(drop1); PINS(xs0); PINS(xs1); PINS(xs2); PINS(oz); PINS(os3); PINS(sO.srd);
    if constexpr (!isA) {
      PINS(sK.srd); PINS(sV.srd); PINS(ks0); PINS(ks1);
#pragma unroll
      for (int i = 0; i < 4; ++i) { PINV(sK.v[i]); PINV(sV.v[i]); PINV(sO.v[i]); }
    }
  }
  const unsigned rb = 1024u * (r16 >> 2) + 64u * (r16 & 3u) + 16u * (g ^ ((r16 >> 2) & 2u));
  const unsigned tb = 64u * ((lane >> 2) & 3u) + 32u * (g & 1u) + 8u * (lane & 3u);
  const unsigned tbV = tb + 1024u * g;
  const unsigned sb0 = w * 2048u + lane * 16u;
  st.base[0] = sb0; st.base[1] = sb0 + 65536u; st.base[2] = sb0 + 131072u;
  AC ac;
  if constexpr (!isA) zero_o<8, 2>(ac);
  {
    f32x4 acc[8][4];
    bf16x8 a0[4], a1[4], bf[4];
    if constexpr (isA) pg_walk<AWSH>(w);
    wait_vm<(isA ? 20 : 9)>();
    barrier();
    proj_start<KQ, 2>(a0, bf, pc, tbV, rb, zq, q_soff(zq + 0x3100u));
    if constexpr (isA) {
      proj_elem<KQ, 2, true,  false, true, W_NT, 63, 63>(acc, a0, a1, bf, pc, sQ);
      proj_elem<KQ, 0, false, false, true, W_NT, 63, 28>(acc, a0, a1, bf, pc, sQ);
      proj_elem<KQ, 1, false, false, true, W_NT, 28, 16>(acc, a0, a1, bf, pc, sQ);
      proj_elem<KQ, 2, false, false, true, W_NT, 16, 16, true>(acc, a0, a1, bf, pc, sQ);
    } else {
      dma_elem<W_NT>(sQ, q_soff(zq + 0x2200u), KQ::US, pc.r01 + SLOT);
      proj_elem<KQ, 2, true,  false, true, W_NT, 13, 20, false, true, true>(acc, a0, a1, bf, pc, sQ, NoX{}, &sX, p1 * 256u, xw + p1 * 16384u, p2 * 256u, xw + p2 * 16384u);
      proj_elem<KQ, 0, false, false, true, W_NT, 24, 28, false, true, false>(acc, a0, a1, bf, pc, sQ, NoX{}, &sX, p3 * 256u, xw + p3 * 16384u);
      proj_elem<KQ, 1, false, false, true, W_NT, 28, 24>(acc, a0, a1, bf, pc, sQ);
      proj_elem<KQ, 2, false, false, true, W_NT, 20, 16, true>(acc, a0, a1, bf, pc, sQ);
    }
    typedef std::conditional_t<isA, EasyX<EASY>, NoX> PX;
    const PX px = [&] { if constexpr (isA) return EasyX<EASY>{tnow, tlim}; else return NoX{}; }();
#pragma unroll 1
    for (unsigned e = 0; e < 3; ++e) {
      proj_elem<KQ, 0, false, false, true, W_NT, 16, 16>(acc, a0, a1, bf, pc, sQ, px);
      proj_elem<KQ, 1, false, false, true, W_NT, 16, 16>(acc, a0, a1, bf, pc, sQ, px);
      proj_elem<KQ, 2, false, false, true, W_NT, 16, 16>(acc, a0, a1, bf, pc, sQ, px);
    }
    if constexpr (isA) {
      proj_elem<KQ, 0, false, false, false, W_NT, 16, 12>(acc, a0, a1, bf, pc, sQ, px);
      proj_elem<KQ, 1, false, false, false, W_NT, 8, 4>(acc, a0, a1, bf, pc, sQ, px);
      proj_elem<KQ, 2, false, true, false, W_NT, 0, 0>(acc, a0, a1, bf, pc, sQ, px);
    } else {
      pc.soff = ks0;
      proj_elem<KT, 0, false, false, true, KV_NT, 16, 16>(acc, a0, a1, bf, pc, sK);
      pc.soff = ks1;
      proj_elem<KT, 1, false, false, true, KV_NT, 16, 16>(acc, a0, a1, bf, pc, sK);
      pc.soff = ks0;
      proj_elem<KT, 2, false, true, true, KV_NT, 16, 16>(acc, a0, a1, bf, pc, sV);
    }
    PINV(entry_flags);
    if (w == 0) {
      const unsigned long long mk = __builtin_amdgcn_ballot_w64(((entry_flags >> (lane & 7u)) & 1u) != 0u);
      const unsigned tok = 0x5a5a5a00u | (~(unsigned)mk & 0xffu);
      if (lane == 0) asm volatile("buffer_store_dword %0, off, %1, %2 sc1" :: "v"(tok), "s"(srdH), "s"(tok_off + xg * 4u) : "memory");
    }
    barrier();
    acc_fence_v(acc);
    q_conv<isA>(ac, acc, st, sK, sV, rk, w * 16384u, make_iseq<16>{});
  }
  ST SA, SB;
  if constexpr (isA) {
    zero_o<4>(ac);
    const unsigned kb = rb, vb = SLOT + tbV;
    unsigned dum = lane, to = ((lane & 8u) * 16u + (lane & 7u) * 4u + (lane >> 4) * 32768u) | (((lane >> 4) ^ w) << 8);
    const unsigned tbo = (b & 3u) * 1024u + (b >> 2) * 131072u;
    PINV(to);
    const char* ta = Wo + (size_t)xg * 524288u + tbo;
    const char* tb2 = Wo2 + (size_t)xg * 524288u + tbo;
    touch(dum, to, ta);
    asm volatile("s_waitcnt lgkmcnt(0)" ::: "memory");
    barrier();
    k_read(ac, kb + kv_pair(0) * 16384u);
    wait_lgkm0();
    GAP();
    qk0<false>(ac, SA, sK, 0u, 0u, make_iseq<32>{});
    k_read(ac, kb + kv_pair(1) * 16384u);
    touch(dum, to, ta);
    sm_first(ac, SA);
    unsigned i4 = 0;
    unsigned blk; const char* tp;
    auto ak = [&](PA& p, auto k) __attribute__((always_inline)) {
      if constexpr (k.value == 0) { PINS(i4); blk = (unsigned)((kv_tab() >> 8) >> i4) << 14; i4 += 4u; PINS(blk); PINS(i4); }
      if constexpr (k.value == 1) { p.rd0 = kb + (blk & 0x3c000u); PINV(p.rd0); }
    };
    auto at = [&](PA& p, auto k) __attribute__((always_inline)) {
      if constexpr (k.value == 0) { PINS(i4); blk = (unsigned)(kv_tab() >> i4) << 14; PINS(blk); }
      if constexpr (k.value == 1) { p.rd0 = vb + (blk & 0x3c000u); PINV(p.rd0); }
      if constexpr (k.value == 2) asm volatile("s_cmp_lt_u32 %1, 24\n\ts_cselect_b64 %0, %2, %3" : "=s"(tp) : "s"(i4), "s"(ta), "s"(tb2 - 32768) : "scc");
      if constexpr (k.value == 3) { p.rd1 = p.rd0 ^ 32u; PINV(p.rd1); asm volatile("global_load_dword %0, %1, %2 nt" : "+v"(dum) : "v"(to), "s"(tp) : "memory"); }
      if constexpr (k.value == 4) asm volatile("global_load_dword %0, %1, %2\n\tv_add_u32_e32 %1, %3, %1" : "+v"(dum), "+v"(to) : "s"(tp), "i"(4096) : "memory");
    };
    while (i4 < 24u) {
      phaseA<true, false, KV_NT, 63>(ac, SA, SB, at, sK, 0u, st);
      phaseB<true, true, false, KV_NT, 63>(ac, SB, ak, sK, 0u, st);
      phaseA<true, false, KV_NT, 63>(ac, SB, SA, at, sK, 0u, st);
      phaseB<true, true, false, KV_NT, 63>(ac, SA, ak, sK, 0u, st);
    }
    barrier();
    phaseA<true, false, KV_NT, 63, 40>(ac, SA, SB, at, sK, 0u, st);
    phaseB<true, true, false, KV_NT, 63, 46>(ac, SB, ak, sK, 0u, st);
    phaseA<true, false, KV_NT, 63, 52>(ac, SB, SA, at, sK, 0u, st);
    phaseB<true, true, false, KV_NT, 63, 58>(ac, SA, ak, sK, 0u, st);
    wait_vm_lgkm<8, 8>(); barrier();
    while (i4 < 56u) {
      phaseA<true, false, KV_NT, 63>(ac, SA, SB, at, sK, 0u, st);
      phaseB<true, true, false, KV_NT, 63>(ac, SB, ak, sK, 0u, st);
      phaseA<true, false, KV_NT, 63>(ac, SB, SA, at, sK, 0u, st);
      phaseB<true, true, false, KV_NT, 63>(ac, SA, ak, sK, 0u, st);
    }
    barrier();
    unsigned v14 = vb + kv_pair(14) * 16384u, x14 = v14 ^ 32u; PINV(v14); PINV(x14);
    phaseA<true, true, W_NT, 63>(ac, SA, SB, [&](PA& p, auto) { p.soff = co * KO::CS; p.fill = pc.r01; p.rd0 = v14; p.rd1 = x14; }, sO, KO::US, st);
    phaseB<true, false, true, W_NT, 63>(ac, SB, [&](PA& p, auto k) __attribute__((always_inline)) {
      p.soff = (co + 1u) * KO::CS; p.fill = pc.r01 + SLOT; p.rd0 = v14 + 16384u; p.rd1 = x14 + 16384u;
      if constexpr (k.value == 1) proj_setup<KO>(pc, tb + 512u * g, lane * 16u, oz, os3); }, sO, KO::US, st);
    dum0 = dum;
  } else {
    const unsigned P0 = pc.r01, P1 = pc.r01 + SLOT, P2 = pc.r2, P3 = w * 16384u;
    unsigned v0 = tbV + P0, v2 = tbV + P2, x0 = v0 ^ 32u, x2 = v2 ^ 32u, k1 = rb + P1, k3 = rb + P3;
    PINV(v0); PINV(v2); PINV(x0); PINV(x2); PINV(k1); PINV(k3);
    wait_vm<48>();
    k_read(ac, k1 - SLOT);
#pragma unroll
    for (int i = 0; i < 8; ++i) { ac.o[i >> 2][i & 3] = f32x4{}; PINV(ac.o[i >> 2][i & 3]); }
    wait_lgkm0();
    GAP();
    qk0<true>(ac, SA, sV, ks1, P0, make_iseq<32>{});
    wait_vm<48>();
    GAP();
    k_read(ac, k1);
    sm_first(ac, SA);
    BS bs{{P0, P1, P2, P3}, {v0, x0, v2, x2, tbV + P3, (tbV + P3) ^ 32u}, {k1, k3}, rk, co * KO::CS, lane * 16u, w};
    PINV(bs.v[4]); PINV(bs.v[5]);
    proj_setup<KO>(pc, tb + 512u * g, lane * 16u, oz, os3);
#define B2(I) b_step<KO, I>(ac, SA, SB, bs, sK, sV, sO, st); b_step<KO, I + 1>(ac, SB, SA, bs, sK, sV, sO, st);
    B2(0) B2(2) B2(4) B2(6) B2(8) B2(10)
    B2(12) b_step<KO, 14>(ac, SA, SB, bs, sK, sV, sO, st);
#undef B2
  }
  {
    const unsigned l16 = lane * 16u;
    unsigned ow = w * 16384u + l16;
    PINV(ow);
    f32x4 acc[8][4];
    bf16x8 a0[4], a1[4], b0[4], bf[4];
    float inv[5];
    asm volatile("" : "=v"(inv[4]));
    unsigned toks;
    asm volatile("buffer_load_dword %0, %1, %2, %3 offen sc1" : "=v"(toks) : "v"(l8), "s"(srdH), "s"(tok_off) : "memory");
    GAP();
    unsigned long long bad;
    phaseE<KO, (isA ? 18 : 19), (isA ? 2 : 1)>(ac, SB, inv, b0, a0, pc, sO, os3 - KO::CS, bad, ow, make_iseq<36>{});
    if (__builtin_expect(bad != 0ull, 0)) {
      asm volatile("s_waitcnt vmcnt(0)" : "+v"(toks) :: "memory");
      acc_fence_a(ac.o);
      redo_head(ac, K + kvo, V + kvo, lane, ow);
#pragma unroll
      for (int q = 0; q < 4; ++q) {
        inv[q] = 1.0f;
        b0[q] = lds128(ow + q * 1024u);
#pragma unroll
        for (int s = 1; s < 4; ++s) {
          const u32x4 t = __builtin_bit_cast(u32x4, lds128(ow + s * 4096u + q * 1024u));
          ac.o[2 * s][q] = f32x4{__uint_as_float(t[0] << 16), __uint_as_float(t[0] & 0xffff0000u), __uint_as_float(t[1] << 16), __uint_as_float(t[1] & 0xffff0000u)};
          ac.o[2 * s + 1][q] = f32x4{__uint_as_float(t[2] << 16), __uint_as_float(t[2] & 0xffff0000u), __uint_as_float(t[3] << 16), __uint_as_float(t[3] & 0xffff0000u)};
        }
      }
      wait_lgkm0();
      acc_fence_a(ac.o);
    }
#pragma unroll
    for (int q = 0; q < 4; ++q) bf[q] = bf16x8{};
    GAP();
    proj_elem<KO, 0, true, false, true, W_NT, 16, 16>(acc, a0, a1, b0, pc, sO, CvX<0>{ac, inv, b0, bf, ow});
    proj_elem<KO, 1, false, false, true, W_NT, 16, 16>(acc, a0, a1, bf, pc, sO, CvX<1>{ac, inv, bf, b0, ow});
    proj_elem<KO, 2, false, false, true, W_NT, 16, 16>(acc, a0, a1, b0, pc, sO, CvX<2>{ac, inv, b0, bf, ow});
    if constexpr (isA) PINV(dum0);
    proj_elem<KO, 0, false, false, true, W_NT, 16, 16, true>(acc, a0, a1, bf, pc, sO, CvX<3>{ac, inv, bf, b0, ow});
    typedef std::conditional_t<isA, EasyX<EASY2, true>, NoX> PX;
    const PX px = [&] { if constexpr (isA) return EasyX<EASY2, true>{tnow, tlim + (EASY2_T - EASY_T)}; else return NoX{}; }();
#pragma unroll 1
    for (unsigned e = 0; e < 3; ++e) {
      proj_elem<KO, 1, false, false, true, W_NT, 16, 16>(acc, a0, a1, bf, pc, sO, px);
      proj_elem<KO, 2, false, false, true, W_NT, 16, 16>(acc, a0, a1, bf, pc, sO, px);
      proj_elem<KO, 0, false, false, true, W_NT, 16, 16>(acc, a0, a1, bf, pc, sO, px);
    }
    Pub pb;
    pb.srd = pb_srd; pb.so = pb_so;
#pragma unroll
    for (int i = 0; i < 16; ++i) pb.pk[i] = u32x4{};
    TL tl;
#pragma unroll
    for (int i = 0; i < 4; ++i) { tl.w[0][0][i] = a0[i]; tl.x[0][0][i] = bf[i]; }
    tl.xs[0] = xs0; tl.xs[1] = xs1; tl.xs[2] = xs2; tl.drop[0] = drop0; tl.drop[1] = drop1; tl.l16 = l16;
    tl_run<KO>(acc, tl, pc, pb, make_iseq<96>{});
    const unsigned lr = ((lane + xg) & 7u) * 4u;
    unsigned fl;
    asm volatile("buffer_load_dword %0, %1, %2, %3 offen sc1" : "=v"(fl) : "v"(lr), "s"(srdH), "s"(flg_off + xg * 128u) : "memory");
    PINV(toks);
    if (w == (xg >> 1)) {
      const unsigned a = pc.r01 + SLOT + l16;
      if (xg & 1u) own_put<8>(pb, a, make_iseq<8>{}); else own_put<0>(pb, a, make_iseq<8>{});
    }
    const unsigned fo = lane * 128u + xg * 4u, tau = __builtin_amdgcn_readlane(toks, xg);
    unsigned oo = ((b * 64u + 16u * w + r16) * 1024u + (isA ? 0u : 512u) + 64u * xg + 8u * g) * 2u;
    PINV(oo);
    wait_lgkm0();
    barrier();
    const unsigned oa = 65536u + SLOT + (xg >> 1) * 16384u + w * 2048u + l16;
    const u32x4 r0 = __builtin_bit_cast(u32x4, lds128(oa)), r1 = __builtin_bit_cast(u32x4, lds128(oa + 1024u));
    GAP();
    wait_vm<9>();
    if (lane == 2u * w) asm volatile("buffer_store_dword %0, %1, %2, %3 offen sc1" :: "v"(toks), "v"(fo), "s"(srdH), "s"(flg_off) : "memory");
    wait_vm<2>();
    if (lane == 2u * w + 1u) asm volatile("buffer_store_dword %0, %1, %2, %3 offen sc1" :: "v"(toks), "v"(fo), "s"(srdH), "s"(flg_off) : "memory");
    f32x16 f[8];
#pragma unroll
    for (int j = 0; j < 8; ++j) { const unsigned t = j < 4 ? r0[j & 3] : r1[j & 3]; f[0][2 * j] = __uint_as_float(t << 16); f[0][2 * j + 1] = __uint_as_float(t & 0xffff0000u); }
    PINV(f[0]);
    asm volatile("s_waitcnt vmcnt(2)" : "+v"(fl) : "v"(pb.pk[0]), "v"(pb.pk[1]), "v"(pb.pk[2]), "v"(pb.pk[3]), "v"(pb.pk[4]), "v"(pb.pk[5]), "v"(pb.pk[6]), "v"(pb.pk[7]),
                 "v"(pb.pk[8]), "v"(pb.pk[9]), "v"(pb.pk[10]), "v"(pb.pk[11]), "v"(pb.pk[12]), "v"(pb.pk[13]), "v"(pb.pk[14]), "v"(pb.pk[15]) : "memory");
    unsigned got = 0xffffff01u, spin = 1u << 21, fresh, so, f2;
#define POLL(R) "buffer_load_dword %[" R "], %[lr], %[sh], %[flg] offen sc1\n\t"
#define SEEN(R) "v_cmp_eq_u32_e32 vcc, %[tau], %[" R "]\n\ts_andn2_b32 %[fr], vcc_lo, %[got]\n\ts_cbranch_scc1 .Lproc_%=\n\t"
    asm volatile(
        SEEN("fl") POLL("fl") "s_sleep 4\n"
        ".Lidle_%=:\n\t" POLL("f2") "s_waitcnt vmcnt(1)\n\t" SEEN("fl") "s_sleep %[slp]\n\t" POLL("fl") "s_waitcnt vmcnt(1)\n\t" SEEN("f2")
        "s_sleep %[slp]\n\ts_sub_u32 %[spin], %[spin], 1\n\ts_cbranch_scc0 .Lidle_%=\n\ts_branch .Ldone_%=\n"
        ".Lproc_%=:\n\ts_or_b32 %[got], %[got], %[fr]\n\t"
        PGS "s_bitcmp1_b32 %[fr], 1\n\ts_cbranch_scc0 .La1_%=\n\ts_add_u32 %[so], %[ro], 65536\n\ts_bitset0_b32 %[so], 19\n\t"
        "buffer_load_dwordx4 v[152:155], %[l16], %[sp], %[so] offen sc1\n\tbuffer_load_dwordx4 v[156:159], %[l16], %[sp], %[so] offen offset:1024 sc1\n.La1_%=:\n\t"
        PGS "s_bitcmp1_b32 %[fr], 2\n\ts_cbranch_scc0 .La2_%=\n\ts_add_u32 %[so], %[ro], 131072\n\ts_bitset0_b32 %[so], 19\n\t"
        "buffer_load_dwordx4 v[168:171], %[l16], %[sp], %[so] offen sc1\n\tbuffer_load_dwordx4 v[172:175], %[l16], %[sp], %[so] offen offset:1024 sc1\n.La2_%=:\n\t"
        PGS "s_bitcmp1_b32 %[fr], 3\n\ts_cbranch_scc0 .La3_%=\n\ts_add_u32 %[so], %[ro], 196608\n\ts_bitset0_b32 %[so], 19\n\t"
        "buffer_load_dwordx4 v[184:187], %[l16], %[sp], %[so] offen sc1\n\tbuffer_load_dwordx4 v[188:191], %[l16], %[sp], %[so] offen offset:1024 sc1\n.La3_%=:\n\t"
        PGS "s_bitcmp1_b32 %[fr], 4\n\ts_cbranch_scc0 .La4_%=\n\ts_add_u32 %[so], %[ro], 262144\n\ts_bitset0_b32 %[so], 19\n\t"
        "buffer_load_dwordx4 v[200:203], %[l16], %[sp], %[so] offen sc1\n\tbuffer_load_dwordx4 v[204:207], %[l16], %[sp], %[so] offen offset:1024 sc1\n.La4_%=:\n\t"
        PGS "s_bitcmp1_b32 %[fr], 5\n\ts_cbranch_scc0 .La5_%=\n\ts_add_u32 %[so], %[ro], 327680\n\ts_bitset0_b32 %[so], 19\n\t"
        "buffer_load_dwordx4 v[216:219], %[l16], %[sp], %[so] offen sc1\n\tbuffer_load_dwordx4 v[220:223], %[l16], %[sp], %[so] offen offset:1024 sc1\n.La5_%=:\n\t"
        PGS "s_bitcmp1_b32 %[fr], 6\n\ts_cbranch_scc0 .La6_%=\n\ts_add_u32 %[so], %[ro], 393216\n\ts_bitset0_b32 %[so], 19\n\t"
        "buffer_load_dwordx4 v[232:235], %[l16], %[sp], %[so] offen sc1\n\tbuffer_load_dwordx4 v[236:239], %[l16], %[sp], %[so] offen offset:1024 sc1\n.La6_%=:\n\t"
        PGS "s_bitcmp1_b32 %[fr], 7\n\ts_cbranch_scc0 .La7_%=\n\ts_add_u32 %[so], %[ro], 458752\n\ts_bitset0_b32 %[so], 19\n\t"
        "buffer_load_dwordx4 v[248:251], %[l16], %[sp], %[so] offen sc1\n\tbuffer_load_dwordx4 v[252:255], %[l16], %[sp], %[so] offen offset:1024 sc1\n.La7_%=:\n\t"
        "s_waitcnt vmcnt(0)\n\ts_cmp_eq_u32 %[got], -1\n\ts_cbranch_scc1 .Ltok_%=\n\t" POLL("fl") "s_branch .Lwide_%=\n"
        ".Ltok_%=:\n\tbuffer_load_dword %[fl], %[l8], %[sh], %[tok] offen sc1\n"
        ".Lwide_%=:\n\t"
        "s_bitcmp1_b32 %[fr], 1\n\ts_cbranch_scc0 .Lb1_%=\n\t"
        PGS "v_lshlrev_b32_e32 v144, 16, v152\n\tv_and_b32_e32 v145, %[hi], v152\n\tv_lshlrev_b32_e32 v146, 16, v153\n\tv_and_b32_e32 v147, %[hi], v153\n\t"
        PGS "v_lshlrev_b32_e32 v148, 16, v154\n\tv_and_b32_e32 v149, %[hi], v154\n\tv_lshlrev_b32_e32 v150, 16, v155\n\tv_and_b32_e32 v151, %[hi], v155\n\t"
        PGS "v_lshlrev_b32_e32 v152, 16, v156\n\tv_and_b32_e32 v153, %[hi], v156\n\tv_lshlrev_b32_e32 v154, 16, v157\n\tv_and_b32_e32 v155, %[hi], v157\n\t"
        PGS "v_lshlrev_b32_e32 v156, 16, v158\n\tv_and_b32_e32 v157, %[hi], v158\n\tv_lshlrev_b32_e32 v158, 16, v159\n\tv_and_b32_e32 v159, %[hi], v159\n\t"
        ".Lb1_%=:\n\t"
        "s_bitcmp1_b32 %[fr], 2\n\ts_cbranch_scc0 .Lb2_%=\n\t"
        PGS "v_lshlrev_b32_e32 v160, 16, v168\n\tv_and_b32_e32 v161, %[hi], v168\n\tv_lshlrev_b32_e32 v162, 16, v169\n\tv_and_b32_e32 v163, %[hi], v169\n\t"
        PGS "v_lshlrev_b32_e32 v164, 16, v170\n\tv_and_b32_e32 v165, %[hi], v170\n\tv_lshlrev_b32_e32 v166, 16, v171\n\tv_and_b32_e32 v167, %[hi], v171\n\t"
        PGS "v_lshlrev_b32_e32 v168, 16, v172\n\tv_and_b32_e32 v169, %[hi], v172\n\tv_lshlrev_b32_e32 v170, 16, v173\n\tv_and_b32_e32 v171, %[hi], v173\n\t"
        PGS "v_lshlrev_b32_e32 v172, 16, v174\n\tv_and_b32_e32 v173, %[hi], v174\n\tv_lshlrev_b32_e32 v174, 16, v175\n\tv_and_b32_e32 v175, %[hi], v175\n\t"
        ".Lb2_%=:\n\t"
        "s_bitcmp1_b32 %[fr], 3\n\ts_cbranch_scc0 .Lb3_%=\n\t"
        PGS "v_lshlrev_b32_e32 v176, 16, v184\n\tv_and_b32_e32 v177, %[hi], v184\n\tv_lshlrev_b32_e32 v178, 16, v185\n\tv_and_b32_e32 v179, %[hi], v185\n\t"
        PGS "v_lshlrev_b32_e32 v180, 16, v186\n\tv_and_b32_e32 v181, %[hi], v186\n\tv_lshlrev_b32_e32 v182, 16, v187\n\tv_and_b32_e32 v183, %[hi], v187\n\t"
        PGS "v_lshlrev_b32_e32 v184, 16, v188\n\tv_and_b32_e32 v185, %[hi], v188\n\tv_lshlrev_b32_e32 v186, 16, v189\n\tv_and_b32_e32 v187, %[hi], v189\n\t"
        PGS "v_lshlrev_b32_e32 v188, 16, v190\n\tv_and_b32_e32 v189, %[hi], v190\n\tv_lshlrev_b32_e32 v190, 16, v191\n\tv_and_b32_e32 v191, %[hi], v191\n\t"
        ".Lb3_%=:\n\t"
        "s_bitcmp1_b32 %[fr], 4\n\ts_cbranch_scc0 .Lb4_%=\n\t"
        PGS "v_lshlrev_b32_e32 v192, 16, v200\n\tv_and_b32_e32 v193, %[hi], v200\n\tv_lshlrev_b32_e32 v194, 16, v201\n\tv_and_b32_e32 v195, %[hi], v201\n\t"
        PGS "v_lshlrev_b32_e32 v196, 16, v202\n\tv_and_b32_e32 v197, %[hi], v202\n\tv_lshlrev_b32_e32 v198, 16, v203\n\tv_and_b32_e32 v199, %[hi], v203\n\t"
        PGS "v_lshlrev_b32_e32 v200, 16, v204\n\tv_and_b32_e32 v201, %[hi], v204\n\tv_lshlrev_b32_e32 v202, 16, v205\n\tv_and_b32_e32 v203, %[hi], v205\n\t"
        PGS "v_lshlrev_b32_e32 v204, 16, v206\n\tv_and_b32_e32 v205, %[hi], v206\n\tv_lshlrev_b32_e32 v206, 16, v207\n\tv_and_b32_e32 v207, %[hi], v207\n\t"
        ".Lb4_%=:\n\t"
        "s_bitcmp1_b32 %[fr], 5\n\ts_cbranch_scc0 .Lb5_%=\n\t"
        PGS "v_lshlrev_b32_e32 v208, 16, v216\n\tv_and_b32_e32 v209, %[hi], v216\n\tv_lshlrev_b32_e32 v210, 16, v217\n\tv_and_b32_e32 v211, %[hi], v217\n\t"
        PGS "v_lshlrev_b32_e32 v212, 16, v218\n\tv_and_b32_e32 v213, %[hi], v218\n\tv_lshlrev_b32_e32 v214, 16, v219\n\tv_and_b32_e32 v215, %[hi], v219\n\t"
        PGS "v_lshlrev_b32_e32 v216, 16, v220\n\tv_and_b32_e32 v217, %[hi], v220\n\tv_lshlrev_b32_e32 v218, 16, v221\n\tv_and_b32_e32 v219, %[hi], v221\n\t"
        PGS "v_lshlrev_b32_e32 v220, 16, v222\n\tv_and_b32_e32 v221, %[hi], v222\n\tv_lshlrev_b32_e32 v222, 16, v223\n\tv_and_b32_e32 v223, %[hi], v223\n\t"
        ".Lb5_%=:\n\t"
        "s_bitcmp1_b32 %[fr], 6\n\ts_cbranch_scc0 .Lb6_%=\n\t"
        PGS "v_lshlrev_b32_e32 v224, 16, v232\n\tv_and_b32_e32 v225, %[hi], v232\n\tv_lshlrev_b32_e32 v226, 16, v233\n\tv_and_b32_e32 v227, %[hi], v233\n\t"
        PGS "v_lshlrev_b32_e32 v228, 16, v234\n\tv_and_b32_e32 v229, %[hi], v234\n\tv_lshlrev_b32_e32 v230, 16, v235\n\tv_and_b32_e32 v231, %[hi], v235\n\t"
        PGS "v_lshlrev_b32_e32 v232, 16, v236\n\tv_and_b32_e32 v233, %[hi], v236\n\tv_lshlrev_b32_e32 v234, 16, v237\n\tv_and_b32_e32 v235, %[hi], v237\n\t"
        PGS "v_lshlrev_b32_e32 v236, 16, v238\n\tv_and_b32_e32 v237, %[hi], v238\n\tv_lshlrev_b32_e32 v238, 16, v239\n\tv_and_b32_e32 v239, %[hi], v239\n\t"
        ".Lb6_%=:\n\t"
        "s_bitcmp1_b32 %[fr], 7\n\ts_cbranch_scc0 .Lb7_%=\n\t"
        PGS "v_lshlrev_b32_e32 v240, 16, v248\n\tv_and_b32_e32 v241, %[hi], v248\n\tv_lshlrev_b32_e32 v242, 16, v249\n\tv_and_b32_e32 v243, %[hi], v249\n\t"
        PGS "v_lshlrev_b32_e32 v244, 16, v250\n\tv_and_b32_e32 v245, %[hi], v250\n\tv_lshlrev_b32_e32 v246, 16, v251\n\tv_and_b32_e32 v247, %[hi], v251\n\t"
        PGS "v_lshlrev_b32_e32 v248, 16, v252\n\tv_and_b32_e32 v249, %[hi], v252\n\tv_lshlrev_b32_e32 v250, 16, v253\n\tv_and_b32_e32 v251, %[hi], v253\n\t"
        PGS "v_lshlrev_b32_e32 v252, 16, v254\n\tv_and_b32_e32 v253, %[hi], v254\n\tv_lshlrev_b32_e32 v254, 16, v255\n\tv_and_b32_e32 v255, %[hi], v255\n\t"
        ".Lb7_%=:\n\t"
        "s_cmp_eq_u32 %[got], -1\n\ts_cbranch_scc0 .Lidle_%=\n"
        ".Ldone_%=:"
        : "=&{v[144:159]}"(f[1]), "=&{v[160:175]}"(f[2]), "=&{v[176:191]}"(f[3]), "=&{v[192:207]}"(f[4]), "=&{v[208:223]}"(f[5]), "=&{v[224:239]}"(f[6]), "=&{v[240:255]}"(f[7]),
          [fl] "+v"(fl), [f2] "=&v"(f2), [got] "+s"(got), [spin] "+s"(spin), [fr] "=&s"(fresh), [so] "=&s"(so)
        : [tau] "s"(tau), [l16] "v"(l16), [l8] "v"(l8), [lr] "v"(lr), [sp] "s"(pb.srd), [sh] "s"(srdH), [ro] "s"(xg * 73728u + w * 2048u), [tok] "s"(tok_off),
          [flg] "s"(flg_off + xg * 128u), [hi] "s"(0xffff0000u), [slp] "i"(isA ? 8 : 0)
        : "vcc", "scc", "memory");
#undef POLL
#undef SEEN
    const f32x16 t = ((f[0] + f[1]) + (f[2] + f[3])) + ((f[4] + f[5]) + (f[6] + f[7]));
#pragma unroll
    for (int s = 0; s < 2; ++s)
    {
      const u32x4 ov = natural8(u32x4{cvtpk(t[8 * s], t[8 * s + 1]), cvtpk(t[8 * s + 2], t[8 * s + 3]), cvtpk(t[8 * s + 4], t[8 * s + 5]), cvtpk(t[8 * s + 6], t[8 * s + 7])});
      __builtin_amdgcn_raw_buffer_store_b128(ov, mk_srd(out, 2097152u), oo + s * 64, 0, 2);
    }
    asm volatile("s_waitcnt vmcnt(2)" : "+v"(fl) :: "memory");
    if ((lane >> 1) == w && fl != toks) asm volatile("buffer_store_dword %0, %1, %2, %3 offen sc1" :: "v"(fl), "v"(fo), "s"(srdH), "s"(flg_off) : "memory");
  }
}

__global__ __launch_bounds__(256, 1) __attribute__((aligned(4096)))
void fused_attn(const char* __restrict__ Ax, const char* __restrict__ Ak, const char* __restrict__ Av, const char* __restrict__ Awq, const char* __restrict__ Awo,
                const char* __restrict__ Bx, const char* __restrict__ Bk, const char* __restrict__ Bv, const char* __restrict__ Bwq, const char* __restrict__ Bwo,
                char* __restrict__ part, char* __restrict__ out) {
  extern __shared__ __attribute__((aligned(16))) char lds[];
  const unsigned bid = blockIdx.x;
  unsigned long long tnow;
  {
    const unsigned xg = bid & 7u, b = (bid >> 3) & 15u;
    const char *px, *pq, *pc; unsigned left;
    asm volatile(PG_DEFS "s_load_dwordx2 %0, %5, %6\n\ts_load_dwordx2 %1, %5, 0x28\n\ts_memrealtime %2\n\ts_getpc_b64 %3\n.Lpc_%=:\n\ts_mov_b32 %4, .Lhot_end-.Lpc_%="
                 : "=&s"(pq), "=&s"(px), "=&s"(tnow), "=&s"(pc), "=&s"(left) : "s"(__builtin_amdgcn_kernarg_segment_ptr()), "s"(bid >= 128u ? 0x18u : 0x40u));
    unsigned tid = threadIdx.x;
    PINV(tid);
    const unsigned lane = tid & 63u, w = rfl(tid >> 6), zq = ((b & 3u) << 14) | ((b >> 2) << 8);
    unsigned q0 = q_soff(zq), q1 = q_soff(zq + 0x1300u), r01 = 65536u + w * 16384u, r2 = 131072u + w * 8192u, xs = (b & 3u) * 256u, xd = w * 4096u + (b & 3u) * 16384u;
    asm volatile("s_mov_b32 m0, %2\n\ts_nop 0\n\tbuffer_load_dword %0, %1, 0 offen lds"
                 :: "v"(bid >= 128u && tid - 192u < WARM ? (b * WARM + tid - 192u) * 128u : 0x80000000u), "s"(mk_srd(pc, left)), "s"(r01 + SLOT) : "memory");
    Src sQ, sX;
    mk_w<false>(sQ, (xg * 4u + w) * 256u, 8192u, lane);
    mk_rows<false>(sX, b * 65536u + w * 16384u, 1024u, lane);
#pragma unroll
    for (int i = 0; i < 4; ++i) PINV(sX.v[i]);
    PINS(xs); PINS(xd);
#pragma unroll
    for (int i = 0; i < 4; ++i) PINV(sQ.v[i]);
    PINS(q0); PINS(q1); PINS(r01); PINS(r2);
    GAP();
    asm volatile("s_waitcnt lgkmcnt(0)" : "+s"(pq), "+s"(px), "+s"(tnow));
    GAP();
    sQ.srd = mk_srd(pq, 4194304u); sX.srd = mk_srd(px, 1048576u);
    dma_unit<1>(sQ, q0, r2);
    dma_unit<W_NT>(sQ, q1, r01);
    asm volatile("s_cmp_ge_u32 %0, 0x80\n\ts_cbranch_scc1 .Lx_%=\n\ts_mov_b32 m0, %1\n\ts_nop 0\n\tbuffer_load_dwordx4 %2, %6, %7 offen lds\n\tbuffer_load_dwordx4 %3, %6, %7 offen offset:1024 lds\n\t"
                 "buffer_load_dwordx4 %4, %6, %7 offen offset:2048 lds\n\tbuffer_load_dwordx4 %5, %6, %7 offen offset:3072 lds\n.Lx_%=:"
                 :: "s"(bid), "s"(xd), "v"(sX.v[0]), "v"(sX.v[1]), "v"(sX.v[2]), "v"(sX.v[3]), "s"(sX.srd), "s"(xs) : "scc", "memory");
    dma_unit<1>(sQ, q0 + 128u, r2 + 4096u);
    dma_unit<W_NT>(sQ, q1 + 128u, r01 + 4096u);
    asm volatile("" :: "v"(lds));
  }
  if (bid < 128u) { body<false>(Bx, Bk, Bv, Bwq, Bwo, Bwo, tnow, part, out); asm volatile(PG_END ".Lhot_end:"); __builtin_amdgcn_endpgm(); }
  body<true>(Ax, Ak, Av, Awq, Awo, Bwo, tnow, part, out);
  asm volatile(PG_END ".rept %0\n\ts_nop 0\n.endr" :: "i"(ASHIFT));
}

extern "C" void kernel_launch(void* const* d_in, const int* in_sizes, int n_in, void* d_out, int out_size, void* d_ws, size_t ws_size, hipStream_t stream) {
  (void)in_sizes; (void)n_in; (void)out_size; (void)ws_size;
  (void)hipFuncSetAttribute((const void*)fused_attn, hipFuncAttributeMaxDynamicSharedMemorySize, (int)LDS_BYTES);
  fused_attn<<<256, 256, LDS_BYTES, stream>>>((const char*)d_in[0], (const char*)d_in[1], (const char*)d_in[2], (const char*)d_in[3], (const char*)d_in[4],
                                              (const char*)d_in[5], (const char*)d_in[6], (const char*)d_in[7], (const char*)d_in[8], (const char*)d_in[9], (char*)d_ws, (char*)d_out);
}
```

```cpp
#include <hip/hip_runtime.h>
#include <hip/hip_bf16.h>
#include <utility>
#include <type_traits>

typedef __bf16 bf16x8 __attribute__((ext_vector_type(8)));
typedef __bf16 bf16x4 __attribute__((ext_vector_type(4)));
typedef __bf16 bf16x2 __attribute__((ext_vector_type(2)));
typedef float f32x4 __attribute__((ext_vector_type(4)));
typedef float f32x2 __attribute__((ext_vector_type(2)));
typedef float f32x16 __attribute__((ext_vector_type(16)));
typedef unsigned u32x4 __attribute__((ext_vector_type(4)));
#define LAS __attribute__((address_space(3)))
#define DI __device__ __forceinline__
typedef __amdgpu_buffer_rsrc_t srd_t;
template <int... Is> using iseq = std::integer_sequence<int, Is...>;
template <int N> using make_iseq = std::make_integer_sequence<int, N>;

constexpr int KV_NT = 1, W_NT = 0, X_NT = 0;
constexpr int SK = 4, SV = 4, SP = 5;
constexpr unsigned EASY_T = 1350, EASY2_T = 3100;
constexpr int EASY = 6, PACE_ATT = 1, EASY2 = 4;
constexpr unsigned WARM = 40;
constexpr int PGB = 9, AWSH = 100, BSHIFT = 0, ASHIFT = 0;

constexpr unsigned SLOT = 8192, LDS_BYTES = 20 * SLOT;
constexpr unsigned HOFF = 16u << 20;
constexpr float SC = 0.12751743082f;
constexpr float LMAX = 1048576.0f;

#define GAP() __builtin_amdgcn_sched_barrier(0)
#define PINS(x) asm volatile("" : "+s"(x))
#define PINV(x) asm volatile("" : "+v"(x))
DI unsigned rfl(unsigned x) { return __builtin_amdgcn_readfirstlane(x); }
DI unsigned cvtpk(float lo, float hi) { f32x2 v = {lo, hi}; bf16x2 b = __builtin_convertvector(v, bf16x2); return __builtin_bit_cast(unsigned, b); }
DI u32x4 pack2(const f32x4& a, const f32x4& b) { return u32x4{cvtpk(a[0], a[1]), cvtpk(a[2], a[3]), cvtpk(b[0], b[1]), cvtpk(b[2], b[3])}; }
DI u32x4 natural8(u32x4 w) {
  auto s0 = __builtin_amdgcn_permlane32_swap(w[0], w[2], false, false);
  auto s1 = __builtin_amdgcn_permlane32_swap(w[1], w[3], false, false);
  auto t0 = __builtin_amdgcn_permlane16_swap(s0[0], s0[1], false, false);
  auto t1 = __builtin_amdgcn_permlane16_swap(s1[0], s1[1], false, false);
  return u32x4{t0[0], t1[0], t0[1], t1[1]};
}
#define KSYM "_Z10fused_attnPKcS0_S0_S0_S0_S0_S0_S0_S0_S0_PcS1_"
#define PGL(c) ".if (((. - " KSYM ") >> 12) & 3) == " #c "\n777" #c ": s_cbranch_execz 777" #c "f\n.endif\n"
#define PG_DEFS ".set pgnext, 4096\n.macro pgstop\n.if (. - " KSYM ") >= pgnext\n.set pgnext, ((. - " KSYM ") | 4095) + 1\n" PGL(0) PGL(1) PGL(2) PGL(3) ".endif\n.endm\n" \
                ".macro mfp a:vararg\nv_mfma_f32_16x16x32_bf16 \\a\npgstop\n.endm\n"
#define PG_END "s_endpgm\n7770:\n7771:\n7772:\n7773:\n\ts_setpc_b64 vcc\n"
#define MF "mfp "
#define PGS "pgstop\n\t"
template <int SH = 0> DI void pg_walk(unsigned w) {
  asm volatile("s_mov_b64 exec, 0\n\ts_call_b64 vcc, .Lw1_%=\n\ts_branch .Lw2_%=\n.Lw1_%=:\n\ts_cmp_eq_u32 %0, 0\n\ts_cbranch_scc1 7770f\n\ts_cmp_eq_u32 %0, 1\n\ts_cbranch_scc1 7771f\n\t"
               "s_cmp_eq_u32 %0, 2\n\ts_cbranch_scc1 7772f\n\ts_branch 7773f\n.rept %1\n\ts_nop 0\n.endr\n.Lw2_%=:\n\ts_mov_b64 exec, -1" :: "s"(w), "i"(SH) : "vcc", "scc", "memory");
}
template <bool Z> DI void mm_vvv(f32x4& c, const bf16x8& a, const bf16x8& b) {
  if constexpr (Z) asm volatile(MF "%0, %1, %2, 0" : "=&v"(c) : "v"(a), "v"(b));
  else             asm volatile(MF "%0, %1, %2, %0" : "+v"(c) : "v"(a), "v"(b));
}
template <bool Z> DI void mm_vva(f32x4& c, const bf16x8& a, const bf16x8& b) {
  if constexpr (Z) asm volatile(MF "%0, %1, %2, 0" : "=&v"(c) : "v"(a), "a"(b));
  else             asm volatile(MF "%0, %1, %2, %0" : "+v"(c) : "v"(a), "a"(b));
}
DI void mm_vca(f32x4& d, const bf16x8& a, const bf16x8& b, const f32x4& c) { asm volatile(MF "%0, %1, %2, %3" : "=&v"(d) : "v"(a), "a"(b), "v"(c)); }
DI void mm_avv(f32x4& c, const bf16x8& a, const bf16x8& b) { asm volatile(MF "%0, %1, %2, %0" : "+a"(c) : "v"(a), "v"(b)); }
DI void set_m0(unsigned v) { asm volatile("s_mov_b32 m0, %0" :: "s"(v) : "memory"); }
template <int NT, int P> DI void dma_p(unsigned voff, srd_t srd, unsigned soff) {
  if constexpr (NT) asm volatile("buffer_load_dwordx4 %0, %1, %2 offen offset:%3 nt lds" :: "v"(voff), "s"(srd), "s"(soff), "i"(1024 * P) : "memory");
  else              asm volatile("buffer_load_dwordx4 %0, %1, %2 offen offset:%3 lds" :: "v"(voff), "s"(srd), "s"(soff), "i"(1024 * P) : "memory");
}
struct Src { unsigned v[4]; srd_t srd; };
template <int NT> DI void dma_unit(const Src& s, unsigned soff, unsigned ldsaddr) {
  asm volatile("s_mov_b32 m0, %0\n\ts_nop 0" :: "s"(ldsaddr) : "memory");
  dma_p<NT, 0>(s.v[0], s.srd, soff); dma_p<NT, 1>(s.v[1], s.srd, soff); dma_p<NT, 2>(s.v[2], s.srd, soff); dma_p<NT, 3>(s.v[3], s.srd, soff);
}
template <int NT> DI void dma_elem(const Src& s, unsigned soff, unsigned ustep, unsigned ldsaddr) { dma_unit<NT>(s, soff, ldsaddr); dma_unit<NT>(s, soff + ustep, ldsaddr + 4096u); }
template <int N> DI void wait_vm() { asm volatile("s_waitcnt vmcnt(%0)" :: "i"(N) : "memory"); }
template <int N, int L> DI void wait_vm_lgkm() { asm volatile("" ::: "memory"); __builtin_amdgcn_s_waitcnt((N & 15) | ((N >> 4) << 14) | 0x70 | (L << 8)); asm volatile("" ::: "memory"); }
DI void wait_lgkm0() { wait_vm_lgkm<63, 0>(); }
DI void barrier() { asm volatile("s_barrier" ::: "memory"); }
DI bf16x8 lds128(unsigned a) { return *(const LAS bf16x8*)(size_t)(a); }
DI bf16x4 lds_tr1(unsigned a) { return __builtin_amdgcn_ds_read_tr16_b64_v4bf16((LAS bf16x4*)(size_t)(a)); }
DI bf16x8 cat(bf16x4 lo, bf16x4 hi) { return __builtin_shufflevector(lo, hi, 0, 1, 2, 3, 4, 5, 6, 7); }
DI bf16x8 lds_tr(unsigned a0, unsigned a1) { return cat(lds_tr1(a0), lds_tr1(a1)); }
DI srd_t mk_srd(const void* base, unsigned bytes) { return __builtin_amdgcn_make_buffer_rsrc((void*)base, (short)0, (int)bytes, 0x00020000); }
template <bool VX> DI void mk_rows(Src& s, unsigned base, unsigned stride, unsigned lane) {
  const unsigned row = (lane >> 2) & 3u, cb = lane >> 4, sl = lane & 3u;
#pragma unroll
  for (int p = 0; p < 4; ++p) s.v[p] = base + row * stride + cb * 64u + ((sl ^ (VX ? 2 * (p & 1) : (p & 2))) << 4) + (4u * stride - 1024u) * p;
}
template <bool VX> DI void mk_w(Src& s, unsigned base, unsigned stride, unsigned lane) {
  const unsigned sub = lane >> 5, cb = (lane >> 4) & 1u, row = (lane >> 2) & 3u, sl = lane & 3u;
#pragma unroll
  for (int p = 0; p < 4; ++p) s.v[p] = base + (4u * sub + row) * stride + cb * 64u + ((sl ^ (VX ? 2u * sub : 2u * (p & 1))) << 4) + (8u * stride - 1024u) * p;
}
DI void touch(unsigned& d, unsigned& voff, const char* base) {
  asm volatile("global_load_dword %0, %1, %2 nt\n\tglobal_load_dword %0, %1, %2\n\tv_add_u32_e32 %1, 0x1000, %1" : "+v"(d), "+v"(voff) : "s"(base) : "memory");
}
struct Geo { int r0, r1, p0, p1, u, t0, t1, t2, t3; };
constexpr Geo GB = {8, 15, 16, 12, 11, 13, 14, 10, 9}, GA = {15, 8, 16, 9, 10, 14, 11, 12, 13};
constexpr unsigned KUS = 1u << GB.u;
template <bool isA> DI unsigned ksoffg(unsigned m, unsigned rk) {
  constexpr Geo G = isA ? GA : GB;
  const unsigned t = m + rk;
  return ((t & 1u) << G.t0) | (((t >> 1) & 1u) << G.t1) | (((t >> 2) & 1u) << G.t2) | (((t >> 3) & 1u) << G.t3);
}
DI unsigned ksoff(unsigned m, unsigned rk) { return ksoffg<false>(m, rk); }
template <bool isA, bool VX> DI void mk_src_kv(Src& s, const char* base, unsigned lane, unsigned w) {
  constexpr Geo G = isA ? GA : GB;
  s.srd = mk_srd(base, 131072u);
  const unsigned row = (lane >> 2) & 3u, sl = lane & 3u, o = ((row & 1u) << G.r0) + ((row >> 1) << G.r1) + (lane >> 4) * 64u;
#pragma unroll
  for (unsigned p = 0; p < (isA ? 2u : 4u); ++p)
    s.v[p] = o + ((sl ^ (VX ? 2u * (p & 1u) : isA ? 2u * (w & 1u) : (p & 2u))) << 4) + ((p & 1u) << G.p0) + ((p >> 1) << G.p1) - (isA ? 0u : 1024u * p);
}

struct PC {
  unsigned r01, r2;
  unsigned w01[2], w2[2];
  unsigned xb, xmask, xa;
  unsigned z, soff;
};
template <unsigned TS_, unsigned QS_, unsigned ZI_, unsigned ZM_, unsigned ZO_, unsigned US_, unsigned CS_, bool ROT_> struct PK {
  static constexpr unsigned TS = TS_, QS = QS_, ZI = ZI_, ZM = ZM_, ZO = ZO_, US = US_, CS = CS_; static constexpr bool ROT = ROT_; };
DI unsigned q_soff(unsigned z) { return ((z & 0xC300u) * 0x440u) & 0x3C0000u; }
template <int POS> DI unsigned ring_lds(const PC& c) { return POS == 2 ? c.r2 : c.r01 + POS * SLOT; }
template <int POS> DI unsigned ring_rd(const PC& c, int d) { return (POS == 2 ? c.w2[d & 1] : c.w01[d & 1] + POS * SLOT) + (d >> 1) * 256u; }
struct NoX { static constexpr bool ACT = true; template <int G> DI void run() const {} };
template <int SL, bool REV = false> struct EasyX {
  static constexpr bool ACT = true;
  unsigned long long& t; unsigned lim;
  template <int G> DI void run() const {
    if constexpr (G == 8) asm volatile("s_memrealtime %0" : "=s"(t));
    if constexpr (G == 16) PINS(t);
    if constexpr (G == 16) asm volatile("s_cmp_lt_i32 %0, 0\n\ts_cbranch_scc1 .Le_%=\n\ts_sleep %1\n.Le_%=:" :: "s"(REV ? lim - (unsigned)t : (unsigned)t - lim), "i"(SL) : "scc");
  }
};
template <class K, int POS, int S, bool Z, bool RD, bool DM, int NT, bool XT, int M, class X>
DI void proj_gap(f32x4 (&acc)[8][4], const bf16x8 (&cur)[4], bf16x8 (&nxt)[4], bf16x4 (&lo)[4], bf16x8 (&bf)[4], PC& c, unsigned soff, const Src& src, const Src& xs, unsigned xsoff, const X& ex) {
  constexpr int qb = M >> 2, db = M & 3;
  mm_vvv<Z>(acc[4 * S + db][qb], cur[db], bf[qb]);
  if constexpr (RD && M < 8) {
    constexpr int d = M >> 1; const unsigned a = S == 0 ? ring_rd<POS>(c, d) + 4096u : ring_rd<(POS + 1) % 3>(c, d);
    if constexpr (M & 1) nxt[d] = cat(lo[d], lds_tr1(a + K::TS)); else lo[d] = lds_tr1(a);
  }
  if constexpr (X::ACT && RD && S == 1 && db == 3) bf[qb] = lds128(c.xa + qb * K::QS);
  if constexpr (XT) {
    if constexpr (M < 8 && !(M & 1)) dma_p<X_NT, M / 2>(xs.v[M / 2], xs.srd, xsoff);
    if constexpr (M == 7) set_m0(ring_lds<POS>(c) + S * 4096u);
    if constexpr (M >= 8 && !(M & 1)) dma_p<NT, (M - 8) / 2>(src.v[(M - 8) / 2], src.srd, soff);
  } else if constexpr (DM && M >= 8 && !(M & 1)) dma_p<NT, (M - 8) / 2>(src.v[(M - 8) / 2], src.srd, soff);
  if constexpr (X::ACT && S == 0 && M == 9) { PINS(c.z); c.z = (c.z + K::ZI) & K::ZM; PINS(c.z); }
  if constexpr (X::ACT && S == 0 && M == 11) { c.xa = (c.z & c.xmask) | c.xb; PINV(c.xa); }
  if constexpr (S == 1 && DM && !K::ROT && M == 9) { PINS(c.soff); c.soff = (c.soff + K::CS) & (16u * K::CS - 1u); PINS(c.soff); }
  if constexpr (S == 1 && DM && K::ROT && M == 9) { PINS(c.z); c.soff = (c.z + 0x3100u) & 0xC300u; PINS(c.soff); }
  if constexpr (S == 1 && DM && K::ROT && M == 13) { PINS(c.soff); c.soff = (c.soff * 0x440u) & 0x3C0000u; PINS(c.soff); }
  ex.template run<16 * S + M>();
  GAP();
}
template <class K, int POS, int S, bool Z, bool RD, bool DM, int NT, int VM, bool BAR, bool XT, class X, int... Ms>
DI void proj_unit(f32x4 (&acc)[8][4], const bf16x8 (&cur)[4], bf16x8 (&nxt)[4], bf16x8 (&bf)[4], PC& c, const Src& src, const Src& xs, unsigned xsoff, unsigned xdst, const X& ex, iseq<Ms...>) {
  wait_vm_lgkm<(RD ? VM : 63), (S == 0 ? 2 : 0)>();
  if constexpr (BAR) barrier();
  const unsigned soff = c.soff + S * K::US;
  if constexpr (XT) set_m0(xdst); else if constexpr (DM) set_m0(ring_lds<POS>(c) + S * 4096u);
  GAP();
  bf16x4 lo[4];
  (proj_gap<K, POS, S, Z, RD, DM, NT, XT, Ms>(acc, cur, nxt, lo, bf, c, soff, src, xs, xsoff, ex), ...);
}
template <class K, int POS, bool Z, bool LAST, bool DM, int NT, int VM0, int VM1, bool BAR1 = false, bool XT0 = false, bool XT1 = false, class X = NoX>
DI void proj_elem(f32x4 (&acc)[8][4], bf16x8 (&a0)[4], bf16x8 (&a1)[4], bf16x8 (&bf)[4], PC& c, const Src& src, const X& ex = X{},
                  const Src* xs = nullptr, unsigned xso0 = 0, unsigned xd0 = 0, unsigned xso1 = 0, unsigned xd1 = 0) {
  proj_unit<K, POS, 0, Z, true, DM, NT, VM0, false, XT0>(acc, a0, a1, bf, c, src, xs ? *xs : src, xso0, xd0, ex, make_iseq<16>{});
  proj_unit<K, POS, 1, Z, !LAST, DM, NT, VM1, BAR1, XT1>(acc, a1, a0, bf, c, src, xs ? *xs : src, xso1, xd1, ex, make_iseq<16>{});
}
template <class K> DI void proj_setup(PC& c, unsigned tb, unsigned xb, unsigned z0, unsigned s3) {
  c.w01[0] = c.r01 + tb; c.w01[1] = c.w01[0] ^ 32u; c.w2[0] = c.r2 + tb; c.w2[1] = c.w2[0] ^ 32u;
  PINV(c.w01[0]); PINV(c.w01[1]); PINV(c.w2[0]); PINV(c.w2[1]);
  c.xb = xb; c.xmask = K::ZO; PINV(c.xmask);
  c.z = z0; c.soff = s3;
}
template <class K, int POS> DI void proj_start(bf16x8 (&a0)[4], bf16x8 (&bf)[4], PC& c, unsigned tb, unsigned xb, unsigned z0, unsigned s3) {
  proj_setup<K>(c, tb, xb, z0, s3);
  c.xa = (c.z & K::ZO) | xb; PINV(c.xa);
#pragma unroll
  for (int d = 0; d < 4; ++d) { const unsigned a = ring_rd<POS>(c, d); a0[d] = lds_tr(a, a + K::TS); }
  GAP();
#pragma unroll
  for (int q = 0; q < 4; ++q) bf[q] = lds128(c.xa + q * K::QS);
  GAP();
}
#define ACC_ALL(C, a) C(a[0][0]), C(a[0][1]), C(a[0][2]), C(a[0][3]), C(a[1][0]), C(a[1][1]), C(a[1][2]), C(a[1][3]), C(a[2][0]), C(a[2][1]), C(a[2][2]), C(a[2][3]), C(a[3][0]), C(a[3][1]), C(a[3][2]), C(a[3][3])
DI void acc_fence_v(f32x4 (&acc)[8][4]) {
  asm volatile("s_nop 7\n\ts_nop 3" : ACC_ALL("+v", acc) :: "memory");
  f32x4 (&hi)[4][4] = *(f32x4 (*)[4][4])&acc[4];
  asm volatile("" : ACC_ALL("+v", hi) :: "memory");
  GAP();
}
DI void acc_fence_a(f32x4 (&acc)[8][4]) {
  asm volatile("s_nop 7\n\ts_nop 3" : ACC_ALL("+a", acc) :: "memory");
  f32x4 (&hi)[4][4] = *(f32x4 (*)[4][4])&acc[4];
  asm volatile("" : ACC_ALL("+a", hi) :: "memory");
  GAP();
}

struct Stash { u32x4 p[64]; unsigned base[3]; };
template <int I> DI void stash_load1(Stash& st, const Src& sK, const Src& sV, unsigned& rk, unsigned wo_, unsigned (&sm)[16]) {
  constexpr int m = I >> 2, isV = (I >> 1) & 1, pc_ = I & 1;
  if constexpr (!isV && !pc_) { PINS(rk); sm[m] = ksoffg<true>(m, rk) + wo_; }
  const unsigned so = sm[m];
  if constexpr (I < 56) asm volatile("buffer_load_dwordx4 %0, %1, %2, %3 offen nt" : "=a"(st.p[I]) : "v"(isV ? sV.v[pc_] : sK.v[pc_]), "s"(isV ? sV.srd : sK.srd), "s"(so) : "memory");
  else                  asm volatile("buffer_load_dwordx4 %0, %1, %2, %3 offen nt" : "=v"(st.p[I]) : "v"(isV ? sV.v[pc_] : sK.v[pc_]), "s"(isV ? sV.srd : sK.srd), "s"(so) : "memory");
}
constexpr int stash_idx(int j) { return 4 * ((j >> 1) & 15) + 2 * (j >> 5) + (j & 1); }
template <int... Is> DI void stash_load(Stash& st, const Src& sK, const Src& sV, unsigned rk, unsigned wo_, iseq<Is...>) { unsigned sm[16]; (stash_load1<stash_idx(Is)>(st, sK, sV, rk, wo_, sm), ...); }
constexpr int kv_pair(int m) { constexpr int t[10] = {0, 1, 2, 3, 8, 9, 4, 5, 6, 7}; return t[m % 10]; }
constexpr unsigned long long kv_tab() { unsigned long long v = 0; for (int i = 0; i < 16; ++i) v |= (unsigned long long)kv_pair(i) << (4 * i); return v; }
template <int I> DI void stash_write1(const Stash& st) {
  constexpr int m = I >> 2, isV = (I >> 1) & 1, pc_ = I & 1, off = kv_pair(m) * 16384 + isV * 8192 + pc_ * 1024;
  if constexpr (I < 56) asm volatile("ds_write_b128 %0, %1 offset:%2" :: "v"(st.base[off >> 16]), "a"(st.p[I]), "i"(off & 65535) : "memory");
  else                  asm volatile("ds_write_b128 %0, %1 offset:%2" :: "v"(st.base[off >> 16]), "v"(st.p[I]), "i"(off & 65535) : "memory");
}

struct AC {
  f32x4 o[8][4];
  bf16x8 q[4][4];
  bf16x8 kf[8], vf[8], pf[4];
  f32x4 negm[4];
  f32x4 l[4];
  bf16x8 ones;
  unsigned ra[2];
};
typedef f32x4 ST[2][4];
constexpr int VR = 32;
constexpr int NEB = 22;
template <int E> DI float sm_val(ST& S) { return S[(E >> 2) & 1][E >> 3][E & 3]; }
template <int E> DI void sm_exp1(ST& S) { float a; asm volatile("v_exp_f32_e32 %0, %1" : "=v"(a) : "v"(sm_val<E>(S))); S[(E >> 2) & 1][E >> 3][E & 3] = a; }
template <int QB, int P> DI void sm_cvt(ST& S, AC& c) {
  u32x4 w = __builtin_bit_cast(u32x4, c.pf[QB]);
  unsigned r;
  asm volatile("v_cvt_pk_bf16_f32 %0, %1, %2" : "=v"(r) : "v"(sm_val<8 * QB + 2 * P>(S)), "v"(sm_val<8 * QB + 2 * P + 1>(S)));
  w[P] = r;
  c.pf[QB] = __builtin_bit_cast(bf16x8, w);
}
constexpr bool wide(int nt) { return nt == W_NT; }
template <bool DM, int NT, int VM, int R, int M> DI void mem_pre(unsigned ldsaddr) {
  if constexpr (M == 6) wait_lgkm0();
  constexpr int F = wide(NT) ? 2 : 6, H = wide(NT) ? 18 : 14;
  if constexpr (DM && (M == F || M == H)) set_m0(ldsaddr + (M == H ? 4096u : 0u));
  if constexpr (M == R && VM < 63) wait_vm<VM>();
}
template <bool DM, int NT, int M> DI void mem_post(const Src& src, unsigned soff, unsigned ustep) {
  if constexpr (wide(NT)) { if constexpr (DM && M < 32 && (M & 3) == 2) dma_p<NT, (M >> 2) & 3>(src.v[(M >> 2) & 3], src.srd, soff + (M >= 16 ? ustep : 0u)); }
  else if constexpr (DM && M >= 6 && M <= 20 && !(M & 1)) dma_p<NT, ((M - 6) / 2) & 3>(src.v[((M - 6) / 2) & 3], src.srd, soff + (M >= 14 ? ustep : 0u));
}
template <int WR, int M> DI void mem_wr(const Stash& st) {
  if constexpr (WR >= 40 && M >= 7 && M < 13) stash_write1<WR + M - 7>(st);
  if constexpr (WR >= 0 && WR < 40 && M >= 8 && M < 16)
    asm volatile("ds_write_b128 %0, %1 offset:%2" :: "v"(st.base[0]), "a"(st.p[WR + M - 8]), "i"((M - 8) * 1024) : "memory");
}
template <int N> using IC = std::integral_constant<int, N>;
struct PA { unsigned rd0, rd1, soff, fill; };
template <bool QK, bool DM, int NT, int VM, int WR, int M, class F>
DI void gapA(AC& c, ST& S, ST& Sn, PA& pa, F& addr, const Src& src, unsigned ustep, const Stash& st, bf16x4& lo) {
  constexpr int R = QK ? 16 : 0;
  if constexpr ((DM || !QK) && M == 0) addr(pa, IC<0>{});
  if constexpr (QK) mem_pre<DM, NT, VM, 16, M>(pa.fill); else if constexpr (M == 0 && VM < 63) wait_vm<VM>();
  if constexpr (QK) {
    constexpr int f = M >> 2, s = f >> 1, kb = f & 1, qb = M & 3;
    if constexpr (s == 0) mm_vca(Sn[kb][qb], c.kf[f], c.q[qb][0], c.negm[qb]); else mm_vva<false>(Sn[kb][qb], c.kf[f], c.q[qb][s]);
  }
  mem_post<DM, NT, M>(src, pa.soff, ustep);
  mem_wr<WR, M>(st);
  if constexpr (QK && !DM && M >= 10 && M < 16) addr(pa, IC<M - 10>{});
  if constexpr (QK && M == 12) asm volatile("" :: "v"(c.negm[0]), "v"(c.negm[1]), "v"(c.negm[2]), "v"(c.negm[3]));
  if constexpr (M >= R && M < R + 16) {
    constexpr int db = (M - R) >> 1; const unsigned a = ((db & 1) ? pa.rd1 : pa.rd0) + (db >> 1) * 256u;
    if constexpr (M & 1) c.vf[db] = cat(lo, lds_tr1(a + 4096u)); else lo = lds_tr1(a);
  }
  if constexpr (NEB + M < 32) sm_exp1<NEB + M>(S);
  if constexpr (M >= 16 && (M & 1)) sm_cvt<2 + ((M - 16) >> 3), ((M - 16) >> 1) & 3>(S, c);
  GAP();
}
template <bool QK, bool DM, int NT, int VM, int WR, class F, int... Ms>
DI void phaseA_(AC& c, ST& S, ST& Sn, F& addr, const Src& src, unsigned ustep, const Stash& st, iseq<Ms...>) {
  PA pa; bf16x4 lo;
  (gapA<QK, DM, NT, VM, WR, Ms>(c, S, Sn, pa, addr, src, ustep, st, lo), ...);
}
template <bool QK, bool DM, int NT, int VM, int WR = -1, class F>
DI void phaseA(AC& c, ST& S, ST& Sn, F addr, const Src& src, unsigned ustep, const Stash& st) {
  phaseA_<QK, DM, NT, VM, WR>(c, S, Sn, addr, src, ustep, st, make_iseq<32>{});
}
template <bool MAX> DI float xlane(float v) {
  auto a = __builtin_amdgcn_permlane32_swap(__float_as_uint(v), __float_as_uint(v), false, false);
  v = MAX ? fmaxf(__uint_as_float(a[0]), __uint_as_float(a[1])) : __uint_as_float(a[0]) + __uint_as_float(a[1]);
  auto b = __builtin_amdgcn_permlane16_swap(__float_as_uint(v), __float_as_uint(v), false, false);
  return MAX ? fmaxf(__uint_as_float(b[0]), __uint_as_float(b[1])) : __uint_as_float(b[0]) + __uint_as_float(b[1]);
}
DI float row_max(const f32x4& x, const f32x4& y) {
  return xlane<true>(fmaxf(fmaxf(fmaxf(x[0], x[1]), fmaxf(x[2], x[3])), fmaxf(fmaxf(y[0], y[1]), fmaxf(y[2], y[3]))));
}
template <int SM, bool KR, bool DM, int NT, int VM, int WR, int M, class F>
DI void gapB(AC& c, ST& Sn, PA& pa, F& addr, const Src& src, unsigned ustep, const Stash& st, bf16x4& lo) {
  mem_pre<DM, NT, VM, (KR ? 18 : VR), M>(pa.fill);
  {
    constexpr int h = M / 18, r = M % 18, qb = 2 * h + (r & 1);
    if constexpr (r < 2) mm_vvv<false>(c.l[qb], c.ones, c.pf[qb]);
    else if constexpr (SM == 2 && r < 6) mm_vvv<false>(c.o[(r - 2) >> 1][qb], c.vf[(r - 2) >> 1], c.pf[qb]);
    else mm_avv(c.o[(r - 2) >> 1][qb], c.vf[(r - 2) >> 1], c.pf[qb]);
  }
  mem_post<DM, NT, M>(src, pa.soff, ustep);
  mem_wr<WR, M>(st);
  if constexpr (M < 4) addr(pa, IC<M>{});
  if constexpr (KR && M >= 18 && M < 34 && !(M & 1)) { constexpr int f = (M - 18) >> 1; c.kf[f] = lds128(pa.rd0 + (f & 1) * 4096u + (f >> 1) * 256u); }
  constexpr int R = VM < 63 ? VR : 20;
  if constexpr (!KR && M >= R) {
    c.ra[0] = pa.rd0; c.ra[1] = pa.rd1;
    constexpr int db = (M - R) >> 1; const unsigned a = ((db & 1) ? pa.rd1 : pa.rd0) + (db >> 1) * 256u;
    if constexpr (M & 1) c.kf[db] = cat(lo, lds_tr1(a + 4096u)); else lo = lds_tr1(a);
  }
  if constexpr (SM) {
    if constexpr (M >= 4 && M < 18) sm_exp1<M - 4>(Sn);
    if constexpr (M >= 19 && M < 35 && (M & 1)) sm_exp1<14 + (M - 19) / 2>(Sn);
    if constexpr (M >= 20 && !(M & 1)) sm_cvt<((M - 20) >> 3), ((M - 20) >> 1) & 3>(Sn, c);
  }
  GAP();
}
template <int SM, bool KR, bool DM, int NT, int VM, int WR, class F, int... Ms>
DI void phaseB_(AC& c, ST& Sn, F& addr, const Src& src, unsigned ustep, const Stash& st, iseq<Ms...>) {
  PA pa; bf16x4 lo;
  (gapB<SM, KR, DM, NT, VM, WR, Ms>(c, Sn, pa, addr, src, ustep, st, lo), ...);
}
template <int SM, bool KR, bool DM, int NT, int VM, int WR = -1, class F>
DI void phaseB(AC& c, ST& Sn, F addr, const Src& src, unsigned ustep, const Stash& st) {
  phaseB_<SM, KR, DM, NT, VM, WR>(c, Sn, addr, src, ustep, st, make_iseq<36>{});
}
DI void k_read(AC& ac, unsigned ka) {
#pragma unroll
  for (int f = 0; f < 8; ++f) ac.kf[f] = lds128(ka + (f & 1) * 4096u + (f >> 1) * 256u);
}
template <bool DM, int M> DI void qk0_gap(AC& ac, ST& SA, const Src& src, unsigned soff, unsigned fill) {
  constexpr int f = M >> 2, qb = M & 3;
  if constexpr (DM && (M == 0 || M == 16)) set_m0(fill + M * 256u);
  mm_vva<(f < 2)>(SA[f & 1][qb], ac.kf[f], ac.q[qb][f >> 1]);
  if constexpr (DM && qb == 0) dma_p<KV_NT, f & 3>(src.v[f & 3], src.srd, soff + (f >> 2) * KUS);
  if constexpr (!DM && (M & 1)) { ac.o[4 + (M >> 3)][(M >> 1) & 3] = f32x4{}; asm volatile("" : "+a"(ac.o[4 + (M >> 3)][(M >> 1) & 3])); }
  GAP();
}
template <bool DM, int... Ms> DI void qk0(AC& ac, ST& SA, const Src& src, unsigned soff, unsigned fill, iseq<Ms...>) { (qk0_gap<DM, Ms>(ac, SA, src, soff, fill), ...); }
template <int... Js> DI void exp_first(ST& SA, iseq<Js...>) { (sm_exp1<Js>(SA), ...); }
template <int... Js> DI void cvt_first(ST& SA, AC& c, iseq<Js...>) { (sm_cvt<(Js >> 2), (Js & 3)>(SA, c), ...); }
DI void sm_first(AC& ac, ST& SA) {
  asm volatile("s_nop 7\n\ts_nop 3" : "+v"(SA[0][0]), "+v"(SA[0][1]), "+v"(SA[0][2]), "+v"(SA[0][3]), "+v"(SA[1][0]), "+v"(SA[1][1]), "+v"(SA[1][2]), "+v"(SA[1][3]) :: "memory");
  GAP();
#pragma unroll
  for (int q = 0; q < 4; ++q) {
    const float m = row_max(SA[0][q], SA[1][q]);
    SA[0][q] -= m; SA[1][q] -= m;
    ac.negm[q] = f32x4{-m, -m, -m, -m};
    ac.l[q] = f32x4{};
    ac.pf[q] = bf16x8{};
  }
  ac.ones = __builtin_bit_cast(bf16x8, u32x4{0x3f803f80u, 0x3f803f80u, 0x3f803f80u, 0x3f803f80u});
  asm volatile("" : "+v"(ac.negm[0]), "+v"(ac.negm[1]), "+v"(ac.negm[2]), "+v"(ac.negm[3]), "+v"(ac.l[0]), "+v"(ac.l[1]), "+v"(ac.l[2]), "+v"(ac.l[3]), "+v"(ac.ones));
  exp_first(SA, make_iseq<NEB>{});
  cvt_first(SA, ac, make_iseq<8>{});
  GAP();
}
struct BS { unsigned p[4], v[6], k[2], rk, wo, l16, w; };
constexpr bool b_dma(int f) { return f != 2 * (SK - 3) && f != 2 * (SV - 2) + 1 && f != 2 * (SP - 2) + 1 && f < 30 && f != 26; }
constexpr int b_vm(int f) {
  const int s = f == 29 ? 27 : f - 3;
  if (!b_dma(s)) return 63;
  int n = !b_dma(f) ? 0 : f == 28 ? 4 : f == 29 ? 8 : 5 + (f & 1);
  for (int i = s + 1; i < f; ++i) n += b_dma(i) ? 8 : 0;
  return n;
}
template <class KO, int I> DI void b_step(AC& ac, ST& S, ST& Sn, BS& b, const Src& sK, const Src& sV, const Src& sO, Stash& st) {
  constexpr int fa = 2 * I, fb = 2 * I + 1, vi = I == SP ? 4 : (I & 1) ? 0 : 2;
  const unsigned pa = b.p[(I & 1) ? 3 : 1], pb = b.p[(I & 1) ? 0 : 2];
  if constexpr (I == PGB) pg_walk<BSHIFT>(b.w);
  if constexpr (I >= 1 && I <= 13) barrier();
  if constexpr (I == SK - 3) st.base[0] = pa + b.l16;
  phaseA<true, b_dma(fa), (I < 14 ? KV_NT : W_NT), b_vm(fa), (I == SK - 3 ? 0 : -1)>(ac, S, Sn, [&](PA& p, auto k) __attribute__((always_inline)) {
    if (k.value) return;
    PINS(b.rk);
    p.soff = I < 14 ? ksoff(I < 13 ? I + 3 : 15, b.rk) : b.wo; p.fill = I == 14 ? b.p[0] : pa;
    p.rd0 = b.v[vi] + (I == SP ? SLOT : 0u); p.rd1 = b.v[vi + 1] + (I == SP ? SLOT : 0u); }, I < 13 ? sK : I < 14 ? sV : sO, I < 14 ? KUS : KO::US, st);
  if constexpr (I == SV - 2) st.base[0] = pb + b.l16;
  phaseB<2, (I < 14), b_dma(fb), (I < 14 ? KV_NT : W_NT), b_vm(fb), (I == SV - 2 ? 8 : -1)>(ac, Sn, [&](PA& p, auto k) __attribute__((always_inline)) {
      if (k.value) return;
      PINS(b.rk);
      p.soff = I < 14 ? ksoff(I + 2, b.rk) : b.wo + KO::CS; p.fill = I == 14 ? b.p[1] : I == 13 ? pa : pb; p.rd0 = I == 14 ? b.v[4] : b.k[(I & 1) ? 0 : 1]; p.rd1 = b.v[5]; }, I < 14 ? sV : sO, I < 14 ? KUS : KO::US, st);
}
template <int CH, int Q, int J, bool V = false> DI void o_word(const AC& c, const float (&inv)[5], bf16x8 (&nb)[4]) {
  u32x4 w = __builtin_bit_cast(u32x4, nb[Q]);
  unsigned r;
  if constexpr (V)
    asm volatile("v_mul_f32_e32 %0, %1, %3\n\tv_mul_f32_e32 %4, %2, %3\n\tv_cvt_pk_bf16_f32 %0, %0, %4"
                 : "=&v"(r) : "v"(c.o[2 * CH + (J >> 1)][Q][2 * (J & 1)]), "v"(c.o[2 * CH + (J >> 1)][Q][2 * (J & 1) + 1]), "v"(inv[Q]), "v"(inv[4]));
  else
  asm volatile("v_accvgpr_read_b32 %0, %1\n\tv_accvgpr_read_b32 %4, %2\n\tv_mul_f32_e32 %0, %0, %3\n\tv_mul_f32_e32 %4, %4, %3\n\tv_cvt_pk_bf16_f32 %0, %0, %4"
               : "=&v"(r) : "a"(c.o[2 * CH + (J >> 1)][Q][2 * (J & 1)]), "a"(c.o[2 * CH + (J >> 1)][Q][2 * (J & 1) + 1]), "v"(inv[Q]), "v"(inv[4]));
  w[J] = r;
  nb[Q] = __builtin_bit_cast(bf16x8, w);
}
template <int CH, int Q> DI void o_put(const bf16x8 (&nb)[4], unsigned ow) { asm volatile("ds_write_b128 %0, %1 offset:%2" :: "v"(ow), "v"(nb[Q]), "i"(CH * 4096 + Q * 1024) : "memory"); }
template <int E> struct CvX {
  static constexpr bool ACT = E == 3;
  const AC& c; const float (&inv)[5]; const bf16x8 (&cur)[4]; bf16x8 (&nb)[4]; unsigned ow;
  template <int G> DI void run() const {
    if constexpr (E < 3 && (G & 1)) o_word<E + 1, (G >> 3), (G >> 1) & 3>(c, inv, nb);
    if constexpr (G == 8 || G == 10) o_put<E, 2 + ((G >> 1) & 1)>(cur, ow);
    if constexpr (E < 3 && (G == 24 || G == 26)) o_put<E + 1, ((G >> 1) & 1)>(nb, ow);
  }
};
template <class K, int VM, int DM, int M> DI void gapE(AC& c, ST& S, float (&inv)[5], bf16x8 (&nb)[4], bf16x8 (&a0)[4], bf16x4& lo, const PC& pc, const Src& src, unsigned soff, unsigned long long& bad, unsigned ow) {
  constexpr int h = M / 18, r = M % 18, qb = 2 * h + (r & 1);
  if constexpr (M == 24) wait_vm<VM>();
  constexpr int D = M - (DM == 2 ? 4 : 0);
  constexpr int NR = DM == 2 ? 0 : VR - 20;
  if constexpr (M == NR + 4) wait_lgkm0();
  if constexpr (M == 6 && DM == 2) barrier();
  if constexpr (DM && (D == 3 || D == 19)) set_m0(pc.r2 + (D == 19 ? 4096u : 0u));
  if constexpr (r < 2) mm_vvv<false>(c.l[qb], c.ones, c.pf[qb]);
  else if constexpr (DM == 1 && r < 6) mm_vvv<false>(c.o[(r - 2) >> 1][qb], c.kf[(r - 2) >> 1], c.pf[qb]);
  else mm_avv(c.o[(r - 2) >> 1][qb], c.kf[(r - 2) >> 1], c.pf[qb]);
  if constexpr (DM && D > 0 && D < 32 && (D & 3) == 3) dma_p<W_NT, (D >> 2) & 3>(src.v[(D >> 2) & 3], src.srd, soff + (D >= 16 ? K::US : 0u));
  if constexpr (M < NR) {
    constexpr int db = (M + 16 - NR) >> 1; const unsigned a = c.ra[db & 1] + (db >> 1) * 256u;
    if constexpr (M & 1) c.kf[db] = cat(lo, lds_tr1(a + 4096u)); else lo = lds_tr1(a);
  }
  if constexpr (M >= 24 && M < 32) {
    constexpr int d = (M - 24) >> 1; const unsigned a = ring_rd<0>(pc, d);
    if constexpr (M & 1) a0[d] = cat(lo, lds_tr1(a + K::TS)); else lo = lds_tr1(a);
  }
  if constexpr (NEB + M < 32) sm_exp1<NEB + M>(S);
  if constexpr (M >= 10 && M < 14) { sm_cvt<2 + ((M - 10) >> 1), 2 * (M & 1)>(S, c); sm_cvt<2 + ((M - 10) >> 1), 2 * (M & 1) + 1>(S, c); }
  if constexpr (M == 14 || M == 15 || M == 22 || M == 23) asm volatile("v_rcp_f32_e32 %0, %1" : "=v"(inv[qb]) : "v"(c.l[qb][0]));
  if constexpr (M >= 16 && M < 22) o_word<0, ((M - 16) >> 2), (M & 3), (DM == 1)>(c, inv, nb);
  if constexpr (M >= 24 && M < 34) o_word<0, ((M - 18) >> 2), ((M - 18) & 3), (DM == 1)>(c, inv, nb);
  if constexpr (M == 32) o_put<0, 0>(nb, ow);
  if constexpr (M == 33) o_put<0, 1>(nb, ow);
  if constexpr (r == 4) asm volatile("" :: "v"(c.l[2 * h]), "v"(c.l[2 * h + 1]));
  if constexpr (M == 34)
    asm volatile("v_add_f32_e32 %1, %2, %3\n\tv_add_f32_e32 %1, %1, %4\n\tv_add_f32_e32 %1, %1, %5\n\tv_cmp_nle_f32_e64 %0, %1, %6"
                 : "=s"(bad) : "v"(inv[4]), "v"(c.l[0][0]), "v"(c.l[1][0]), "v"(c.l[2][0]), "v"(c.l[3][0]), "s"(LMAX));
  GAP();
}
template <class K, int VM, int DM, int... Ms> DI void phaseE(AC& c, ST& S, float (&inv)[5], bf16x8 (&nb)[4], bf16x8 (&a0)[4], const PC& pc, const Src& src, unsigned soff, unsigned long long& bad, unsigned ow, iseq<Ms...>) {
  bf16x4 lo;
#pragma unroll
  for (int q = 0; q < 4; ++q) nb[q] = bf16x8{};
  (gapE<K, VM, DM, Ms>(c, S, inv, nb, a0, lo, pc, src, soff, bad, ow), ...);
}
struct Pub { u32x4 pk[16]; srd_t srd; unsigned v[4], so; };
struct TL { bf16x8 w[2][3][4], x[2][3][4]; bf16x4 lo[2]; unsigned xa[3], xs[3], drop[2], l16; };
template <int S, int QB, int J> DI void pub_word(const f32x4 (&acc)[8][4], Pub& p) {
  const f32x4& a = acc[4 * S + (J >> 1)][QB];
  unsigned r;
  asm volatile("v_cvt_pk_bf16_f32 %0, %1, %2" : "=v"(r) : "v"(a[2 * (J & 1)]), "v"(a[2 * (J & 1) + 1]));
  p.pk[8 * S + 2 * QB + (J >> 2)][J & 3] = r;
}
template <int F> DI void pub_store(const Pub& p) {
  asm volatile("buffer_store_dwordx4 %0, %1, %2, %3 offen offset:%4 sc0 sc1" :: "v"(p.pk[F]), "v"(p.v[F >> 2]), "s"(p.srd), "s"(p.so), "i"((F & 3) * 1024) : "memory");
}
template <class K, int S, int E, int R> DI void tl_w(TL& t, const PC& c) {
  const unsigned a = ring_rd<(E + 1) % 3>(c, R >> 1) + S * 4096u;
  if constexpr (R & 1) t.w[S][E][R >> 1] = cat(t.lo[S], lds_tr1(a + K::TS)); else t.lo[S] = lds_tr1(a);
}
template <class K, int G> DI void tl_gap(f32x4 (&acc)[8][4], TL& t, const PC& c, Pub& p) {
  constexpr int S = G >= 48, qb = G < 16 ? G >> 2 : G < 48 ? (G - 16) >> 3 : (G - 48) / 12, e = G < 16 ? 0 : G < 48 ? 1 + ((G >> 2) & 1) : G % 3, db = G < 48 ? G & 3 : (G % 12) / 3;
  if constexpr (G == 10) wait_vm<4>();
  if constexpr (G == 27) wait_vm<0>();
  mm_vvv<false>(acc[4 * S + db][qb], t.w[S][e][db], t.x[S][e][qb]);
  if constexpr (G >= 28 && G <= 56 && !(G & 3)) pub_store<(G - 28) / 4>(p);
  if constexpr (G >= 59 && G % 6 == 5) pub_store<8 + (G - 59) / 6>(p);
  if constexpr (G < 8) tl_w<K, 0, 1, G>(t, c);
  if constexpr (G >= 10 && G < 18) tl_w<K, 0, 2, G - 10>(t, c);
  if constexpr (G >= 16 && G < 24) tl_w<K, 1, 0, G - 16>(t, c);
  if constexpr (G >= 25 && G < 56 && (G & 1)) tl_w<K, 1, 1 + (((G - 25) >> 2) & 1), 2 * ((G - 25) >> 3) + (((G - 25) >> 1) & 1)>(t, c);
  if constexpr (G < 34 && (G & 3) == 2) t.x[0][1 + ((G >> 2) & 1)][G >> 3] = lds128(t.xa[1 + ((G >> 2) & 1)] + (G >> 3) * K::QS);
  if constexpr (G >= 34 && G < 82 && (G & 3) == 2) t.x[1][((G - 34) % 12) >> 2][(G - 34) / 12] = lds128(t.xa[((G - 34) % 12) >> 2] + ((G - 34) / 12) * K::QS);
  if constexpr (G >= 24 && G < 56) pub_word<0, ((G - 24) >> 3), (G & 7)>(acc, p);
  if constexpr (G >= 54 && G % 3 < 2) pub_word<1, (G - 54) / 12, 2 * (((G - 54) % 12) / 3) + G % 3>(acc, p);
  if constexpr (G == 0 || G == 1 || G == 3) { t.xa[(G + 1) & 3] = t.xs[(G + 1) & 3] | t.l16; PINV(t.xa[(G + 1) & 3]); }
  if constexpr (G == 4 || G == 5) { p.v[2 * (G & 1)] = t.drop[G & 1] | t.l16; PINV(p.v[2 * (G & 1)]); }
  if constexpr (G == 7 || G == 8) { p.v[2 * (G & 1) + 1] = p.v[2 * (G & 1)] | 4096u; PINV(p.v[2 * (G & 1) + 1]); }
  GAP();
}
template <class K, int... Gs> DI void tl_run(f32x4 (&acc)[8][4], TL& t, const PC& c, Pub& p, iseq<Gs...>) {
  wait_vm_lgkm<12, 2>();
  GAP();
  (tl_gap<K, Gs>(acc, t, c, p), ...);
  pub_word<1, 3, 4>(acc, p); pub_word<1, 3, 5>(acc, p);
  asm volatile("s_nop 4");
  pub_word<1, 3, 6>(acc, p); pub_word<1, 3, 7>(acc, p); pub_store<15>(p);
}
template <int I> DI void own_put1(const u32x4& v, unsigned a) { asm volatile("ds_write_b128 %0, %1 offset:%2 ; %3" :: "v"(a), "v"(v), "i"((I & 7) * 1024), "i"(I) : "memory"); }
template <int B, int... Is> DI void own_put(const Pub& p, unsigned a, iseq<Is...>) { (own_put1<B + Is>(p.pk[B + Is], a), ...); }
DI void redo_head(const AC& ac, const char* __restrict__ Kp, const char* __restrict__ Vp, unsigned lane, unsigned ow) {
  const unsigned r16 = lane & 15u, g = lane >> 4;
#pragma unroll 1
  for (unsigned qb = 0; qb < 4; ++qb) {
    bf16x8 qq[4];
#pragma unroll
    for (int s = 0; s < 4; ++s) qq[s] = qb == 0 ? ac.q[0][s] : qb == 1 ? ac.q[1][s] : qb == 2 ? ac.q[2][s] : ac.q[3][s];
    f32x4 o[8];
#pragma unroll
    for (int d = 0; d < 8; ++d) o[d] = f32x4{};
    float m = -__builtin_inff(), l = 0.f;
#pragma unroll 1
    for (unsigned t = 0; t < 16; ++t) {
      f32x4 s0 = {}, s1 = {};
      const char* kp = Kp + (t * 32u + r16) * 256u + g * 16u;
#pragma unroll
      for (int s = 0; s < 4; ++s) {
        s0 = __builtin_amdgcn_mfma_f32_16x16x32_bf16(*(const bf16x8*)(kp + s * 64), qq[s], s0, 0, 0, 0);
        s1 = __builtin_amdgcn_mfma_f32_16x16x32_bf16(*(const bf16x8*)(kp + s * 64 + 4096), qq[s], s1, 0, 0, 0);
      }
      const float mn = fmaxf(m, row_max(s0, s1)), al = __builtin_amdgcn_exp2f(m - mn);
      m = mn;
#pragma unroll
      for (int j = 0; j < 4; ++j) { s0[j] = __builtin_amdgcn_exp2f(s0[j] - mn); s1[j] = __builtin_amdgcn_exp2f(s1[j] - mn); }
      l = l * al + ((s0[0] + s0[1]) + (s0[2] + s0[3])) + ((s1[0] + s1[1]) + (s1[2] + s1[3]));
      const bf16x8 pf = __builtin_bit_cast(bf16x8, pack2(s0, s1));
      const __bf16* vp = (const __bf16*)(Vp + (t * 32u + g * 4u) * 256u) + r16;
#pragma unroll
      for (int d = 0; d < 8; ++d) {
        bf16x8 vf;
#pragma unroll
        for (int j = 0; j < 8; ++j) vf[j] = vp[((j & 3) + (j >> 2) * 16) * 128 + d * 16];
        o[d] = __builtin_amdgcn_mfma_f32_16x16x32_bf16(vf, pf, o[d] * al, 0, 0, 0);
      }
    }
    const float inv = 1.0f / xlane<false>(l);
#pragma unroll
    for (int s = 0; s < 4; ++s) *(LAS u32x4*)(size_t)(ow + s * 4096u + qb * 1024u) = pack2(o[2 * s] * inv, o[2 * s + 1] * inv);
  }
}
template <bool isA, int J> DI void q_conv1(AC& ac, f32x4 (&acc)[8][4], Stash& st, const Src& sK, const Src& sV, unsigned rk, unsigned p3) {
  constexpr int s = J >> 2, qb = J & 3;
  if constexpr (isA) { stash_write1<(5 * J) / 2>(st); stash_write1<(5 * J) / 2 + 1>(st); if constexpr (J & 1) stash_write1<(5 * J) / 2 + 2>(st); }
  else {
    constexpr int p = 2 * (J & 1);
    const Src& src = s < 2 ? sK : sV;
    const unsigned so = ksoff(s == 0 ? 2 : s == 1 ? SK : s == 2 ? SV : SP, rk) + ((J >> 1) & 1) * KUS;
    if constexpr (s == 0 || s == 3) {
      if constexpr (p == 0) asm volatile("s_mov_b32 m0, %0\n\ts_nop 0" :: "s"(p3 + (s == 3 ? SLOT : 0u) + (J & 2) * 2048u) : "memory");
      dma_p<KV_NT, p>(src.v[p], src.srd, so); dma_p<KV_NT, p + 1>(src.v[p + 1], src.srd, so);
    } else {
      asm volatile("buffer_load_dwordx4 %0, %1, %2, %3 offen offset:%4 nt" : "=a"(st.p[2 * J - 8]) : "v"(src.v[p]), "s"(src.srd), "s"(so), "i"(1024 * p) : "memory");
      asm volatile("buffer_load_dwordx4 %0, %1, %2, %3 offen offset:%4 nt" : "=a"(st.p[2 * J - 7]) : "v"(src.v[p + 1]), "s"(src.srd), "s"(so), "i"(1024 * p + 1024) : "memory");
    }
  }
  ac.q[qb][s] = __builtin_bit_cast(bf16x8, natural8(pack2(acc[2 * s][qb] * SC, acc[2 * s + 1][qb] * SC)));
  asm volatile("" : "+a"(ac.q[qb][s]));
  GAP();
}
template <bool isA, int... Js> DI void q_conv(AC& ac, f32x4 (&acc)[8][4], Stash& st, const Src& sK, const Src& sV, unsigned rk, unsigned p3, iseq<Js...>) { (q_conv1<isA, Js>(ac, acc, st, sK, sV, rk, p3), ...); }
template <int N, int N0 = 0> DI void zero_o(AC& ac) {
#pragma unroll
  for (int i = N0; i < N; ++i)
#pragma unroll
    for (int q = 0; q < 4; ++q) { ac.o[i][q] = f32x4{}; asm volatile("" : "+a"(ac.o[i][q])); }
}

template <bool isA>
DI void body(const char* __restrict__ X, const char* __restrict__ K, const char* __restrict__ V, const char* __restrict__ Wq, const char* __restrict__ Wo,
             const char* __restrict__ Wo2, unsigned long long tnow, char* __restrict__ part, char* __restrict__ out) {
  const unsigned tlim = (unsigned)tnow + EASY_T;
  unsigned tid = threadIdx.x;
  PINV(tid);
  const unsigned lane = tid & 63u, w = rfl(tid >> 6);
  const unsigned bid = blockIdx.x, xg = bid & 7u, rr = bid >> 3, b = rr & 15u, h = xg * 4u + w;
  const unsigned r16 = lane & 15u, g = lane >> 4;
  const size_t kvo = isA ? (size_t)(b * 8u + xg) * 131072u : (size_t)(b * 32u + h) * 131072u;
  const unsigned rk = isA ? 7u * b : 5u * b + 8u * w;
  typedef PK<512u, 4096u, 0x1300u, 0xF300u, 0xC300u, 128u, 32u * 8192u, true> KQ;
  typedef PK<512u, 4096u, 0x1300u, 0xF300u, 0xC300u, KUS, 8192u, false> KT;
  typedef PK<2048u, 1024u, 0x1000u, 0xF000u, 0xF000u, 128u, 32u * 1024u, false> KO;
  PC pc;
  pc.r01 = 65536u + w * 16384u; pc.r2 = 131072u + w * 8192u;
  const unsigned zq = ((b & 3u) << 14) | ((b >> 2) << 8), co = 4u * w;
  const srd_t srdH = mk_srd(part + HOFF, 4096u + 256u * 128u);
  const unsigned tok_off = rr * 128u, flg_off = 4096u + rr * 1024u;
  const unsigned l8 = (lane & 7u) * 4u;
  unsigned entry_flags, dum0 = lane;
  Src sQ, sX, sK, sV, sO;
  const unsigned p0 = b & 3u, p1 = (p0 + 1u) & 3u, p2 = (p0 + 2u) & 3u, p3 = (p0 + 3u) & 3u, xw = w * 4096u;
  Stash st;
  mk_src_kv<isA, false>(sK, K + kvo, lane, w);
  mk_src_kv<isA, true>(sV, V + kvo, lane, w);
  if constexpr (isA) { stash_load(st, sK, sV, rk, ((w & 1u) << GA.p1) + ((w >> 1) << GA.u), make_iseq<64>{}); GAP(); }
  asm volatile("buffer_load_dword %0, %1, %2, %3 offen sc1" : "=v"(entry_flags) : "v"(l8), "s"(srdH), "s"(flg_off + xg * 128u) : "memory");
  sQ.srd = mk_srd(Wq, 4194304u); sX.srd = mk_srd(X, 1048576u);
  mk_w<false>(sQ, h * 256u, 8192u, lane);
  mk_rows<false>(sX, b * 65536u + w * 16384u, 1024u, lane);
  sO.srd = mk_srd(Wo + (size_t)xg * 524288u, 524288u);
  mk_w<true>(sO, w * 256u, 1024u, lane);
  if constexpr (isA) {
    dma_unit<X_NT>(sX, p0 * 256u, xw + p0 * 16384u);
    dma_elem<W_NT>(sQ, q_soff(zq + 0x2200u), KQ::US, pc.r01 + SLOT);
    dma_unit<X_NT>(sX, p1 * 256u, xw + p1 * 16384u);
    dma_unit<X_NT>(sX, p2 * 256u, xw + p2 * 16384u);
    dma_unit<X_NT>(sX, p3 * 256u, xw + p3 * 16384u);
  }
  srd_t pb_srd = mk_srd(part + (size_t)rr * 524288u, 524288u);
  unsigned pb_so = xg * 65536u + w * 16384u, drop0 = 2u * w == xg ? 0x80000000u : 0u, drop1 = 2u * w + 1u == xg ? 0x80002000u : 0x2000u;
  unsigned xs0 = ((co + 13u) & 15u) << 12, xs1 = ((co + 14u) & 15u) << 12, xs2 = ((co + 15u) & 15u) << 12, oz = (co + 3u) << 12, os3 = (co + 3u) * KO::CS;
  unsigned ks0 = ksoff(0u, rk), ks1 = ksoff(1u, rk);
  {
    PINS(pb_srd); PINS(pb_so); PINS(drop0); PINS(drop1); PINS(xs0); PINS(xs1); PINS(xs2); PINS(oz); PINS(os3); PINS(sO.srd);
    if constexpr (!isA) {
      PINS(sK.srd); PINS(sV.srd); PINS(ks0); PINS(ks1);
#pragma unroll
      for (int i = 0; i < 4; ++i) { PINV(sK.v[i]); PINV(sV.v[i]); PINV(sO.v[i]); }
    }
  }
  const unsigned rb = 1024u * (r16 >> 2) + 64u * (r16 & 3u) + 16u * (g ^ ((r16 >> 2) & 2u));
  const unsigned tb = 64u * ((lane >> 2) & 3u) + 32u * (g & 1u) + 8u * (lane & 3u);
  const unsigned tbV = tb + 1024u * g;
  const unsigned sb0 = w * 2048u + lane * 16u;
  st.base[0] = sb0; st.base[1] = sb0 + 65536u; st.base[2] = sb0 + 131072u;
  AC ac;
  if constexpr (!isA) zero_o<8, 2>(ac);
  {
    f32x4 acc[8][4];
    bf16x8 a0[4], a1[4], bf[4];
    if constexpr (isA) pg_walk<AWSH>(w);
    wait_vm<(isA ? 20 : 9)>();
    barrier();
    proj_start<KQ, 2>(a0, bf, pc, tbV, rb, zq, q_soff(zq + 0x3100u));
    if constexpr (isA) {
      proj_elem<KQ, 2, true,  false, true, W_NT, 63, 63>(acc, a0, a1, bf, pc, sQ);
      proj_elem<KQ, 0, false, false, true, W_NT, 63, 28>(acc, a0, a1, bf, pc, sQ);
      proj_elem<KQ, 1, false, false, true, W_NT, 28, 16>(acc, a0, a1, bf, pc, sQ);
      proj_elem<KQ, 2, false, false, true, W_NT, 16, 16, true>(acc, a0, a1, bf, pc, sQ);
    } else {
      dma_elem<W_NT>(sQ, q_soff(zq + 0x2200u), KQ::US, pc.r01 + SLOT);
      proj_elem<KQ, 2, true,  false, true, W_NT, 13, 20, false, true, true>(acc, a0, a1, bf, pc, sQ, NoX{}, &sX, p1 * 256u, xw + p1 * 16384u, p2 * 256u, xw + p2 * 16384u);
      proj_elem<KQ, 0, false, false, true, W_NT, 24, 28, false, true, false>(acc, a0, a1, bf, pc, sQ, NoX{}, &sX, p3 * 256u, xw + p3 * 16384u);
      proj_elem<KQ, 1, false, false, true, W_NT, 28, 24>(acc, a0, a1, bf, pc, sQ);
      proj_elem<KQ, 2, false, false, true, W_NT, 20, 16, true>(acc, a0, a1, bf, pc, sQ);
    }
    typedef std::conditional_t<isA, EasyX<EASY>, NoX> PX;
    const PX px = [&] { if constexpr (isA) return EasyX<EASY>{tnow, tlim}; else return NoX{}; }();
#pragma unroll 1
    for (unsigned e = 0; e < 3; ++e) {
      proj_elem<KQ, 0, false, false, true, W_NT, 16, 16>(acc, a0, a1, bf, pc, sQ, px);
      proj_elem<KQ, 1, false, false, true, W_NT, 16, 16>(acc, a0, a1, bf, pc, sQ, px);
      proj_elem<KQ, 2, false, false, true, W_NT, 16, 16>(acc, a0, a1, bf, pc, sQ, px);
    }
    if constexpr (isA) {
      proj_elem<KQ, 0, false, false, false, W_NT, 16, 12>(acc, a0, a1, bf, pc, sQ, px);
      proj_elem<KQ, 1, false, false, false, W_NT, 8, 4>(acc, a0, a1, bf, pc, sQ, px);
      proj_elem<KQ, 2, false, true, false, W_NT, 0, 0>(acc, a0, a1, bf, pc, sQ, px);
    } else {
      pc.soff = ks0;
      proj_elem<KT, 0, false, false, true, KV_NT, 16, 16>(acc, a0, a1, bf, pc, sK);
      pc.soff = ks1;
      proj_elem<KT, 1, false, false, true, KV_NT, 16, 16>(acc, a0, a1, bf, pc, sK);
      pc.soff = ks0;
      proj_elem<KT, 2, false, true, true, KV_NT, 16, 16>(acc, a0, a1, bf, pc, sV);
    }
    PINV(entry_flags);
    if (w == 0) {
      const unsigned long long mk = __builtin_amdgcn_ballot_w64(((entry_flags >> (lane & 7u)) & 1u) != 0u);
      const unsigned tok = 0x5a5a5a00u | (~(unsigned)mk & 0xffu);
      if (lane == 0) asm volatile("buffer_store_dword %0, off, %1, %2 sc1" :: "v"(tok), "s"(srdH), "s"(tok_off + xg * 4u) : "memory");
    }
    barrier();
    acc_fence_v(acc);
    q_conv<isA>(ac, acc, st, sK, sV, rk, w * 16384u, make_iseq<16>{});
  }
  ST SA, SB;
  if constexpr (isA) {
    zero_o<4>(ac);
    const unsigned kb = rb, vb = SLOT + tbV;
    unsigned dum = lane, to = ((lane & 8u) * 16u + (lane & 7u) * 4u + (lane >> 4) * 32768u) | (((lane >> 4) ^ w) << 8);
    const unsigned tbo = (b & 3u) * 1024u + (b >> 2) * 131072u;
    PINV(to);
    const char* ta = Wo + (size_t)xg * 524288u + tbo;
    const char* tb2 = Wo2 + (size_t)xg * 524288u + tbo;
    touch(dum, to, ta);
    asm volatile("s_waitcnt lgkmcnt(0)" ::: "memory");
    barrier();
    k_read(ac, kb + kv_pair(0) * 16384u);
    wait_lgkm0();
    GAP();
    qk0<false>(ac, SA, sK, 0u, 0u, make_iseq<32>{});
    k_read(ac, kb + kv_pair(1) * 16384u);
    touch(dum, to, ta);
    sm_first(ac, SA);
    unsigned i4 = 0;
    unsigned blk; const char* tp;
    auto ak = [&](PA& p, auto k) __attribute__((always_inline)) {
      if constexpr (k.value == 0) { __builtin_amdgcn_s_sleep(PACE_ATT); PINS(i4); blk = (unsigned)((kv_tab() >> 8) >> i4) << 14; i4 += 4u; PINS(blk); PINS(i4); }
      if constexpr (k.value == 1) { p.rd0 = kb + (blk & 0x3c000u); PINV(p.rd0); }
    };
    auto at = [&](PA& p, auto k) __attribute__((always_inline)) {
      if constexpr (k.value == 0) { PINS(i4); blk = (unsigned)(kv_tab() >> i4) << 14; PINS(blk); }
      if constexpr (k.value == 1) { p.rd0 = vb + (blk & 0x3c000u); PINV(p.rd0); }
      if constexpr (k.value == 2) asm volatile("s_cmp_lt_u32 %1, 24\n\ts_cselect_b64 %0, %2, %3" : "=s"(tp) : "s"(i4), "s"(ta), "s"(tb2 - 32768) : "scc");
      if constexpr (k.value == 3) { p.rd1 = p.rd0 ^ 32u; PINV(p.rd1); asm volatile("global_load_dword %0, %1, %2 nt" : "+v"(dum) : "v"(to), "s"(tp) : "memory"); }
      if constexpr (k.value == 4) asm volatile("global_load_dword %0, %1, %2\n\tv_add_u32_e32 %1, %3, %1" : "+v"(dum), "+v"(to) : "s"(tp), "i"(4096) : "memory");
    };
    while (i4 < 24u) {
      phaseA<true, false, KV_NT, 63>(ac, SA, SB, at, sK, 0u, st);
      phaseB<true, true, false, KV_NT, 63>(ac, SB, ak, sK, 0u, st);
      phaseA<true, false, KV_NT, 63>(ac, SB, SA, at, sK, 0u, st);
      phaseB<true, true, false, KV_NT, 63>(ac, SA, ak, sK, 0u, st);
    }
    barrier();
    phaseA<true, false, KV_NT, 63, 40>(ac, SA, SB, at, sK, 0u, st);
    phaseB<true, true, false, KV_NT, 63, 46>(ac, SB, ak, sK, 0u, st);
    phaseA<true, false, KV_NT, 63, 52>(ac, SB, SA, at, sK, 0u, st);
    phaseB<true, true, false, KV_NT, 63, 58>(ac, SA, ak, sK, 0u, st);
    wait_vm_lgkm<8, 8>(); barrier();
    while (i4 < 56u) {
      phaseA<true, false, KV_NT, 63>(ac, SA, SB, at, sK, 0u, st);
      phaseB<true, true, false, KV_NT, 63>(ac, SB, ak, sK, 0u, st);
      phaseA<true, false, KV_NT, 63>(ac, SB, SA, at, sK, 0u, st);
      phaseB<true, true, false, KV_NT, 63>(ac, SA, ak, sK, 0u, st);
    }
    barrier();
    unsigned v14 = vb + kv_pair(14) * 16384u, x14 = v14 ^ 32u; PINV(v14); PINV(x14);
    phaseA<true, true, W_NT, 63>(ac, SA, SB, [&](PA& p, auto) { p.soff = co * KO::CS; p.fill = pc.r01; p.rd0 = v14; p.rd1 = x14; }, sO, KO::US, st);
    phaseB<true, false, true, W_NT, 63>(ac, SB, [&](PA& p, auto k) __attribute__((always_inline)) {
      p.soff = (co + 1u) * KO::CS; p.fill = pc.r01 + SLOT; p.rd0 = v14 + 16384u; p.rd1 = x14 + 16384u;
      if constexpr (k.value == 1) proj_setup<KO>(pc, tb + 512u * g, lane * 16u, oz, os3); }, sO, KO::US, st);
    dum0 = dum;
  } else {
    const unsigned P0 = pc.r01, P1 = pc.r01 + SLOT, P2 = pc.r2, P3 = w * 16384u;
    unsigned v0 = tbV + P0, v2 = tbV + P2, x0 = v0 ^ 32u, x2 = v2 ^ 32u, k1 = rb + P1, k3 = rb + P3;
    PINV(v0); PINV(v2); PINV(x0); PINV(x2); PINV(k1); PINV(k3);
    wait_vm<48>();
    k_read(ac, k1 - SLOT);
#pragma unroll
    for (int i = 0; i < 8; ++i) { ac.o[i >> 2][i & 3] = f32x4{}; PINV(ac.o[i >> 2][i & 3]); }
    wait_lgkm0();
    GAP();
    qk0<true>(ac, SA, sV, ks1, P0, make_iseq<32>{});
    wait_vm<48>();
    GAP();
    k_read(ac, k1);
    sm_first(ac, SA);
    BS bs{{P0, P1, P2, P3}, {v0, x0, v2, x2, tbV + P3, (tbV + P3) ^ 32u}, {k1, k3}, rk, co * KO::CS, lane * 16u, w};
    PINV(bs.v[4]); PINV(bs.v[5]);
    proj_setup<KO>(pc, tb + 512u * g, lane * 16u, oz, os3);
#define B2(I) b_step<KO, I>(ac, SA, SB, bs, sK, sV, sO, st); b_step<KO, I + 1>(ac, SB, SA, bs, sK, sV, sO, st);
    B2(0) B2(2) B2(4) B2(6) B2(8) B2(10)
    B2(12) b_step<KO, 14>(ac, SA, SB, bs, sK, sV, sO, st);
#undef B2
  }
  {
    const unsigned l16 = lane * 16u;
    unsigned ow = w * 16384u + l16;
    PINV(ow);
    f32x4 acc[8][4];
    bf16x8 a0[4], a1[4], b0[4], bf[4];
    float inv[5];
    asm volatile("" : "=v"(inv[4]));
    unsigned toks;
    asm volatile("buffer_load_dword %0, %1, %2, %3 offen sc1" : "=v"(toks) : "v"(l8), "s"(srdH), "s"(tok_off) : "memory");
    GAP();
    unsigned long long bad;
    phaseE<KO, (isA ? 18 : 19), (isA ? 2 : 1)>(ac, SB, inv, b0, a0, pc, sO, os3 - KO::CS, bad, ow, make_iseq<36>{});
    if (__builtin_expect(bad != 0ull, 0)) {
      asm volatile("s_waitcnt vmcnt(0)" : "+v"(toks) :: "memory");
      acc_fence_a(ac.o);
      redo_head(ac, K + kvo, V + kvo, lane, ow);
#pragma unroll
      for (int q = 0; q < 4; ++q) {
        inv[q] = 1.0f;
        b0[q] = lds128(ow + q * 1024u);
#pragma unroll
        for (int s = 1; s < 4; ++s) {
          const u32x4 t = __builtin_bit_cast(u32x4, lds128(ow + s * 4096u + q * 1024u));
          ac.o[2 * s][q] = f32x4{__uint_as_float(t[0] << 16), __uint_as_float(t[0] & 0xffff0000u), __uint_as_float(t[1] << 16), __uint_as_float(t[1] & 0xffff0000u)};
          ac.o[2 * s + 1][q] = f32x4{__uint_as_float(t[2] << 16), __uint_as_float(t[2] & 0xffff0000u), __uint_as_float(t[3] << 16), __uint_as_float(t[3] & 0xffff0000u)};
        }
      }
      wait_lgkm0();
      acc_fence_a(ac.o);
    }
#pragma unroll
    for (int q = 0; q < 4; ++q) bf[q] = bf16x8{};
    GAP();
    proj_elem<KO, 0, true, false, true, W_NT, 16, 16>(acc, a0, a1, b0, pc, sO, CvX<0>{ac, inv, b0, bf, ow});
    proj_elem<KO, 1, false, false, true, W_NT, 16, 16>(acc, a0, a1, bf, pc, sO, CvX<1>{ac, inv, bf, b0, ow});
    proj_elem<KO, 2, false, false, true, W_NT, 16, 16>(acc, a0, a1, b0, pc, sO, CvX<2>{ac, inv, b0, bf, ow});
    if constexpr (isA) PINV(dum0);
    proj_elem<KO, 0, false, false, true, W_NT, 16, 16, true>(acc, a0, a1, bf, pc, sO, CvX<3>{ac, inv, bf, b0, ow});
    typedef std::conditional_t<isA, EasyX<EASY2, true>, NoX> PX;
    const PX px = [&] { if constexpr (isA) return EasyX<EASY2, true>{tnow, tlim + (EASY2_T - EASY_T)}; else return NoX{}; }();
#pragma unroll 1
    for (unsigned e = 0; e < 3; ++e) {
      proj_elem<KO, 1, false, false, true, W_NT, 16, 16>(acc, a0, a1, bf, pc, sO, px);
      proj_elem<KO, 2, false, false, true, W_NT, 16, 16>(acc, a0, a1, bf, pc, sO, px);
      proj_elem<KO, 0, false, false, true, W_NT, 16, 16>(acc, a0, a1, bf, pc, sO, px);
    }
    Pub pb;
    pb.srd = pb_srd; pb.so = pb_so;
#pragma unroll
    for (int i = 0; i < 16; ++i) pb.pk[i] = u32x4{};
    TL tl;
#pragma unroll
    for (int i = 0; i < 4; ++i) { tl.w[0][0][i] = a0[i]; tl.x[0][0][i] = bf[i]; }
    tl.xs[0] = xs0; tl.xs[1] = xs1; tl.xs[2] = xs2; tl.drop[0] = drop0; tl.drop[1] = drop1; tl.l16 = l16;
    tl_run<KO>(acc, tl, pc, pb, make_iseq<96>{});
    unsigned long long t5_; asm volatile("s_memrealtime %0\n\ts_waitcnt lgkmcnt(0)" : "=s"(t5_) :: "memory");
    const unsigned lr = ((lane + xg) & 7u) * 4u;
    unsigned fl;
    asm volatile("buffer_load_dword %0, %1, %2, %3 offen sc1" : "=v"(fl) : "v"(lr), "s"(srdH), "s"(flg_off + xg * 128u) : "memory");
    PINV(toks);
    if (w == (xg >> 1)) {
      const unsigned a = pc.r01 + SLOT + l16;
      if (xg & 1u) own_put<8>(pb, a, make_iseq<8>{}); else own_put<0>(pb, a, make_iseq<8>{});
    }
    const unsigned fo = lane * 128u + xg * 4u, tau = __builtin_amdgcn_readlane(toks, xg);
    unsigned oo = ((b * 64u + 16u * w + r16) * 1024u + (isA ? 0u : 512u) + 64u * xg + 8u * g) * 2u;
    PINV(oo);
    wait_lgkm0();
    barrier();
    const unsigned oa = 65536u + SLOT + (xg >> 1) * 16384u + w * 2048u + l16;
    const u32x4 r0 = __builtin_bit_cast(u32x4, lds128(oa)), r1 = __builtin_bit_cast(u32x4, lds128(oa + 1024u));
    GAP();
    wait_vm<9>();
    if (lane == 2u * w) asm volatile("buffer_store_dword %0, %1, %2, %3 offen sc1" :: "v"(toks), "v"(fo), "s"(srdH), "s"(flg_off) : "memory");
    wait_vm<2>();
    if (lane == 2u * w + 1u) asm volatile("buffer_store_dword %0, %1, %2, %3 offen sc1" :: "v"(toks), "v"(fo), "s"(srdH), "s"(flg_off) : "memory");
    f32x16 f[8];
#pragma unroll
    for (int j = 0; j < 8; ++j) { const unsigned t = j < 4 ? r0[j & 3] : r1[j & 3]; f[0][2 * j] = __uint_as_float(t << 16); f[0][2 * j + 1] = __uint_as_float(t & 0xffff0000u); }
    PINV(f[0]);
    asm volatile("s_waitcnt vmcnt(2)" : "+v"(fl) : "v"(pb.pk[0]), "v"(pb.pk[1]), "v"(pb.pk[2]), "v"(pb.pk[3]), "v"(pb.pk[4]), "v"(pb.pk[5]), "v"(pb.pk[6]), "v"(pb.pk[7]),
                 "v"(pb.pk[8]), "v"(pb.pk[9]), "v"(pb.pk[10]), "v"(pb.pk[11]), "v"(pb.pk[12]), "v"(pb.pk[13]), "v"(pb.pk[14]), "v"(pb.pk[15]) : "memory");
    unsigned got = 0xffffff01u, spin = 1u << 21, fresh, so, f2;
#define POLL(R) "buffer_load_dword %[" R "], %[lr], %[sh], %[flg] offen sc1\n\t"
#define SEEN(R) "v_cmp_eq_u32_e32 vcc, %[tau], %[" R "]\n\ts_andn2_b32 %[fr], vcc_lo, %[got]\n\ts_cbranch_scc1 .Lproc_%=\n\t"
    asm volatile(
        SEEN("fl") POLL("fl") "s_sleep 4\n"
        ".Lidle_%=:\n\t" POLL("f2") "s_waitcnt vmcnt(1)\n\t" SEEN("fl") "s_sleep %[slp]\n\t" POLL("fl") "s_waitcnt vmcnt(1)\n\t" SEEN("f2")
        "s_sleep %[slp]\n\ts_sub_u32 %[spin], %[spin], 1\n\ts_cbranch_scc0 .Lidle_%=\n\ts_branch .Ldone_%=\n"
        ".Lproc_%=:\n\ts_or_b32 %[got], %[got], %[fr]\n\t"
        PGS "s_bitcmp1_b32 %[fr], 1\n\ts_cbranch_scc0 .La1_%=\n\ts_add_u32 %[so], %[ro], 65536\n\ts_bitset0_b32 %[so], 19\n\t"
        "buffer_load_dwordx4 v[152:155], %[l16], %[sp], %[so] offen sc1\n\tbuffer_load_dwordx4 v[156:159], %[l16], %[sp], %[so] offen offset:1024 sc1\n.La1_%=:\n\t"
        PGS "s_bitcmp1_b32 %[fr], 2\n\ts_cbranch_scc0 .La2_%=\n\ts_add_u32 %[so], %[ro], 131072\n\ts_bitset0_b32 %[so], 19\n\t"
        "buffer_load_dwordx4 v[168:171], %[l16], %[sp], %[so] offen sc1\n\tbuffer_load_dwordx4 v[172:175], %[l16], %[sp], %[so] offen offset:1024 sc1\n.La2_%=:\n\t"
        PGS "s_bitcmp1_b32 %[fr], 3\n\ts_cbranch_scc0 .La3_%=\n\ts_add_u32 %[so], %[ro], 196608\n\ts_bitset0_b32 %[so], 19\n\t"
        "buffer_load_dwordx4 v[184:187], %[l16], %[sp], %[so] offen sc1\n\tbuffer_load_dwordx4 v[188:191], %[l16], %[sp], %[so] offen offset:1024 sc1\n.La3_%=:\n\t"
        PGS "s_bitcmp1_b32 %[fr], 4\n\ts_cbranch_scc0 .La4_%=\n\ts_add_u32 %[so], %[ro], 262144\n\ts_bitset0_b32 %[so], 19\n\t"
        "buffer_load_dwordx4 v[200:203], %[l16], %[sp], %[so] offen sc1\n\tbuffer_load_dwordx4 v[204:207], %[l16], %[sp], %[so] offen offset:1024 sc1\n.La4_%=:\n\t"
        PGS "s_bitcmp1_b32 %[fr], 5\n\ts_cbranch_scc0 .La5_%=\n\ts_add_u32 %[so], %[ro], 327680\n\ts_bitset0_b32 %[so], 19\n\t"
        "buffer_load_dwordx4 v[216:219], %[l16], %[sp], %[so] offen sc1\n\tbuffer_load_dwordx4 v[220:223], %[l16], %[sp], %[so] offen offset:1024 sc1\n.La5_%=:\n\t"
        PGS "s_bitcmp1_b32 %[fr], 6\n\ts_cbranch_scc0 .La6_%=\n\ts_add_u32 %[so], %[ro], 393216\n\ts_bitset0_b32 %[so], 19\n\t"
        "buffer_load_dwordx4 v[232:235], %[l16], %[sp], %[so] offen sc1\n\tbuffer_load_dwordx4 v[236:239], %[l16], %[sp], %[so] offen offset:1024 sc1\n.La6_%=:\n\t"
        PGS "s_bitcmp1_b32 %[fr], 7\n\ts_cbranch_scc0 .La7_%=\n\ts_add_u32 %[so], %[ro], 458752\n\ts_bitset0_b32 %[so], 19\n\t"
        "buffer_load_dwordx4 v[248:251], %[l16], %[sp], %[so] offen sc1\n\tbuffer_load_dwordx4 v[252:255], %[l16], %[sp], %[so] offen offset:1024 sc1\n.La7_%=:\n\t"
        "s_waitcnt vmcnt(0)\n\ts_cmp_eq_u32 %[got], -1\n\ts_cbranch_scc1 .Ltok_%=\n\t" POLL("fl") "s_branch .Lwide_%=\n"
        ".Ltok_%=:\n\tbuffer_load_dword %[fl], %[l8], %[sh], %[tok] offen sc1\n"
        ".Lwide_%=:\n\t"
        "s_bitcmp1_b32 %[fr], 1\n\ts_cbranch_scc0 .Lb1_%=\n\t"
        PGS "v_lshlrev_b32_e32 v144, 16, v152\n\tv_and_b32_e32 v145, %[hi], v152\n\tv_lshlrev_b32_e32 v146, 16, v153\n\tv_and_b32_e32 v147, %[hi], v153\n\t"
        PGS "v_lshlrev_b32_e32 v148, 16, v154\n\tv_and_b32_e32 v149, %[hi], v154\n\tv_lshlrev_b32_e32 v150, 16, v155\n\tv_and_b32_e32 v151, %[hi], v155\n\t"
        PGS "v_lshlrev_b32_e32 v152, 16, v156\n\tv_and_b32_e32 v153, %[hi], v156\n\tv_lshlrev_b32_e32 v154, 16, v157\n\tv_and_b32_e32 v155, %[hi], v157\n\t"
        PGS "v_lshlrev_b32_e32 v156, 16, v158\n\tv_and_b32_e32 v157, %[hi], v158\n\tv_lshlrev_b32_e32 v158, 16, v159\n\tv_and_b32_e32 v159, %[hi], v159\n\t"
        ".Lb1_%=:\n\t"
        "s_bitcmp1_b32 %[fr], 2\n\ts_cbranch_scc0 .Lb2_%=\n\t"
        PGS "v_lshlrev_b32_e32 v160, 16, v168\n\tv_and_b32_e32 v161, %[hi], v168\n\tv_lshlrev_b32_e32 v162, 16, v169\n\tv_and_b32_e32 v163, %[hi], v169\n\t"
        PGS "v_lshlrev_b32_e32 v164, 16, v170\n\tv_and_b32_e32 v165, %[hi], v170\n\tv_lshlrev_b32_e32 v166, 16, v171\n\tv_and_b32_e32 v167, %[hi], v171\n\t"
        PGS "v_lshlrev_b32_e32 v168, 16, v172\n\tv_and_b32_e32 v169, %[hi], v172\n\tv_lshlrev_b32_e32 v170, 16, v173\n\tv_and_b32_e32 v171, %[hi], v173\n\t"
        PGS "v_lshlrev_b32_e32 v172, 16, v174\n\tv_and_b32_e32 v173, %[hi], v174\n\tv_lshlrev_b32_e32 v174, 16, v175\n\tv_and_b32_e32 v175, %[hi], v175\n\t"
        ".Lb2_%=:\n\t"
        "s_bitcmp1_b32 %[fr], 3\n\ts_cbranch_scc0 .Lb3_%=\n\t"
        PGS "v_lshlrev_b32_e32 v176, 16, v184\n\tv_and_b32_e32 v177, %[hi], v184\n\tv_lshlrev_b32_e32 v178, 16, v185\n\tv_and_b32_e32 v179, %[hi], v185\n\t"
        PGS "v_lshlrev_b32_e32 v180, 16, v186\n\tv_and_b32_e32 v181, %[hi], v186\n\tv_lshlrev_b32_e32 v182, 16, v187\n\tv_and_b32_e32 v183, %[hi], v187\n\t"
        PGS "v_lshlrev_b32_e32 v184, 16, v188\n\tv_and_b32_e32 v185, %[hi], v188\n\tv_lshlrev_b32_e32 v186, 16, v189\n\tv_and_b32_e32 v187, %[hi], v189\n\t"
        PGS "v_lshlrev_b32_e32 v188, 16, v190\n\tv_and_b32_e32 v189, %[hi], v190\n\tv_lshlrev_b32_e32 v190, 16, v191\n\tv_and_b32_e32 v191, %[hi], v191\n\t"
        ".Lb3_%=:\n\t"
        "s_bitcmp1_b32 %[fr], 4\n\ts_cbranch_scc0 .Lb4_%=\n\t"
        PGS "v_lshlrev_b32_e32 v192, 16, v200\n\tv_and_b32_e32 v193, %[hi], v200\n\tv_lshlrev_b32_e32 v194, 16, v201\n\tv_and_b32_e32 v195, %[hi], v201\n\t"
        PGS "v_lshlrev_b32_e32 v196, 16, v202\n\tv_and_b32_e32 v197, %[hi], v202\n\tv_lshlrev_b32_e32 v198, 16, v203\n\tv_and_b32_e32 v199, %[hi], v203\n\t"
        PGS "v_lshlrev_b32_e32 v200, 16, v204\n\tv_and_b32_e32 v201, %[hi], v204\n\tv_lshlrev_b32_e32 v202, 16, v205\n\tv_and_b32_e32 v203, %[hi], v205\n\t"
        PGS "v_lshlrev_b32_e32 v204, 16, v206\n\tv_and_b32_e32 v205, %[hi], v206\n\tv_lshlrev_b32_e32 v206, 16, v207\n\tv_and_b32_e32 v207, %[hi], v207\n\t"
        ".Lb4_%=:\n\t"
        "s_bitcmp1_b32 %[fr], 5\n\ts_cbranch_scc0 .Lb5_%=\n\t"
        PGS "v_lshlrev_b32_e32 v208, 16, v216\n\tv_and_b32_e32 v209, %[hi], v216\n\tv_lshlrev_b32_e32 v210, 16, v217\n\tv_and_b32_e32 v211, %[hi], v217\n\t"
        PGS "v_lshlrev_b32_e32 v212, 16, v218\n\tv_and_b32_e32 v213, %[hi], v218\n\tv_lshlrev_b32_e32 v214, 16, v219\n\tv_and_b32_e32 v215, %[hi], v219\n\t"
        PGS "v_lshlrev_b32_e32 v216, 16, v220\n\tv_and_b32_e32 v217, %[hi], v220\n\tv_lshlrev_b32_e32 v218, 16, v221\n\tv_and_b32_e32 v219, %[hi], v221\n\t"
        PGS "v_lshlrev_b32_e32 v220, 16, v222\n\tv_and_b32_e32 v221, %[hi], v222\n\tv_lshlrev_b32_e32 v222, 16, v223\n\tv_and_b32_e32 v223, %[hi], v223\n\t"
        ".Lb5_%=:\n\t"
        "s_bitcmp1_b32 %[fr], 6\n\ts_cbranch_scc0 .Lb6_%=\n\t"
        PGS "v_lshlrev_b32_e32 v224, 16, v232\n\tv_and_b32_e32 v225, %[hi], v232\n\tv_lshlrev_b32_e32 v226, 16, v233\n\tv_and_b32_e32 v227, %[hi], v233\n\t"
        PGS "v_lshlrev_b32_e32 v228, 16, v234\n\tv_and_b32_e32 v229, %[hi], v234\n\tv_lshlrev_b32_e32 v230, 16, v235\n\tv_and_b32_e32 v231, %[hi], v235\n\t"
        PGS "v_lshlrev_b32_e32 v232, 16, v236\n\tv_and_b32_e32 v233, %[hi], v236\n\tv_lshlrev_b32_e32 v234, 16, v237\n\tv_and_b32_e32 v235, %[hi], v237\n\t"
        PGS "v_lshlrev_b32_e32 v236, 16, v238\n\tv_and_b32_e32 v237, %[hi], v238\n\tv_lshlrev_b32_e32 v238, 16, v239\n\tv_and_b32_e32 v239, %[hi], v239\n\t"
        ".Lb6_%=:\n\t"
        "s_bitcmp1_b32 %[fr], 7\n\ts_cbranch_scc0 .Lb7_%=\n\t"
        PGS "v_lshlrev_b32_e32 v240, 16, v248\n\tv_and_b32_e32 v241, %[hi], v248\n\tv_lshlrev_b32_e32 v242, 16, v249\n\tv_and_b32_e32 v243, %[hi], v249\n\t"
        PGS "v_lshlrev_b32_e32 v244, 16, v250\n\tv_and_b32_e32 v245, %[hi], v250\n\tv_lshlrev_b32_e32 v246, 16, v251\n\tv_and_b32_e32 v247, %[hi], v251\n\t"
        PGS "v_lshlrev_b32_e32 v248, 16, v252\n\tv_and_b32_e32 v249, %[hi], v252\n\tv_lshlrev_b32_e32 v250, 16, v253\n\tv_and_b32_e32 v251, %[hi], v253\n\t"
        PGS "v_lshlrev_b32_e32 v252, 16, v254\n\tv_and_b32_e32 v253, %[hi], v254\n\tv_lshlrev_b32_e32 v254, 16, v255\n\tv_and_b32_e32 v255, %[hi], v255\n\t"
        ".Lb7_%=:\n\t"
        "s_cmp_eq_u32 %[got], -1\n\ts_cbranch_scc0 .Lidle_%=\n"
        ".Ldone_%=:"
        : "=&{v[144:159]}"(f[1]), "=&{v[160:175]}"(f[2]), "=&{v[176:191]}"(f[3]), "=&{v[192:207]}"(f[4]), "=&{v[208:223]}"(f[5]), "=&{v[224:239]}"(f[6]), "=&{v[240:255]}"(f[7]),
          [fl] "+v"(fl), [f2] "=&v"(f2), [got] "+s"(got), [spin] "+s"(spin), [fr] "=&s"(fresh), [so] "=&s"(so)
        : [tau] "s"(tau), [l16] "v"(l16), [l8] "v"(l8), [lr] "v"(lr), [sp] "s"(pb.srd), [sh] "s"(srdH), [ro] "s"(xg * 73728u + w * 2048u), [tok] "s"(tok_off),
          [flg] "s"(flg_off + xg * 128u), [hi] "s"(0xffff0000u), [slp] "i"(isA ? 8 : 0)
        : "vcc", "scc", "memory");
#undef POLL
#undef SEEN
    const f32x16 t = ((f[0] + f[1]) + (f[2] + f[3])) + ((f[4] + f[5]) + (f[6] + f[7]));
#pragma unroll
    for (int s = 0; s < 2; ++s)
    {
      const u32x4 ov = natural8(u32x4{cvtpk(t[8 * s], t[8 * s + 1]), cvtpk(t[8 * s + 2], t[8 * s + 3]), cvtpk(t[8 * s + 4], t[8 * s + 5]), cvtpk(t[8 * s + 6], t[8 * s + 7])});
      __builtin_amdgcn_raw_buffer_store_b128(ov, mk_srd(out, 2097152u), oo + s * 64, 0, 2);
    }
    asm volatile("s_waitcnt vmcnt(2)" : "+v"(fl) :: "memory");
    if ((lane >> 1) == w && fl != toks) asm volatile("buffer_store_dword %0, %1, %2, %3 offen sc1" :: "v"(fl), "v"(fo), "s"(srdH), "s"(flg_off) : "memory");
    if constexpr (!isA) {
      unsigned long long t12_; asm volatile("s_waitcnt vmcnt(0)\n\ts_memrealtime %0\n\ts_waitcnt lgkmcnt(0)" : "=s"(t12_) :: "memory");
      const unsigned t0_ = tlim - EASY_T, d5 = (unsigned)t5_ - t0_, d12 = (unsigned)t12_ - (unsigned)t5_;
      const unsigned target = 10000u + (0u * (d5 + d12));
      for (int i = 0; i < 1000000; ++i) { unsigned long long t_; asm volatile("s_memrealtime %0\n\ts_waitcnt lgkmcnt(0)" : "=s"(t_) :: "memory"); if ((unsigned)t_ - t0_ >= target) break; }
    }
  }
}

__global__ __launch_bounds__(256, 1) __attribute__((aligned(4096)))
void fused_attn(const char* __restrict__ Ax, const char* __restrict__ Ak, const char* __restrict__ Av, const char* __restrict__ Awq, const char* __restrict__ Awo,
                const char* __restrict__ Bx, const char* __restrict__ Bk, const char* __restrict__ Bv, const char* __restrict__ Bwq, const char* __restrict__ Bwo,
                char* __restrict__ part, char* __restrict__ out) {
  extern __shared__ __attribute__((aligned(16))) char lds[];
  const unsigned bid = blockIdx.x;
  unsigned long long tnow;
  {
    const unsigned xg = bid & 7u, b = (bid >> 3) & 15u;
    const char *px, *pq, *pc; unsigned left;
    asm volatile(PG_DEFS "s_load_dwordx2 %0, %5, %6\n\ts_load_dwordx2 %1, %5, 0x28\n\ts_memrealtime %2\n\ts_getpc_b64 %3\n.Lpc_%=:\n\ts_mov_b32 %4, .Lhot_end-.Lpc_%="
                 : "=&s"(pq), "=&s"(px), "=&s"(tnow), "=&s"(pc), "=&s"(left) : "s"(__builtin_amdgcn_kernarg_segment_ptr()), "s"(bid >= 128u ? 0x18u : 0x40u));
    unsigned tid = threadIdx.x;
    PINV(tid);
    const unsigned lane = tid & 63u, w = rfl(tid >> 6), zq = ((b & 3u) << 14) | ((b >> 2) << 8);
    unsigned q0 = q_soff(zq), q1 = q_soff(zq + 0x1300u), r01 = 65536u + w * 16384u, r2 = 131072u + w * 8192u, xs = (b & 3u) * 256u, xd = w * 4096u + (b & 3u) * 16384u;
    asm volatile("s_mov_b32 m0, %2\n\ts_nop 0\n\tbuffer_load_dword %0, %1, 0 offen lds"
                 :: "v"(bid >= 128u && tid - 192u < WARM ? (b * WARM + tid - 192u) * 128u : 0x80000000u), "s"(mk_srd(pc, left)), "s"(r01 + SLOT) : "memory");
    Src sQ, sX;
    mk_w<false>(sQ, (xg * 4u + w) * 256u, 8192u, lane);
    mk_rows<false>(sX, b * 65536u + w * 16384u, 1024u, lane);
#pragma unroll
    for (int i = 0; i < 4; ++i) PINV(sX.v[i]);
    PINS(xs); PINS(xd);
#pragma unroll
    for (int i = 0; i < 4; ++i) PINV(sQ.v[i]);
    PINS(q0); PINS(q1); PINS(r01); PINS(r2);
    GAP();
    asm volatile("s_waitcnt lgkmcnt(0)" : "+s"(pq), "+s"(px), "+s"(tnow));
    GAP();
    sQ.srd = mk_srd(pq, 4194304u); sX.srd = mk_srd(px, 1048576u);
    dma_unit<1>(sQ, q0, r2);
    dma_unit<W_NT>(sQ, q1, r01);
    asm volatile("s_cmp_ge_u32 %0, 0x80\n\ts_cbranch_scc1 .Lx_%=\n\ts_mov_b32 m0, %1\n\ts_nop 0\n\tbuffer_load_dwordx4 %2, %6, %7 offen lds\n\tbuffer_load_dwordx4 %3, %6, %7 offen offset:1024 lds\n\t"
                 "buffer_load_dwordx4 %4, %6, %7 offen offset:2048 lds\n\tbuffer_load_dwordx4 %5, %6, %7 offen offset:3072 lds\n.Lx_%=:"
                 :: "s"(bid), "s"(xd), "v"(sX.v[0]), "v"(sX.v[1]), "v"(sX.v[2]), "v"(sX.v[3]), "s"(sX.srd), "s"(xs) : "scc", "memory");
    dma_unit<1>(sQ, q0 + 128u, r2 + 4096u);
    dma_unit<W_NT>(sQ, q1 + 128u, r01 + 4096u);
    asm volatile("" :: "v"(lds));
  }
  if (bid < 128u) { body<false>(Bx, Bk, Bv, Bwq, Bwo, Bwo, tnow, part, out); asm volatile(PG_END ".Lhot_end:"); __builtin_amdgcn_endpgm(); }
  body<true>(Ax, Ak, Av, Awq, Awo, Bwo, tnow, part, out);
  asm volatile(PG_END ".rept %0\n\ts_nop 0\n.endr" :: "i"(ASHIFT));
}

extern "C" void kernel_launch(void* const* d_in, const int* in_sizes, int n_in, void* d_out, int out_size, void* d_ws, size_t ws_size, hipStream_t stream) {
  (void)in_sizes; (void)n_in; (void)out_size; (void)ws_size;
  (void)hipFuncSetAttribute((const void*)fused_attn, hipFuncAttributeMaxDynamicSharedMemorySize, (int)LDS_BYTES);
  fused_attn<<<256, 256, LDS_BYTES, stream>>>((const char*)d_in[0], (const char*)d_in[1], (const char*)d_in[2], (const char*)d_in[3], (const char*)d_in[4],
                                              (const char*)d_in[5], (const char*)d_in[6], (const char*)d_in[7], (const char*)d_in[8], (const char*)d_in[9], (char*)d_ws, (char*)d_out);
}
```

```cpp
#include <hip/hip_runtime.h>
#include <hip/hip_bf16.h>
#include <utility>
#include <type_traits>

typedef __bf16 bf16x8 __attribute__((ext_vector_type(8)));
typedef __bf16 bf16x4 __attribute__((ext_vector_type(4)));
typedef __bf16 bf16x2 __attribute__((ext_vector_type(2)));
typedef float f32x4 __attribute__((ext_vector_type(4)));
typedef float f32x2 __attribute__((ext_vector_type(2)));
typedef float f32x16 __attribute__((ext_vector_type(16)));
typedef unsigned u32x4 __attribute__((ext_vector_type(4)));
#define LAS __attribute__((address_space(3)))
#define DI __device__ __forceinline__
typedef __amdgpu_buffer_rsrc_t srd_t;
template <int... Is> using iseq = std::integer_sequence<int, Is...>;
template <int N> using make_iseq = std::make_integer_sequence<int, N>;

constexpr int KV_NT = 1, W_NT = 0, X_NT = 0;
constexpr int SK = 4, SV = 4, SP = 5;
constexpr unsigned EASY_T = 1350, EASY_L = 1000, EASY2_F = 2000, EASY2_T = 3250;
constexpr int EASY = 6, PACE_ATT = 1, EASY2 = 6;
constexpr unsigned WARM = 40;
constexpr int PGB = 9, AWSH = 48, BSHIFT = 0, ASHIFT = 0;

constexpr unsigned SLOT = 8192, LDS_BYTES = 20 * SLOT;
constexpr unsigned HOFF = 16u << 20;
constexpr float SC = 0.12751743082f;
constexpr float LMAX = 1048576.0f;

#define GAP() __builtin_amdgcn_sched_barrier(0)
#define PINS(x) asm volatile("" : "+s"(x))
#define PINV(x) asm volatile("" : "+v"(x))
DI unsigned rfl(unsigned x) { return __builtin_amdgcn_readfirstlane(x); }
DI unsigned cvtpk(float lo, float hi) { f32x2 v = {lo, hi}; bf16x2 b = __builtin_convertvector(v, bf16x2); return __builtin_bit_cast(unsigned, b); }
DI u32x4 pack2(const f32x4& a, const f32x4& b) { return u32x4{cvtpk(a[0], a[1]), cvtpk(a[2], a[3]), cvtpk(b[0], b[1]), cvtpk(b[2], b[3])}; }
DI u32x4 natural8(u32x4 w) {
  auto s0 = __builtin_amdgcn_permlane32_swap(w[0], w[2], false, false);
  auto s1 = __builtin_amdgcn_permlane32_swap(w[1], w[3], false, false);
  auto t0 = __builtin_amdgcn_permlane16_swap(s0[0], s0[1], false, false);
  auto t1 = __builtin_amdgcn_permlane16_swap(s1[0], s1[1], false, false);
  return u32x4{t0[0], t1[0], t0[1], t1[1]};
}
#define KSYM "_Z10fused_attnPKcS0_S0_S0_S0_S0_S0_S0_S0_S0_PcS1_"
#define PGL(c) ".if (((. - " KSYM ") >> 12) & 3) == " #c "\n777" #c ": s_cbranch_execz 777" #c "f\n.endif\n"
#define PG_DEFS ".set pgnext, 4096\n.macro pgstop\n.if (. - " KSYM ") >= pgnext\n.set pgnext, ((. - " KSYM ") | 4095) + 1\n" PGL(0) PGL(1) PGL(2) PGL(3) ".endif\n.endm\n" \
                ".macro mfp a:vararg\nv_mfma_f32_16x16x32_bf16 \\a\npgstop\n.endm\n"
#define PG_END "s_endpgm\n7770:\n7771:\n7772:\n7773:\n\ts_setpc_b64 vcc\n"
#define MF "mfp "
#define PGS "pgstop\n\t"
template <int SH = 0> DI void pg_walk(unsigned w) {
  asm volatile("s_mov_b64 exec, 0\n\ts_call_b64 vcc, .Lw1_%=\n\ts_branch .Lw2_%=\n.Lw1_%=:\n\ts_cmp_eq_u32 %0, 0\n\ts_cbranch_scc1 7770f\n\ts_cmp_eq_u32 %0, 1\n\ts_cbranch_scc1 7771f\n\t"
               "s_cmp_eq_u32 %0, 2\n\ts_cbranch_scc1 7772f\n\ts_branch 7773f\n.rept %1\n\ts_nop 0\n.endr\n.Lw2_%=:\n\ts_mov_b64 exec, -1" :: "s"(w), "i"(SH) : "vcc", "scc", "memory");
}
template <bool Z> DI void mm_vvv(f32x4& c, const bf16x8& a, const bf16x8& b) {
  if constexpr (Z) asm volatile(MF "%0, %1, %2, 0" : "=&v"(c) : "v"(a), "v"(b));
  else             asm volatile(MF "%0, %1, %2, %0" : "+v"(c) : "v"(a), "v"(b));
}
template <bool Z> DI void mm_vva(f32x4& c, const bf16x8& a, const bf16x8& b) {
  if constexpr (Z) asm volatile(MF "%0, %1, %2, 0" : "=&v"(c) : "v"(a), "a"(b));
  else             asm volatile(MF "%0, %1, %2, %0" : "+v"(c) : "v"(a), "a"(b));
}
DI void mm_vca(f32x4& d, const bf16x8& a, const bf16x8& b, const f32x4& c) { asm volatile(MF "%0, %1, %2, %3" : "=&v"(d) : "v"(a), "a"(b), "v"(c)); }
template <bool Z = false> DI void mm_avv(f32x4& c, const bf16x8& a, const bf16x8& b) {
  if constexpr (Z) asm volatile(MF "%0, %1, %2, 0" : "=a"(c) : "v"(a), "v"(b));
  else             asm volatile(MF "%0, %1, %2, %0" : "+a"(c) : "v"(a), "v"(b));
}
DI void set_m0(unsigned v) { asm volatile("s_mov_b32 m0, %0" :: "s"(v) : "memory"); }
template <int NT, int P> DI void dma_p(unsigned voff, srd_t srd, unsigned soff) {
  if constexpr (NT) asm volatile("buffer_load_dwordx4 %0, %1, %2 offen offset:%3 nt lds" :: "v"(voff), "s"(srd), "s"(soff), "i"(1024 * P) : "memory");
  else              asm volatile("buffer_load_dwordx4 %0, %1, %2 offen offset:%3 lds" :: "v"(voff), "s"(srd), "s"(soff), "i"(1024 * P) : "memory");
}
struct Src { unsigned v[4]; srd_t srd; };
template <int NT> DI void dma_unit(const Src& s, unsigned soff, unsigned ldsaddr) {
  asm volatile("s_mov_b32 m0, %0\n\ts_nop 0" :: "s"(ldsaddr) : "memory");
  dma_p<NT, 0>(s.v[0], s.srd, soff); dma_p<NT, 1>(s.v[1], s.srd, soff); dma_p<NT, 2>(s.v[2], s.srd, soff); dma_p<NT, 3>(s.v[3], s.srd, soff);
}
template <int NT> DI void dma_elem(const Src& s, unsigned soff, unsigned ustep, unsigned ldsaddr) { dma_unit<NT>(s, soff, ldsaddr); dma_unit<NT>(s, soff + ustep, ldsaddr + 4096u); }
template <int N> DI void wait_vm() { asm volatile("s_waitcnt vmcnt(%0)" :: "i"(N) : "memory"); }
template <int N, int L> DI void wait_vm_lgkm() { asm volatile("" ::: "memory"); __builtin_amdgcn_s_waitcnt((N & 15) | ((N >> 4) << 14) | 0x70 | (L << 8)); asm volatile("" ::: "memory"); }
DI void wait_lgkm0() { wait_vm_lgkm<63, 0>(); }
DI void barrier() { asm volatile("s_barrier" ::: "memory"); }
DI bf16x8 lds128(unsigned a) { return *(const LAS bf16x8*)(size_t)(a); }
DI bf16x4 lds_tr1(unsigned a) { return __builtin_amdgcn_ds_read_tr16_b64_v4bf16((LAS bf16x4*)(size_t)(a)); }
DI bf16x8 cat(bf16x4 lo, bf16x4 hi) { return __builtin_shufflevector(lo, hi, 0, 1, 2, 3, 4, 5, 6, 7); }
DI bf16x8 lds_tr(unsigned a0, unsigned a1) { return cat(lds_tr1(a0), lds_tr1(a1)); }
DI srd_t mk_srd(const void* base, unsigned bytes) { return __builtin_amdgcn_make_buffer_rsrc((void*)base, (short)0, (int)bytes, 0x00020000); }
template <bool VX> DI void mk_rows(Src& s, unsigned base, unsigned stride, unsigned lane) {
  const unsigned row = (lane >> 2) & 3u, cb = lane >> 4, sl = lane & 3u;
#pragma unroll
  for (int p = 0; p < 4; ++p) s.v[p] = base + row * stride + cb * 64u + ((sl ^ (VX ? 2 * (p & 1) : (p & 2))) << 4) + (4u * stride - 1024u) * p;
}
template <bool VX> DI void mk_w(Src& s, unsigned base, unsigned stride, unsigned lane) {
  const unsigned sub = lane >> 5, cb = (lane >> 4) & 1u, row = (lane >> 2) & 3u, sl = lane & 3u;
#pragma unroll
  for (int p = 0; p < 4; ++p) s.v[p] = base + (4u * sub + row) * stride + cb * 64u + ((sl ^ (VX ? 2u * sub : 2u * (p & 1))) << 4) + (8u * stride - 1024u) * p;
}
DI void touch(unsigned& d, unsigned& voff, const char* base) {
  asm volatile("global_load_dword %0, %1, %2 nt\n\tglobal_load_dword %0, %1, %2\n\tv_add_u32_e32 %1, 0x1000, %1" : "+v"(d), "+v"(voff) : "s"(base) : "memory");
}
struct Geo { int r0, r1, p0, p1, u, t0, t1, t2, t3; };
constexpr Geo GB = {8, 15, 16, 12, 11, 13, 14, 10, 9}, GA = {15, 8, 16, 9, 10, 14, 11, 12, 13};
constexpr unsigned KUS = 1u << GB.u;
template <bool isA> DI unsigned ksoffg(unsigned m, unsigned rk) {
  constexpr Geo G = isA ? GA : GB;
  const unsigned t = m + rk;
  return ((t & 1u) << G.t0) | (((t >> 1) & 1u) << G.t1) | (((t >> 2) & 1u) << G.t2) | (((t >> 3) & 1u) << G.t3);
}
DI unsigned ksoff(unsigned m, unsigned rk) { return ksoffg<false>(m, rk); }
template <bool isA, bool VX> DI void mk_src_kv(Src& s, const char* base, unsigned lane, unsigned w) {
  constexpr Geo G = isA ? GA : GB;
  s.srd = mk_srd(base, 131072u);
  const unsigned row = (lane >> 2) & 3u, sl = lane & 3u, o = ((row & 1u) << G.r0) + ((row >> 1) << G.r1) + (lane >> 4) * 64u;
#pragma unroll
  for (unsigned p = 0; p < (isA ? 2u : 4u); ++p)
    s.v[p] = o + ((sl ^ (VX ? 2u * (p & 1u) : isA ? 2u * (w & 1u) : (p & 2u))) << 4) + ((p & 1u) << G.p0) + ((p >> 1) << G.p1) - (isA ? 0u : 1024u * p);
}

struct PC {
  unsigned r01, r2;
  unsigned w01[2], w2[2];
  unsigned xb, xmask, xa;
  unsigned z, soff;
};
template <unsigned TS_, unsigned QS_, unsigned ZI_, unsigned ZM_, unsigned ZO_, unsigned US_, unsigned CS_, bool ROT_> struct PK {
  static constexpr unsigned TS = TS_, QS = QS_, ZI = ZI_, ZM = ZM_, ZO = ZO_, US = US_, CS = CS_; static constexpr bool ROT = ROT_; };
DI unsigned q_soff(unsigned z) { return ((z & 0xC300u) * 0x440u) & 0x3C0000u; }
template <int POS> DI unsigned ring_lds(const PC& c) { return POS == 2 ? c.r2 : c.r01 + POS * SLOT; }
template <int POS> DI unsigned ring_rd(const PC& c, int d) { return (POS == 2 ? c.w2[d & 1] : c.w01[d & 1] + POS * SLOT) + (d >> 1) * 256u; }
struct NoX { static constexpr bool ACT = true; template <int G> DI void run() const {} };
template <int SL, unsigned LEN> struct EasyX {
  static constexpr bool ACT = true;
  unsigned long long& t; unsigned from;
  template <int G> DI void run() const {
    if constexpr (G == 8) asm volatile("s_memrealtime %0" : "=s"(t));
    if constexpr (G == 16) PINS(t);
    if constexpr (G == 16) asm volatile("s_cmp_ge_u32 %0, %2\n\ts_cbranch_scc1 .Le_%=\n\ts_sleep %1\n.Le_%=:" :: "s"((unsigned)t - from), "i"(SL), "i"(LEN) : "scc");
  }
};
template <class K, int POS, int S, bool Z, bool RD, bool DM, int NT, bool XT, int M, class X>
DI void proj_gap(f32x4 (&acc)[8][4], const bf16x8 (&cur)[4], bf16x8 (&nxt)[4], bf16x4 (&lo)[4], bf16x8 (&bf)[4], PC& c, unsigned soff, const Src& src, const Src& xs, unsigned xsoff, const X& ex) {
  constexpr int qb = M >> 2, db = M & 3;
  mm_vvv<Z>(acc[4 * S + db][qb], cur[db], bf[qb]);
  if constexpr (RD && M < 8) {
    constexpr int d = M >> 1; const unsigned a = S == 0 ? ring_rd<POS>(c, d) + 4096u : ring_rd<(POS + 1) % 3>(c, d);
    if constexpr (M & 1) nxt[d] = cat(lo[d], lds_tr1(a + K::TS)); else lo[d] = lds_tr1(a);
  }
  if constexpr (X::ACT && RD && S == 1 && db == 3) bf[qb] = lds128(c.xa + qb * K::QS);
  if constexpr (XT) {
    if constexpr (M < 8 && !(M & 1)) dma_p<X_NT, M / 2>(xs.v[M / 2], xs.srd, xsoff);
    if constexpr (M == 7) set_m0(ring_lds<POS>(c) + S * 4096u);
    if constexpr (M >= 8 && !(M & 1)) dma_p<NT, (M - 8) / 2>(src.v[(M - 8) / 2], src.srd, soff);
  } else if constexpr (DM && M >= 8 && !(M & 1)) dma_p<NT, (M - 8) / 2>(src.v[(M - 8) / 2], src.srd, soff);
  if constexpr (X::ACT && S == 0 && M == 9) { PINS(c.z); c.z = (c.z + K::ZI) & K::ZM; PINS(c.z); }
  if constexpr (X::ACT && S == 0 && M == 11) { c.xa = (c.z & c.xmask) | c.xb; PINV(c.xa); }
  if constexpr (S == 1 && DM && !K::ROT && M == 9) { PINS(c.soff); c.soff = (c.soff + K::CS) & (16u * K::CS - 1u); PINS(c.soff); }
  if constexpr (S == 1 && DM && K::ROT && M == 9) { PINS(c.z); c.soff = (c.z + 0x3100u) & 0xC300u; PINS(c.soff); }
  if constexpr (S == 1 && DM && K::ROT && M == 13) { PINS(c.soff); c.soff = (c.soff * 0x440u) & 0x3C0000u; PINS(c.soff); }
  ex.template run<16 * S + M>();
  GAP();
}
template <class K, int POS, int S, bool Z, bool RD, bool DM, int NT, int VM, bool BAR, bool XT, class X, int... Ms>
DI void proj_unit(f32x4 (&acc)[8][4], const bf16x8 (&cur)[4], bf16x8 (&nxt)[4], bf16x8 (&bf)[4], PC& c, const Src& src, const Src& xs, unsigned xsoff, unsigned xdst, const X& ex, iseq<Ms...>) {
  wait_vm_lgkm<(RD ? VM : 63), (S == 0 ? 2 : 0)>();
  if constexpr (BAR) barrier();
  const unsigned soff = c.soff + S * K::US;
  if constexpr (XT) set_m0(xdst); else if constexpr (DM) set_m0(ring_lds<POS>(c) + S * 4096u);
  GAP();
  bf16x4 lo[4];
  (proj_gap<K, POS, S, Z, RD, DM, NT, XT, Ms>(acc, cur, nxt, lo, bf, c, soff, src, xs, xsoff, ex), ...);
}
template <class K, int POS, bool Z, bool LAST, bool DM, int NT, int VM0, int VM1, bool BAR1 = false, bool XT0 = false, bool XT1 = false, class X = NoX>
DI void proj_elem(f32x4 (&acc)[8][4], bf16x8 (&a0)[4], bf16x8 (&a1)[4], bf16x8 (&bf)[4], PC& c, const Src& src, const X& ex = X{},
                  const Src* xs = nullptr, unsigned xso0 = 0, unsigned xd0 = 0, unsigned xso1 = 0, unsigned xd1 = 0) {
  proj_unit<K, POS, 0, Z, true, DM, NT, VM0, false, XT0>(acc, a0, a1, bf, c, src, xs ? *xs : src, xso0, xd0, ex, make_iseq<16>{});
  proj_unit<K, POS, 1, Z, !LAST, DM, NT, VM1, BAR1, XT1>(acc, a1, a0, bf, c, src, xs ? *xs : src, xso1, xd1, ex, make_iseq<16>{});
}
template <class K> DI void proj_setup(PC& c, unsigned tb, unsigned xb, unsigned z0, unsigned s3) {
  c.w01[0] = c.r01 + tb; c.w01[1] = c.w01[0] ^ 32u; c.w2[0] = c.r2 + tb; c.w2[1] = c.w2[0] ^ 32u;
  PINV(c.w01[0]); PINV(c.w01[1]); PINV(c.w2[0]); PINV(c.w2[1]);
  c.xb = xb; c.xmask = K::ZO; PINV(c.xmask);
  c.z = z0; c.soff = s3;
}
template <class K, int POS> DI void proj_start(bf16x8 (&a0)[4], bf16x8 (&bf)[4], PC& c, unsigned tb, unsigned xb, unsigned z0, unsigned s3) {
  proj_setup<K>(c, tb, xb, z0, s3);
  c.xa = (c.z & K::ZO) | xb; PINV(c.xa);
#pragma unroll
  for (int d = 0; d < 4; ++d) { const unsigned a = ring_rd<POS>(c, d); a0[d] = lds_tr(a, a + K::TS); }
  GAP();
#pragma unroll
  for (int q = 0; q < 4; ++q) bf[q] = lds128(c.xa + q * K::QS);
  GAP();
}
#define ACC_ALL(C, a) C(a[0][0]), C(a[0][1]), C(a[0][2]), C(a[0][3]), C(a[1][0]), C(a[1][1]), C(a[1][2]), C(a[1][3]), C(a[2][0]), C(a[2][1]), C(a[2][2]), C(a[2][3]), C(a[3][0]), C(a[3][1]), C(a[3][2]), C(a[3][3])
DI void acc_fence_v(f32x4 (&acc)[8][4]) {
  asm volatile("s_nop 7\n\ts_nop 3" : ACC_ALL("+v", acc) :: "memory");
  f32x4 (&hi)[4][4] = *(f32x4 (*)[4][4])&acc[4];
  asm volatile("" : ACC_ALL("+v", hi) :: "memory");
  GAP();
}
DI void acc_fence_a(f32x4 (&acc)[8][4]) {
  asm volatile("s_nop 7\n\ts_nop 3" : ACC_ALL("+a", acc) :: "memory");
  f32x4 (&hi)[4][4] = *(f32x4 (*)[4][4])&acc[4];
  asm volatile("" : ACC_ALL("+a", hi) :: "memory");
  GAP();
}

struct Stash { u32x4 p[64]; unsigned base[3]; };
template <int I> DI void stash_load1(Stash& st, const Src& sK, const Src& sV, unsigned& rk, unsigned wo_, unsigned (&sm)[16]) {
  constexpr int m = I >> 2, isV = (I >> 1) & 1, pc_ = I & 1;
  if constexpr (!isV && !pc_) { PINS(rk); sm[m] = ksoffg<true>(m, rk) + wo_; }
  const unsigned so = sm[m];
  if constexpr (I < 56) asm volatile("buffer_load_dwordx4 %0, %1, %2, %3 offen nt" : "=a"(st.p[I]) : "v"(isV ? sV.v[pc_] : sK.v[pc_]), "s"(isV ? sV.srd : sK.srd), "s"(so) : "memory");
  else                  asm volatile("buffer_load_dwordx4 %0, %1, %2, %3 offen nt" : "=v"(st.p[I]) : "v"(isV ? sV.v[pc_] : sK.v[pc_]), "s"(isV ? sV.srd : sK.srd), "s"(so) : "memory");
}
constexpr int stash_idx(int j) { return 4 * ((j >> 1) & 15) + 2 * (j >> 5) + (j & 1); }
template <int... Is> DI void stash_load(Stash& st, const Src& sK, const Src& sV, unsigned rk, unsigned wo_, iseq<Is...>) { unsigned sm[16]; (stash_load1<stash_idx(Is)>(st, sK, sV, rk, wo_, sm), ...); }
constexpr int kv_pair(int m) { constexpr int t[10] = {0, 1, 2, 3, 8, 9, 4, 5, 6, 7}; return t[m % 10]; }
constexpr unsigned long long kv_tab() { unsigned long long v = 0; for (int i = 0; i < 16; ++i) v |= (unsigned long long)kv_pair(i) << (4 * i); return v; }
template <int I> DI void stash_write1(const Stash& st) {
  constexpr int m = I >> 2, isV = (I >> 1) & 1, pc_ = I & 1, off = kv_pair(m) * 16384 + isV * 8192 + pc_ * 1024;
  if constexpr (I < 56) asm volatile("ds_write_b128 %0, %1 offset:%2" :: "v"(st.base[off >> 16]), "a"(st.p[I]), "i"(off & 65535) : "memory");
  else                  asm volatile("ds_write_b128 %0, %1 offset:%2" :: "v"(st.base[off >> 16]), "v"(st.p[I]), "i"(off & 65535) : "memory");
}

struct AC {
  f32x4 o[8][4];
  bf16x8 q[4][4];
  bf16x8 kf[8], vf[8], pf[4];
  f32x4 negm[4];
  f32x4 l[4];
  bf16x8 ones;
  unsigned ra[2];
};
typedef f32x4 ST[2][4];
constexpr int VR = 32;
constexpr int NEB = 22;
template <int E> DI float sm_val(ST& S) { return S[(E >> 2) & 1][E >> 3][E & 3]; }
template <int E> DI void sm_exp1(ST& S) { float a; asm volatile("v_exp_f32_e32 %0, %1" : "=v"(a) : "v"(sm_val<E>(S))); S[(E >> 2) & 1][E >> 3][E & 3] = a; }
template <int QB, int P> DI void sm_cvt(ST& S, AC& c) {
  u32x4 w = __builtin_bit_cast(u32x4, c.pf[QB]);
  unsigned r;
  asm volatile("v_cvt_pk_bf16_f32 %0, %1, %2" : "=v"(r) : "v"(sm_val<8 * QB + 2 * P>(S)), "v"(sm_val<8 * QB + 2 * P + 1>(S)));
  w[P] = r;
  c.pf[QB] = __builtin_bit_cast(bf16x8, w);
}
constexpr bool wide(int nt) { return nt == W_NT; }
template <bool DM, int NT, int VM, int R, int M> DI void mem_pre(unsigned ldsaddr) {
  if constexpr (M == 6) wait_lgkm0();
  constexpr int F = wide(NT) ? 2 : 6, H = wide(NT) ? 18 : 14;
  if constexpr (DM && (M == F || M == H)) set_m0(ldsaddr + (M == H ? 4096u : 0u));
  if constexpr (M == R && VM < 63) wait_vm<VM>();
}
template <bool DM, int NT, int M> DI void mem_post(const Src& src, unsigned soff, unsigned ustep) {
  if constexpr (wide(NT)) { if constexpr (DM && M < 32 && (M & 3) == 2) dma_p<NT, (M >> 2) & 3>(src.v[(M >> 2) & 3], src.srd, soff + (M >= 16 ? ustep : 0u)); }
  else if constexpr (DM && M >= 6 && M <= 20 && !(M & 1)) dma_p<NT, ((M - 6) / 2) & 3>(src.v[((M - 6) / 2) & 3], src.srd, soff + (M >= 14 ? ustep : 0u));
}
template <int WR, int M> DI void mem_wr(const Stash& st) {
  if constexpr (WR >= 32 && M >= 7 && M < 19 && (M & 3) == 3) stash_write1<WR + (M - 7) / 4>(st);
  if constexpr (WR >= 0 && WR < 32 && M >= 8 && M < 16)
    asm volatile("ds_write_b128 %0, %1 offset:%2" :: "v"(st.base[0]), "a"(st.p[WR + M - 8]), "i"((M - 8) * 1024) : "memory");
}
template <int N> using IC = std::integral_constant<int, N>;
struct PA { unsigned rd0, rd1, soff, fill; };
template <bool QK, bool DM, int NT, int VM, int WR, int M, class F>
DI void gapA(AC& c, ST& S, ST& Sn, PA& pa, F& addr, const Src& src, unsigned ustep, const Stash& st, bf16x4& lo) {
  constexpr int R = QK ? 16 : 0;
  if constexpr ((DM || !QK) && M == 0) addr(pa, IC<0>{});
  if constexpr (QK) mem_pre<DM, NT, VM, 16, M>(pa.fill); else if constexpr (M == 0 && VM < 63) wait_vm<VM>();
  if constexpr (QK) {
    constexpr int f = M >> 2, s = f >> 1, kb = f & 1, qb = M & 3;
    if constexpr (s == 0) mm_vca(Sn[kb][qb], c.kf[f], c.q[qb][0], c.negm[qb]); else mm_vva<false>(Sn[kb][qb], c.kf[f], c.q[qb][s]);
  }
  mem_post<DM, NT, M>(src, pa.soff, ustep);
  mem_wr<WR, M>(st);
  if constexpr (QK && !DM && M >= 10 && M < 16) addr(pa, IC<M - 10>{});
  if constexpr (QK && M == 12) asm volatile("" :: "v"(c.negm[0]), "v"(c.negm[1]), "v"(c.negm[2]), "v"(c.negm[3]));
  if constexpr (M >= R && M < R + 16) {
    constexpr int db = (M - R) >> 1; const unsigned a = ((db & 1) ? pa.rd1 : pa.rd0) + (db >> 1) * 256u;
    if constexpr (M & 1) c.vf[db] = cat(lo, lds_tr1(a + 4096u)); else lo = lds_tr1(a);
  }
  if constexpr (NEB + M < 32) sm_exp1<NEB + M>(S);
  if constexpr (M >= 16 && (M & 1)) sm_cvt<2 + ((M - 16) >> 3), ((M - 16) >> 1) & 3>(S, c);
  GAP();
}
template <bool QK, bool DM, int NT, int VM, int WR, class F, int... Ms>
DI void phaseA_(AC& c, ST& S, ST& Sn, F& addr, const Src& src, unsigned ustep, const Stash& st, iseq<Ms...>) {
  PA pa; bf16x4 lo;
  (gapA<QK, DM, NT, VM, WR, Ms>(c, S, Sn, pa, addr, src, ustep, st, lo), ...);
}
template <bool QK, bool DM, int NT, int VM, int WR = -1, class F>
DI void phaseA(AC& c, ST& S, ST& Sn, F addr, const Src& src, unsigned ustep, const Stash& st) {
  phaseA_<QK, DM, NT, VM, WR>(c, S, Sn, addr, src, ustep, st, make_iseq<32>{});
}
template <bool MAX> DI float xlane(float v) {
  auto a = __builtin_amdgcn_permlane32_swap(__float_as_uint(v), __float_as_uint(v), false, false);
  v = MAX ? fmaxf(__uint_as_float(a[0]), __uint_as_float(a[1])) : __uint_as_float(a[0]) + __uint_as_float(a[1]);
  auto b = __builtin_amdgcn_permlane16_swap(__float_as_uint(v), __float_as_uint(v), false, false);
  return MAX ? fmaxf(__uint_as_float(b[0]), __uint_as_float(b[1])) : __uint_as_float(b[0]) + __uint_as_float(b[1]);
}
DI float row_max(const f32x4& x, const f32x4& y) {
  return xlane<true>(fmaxf(fmaxf(fmaxf(x[0], x[1]), fmaxf(x[2], x[3])), fmaxf(fmaxf(y[0], y[1]), fmaxf(y[2], y[3]))));
}
template <int SM, bool KR, bool DM, int NT, int VM, int WR, int M, class F>
DI void gapB(AC& c, ST& Sn, PA& pa, F& addr, const Src& src, unsigned ustep, const Stash& st, bf16x4& lo) {
  mem_pre<DM, NT, VM, (KR ? 18 : VR), M>(pa.fill);
  {
    constexpr int h = M / 18, r = M % 18, qb = 2 * h + (r & 1);
    if constexpr (r < 2) mm_vvv<SM == 3>(c.l[qb], c.ones, c.pf[qb]);
    else if constexpr (SM == 2 && r < 6) mm_vvv<false>(c.o[(r - 2) >> 1][qb], c.vf[(r - 2) >> 1], c.pf[qb]);
    else mm_avv<SM == 3>(c.o[(r - 2) >> 1][qb], c.vf[(r - 2) >> 1], c.pf[qb]);
  }
  mem_post<DM, NT, M>(src, pa.soff, ustep);
  mem_wr<WR, M>(st);
  if constexpr (M < 4) addr(pa, IC<M>{});
  if constexpr (KR && M >= 18 && M < 34 && !(M & 1)) { constexpr int f = (M - 18) >> 1; c.kf[f] = lds128(pa.rd0 + (f & 1) * 4096u + (f >> 1) * 256u); }
  constexpr int R = VM < 63 ? VR : 20;
  if constexpr (!KR && M >= R) {
    c.ra[0] = pa.rd0; c.ra[1] = pa.rd1;
    constexpr int db = (M - R) >> 1; const unsigned a = ((db & 1) ? pa.rd1 : pa.rd0) + (db >> 1) * 256u;
    if constexpr (M & 1) c.kf[db] = cat(lo, lds_tr1(a + 4096u)); else lo = lds_tr1(a);
  }
  if constexpr (SM) {
    if constexpr (M >= 4 && M < 18) sm_exp1<M - 4>(Sn);
    if constexpr (M >= 19 && M < 35 && (M & 1)) sm_exp1<14 + (M - 19) / 2>(Sn);
    if constexpr (M >= 20 && !(M & 1)) sm_cvt<((M - 20) >> 3), ((M - 20) >> 1) & 3>(Sn, c);
  }
  GAP();
}
template <int SM, bool KR, bool DM, int NT, int VM, int WR, class F, int... Ms>
DI void phaseB_(AC& c, ST& Sn, F& addr, const Src& src, unsigned ustep, const Stash& st, iseq<Ms...>) {
  PA pa; bf16x4 lo;
  (gapB<SM, KR, DM, NT, VM, WR, Ms>(c, Sn, pa, addr, src, ustep, st, lo), ...);
}
template <int SM, bool KR, bool DM, int NT, int VM, int WR = -1, class F>
DI void phaseB(AC& c, ST& Sn, F addr, const Src& src, unsigned ustep, const Stash& st) {
  phaseB_<SM, KR, DM, NT, VM, WR>(c, Sn, addr, src, ustep, st, make_iseq<36>{});
}
DI void k_read(AC& ac, unsigned ka) {
#pragma unroll
  for (int f = 0; f < 8; ++f) ac.kf[f] = lds128(ka + (f & 1) * 4096u + (f >> 1) * 256u);
}
constexpr int NW = 29;
template <bool DM, int M> DI void qk0_gap(AC& ac, ST& SA, const Src& src, unsigned soff, unsigned fill, const Stash& st) {
  constexpr int f = M >> 2, qb = M & 3;
  if constexpr (DM && (M == 0 || M == 16)) set_m0(fill + M * 256u);
  mm_vva<(f < 2)>(SA[f & 1][qb], ac.kf[f], ac.q[qb][f >> 1]);
  if constexpr (DM && qb == 0) dma_p<KV_NT, f & 3>(src.v[f & 3], src.srd, soff + (f >> 2) * KUS);
  if constexpr (!DM && qb == 0) stash_write1<NW + f>(st);
  GAP();
}
template <bool DM, int... Ms> DI void qk0(AC& ac, ST& SA, const Src& src, unsigned soff, unsigned fill, const Stash& st, iseq<Ms...>) { (qk0_gap<DM, Ms>(ac, SA, src, soff, fill, st), ...); }
template <int... Js> DI void exp_first(ST& SA, iseq<Js...>) { (sm_exp1<Js>(SA), ...); }
template <int... Js> DI void cvt_first(ST& SA, AC& c, iseq<Js...>) { (sm_cvt<(Js >> 2), (Js & 3)>(SA, c), ...); }
DI float max3(float a, float b, float c) { float r; asm("v_max3_f32 %0, %1, %2, %3" : "=v"(r) : "v"(a), "v"(b), "v"(c)); return r; }
DI float max2(float a, float b) { float r; asm("v_max_f32_e32 %0, %1, %2" : "=v"(r) : "v"(a), "v"(b)); return r; }
template <bool ZL> DI void sm_first(AC& ac, ST& SA) {
  asm volatile("s_nop 7\n\ts_nop 3" : "+v"(SA[0][0]), "+v"(SA[0][1]), "+v"(SA[0][2]), "+v"(SA[0][3]), "+v"(SA[1][0]), "+v"(SA[1][1]), "+v"(SA[1][2]), "+v"(SA[1][3]) :: "memory");
  GAP();
  float m[4], n[4];
#pragma unroll
  for (int q = 0; q < 4; ++q) { m[q] = max3(SA[0][q][0], SA[0][q][1], SA[0][q][2]); n[q] = max3(SA[1][q][0], SA[1][q][1], SA[1][q][2]); }
  GAP();
#pragma unroll
  for (int q = 0; q < 4; ++q) m[q] = max3(m[q], SA[0][q][3], SA[1][q][3]);
  GAP();
#pragma unroll
  for (int k = 0; k < 2; ++k) {
#pragma unroll
    for (int q = 0; q < 4; ++q) { const float a = m[q], b = n[q]; m[q] = max2(a, b); n[q] = max2(b, a); }
    GAP();
#pragma unroll
    for (int q = 0; q < 4; ++q) {
      auto a = k ? __builtin_amdgcn_permlane16_swap(__float_as_uint(m[q]), __float_as_uint(n[q]), false, false)
                 : __builtin_amdgcn_permlane32_swap(__float_as_uint(m[q]), __float_as_uint(n[q]), false, false);
      m[q] = __uint_as_float(a[0]); n[q] = __uint_as_float(a[1]);
    }
    GAP();
  }
#pragma unroll
  for (int q = 0; q < 4; ++q) m[q] = max2(m[q], n[q]);
  GAP();
#pragma unroll
  for (int q = 0; q < 4; ++q) {
    SA[0][q] -= m[q]; SA[1][q] -= m[q];
    ac.negm[q] = f32x4{-m[q], -m[q], -m[q], -m[q]};
    if constexpr (ZL) { ac.l[q] = f32x4{}; PINV(ac.l[q]); }
    ac.pf[q] = bf16x8{};
  }
  ac.ones = __builtin_bit_cast(bf16x8, u32x4{0x3f803f80u, 0x3f803f80u, 0x3f803f80u, 0x3f803f80u});
  asm volatile("" : "+v"(ac.negm[0]), "+v"(ac.negm[1]), "+v"(ac.negm[2]), "+v"(ac.negm[3]), "+v"(ac.ones));
  exp_first(SA, make_iseq<NEB>{});
  cvt_first(SA, ac, make_iseq<8>{});
  GAP();
}
struct BS { unsigned p[4], v[6], k[2], rk, wo, l16, w; };
constexpr bool b_dma(int f) { return f != 2 * (SK - 3) && f != 2 * (SV - 2) + 1 && f != 2 * (SP - 2) + 1 && f < 30 && f != 26; }
constexpr int b_vm(int f) {
  const int s = f == 29 ? 27 : f - 3;
  if (!b_dma(s)) return 63;
  int n = !b_dma(f) ? 0 : f == 28 ? 4 : f == 29 ? 8 : 5 + (f & 1);
  for (int i = s + 1; i < f; ++i) n += b_dma(i) ? 8 : 0;
  return n;
}
template <class KO, int I> DI void b_step(AC& ac, ST& S, ST& Sn, BS& b, const Src& sK, const Src& sV, const Src& sO, Stash& st) {
  constexpr int fa = 2 * I, fb = 2 * I + 1, vi = I == SP ? 4 : (I & 1) ? 0 : 2;
  const unsigned pa = b.p[(I & 1) ? 3 : 1], pb = b.p[(I & 1) ? 0 : 2];
  if constexpr (I == PGB) pg_walk<BSHIFT>(b.w);
  if constexpr (I >= 1 && I <= 13) barrier();
  if constexpr (I == SK - 3) st.base[0] = pa + b.l16;
  phaseA<true, b_dma(fa), (I < 14 ? KV_NT : W_NT), b_vm(fa), (I == SK - 3 ? 0 : -1)>(ac, S, Sn, [&](PA& p, auto k) __attribute__((always_inline)) {
    if (k.value) return;
    PINS(b.rk);
    p.soff = I < 14 ? ksoff(I < 13 ? I + 3 : 15, b.rk) : b.wo; p.fill = I == 14 ? b.p[0] : pa;
    p.rd0 = b.v[vi] + (I == SP ? SLOT : 0u); p.rd1 = b.v[vi + 1] + (I == SP ? SLOT : 0u); }, I < 13 ? sK : I < 14 ? sV : sO, I < 14 ? KUS : KO::US, st);
  if constexpr (I == SV - 2) st.base[0] = pb + b.l16;
  phaseB<2, (I < 14), b_dma(fb), (I < 14 ? KV_NT : W_NT), b_vm(fb), (I == SV - 2 ? 8 : -1)>(ac, Sn, [&](PA& p, auto k) __attribute__((always_inline)) {
      if (k.value) return;
      PINS(b.rk);
      p.soff = I < 14 ? ksoff(I + 2, b.rk) : b.wo + KO::CS; p.fill = I == 14 ? b.p[1] : I == 13 ? pa : pb; p.rd0 = I == 14 ? b.v[4] : b.k[(I & 1) ? 0 : 1]; p.rd1 = b.v[5]; }, I < 14 ? sV : sO, I < 14 ? KUS : KO::US, st);
}
template <int CH, int Q, int J, bool V = false> DI void o_word(const AC& c, const float (&inv)[5], bf16x8 (&nb)[4]) {
  u32x4 w = __builtin_bit_cast(u32x4, nb[Q]);
  unsigned r;
  if constexpr (V)
    asm volatile("v_mul_f32_e32 %0, %1, %3\n\tv_mul_f32_e32 %4, %2, %3\n\tv_cvt_pk_bf16_f32 %0, %0, %4"
                 : "=&v"(r) : "v"(c.o[2 * CH + (J >> 1)][Q][2 * (J & 1)]), "v"(c.o[2 * CH + (J >> 1)][Q][2 * (J & 1) + 1]), "v"(inv[Q]), "v"(inv[4]));
  else
  asm volatile("v_accvgpr_read_b32 %0, %1\n\tv_accvgpr_read_b32 %4, %2\n\tv_mul_f32_e32 %0, %0, %3\n\tv_mul_f32_e32 %4, %4, %3\n\tv_cvt_pk_bf16_f32 %0, %0, %4"
               : "=&v"(r) : "a"(c.o[2 * CH + (J >> 1)][Q][2 * (J & 1)]), "a"(c.o[2 * CH + (J >> 1)][Q][2 * (J & 1) + 1]), "v"(inv[Q]), "v"(inv[4]));
  w[J] = r;
  nb[Q] = __builtin_bit_cast(bf16x8, w);
}
template <int CH, int Q> DI void o_put(const bf16x8 (&nb)[4], unsigned ow) { asm volatile("ds_write_b128 %0, %1 offset:%2" :: "v"(ow), "v"(nb[Q]), "i"(CH * 4096 + Q * 1024) : "memory"); }
template <int E> struct CvX {
  static constexpr bool ACT = E == 3;
  const AC& c; const float (&inv)[5]; const bf16x8 (&cur)[4]; bf16x8 (&nb)[4]; unsigned ow;
  template <int G> DI void run() const {
    if constexpr (E < 3 && (G & 1)) o_word<E + 1, (G >> 3), (G >> 1) & 3>(c, inv, nb);
    if constexpr (G == 8 || G == 10) o_put<E, 2 + ((G >> 1) & 1)>(cur, ow);
    if constexpr (E < 3 && (G == 24 || G == 26)) o_put<E + 1, ((G >> 1) & 1)>(nb, ow);
  }
};
template <class K, int VM, int DM, int M> DI void gapE(AC& c, ST& S, float (&inv)[5], bf16x8 (&nb)[4], bf16x8 (&a0)[4], bf16x4& lo, const PC& pc, const Src& src, unsigned soff, unsigned long long& bad, unsigned ow) {
  constexpr int h = M / 18, r = M % 18, qb = 2 * h + (r & 1);
  if constexpr (M == 24) wait_vm<VM>();
  constexpr int D = M - (DM == 2 ? 4 : 0);
  constexpr int NR = DM == 2 ? 0 : VR - 20;
  if constexpr (M == NR + 4) wait_lgkm0();
  if constexpr (M == 6 && DM == 2) barrier();
  if constexpr (DM && (D == 3 || D == 19)) set_m0(pc.r2 + (D == 19 ? 4096u : 0u));
  if constexpr (r < 2) mm_vvv<false>(c.l[qb], c.ones, c.pf[qb]);
  else if constexpr (DM == 1 && r < 6) mm_vvv<false>(c.o[(r - 2) >> 1][qb], c.kf[(r - 2) >> 1], c.pf[qb]);
  else mm_avv(c.o[(r - 2) >> 1][qb], c.kf[(r - 2) >> 1], c.pf[qb]);
  if constexpr (DM && D > 0 && D < 32 && (D & 3) == 3) dma_p<W_NT, (D >> 2) & 3>(src.v[(D >> 2) & 3], src.srd, soff + (D >= 16 ? K::US : 0u));
  if constexpr (M < NR) {
    constexpr int db = (M + 16 - NR) >> 1; const unsigned a = c.ra[db & 1] + (db >> 1) * 256u;
    if constexpr (M & 1) c.kf[db] = cat(lo, lds_tr1(a + 4096u)); else lo = lds_tr1(a);
  }
  if constexpr (M >= 24 && M < 32) {
    constexpr int d = (M - 24) >> 1; const unsigned a = ring_rd<0>(pc, d);
    if constexpr (M & 1) a0[d] = cat(lo, lds_tr1(a + K::TS)); else lo = lds_tr1(a);
  }
  if constexpr (NEB + M < 32) sm_exp1<NEB + M>(S);
  if constexpr (M >= 10 && M < 14) { sm_cvt<2 + ((M - 10) >> 1), 2 * (M & 1)>(S, c); sm_cvt<2 + ((M - 10) >> 1), 2 * (M & 1) + 1>(S, c); }
  if constexpr (M == 14 || M == 15 || M == 22 || M == 23) asm volatile("v_rcp_f32_e32 %0, %1" : "=v"(inv[qb]) : "v"(c.l[qb][0]));
  if constexpr (M >= 16 && M < 22) o_word<0, ((M - 16) >> 2), (M & 3), (DM == 1)>(c, inv, nb);
  if constexpr (M >= 24 && M < 34) o_word<0, ((M - 18) >> 2), ((M - 18) & 3), (DM == 1)>(c, inv, nb);
  if constexpr (M == 32) o_put<0, 0>(nb, ow);
  if constexpr (M == 33) o_put<0, 1>(nb, ow);
  if constexpr (r == 4) asm volatile("" :: "v"(c.l[2 * h]), "v"(c.l[2 * h + 1]));
  if constexpr (M == 34)
    asm volatile("v_add_f32_e32 %1, %2, %3\n\tv_add_f32_e32 %1, %1, %4\n\tv_add_f32_e32 %1, %1, %5\n\tv_cmp_nle_f32_e64 %0, %1, %6"
                 : "=s"(bad) : "v"(inv[4]), "v"(c.l[0][0]), "v"(c.l[1][0]), "v"(c.l[2][0]), "v"(c.l[3][0]), "s"(LMAX));
  GAP();
}
template <class K, int VM, int DM, int... Ms> DI void phaseE(AC& c, ST& S, float (&inv)[5], bf16x8 (&nb)[4], bf16x8 (&a0)[4], const PC& pc, const Src& src, unsigned soff, unsigned long long& bad, unsigned ow, iseq<Ms...>) {
  bf16x4 lo;
#pragma unroll
  for (int q = 0; q < 4; ++q) nb[q] = bf16x8{};
  (gapE<K, VM, DM, Ms>(c, S, inv, nb, a0, lo, pc, src, soff, bad, ow), ...);
}
struct Pub { u32x4 pk[16]; srd_t srd; unsigned v[4], so; };
struct TL { bf16x8 w[2][3][4], x[2][3][4]; bf16x4 lo[2]; unsigned xa[3], xs[3], drop[2], l16; };
template <int S, int QB, int J> DI void pub_word(const f32x4 (&acc)[8][4], Pub& p) {
  const f32x4& a = acc[4 * S + (J >> 1)][QB];
  unsigned r;
  asm volatile("v_cvt_pk_bf16_f32 %0, %1, %2" : "=v"(r) : "v"(a[2 * (J & 1)]), "v"(a[2 * (J & 1) + 1]));
  p.pk[8 * S + 2 * QB + (J >> 2)][J & 3] = r;
}
template <int F> DI void pub_store(const Pub& p) {
  asm volatile("buffer_store_dwordx4 %0, %1, %2, %3 offen offset:%4 sc0 sc1" :: "v"(p.pk[F]), "v"(p.v[F >> 2]), "s"(p.srd), "s"(p.so), "i"((F & 3) * 1024) : "memory");
}
template <class K, int S, int E, int R> DI void tl_w(TL& t, const PC& c) {
  const unsigned a = ring_rd<(E + 1) % 3>(c, R >> 1) + S * 4096u;
  if constexpr (R & 1) t.w[S][E][R >> 1] = cat(t.lo[S], lds_tr1(a + K::TS)); else t.lo[S] = lds_tr1(a);
}
template <class K, int G> DI void tl_gap(f32x4 (&acc)[8][4], TL& t, const PC& c, Pub& p) {
  constexpr int S = G >= 48, qb = G < 16 ? G >> 2 : G < 48 ? (G - 16) >> 3 : (G - 48) / 12, e = G < 16 ? 0 : G < 48 ? 1 + ((G >> 2) & 1) : G % 3, db = G < 48 ? G & 3 : (G % 12) / 3;
  if constexpr (G == 10) wait_vm<4>();
  if constexpr (G == 27) wait_vm<0>();
  mm_vvv<false>(acc[4 * S + db][qb], t.w[S][e][db], t.x[S][e][qb]);
  if constexpr (G >= 28 && G <= 56 && !(G & 3)) pub_store<(G - 28) / 4>(p);
  if constexpr (G >= 59 && G % 6 == 5) pub_store<8 + (G - 59) / 6>(p);
  if constexpr (G < 8) tl_w<K, 0, 1, G>(t, c);
  if constexpr (G >= 10 && G < 18) tl_w<K, 0, 2, G - 10>(t, c);
  if constexpr (G >= 16 && G < 24) tl_w<K, 1, 0, G - 16>(t, c);
  if constexpr (G >= 25 && G < 56 && (G & 1)) tl_w<K, 1, 1 + (((G - 25) >> 2) & 1), 2 * ((G - 25) >> 3) + (((G - 25) >> 1) & 1)>(t, c);
  if constexpr (G < 34 && (G & 3) == 2) t.x[0][1 + ((G >> 2) & 1)][G >> 3] = lds128(t.xa[1 + ((G >> 2) & 1)] + (G >> 3) * K::QS);
  if constexpr (G >= 34 && G < 82 && (G & 3) == 2) t.x[1][((G - 34) % 12) >> 2][(G - 34) / 12] = lds128(t.xa[((G - 34) % 12) >> 2] + ((G - 34) / 12) * K::QS);
  if constexpr (G >= 24 && G < 56) pub_word<0, ((G - 24) >> 3), (G & 7)>(acc, p);
  if constexpr (G >= 54 && G % 3 < 2) pub_word<1, (G - 54) / 12, 2 * (((G - 54) % 12) / 3) + G % 3>(acc, p);
  if constexpr (G == 0 || G == 1 || G == 3) { t.xa[(G + 1) & 3] = t.xs[(G + 1) & 3] | t.l16; PINV(t.xa[(G + 1) & 3]); }
  if constexpr (G == 4 || G == 5) { p.v[2 * (G & 1)] = t.drop[G & 1] | t.l16; PINV(p.v[2 * (G & 1)]); }
  if constexpr (G == 7 || G == 8) { p.v[2 * (G & 1) + 1] = p.v[2 * (G & 1)] | 4096u; PINV(p.v[2 * (G & 1) + 1]); }
  GAP();
}
template <class K, int... Gs> DI void tl_run(f32x4 (&acc)[8][4], TL& t, const PC& c, Pub& p, iseq<Gs...>) {
  wait_vm_lgkm<12, 2>();
  GAP();
  (tl_gap<K, Gs>(acc, t, c, p), ...);
  pub_word<1, 3, 4>(acc, p); pub_word<1, 3, 5>(acc, p);
  asm volatile("s_nop 4");
  pub_word<1, 3, 6>(acc, p); pub_word<1, 3, 7>(acc, p); pub_store<15>(p);
}
template <int I> DI void own_put1(const u32x4& v, unsigned a) { asm volatile("ds_write_b128 %0, %1 offset:%2 ; %3" :: "v"(a), "v"(v), "i"((I & 7) * 1024), "i"(I) : "memory"); }
template <int B, int... Is> DI void own_put(const Pub& p, unsigned a, iseq<Is...>) { (own_put1<B + Is>(p.pk[B + Is], a), ...); }
DI void redo_head(const AC& ac, const char* __restrict__ Kp, const char* __restrict__ Vp, unsigned lane, unsigned ow) {
  const unsigned r16 = lane & 15u, g = lane >> 4;
#pragma unroll 1
  for (unsigned qb = 0; qb < 4; ++qb) {
    bf16x8 qq[4];
#pragma unroll
    for (int s = 0; s < 4; ++s) qq[s] = qb == 0 ? ac.q[0][s] : qb == 1 ? ac.q[1][s] : qb == 2 ? ac.q[2][s] : ac.q[3][s];
    f32x4 o[8];
#pragma unroll
    for (int d = 0; d < 8; ++d) o[d] = f32x4{};
    float m = -__builtin_inff(), l = 0.f;
#pragma unroll 1
    for (unsigned t = 0; t < 16; ++t) {
      f32x4 s0 = {}, s1 = {};
      const char* kp = Kp + (t * 32u + r16) * 256u + g * 16u;
#pragma unroll
      for (int s = 0; s < 4; ++s) {
        s0 = __builtin_amdgcn_mfma_f32_16x16x32_bf16(*(const bf16x8*)(kp + s * 64), qq[s], s0, 0, 0, 0);
        s1 = __builtin_amdgcn_mfma_f32_16x16x32_bf16(*(const bf16x8*)(kp + s * 64 + 4096), qq[s], s1, 0, 0, 0);
      }
      const float mn = fmaxf(m, row_max(s0, s1)), al = __builtin_amdgcn_exp2f(m - mn);
      m = mn;
#pragma unroll
      for (int j = 0; j < 4; ++j) { s0[j] = __builtin_amdgcn_exp2f(s0[j] - mn); s1[j] = __builtin_amdgcn_exp2f(s1[j] - mn); }
      l = l * al + ((s0[0] + s0[1]) + (s0[2] + s0[3])) + ((s1[0] + s1[1]) + (s1[2] + s1[3]));
      const bf16x8 pf = __builtin_bit_cast(bf16x8, pack2(s0, s1));
      const __bf16* vp = (const __bf16*)(Vp + (t * 32u + g * 4u) * 256u) + r16;
#pragma unroll
      for (int d = 0; d < 8; ++d) {
        bf16x8 vf;
#pragma unroll
        for (int j = 0; j < 8; ++j) vf[j] = vp[((j & 3) + (j >> 2) * 16) * 128 + d * 16];
        o[d] = __builtin_amdgcn_mfma_f32_16x16x32_bf16(vf, pf, o[d] * al, 0, 0, 0);
      }
    }
    const float inv = 1.0f / xlane<false>(l);
#pragma unroll
    for (int s = 0; s < 4; ++s) *(LAS u32x4*)(size_t)(ow + s * 4096u + qb * 1024u) = pack2(o[2 * s] * inv, o[2 * s + 1] * inv);
  }
}
template <bool isA, int J> DI void q_conv1(AC& ac, f32x4 (&acc)[8][4], Stash& st, const Src& sK, const Src& sV, unsigned rk, unsigned p3, u32x4 (&t)[2]) {
  constexpr int s = J >> 2, qb = J & 3;
  if constexpr (isA) { if constexpr (2 * J < NW) stash_write1<2 * J>(st); }
  else {
    constexpr int p = 2 * (J & 1);
    const Src& src = s < 2 ? sK : sV;
    const unsigned so = ksoff(s == 0 ? 2 : s == 1 ? SK : s == 2 ? SV : SP, rk) + ((J >> 1) & 1) * KUS;
    if constexpr (s == 0 || s == 3) {
      if constexpr (p == 0) asm volatile("s_mov_b32 m0, %0\n\ts_nop 0" :: "s"(p3 + (s == 3 ? SLOT : 0u) + (J & 2) * 2048u) : "memory");
      dma_p<KV_NT, p>(src.v[p], src.srd, so); dma_p<KV_NT, p + 1>(src.v[p + 1], src.srd, so);
    } else {
      asm volatile("buffer_load_dwordx4 %0, %1, %2, %3 offen offset:%4 nt" : "=a"(st.p[2 * J - 8]) : "v"(src.v[p]), "s"(src.srd), "s"(so), "i"(1024 * p) : "memory");
      asm volatile("buffer_load_dwordx4 %0, %1, %2, %3 offen offset:%4 nt" : "=a"(st.p[2 * J - 7]) : "v"(src.v[p + 1]), "s"(src.srd), "s"(so), "i"(1024 * p + 1024) : "memory");
    }
  }
  t[J & 1] = pack2(acc[2 * s][qb] * SC, acc[2 * s + 1][qb] * SC);
  PINV(t[J & 1]);
  GAP();
  if constexpr (J & 1) {
#pragma unroll
    for (int k = 0; k < 2; ++k) {
#pragma unroll
      for (int i = 0; i < 4; ++i) {
        u32x4& w = t[i >> 1];
        auto a = k ? __builtin_amdgcn_permlane16_swap(w[i & 1], w[2 + (i & 1)], false, false) : __builtin_amdgcn_permlane32_swap(w[i & 1], w[2 + (i & 1)], false, false);
        w[i & 1] = a[0]; w[2 + (i & 1)] = a[1];
      }
      if constexpr (isA) { if (k == 0) { if constexpr (2 * J - 1 < NW) stash_write1<2 * J - 1>(st); } else { if constexpr (2 * J + 1 < NW) stash_write1<2 * J + 1>(st); } }
      GAP();
    }
    ac.q[qb - 1][s] = __builtin_bit_cast(bf16x8, t[0]); ac.q[qb][s] = __builtin_bit_cast(bf16x8, t[1]);
    asm volatile("" : "+a"(ac.q[qb - 1][s]), "+a"(ac.q[qb][s]));
    GAP();
  }
}
template <bool isA, int... Js> DI void q_conv(AC& ac, f32x4 (&acc)[8][4], Stash& st, const Src& sK, const Src& sV, unsigned rk, unsigned p3, iseq<Js...>) { u32x4 t[2]; (q_conv1<isA, Js>(ac, acc, st, sK, sV, rk, p3, t), ...); }
template <int N, int N0 = 0> DI void zero_o(AC& ac) {
#pragma unroll
  for (int i = N0; i < N; ++i)
#pragma unroll
    for (int q = 0; q < 4; ++q) { ac.o[i][q] = f32x4{}; asm volatile("" : "+a"(ac.o[i][q])); }
}

template <bool isA>
DI void body(const char* __restrict__ X, const char* __restrict__ K, const char* __restrict__ V, const char* __restrict__ Wq, const char* __restrict__ Wo,
             const char* __restrict__ Wo2, unsigned long long tnow, char* __restrict__ part, char* __restrict__ out) {
  const unsigned tlim = (unsigned)tnow + EASY_T;
  unsigned tid = threadIdx.x;
  PINV(tid);
  const unsigned lane = tid & 63u, w = rfl(tid >> 6);
  const unsigned bid = blockIdx.x, xg = bid & 7u, rr = bid >> 3, b = rr & 15u, h = xg * 4u + w;
  const unsigned r16 = lane & 15u, g = lane >> 4;
  const size_t kvo = isA ? (size_t)(b * 8u + xg) * 131072u : (size_t)(b * 32u + h) * 131072u;
  const unsigned rk = isA ? 7u * b : 5u * b + 8u * w;
  typedef PK<512u, 4096u, 0x1300u, 0xF300u, 0xC300u, 128u, 32u * 8192u, true> KQ;
  typedef PK<512u, 4096u, 0x1300u, 0xF300u, 0xC300u, KUS, 8192u, false> KT;
  typedef PK<2048u, 1024u, 0x1000u, 0xF000u, 0xF000u, 128u, 32u * 1024u, false> KO;
  PC pc;
  pc.r01 = 65536u + w * 16384u; pc.r2 = 131072u + w * 8192u;
  const unsigned zq = ((b & 3u) << 14) | ((b >> 2) << 8), co = 4u * w;
  const srd_t srdH = mk_srd(part + HOFF, 4096u + 256u * 128u);
  const unsigned tok_off = rr * 128u, flg_off = 4096u + rr * 1024u;
  const unsigned l8 = (lane & 7u) * 4u;
  unsigned entry_flags, dum0 = lane;
  Src sQ, sX, sK, sV, sO;
  const unsigned p0 = b & 3u, p1 = (p0 + 1u) & 3u, p2 = (p0 + 2u) & 3u, p3 = (p0 + 3u) & 3u, xw = w * 4096u;
  Stash st;
  mk_src_kv<isA, false>(sK, K + kvo, lane, w);
  mk_src_kv<isA, true>(sV, V + kvo, lane, w);
  if constexpr (isA) { stash_load(st, sK, sV, rk, ((w & 1u) << GA.p1) + ((w >> 1) << GA.u), make_iseq<64>{}); GAP(); }
  asm volatile("buffer_load_dword %0, %1, %2, %3 offen sc1" : "=v"(entry_flags) : "v"(l8), "s"(srdH), "s"(flg_off + xg * 128u) : "memory");
  sQ.srd = mk_srd(Wq, 4194304u); sX.srd = mk_srd(X, 1048576u);
  mk_w<false>(sQ, h * 256u, 8192u, lane);
  mk_rows<false>(sX, b * 65536u + w * 16384u, 1024u, lane);
  sO.srd = mk_srd(Wo + (size_t)xg * 524288u, 524288u);
  mk_w<true>(sO, w * 256u, 1024u, lane);
  if constexpr (isA) {
    dma_unit<X_NT>(sX, p0 * 256u, xw + p0 * 16384u);
    dma_elem<W_NT>(sQ, q_soff(zq + 0x2200u), KQ::US, pc.r01 + SLOT);
    dma_unit<X_NT>(sX, p1 * 256u, xw + p1 * 16384u);
    dma_unit<X_NT>(sX, p2 * 256u, xw + p2 * 16384u);
    dma_unit<X_NT>(sX, p3 * 256u, xw + p3 * 16384u);
  }
  srd_t pb_srd = mk_srd(part + (size_t)rr * 524288u, 524288u);
  unsigned pb_so = xg * 65536u + w * 16384u, drop0 = 2u * w == xg ? 0x80000000u : 0u, drop1 = 2u * w + 1u == xg ? 0x80002000u : 0x2000u;
  unsigned xs0 = ((co + 13u) & 15u) << 12, xs1 = ((co + 14u) & 15u) << 12, xs2 = ((co + 15u) & 15u) << 12, oz = (co + 3u) << 12, os3 = (co + 3u) * KO::CS;
  unsigned ks0 = ksoff(0u, rk), ks1 = ksoff(1u, rk);
  {
    PINS(pb_srd); PINS(pb_so); PINS(drop0); PINS(drop1); PINS(xs0); PINS(xs1); PINS(xs2); PINS(oz); PINS(os3); PINS(sO.srd);
    if constexpr (!isA) {
      PINS(sK.srd); PINS(sV.srd); PINS(ks0); PINS(ks1);
#pragma unroll
      for (int i = 0; i < 4; ++i) { PINV(sK.v[i]); PINV(sV.v[i]); PINV(sO.v[i]); }
    }
  }
  const unsigned rb = 1024u * (r16 >> 2) + 64u * (r16 & 3u) + 16u * (g ^ ((r16 >> 2) & 2u));
  const unsigned tb = 64u * ((lane >> 2) & 3u) + 32u * (g & 1u) + 8u * (lane & 3u);
  const unsigned tbV = tb + 1024u * g;
  const unsigned sb0 = w * 2048u + lane * 16u;
  st.base[0] = sb0; st.base[1] = sb0 + 65536u; st.base[2] = sb0 + 131072u;
  AC ac;
  if constexpr (!isA) zero_o<8, 2>(ac);
  {
    f32x4 acc[8][4];
    bf16x8 a0[4], a1[4], bf[4];
    if constexpr (isA) pg_walk<AWSH>(w);
    wait_vm<(isA ? 20 : 9)>();
    barrier();
    proj_start<KQ, 2>(a0, bf, pc, tbV, rb, zq, q_soff(zq + 0x3100u));
    if constexpr (isA) {
      proj_elem<KQ, 2, true,  false, true, W_NT, 63, 63>(acc, a0, a1, bf, pc, sQ);
      proj_elem<KQ, 0, false, false, true, W_NT, 63, 28>(acc, a0, a1, bf, pc, sQ);
      proj_elem<KQ, 1, false, false, true, W_NT, 28, 16>(acc, a0, a1, bf, pc, sQ);
      proj_elem<KQ, 2, false, false, true, W_NT, 16, 16, true>(acc, a0, a1, bf, pc, sQ);
    } else {
      dma_elem<W_NT>(sQ, q_soff(zq + 0x2200u), KQ::US, pc.r01 + SLOT);
      proj_elem<KQ, 2, true,  false, true, W_NT, 13, 20, false, true, true>(acc, a0, a1, bf, pc, sQ, NoX{}, &sX, p1 * 256u, xw + p1 * 16384u, p2 * 256u, xw + p2 * 16384u);
      proj_elem<KQ, 0, false, false, true, W_NT, 24, 28, false, true, false>(acc, a0, a1, bf, pc, sQ, NoX{}, &sX, p3 * 256u, xw + p3 * 16384u);
      proj_elem<KQ, 1, false, false, true, W_NT, 28, 24>(acc, a0, a1, bf, pc, sQ);
      proj_elem<KQ, 2, false, false, true, W_NT, 20, 16, true>(acc, a0, a1, bf, pc, sQ);
    }
    typedef std::conditional_t<isA, EasyX<EASY, EASY_L>, NoX> PX;
    const PX px = [&] { if constexpr (isA) return EasyX<EASY, EASY_L>{tnow, tlim}; else return NoX{}; }();
#pragma unroll 1
    for (unsigned e = 0; e < 3; ++e) {
      proj_elem<KQ, 0, false, false, true, W_NT, 16, 16>(acc, a0, a1, bf, pc, sQ, px);
      proj_elem<KQ, 1, false, false, true, W_NT, 16, 16>(acc, a0, a1, bf, pc, sQ, px);
      proj_elem<KQ, 2, false, false, true, W_NT, 16, 16>(acc, a0, a1, bf, pc, sQ, px);
    }
    if constexpr (isA) {
      proj_elem<KQ, 0, false, false, false, W_NT, 16, 12>(acc, a0, a1, bf, pc, sQ, px);
      proj_elem<KQ, 1, false, false, false, W_NT, 8, 4>(acc, a0, a1, bf, pc, sQ, px);
      proj_elem<KQ, 2, false, true, false, W_NT, 0, 0>(acc, a0, a1, bf, pc, sQ, px);
    } else {
      pc.soff = ks0;
      proj_elem<KT, 0, false, false, true, KV_NT, 16, 16>(acc, a0, a1, bf, pc, sK);
      pc.soff = ks1;
      proj_elem<KT, 1, false, false, true, KV_NT, 16, 16>(acc, a0, a1, bf, pc, sK);
      pc.soff = ks0;
      proj_elem<KT, 2, false, true, true, KV_NT, 16, 16>(acc, a0, a1, bf, pc, sV);
    }
    PINV(entry_flags);
    if (w == 0) {
      const unsigned long long mk = __builtin_amdgcn_ballot_w64(((entry_flags >> (lane & 7u)) & 1u) != 0u);
      const unsigned tok = 0x5a5a5a00u | (~(unsigned)mk & 0xffu);
      if (lane == 0) asm volatile("buffer_store_dword %0, off, %1, %2 sc1" :: "v"(tok), "s"(srdH), "s"(tok_off + xg * 4u) : "memory");
    }
    barrier();
    acc_fence_v(acc);
    q_conv<isA>(ac, acc, st, sK, sV, rk, w * 16384u, make_iseq<16>{});
  }
  ST SA, SB;
  if constexpr (isA) {
    const unsigned kb = rb, vb = SLOT + tbV;
    unsigned dum = lane, to = ((lane & 8u) * 16u + (lane & 7u) * 4u + (lane >> 4) * 32768u) | (((lane >> 4) ^ w) << 8);
    const unsigned tbo = (b & 3u) * 1024u + (b >> 2) * 131072u;
    PINV(to);
    const char* ta = Wo + (size_t)xg * 524288u + tbo;
    const char* tb2 = Wo2 + (size_t)xg * 524288u + tbo;
    touch(dum, to, ta);
    asm volatile("s_waitcnt lgkmcnt(0)" ::: "memory");
    barrier();
    k_read(ac, kb + kv_pair(0) * 16384u);
    wait_lgkm0();
    GAP();
    qk0<false>(ac, SA, sK, 0u, 0u, st, make_iseq<32>{});
    k_read(ac, kb + kv_pair(1) * 16384u);
    touch(dum, to, ta);
    sm_first<!isA>(ac, SA);
    unsigned i4 = 0;
    unsigned blk; const char* tp;
    auto ak = [&](PA& p, auto k) __attribute__((always_inline)) {
      if constexpr (k.value == 0) { __builtin_amdgcn_s_sleep(PACE_ATT); PINS(i4); blk = (unsigned)((kv_tab() >> 8) >> i4) << 14; i4 += 4u; PINS(blk); PINS(i4); }
      if constexpr (k.value == 1) { p.rd0 = kb + (blk & 0x3c000u); PINV(p.rd0); }
    };
    auto at = [&](PA& p, auto k) __attribute__((always_inline)) {
      if constexpr (k.value == 0) { PINS(i4); blk = (unsigned)(kv_tab() >> i4) << 14; PINS(blk); }
      if constexpr (k.value == 1) { p.rd0 = vb + (blk & 0x3c000u); PINV(p.rd0); }
      if constexpr (k.value == 2) asm volatile("s_cmp_lt_u32 %1, 24\n\ts_cselect_b64 %0, %2, %3" : "=s"(tp) : "s"(i4), "s"(ta), "s"(tb2 - 32768) : "scc");
      if constexpr (k.value == 3) { p.rd1 = p.rd0 ^ 32u; PINV(p.rd1); asm volatile("global_load_dword %0, %1, %2 nt" : "+v"(dum) : "v"(to), "s"(tp) : "memory"); }
      if constexpr (k.value == 4) asm volatile("global_load_dword %0, %1, %2\n\tv_add_u32_e32 %1, %3, %1" : "+v"(dum), "+v"(to) : "s"(tp), "i"(4096) : "memory");
    };
    phaseA<true, false, KV_NT, 63, NW + 8>(ac, SA, SB, at, sK, 0u, st);
    phaseB<3, true, false, KV_NT, 63>(ac, SB, ak, sK, 0u, st);
    phaseA<true, false, KV_NT, 63>(ac, SB, SA, at, sK, 0u, st);
    phaseB<true, true, false, KV_NT, 63>(ac, SA, ak, sK, 0u, st);
    barrier();
    while (i4 < 24u) {
      phaseA<true, false, KV_NT, 63>(ac, SA, SB, at, sK, 0u, st);
      phaseB<true, true, false, KV_NT, 63>(ac, SB, ak, sK, 0u, st);
      phaseA<true, false, KV_NT, 63>(ac, SB, SA, at, sK, 0u, st);
      phaseB<true, true, false, KV_NT, 63>(ac, SA, ak, sK, 0u, st);
    }
    barrier();
    phaseA<true, false, KV_NT, 63, 40>(ac, SA, SB, at, sK, 0u, st);
    phaseB<true, true, false, KV_NT, 63, 43>(ac, SB, ak, sK, 0u, st);
    phaseA<true, false, KV_NT, 63, 46>(ac, SB, SA, at, sK, 0u, st);
    phaseB<true, true, false, KV_NT, 63, 49>(ac, SA, ak, sK, 0u, st);
    wait_vm_lgkm<8, 8>(); barrier();
    phaseA<true, false, KV_NT, 63, 52>(ac, SA, SB, at, sK, 0u, st);
    phaseB<true, true, false, KV_NT, 63, 55>(ac, SB, ak, sK, 0u, st);
    phaseA<true, false, KV_NT, 63, 58>(ac, SB, SA, at, sK, 0u, st);
    phaseB<true, true, false, KV_NT, 63, 61>(ac, SA, ak, sK, 0u, st);
    wait_vm_lgkm<63, 8>(); barrier();
    while (i4 < 56u) {
      phaseA<true, false, KV_NT, 63>(ac, SA, SB, at, sK, 0u, st);
      phaseB<true, true, false, KV_NT, 63>(ac, SB, ak, sK, 0u, st);
      phaseA<true, false, KV_NT, 63>(ac, SB, SA, at, sK, 0u, st);
      phaseB<true, true, false, KV_NT, 63>(ac, SA, ak, sK, 0u, st);
    }
    barrier();
    unsigned v14 = vb + kv_pair(14) * 16384u, x14 = v14 ^ 32u; PINV(v14); PINV(x14);
    phaseA<true, true, W_NT, 63>(ac, SA, SB, [&](PA& p, auto) { p.soff = co * KO::CS; p.fill = pc.r01; p.rd0 = v14; p.rd1 = x14; }, sO, KO::US, st);
    phaseB<true, false, true, W_NT, 63>(ac, SB, [&](PA& p, auto k) __attribute__((always_inline)) {
      p.soff = (co + 1u) * KO::CS; p.fill = pc.r01 + SLOT; p.rd0 = v14 + 16384u; p.rd1 = x14 + 16384u;
      if constexpr (k.value == 1) proj_setup<KO>(pc, tb + 512u * g, lane * 16u, oz, os3); }, sO, KO::US, st);
    dum0 = dum;
  } else {
    const unsigned P0 = pc.r01, P1 = pc.r01 + SLOT, P2 = pc.r2, P3 = w * 16384u;
    unsigned v0 = tbV + P0, v2 = tbV + P2, x0 = v0 ^ 32u, x2 = v2 ^ 32u, k1 = rb + P1, k3 = rb + P3;
    PINV(v0); PINV(v2); PINV(x0); PINV(x2); PINV(k1); PINV(k3);
    wait_vm<48>();
    k_read(ac, k1 - SLOT);
#pragma unroll
    for (int i = 0; i < 8; ++i) { ac.o[i >> 2][i & 3] = f32x4{}; PINV(ac.o[i >> 2][i & 3]); }
    wait_lgkm0();
    GAP();
    qk0<true>(ac, SA, sV, ks1, P0, st, make_iseq<32>{});
    wait_vm<48>();
    GAP();
    k_read(ac, k1);
    sm_first<!isA>(ac, SA);
    BS bs{{P0, P1, P2, P3}, {v0, x0, v2, x2, tbV + P3, (tbV + P3) ^ 32u}, {k1, k3}, rk, co * KO::CS, lane * 16u, w};
    PINV(bs.v[4]); PINV(bs.v[5]);
    proj_setup<KO>(pc, tb + 512u * g, lane * 16u, oz, os3);
#define B2(I) b_step<KO, I>(ac, SA, SB, bs, sK, sV, sO, st); b_step<KO, I + 1>(ac, SB, SA, bs, sK, sV, sO, st);
    B2(0) B2(2) B2(4) B2(6) B2(8) B2(10)
    B2(12) b_step<KO, 14>(ac, SA, SB, bs, sK, sV, sO, st);
#undef B2
  }
  {
    const unsigned l16 = lane * 16u;
    unsigned ow = w * 16384u + l16;
    PINV(ow);
    f32x4 acc[8][4];
    bf16x8 a0[4], a1[4], b0[4], bf[4];
    float inv[5];
    asm volatile("" : "=v"(inv[4]));
    unsigned toks;
    asm volatile("buffer_load_dword %0, %1, %2, %3 offen sc1" : "=v"(toks) : "v"(l8), "s"(srdH), "s"(tok_off) : "memory");
    GAP();
    unsigned long long bad;
    phaseE<KO, (isA ? 18 : 19), (isA ? 2 : 1)>(ac, SB, inv, b0, a0, pc, sO, os3 - KO::CS, bad, ow, make_iseq<36>{});
    if (__builtin_expect(bad != 0ull, 0)) {
      asm volatile("s_waitcnt vmcnt(0)" : "+v"(toks) :: "memory");
      acc_fence_a(ac.o);
      redo_head(ac, K + kvo, V + kvo, lane, ow);
#pragma unroll
      for (int q = 0; q < 4; ++q) {
        inv[q] = 1.0f;
        b0[q] = lds128(ow + q * 1024u);
#pragma unroll
        for (int s = 1; s < 4; ++s) {
          const u32x4 t = __builtin_bit_cast(u32x4, lds128(ow + s * 4096u + q * 1024u));
          ac.o[2 * s][q] = f32x4{__uint_as_float(t[0] << 16), __uint_as_float(t[0] & 0xffff0000u), __uint_as_float(t[1] << 16), __uint_as_float(t[1] & 0xffff0000u)};
          ac.o[2 * s + 1][q] = f32x4{__uint_as_float(t[2] << 16), __uint_as_float(t[2] & 0xffff0000u), __uint_as_float(t[3] << 16), __uint_as_float(t[3] & 0xffff0000u)};
        }
      }
      wait_lgkm0();
      acc_fence_a(ac.o);
    }
#pragma unroll
    for (int q = 0; q < 4; ++q) bf[q] = bf16x8{};
    GAP();
    proj_elem<KO, 0, true, false, true, W_NT, 16, 16>(acc, a0, a1, b0, pc, sO, CvX<0>{ac, inv, b0, bf, ow});
    proj_elem<KO, 1, false, false, true, W_NT, 16, 16>(acc, a0, a1, bf, pc, sO, CvX<1>{ac, inv, bf, b0, ow});
    proj_elem<KO, 2, false, false, true, W_NT, 16, 16>(acc, a0, a1, b0, pc, sO, CvX<2>{ac, inv, b0, bf, ow});
    if constexpr (isA) PINV(dum0);
    proj_elem<KO, 0, false, false, true, W_NT, 16, 16, true>(acc, a0, a1, bf, pc, sO, CvX<3>{ac, inv, bf, b0, ow});
    typedef std::conditional_t<isA, EasyX<EASY2, EASY2_T - EASY2_F>, NoX> PX;
    const PX px = [&] { if constexpr (isA) return EasyX<EASY2, EASY2_T - EASY2_F>{tnow, tlim + (EASY2_F - EASY_T)}; else return NoX{}; }();
#pragma unroll 1
    for (unsigned e = 0; e < 3; ++e) {
      proj_elem<KO, 1, false, false, true, W_NT, 16, 16>(acc, a0, a1, bf, pc, sO, px);
      proj_elem<KO, 2, false, false, true, W_NT, 16, 16>(acc, a0, a1, bf, pc, sO, px);
      proj_elem<KO, 0, false, false, true, W_NT, 16, 16>(acc, a0, a1, bf, pc, sO, px);
    }
    Pub pb;
    pb.srd = pb_srd; pb.so = pb_so;
#pragma unroll
    for (int i = 0; i < 16; ++i) pb.pk[i] = u32x4{};
    TL tl;
#pragma unroll
    for (int i = 0; i < 4; ++i) { tl.w[0][0][i] = a0[i]; tl.x[0][0][i] = bf[i]; }
    tl.xs[0] = xs0; tl.xs[1] = xs1; tl.xs[2] = xs2; tl.drop[0] = drop0; tl.drop[1] = drop1; tl.l16 = l16;
    tl_run<KO>(acc, tl, pc, pb, make_iseq<96>{});
    const unsigned lr = ((lane + xg) & 7u) * 4u;
    unsigned fl;
    asm volatile("buffer_load_dword %0, %1, %2, %3 offen sc1" : "=v"(fl) : "v"(lr), "s"(srdH), "s"(flg_off + xg * 128u) : "memory");
    PINV(toks);
    if (w == (xg >> 1)) {
      const unsigned a = pc.r01 + SLOT + l16;
      if (xg & 1u) own_put<8>(pb, a, make_iseq<8>{}); else own_put<0>(pb, a, make_iseq<8>{});
    }
    const unsigned fo = lane * 128u + xg * 4u, tau = __builtin_amdgcn_readlane(toks, xg);
    unsigned oo = ((b * 64u + 16u * w + r16) * 1024u + (isA ? 0u : 512u) + 64u * xg + 8u * g) * 2u;
    PINV(oo);
    wait_lgkm0();
    barrier();
    const unsigned oa = 65536u + SLOT + (xg >> 1) * 16384u + w * 2048u + l16;
    const u32x4 r0 = __builtin_bit_cast(u32x4, lds128(oa)), r1 = __builtin_bit_cast(u32x4, lds128(oa + 1024u));
    GAP();
    wait_vm<9>();
    if (lane == 2u * w) asm volatile("buffer_store_dword %0, %1, %2, %3 offen sc1" :: "v"(toks), "v"(fo), "s"(srdH), "s"(flg_off) : "memory");
    wait_vm<2>();
    if (lane == 2u * w + 1u) asm volatile("buffer_store_dword %0, %1, %2, %3 offen sc1" :: "v"(toks), "v"(fo), "s"(srdH), "s"(flg_off) : "memory");
    f32x16 f[8];
#pragma unroll
    for (int j = 0; j < 8; ++j) { const unsigned t = j < 4 ? r0[j & 3] : r1[j & 3]; f[0][2 * j] = __uint_as_float(t << 16); f[0][2 * j + 1] = __uint_as_float(t & 0xffff0000u); }
    PINV(f[0]);
    asm volatile("s_waitcnt vmcnt(2)" : "+v"(fl) : "v"(pb.pk[0]), "v"(pb.pk[1]), "v"(pb.pk[2]), "v"(pb.pk[3]), "v"(pb.pk[4]), "v"(pb.pk[5]), "v"(pb.pk[6]), "v"(pb.pk[7]),
                 "v"(pb.pk[8]), "v"(pb.pk[9]), "v"(pb.pk[10]), "v"(pb.pk[11]), "v"(pb.pk[12]), "v"(pb.pk[13]), "v"(pb.pk[14]), "v"(pb.pk[15]) : "memory");
    unsigned got = 0xffffff01u, spin = 1u << 21, fresh, so, f2;
#define POLL(R) "buffer_load_dword %[" R "], %[lr], %[sh], %[flg] offen sc1\n\t"
#define SEEN(R) "v_cmp_eq_u32_e32 vcc, %[tau], %[" R "]\n\ts_andn2_b32 %[fr], vcc_lo, %[got]\n\ts_cbranch_scc1 .Lproc_%=\n\t"
    asm volatile(
        SEEN("fl") POLL("fl") "s_sleep 4\n"
        ".Lidle_%=:\n\t" POLL("f2") "s_waitcnt vmcnt(1)\n\t" SEEN("fl") "s_sleep %[slp]\n\t" POLL("fl") "s_waitcnt vmcnt(1)\n\t" SEEN("f2")
        "s_sleep %[slp]\n\ts_sub_u32 %[spin], %[spin], 1\n\ts_cbranch_scc0 .Lidle_%=\n\ts_branch .Ldone_%=\n"
        ".Lproc_%=:\n\ts_or_b32 %[got], %[got], %[fr]\n\t"
        PGS "s_bitcmp1_b32 %[fr], 1\n\ts_cbranch_scc0 .La1_%=\n\ts_add_u32 %[so], %[ro], 65536\n\ts_bitset0_b32 %[so], 19\n\t"
        "buffer_load_dwordx4 v[152:155], %[l16], %[sp], %[so] offen sc1\n\tbuffer_load_dwordx4 v[156:159], %[l16], %[sp], %[so] offen offset:1024 sc1\n.La1_%=:\n\t"
        PGS "s_bitcmp1_b32 %[fr], 2\n\ts_cbranch_scc0 .La2_%=\n\ts_add_u32 %[so], %[ro], 131072\n\ts_bitset0_b32 %[so], 19\n\t"
        "buffer_load_dwordx4 v[168:171], %[l16], %[sp], %[so] offen sc1\n\tbuffer_load_dwordx4 v[172:175], %[l16], %[sp], %[so] offen offset:1024 sc1\n.La2_%=:\n\t"
        PGS "s_bitcmp1_b32 %[fr], 3\n\ts_cbranch_scc0 .La3_%=\n\ts_add_u32 %[so], %[ro], 196608\n\ts_bitset0_b32 %[so], 19\n\t"
        "buffer_load_dwordx4 v[184:187], %[l16], %[sp], %[so] offen sc1\n\tbuffer_load_dwordx4 v[188:191], %[l16], %[sp], %[so] offen offset:1024 sc1\n.La3_%=:\n\t"
        PGS "s_bitcmp1_b32 %[fr], 4\n\ts_cbranch_scc0 .La4_%=\n\ts_add_u32 %[so], %[ro], 262144\n\ts_bitset0_b32 %[so], 19\n\t"
        "buffer_load_dwordx4 v[200:203], %[l16], %[sp], %[so] offen sc1\n\tbuffer_load_dwordx4 v[204:207], %[l16], %[sp], %[so] offen offset:1024 sc1\n.La4_%=:\n\t"
        PGS "s_bitcmp1_b32 %[fr], 5\n\ts_cbranch_scc0 .La5_%=\n\ts_add_u32 %[so], %[ro], 327680\n\ts_bitset0_b32 %[so], 19\n\t"
        "buffer_load_dwordx4 v[216:219], %[l16], %[sp], %[so] offen sc1\n\tbuffer_load_dwordx4 v[220:223], %[l16], %[sp], %[so] offen offset:1024 sc1\n.La5_%=:\n\t"
        PGS "s_bitcmp1_b32 %[fr], 6\n\ts_cbranch_scc0 .La6_%=\n\ts_add_u32 %[so], %[ro], 393216\n\ts_bitset0_b32 %[so], 19\n\t"
        "buffer_load_dwordx4 v[232:235], %[l16], %[sp], %[so] offen sc1\n\tbuffer_load_dwordx4 v[236:239], %[l16], %[sp], %[so] offen offset:1024 sc1\n.La6_%=:\n\t"
        PGS "s_bitcmp1_b32 %[fr], 7\n\ts_cbranch_scc0 .La7_%=\n\ts_add_u32 %[so], %[ro], 458752\n\ts_bitset0_b32 %[so], 19\n\t"
        "buffer_load_dwordx4 v[248:251], %[l16], %[sp], %[so] offen sc1\n\tbuffer_load_dwordx4 v[252:255], %[l16], %[sp], %[so] offen offset:1024 sc1\n.La7_%=:\n\t"
        "s_waitcnt vmcnt(0)\n\ts_cmp_eq_u32 %[got], -1\n\ts_cbranch_scc1 .Ltok_%=\n\t" POLL("fl") "s_branch .Lwide_%=\n"
        ".Ltok_%=:\n\tbuffer_load_dword %[fl], %[l8], %[sh], %[tok] offen sc1\n"
        ".Lwide_%=:\n\t"
        "s_bitcmp1_b32 %[fr], 1\n\ts_cbranch_scc0 .Lb1_%=\n\t"
        PGS "v_lshlrev_b32_e32 v144, 16, v152\n\tv_and_b32_e32 v145, %[hi], v152\n\tv_lshlrev_b32_e32 v146, 16, v153\n\tv_and_b32_e32 v147, %[hi], v153\n\t"
        PGS "v_lshlrev_b32_e32 v148, 16, v154\n\tv_and_b32_e32 v149, %[hi], v154\n\tv_lshlrev_b32_e32 v150, 16, v155\n\tv_and_b32_e32 v151, %[hi], v155\n\t"
        PGS "v_lshlrev_b32_e32 v152, 16, v156\n\tv_and_b32_e32 v153, %[hi], v156\n\tv_lshlrev_b32_e32 v154, 16, v157\n\tv_and_b32_e32 v155, %[hi], v157\n\t"
        PGS "v_lshlrev_b32_e32 v156, 16, v158\n\tv_and_b32_e32 v157, %[hi], v158\n\tv_lshlrev_b32_e32 v158, 16, v159\n\tv_and_b32_e32 v159, %[hi], v159\n\t"
        ".Lb1_%=:\n\t"
        "s_bitcmp1_b32 %[fr], 2\n\ts_cbranch_scc0 .Lb2_%=\n\t"
        PGS "v_lshlrev_b32_e32 v160, 16, v168\n\tv_and_b32_e32 v161, %[hi], v168\n\tv_lshlrev_b32_e32 v162, 16, v169\n\tv_and_b32_e32 v163, %[hi], v169\n\t"
        PGS "v_lshlrev_b32_e32 v164, 16, v170\n\tv_and_b32_e32 v165, %[hi], v170\n\tv_lshlrev_b32_e32 v166, 16, v171\n\tv_and_b32_e32 v167, %[hi], v171\n\t"
        PGS "v_lshlrev_b32_e32 v168, 16, v172\n\tv_and_b32_e32 v169, %[hi], v172\n\tv_lshlrev_b32_e32 v170, 16, v173\n\tv_and_b32_e32 v171, %[hi], v173\n\t"
        PGS "v_lshlrev_b32_e32 v172, 16, v174\n\tv_and_b32_e32 v173, %[hi], v174\n\tv_lshlrev_b32_e32 v174, 16, v175\n\tv_and_b32_e32 v175, %[hi], v175\n\t"
        ".Lb2_%=:\n\t"
        "s_bitcmp1_b32 %[fr], 3\n\ts_cbranch_scc0 .Lb3_%=\n\t"
        PGS "v_lshlrev_b32_e32 v176, 16, v184\n\tv_and_b32_e32 v177, %[hi], v184\n\tv_lshlrev_b32_e32 v178, 16, v185\n\tv_and_b32_e32 v179, %[hi], v185\n\t"
        PGS "v_lshlrev_b32_e32 v180, 16, v186\n\tv_and_b32_e32 v181, %[hi], v186\n\tv_lshlrev_b32_e32 v182, 16, v187\n\tv_and_b32_e32 v183, %[hi], v187\n\t"
        PGS "v_lshlrev_b32_e32 v184, 16, v188\n\tv_and_b32_e32 v185, %[hi], v188\n\tv_lshlrev_b32_e32 v186, 16, v189\n\tv_and_b32_e32 v187, %[hi], v189\n\t"
        PGS "v_lshlrev_b32_e32 v188, 16, v190\n\tv_and_b32_e32 v189, %[hi], v190\n\tv_lshlrev_b32_e32 v190, 16, v191\n\tv_and_b32_e32 v191, %[hi], v191\n\t"
        ".Lb3_%=:\n\t"
        "s_bitcmp1_b32 %[fr], 4\n\ts_cbranch_scc0 .Lb4_%=\n\t"
        PGS "v_lshlrev_b32_e32 v192, 16, v200\n\tv_and_b32_e32 v193, %[hi], v200\n\tv_lshlrev_b32_e32 v194, 16, v201\n\tv_and_b32_e32 v195, %[hi], v201\n\t"
        PGS "v_lshlrev_b32_e32 v196, 16, v202\n\tv_and_b32_e32 v197, %[hi], v202\n\tv_lshlrev_b32_e32 v198, 16, v203\n\tv_and_b32_e32 v199, %[hi], v203\n\t"
        PGS "v_lshlrev_b32_e32 v200, 16, v204\n\tv_and_b32_e32 v201, %[hi], v204\n\tv_lshlrev_b32_e32 v202, 16, v205\n\tv_and_b32_e32 v203, %[hi], v205\n\t"
        PGS "v_lshlrev_b32_e32 v204, 16, v206\n\tv_and_b32_e32 v205, %[hi], v206\n\tv_lshlrev_b32_e32 v206, 16, v207\n\tv_and_b32_e32 v207, %[hi], v207\n\t"
        ".Lb4_%=:\n\t"
        "s_bitcmp1_b32 %[fr], 5\n\ts_cbranch_scc0 .Lb5_%=\n\t"
        PGS "v_lshlrev_b32_e32 v208, 16, v216\n\tv_and_b32_e32 v209, %[hi], v216\n\tv_lshlrev_b32_e32 v210, 16, v217\n\tv_and_b32_e32 v211, %[hi], v217\n\t"
        PGS "v_lshlrev_b32_e32 v212, 16, v218\n\tv_and_b32_e32 v213, %[hi], v218\n\tv_lshlrev_b32_e32 v214, 16, v219\n\tv_and_b32_e32 v215, %[hi], v219\n\t"
        PGS "v_lshlrev_b32_e32 v216, 16, v220\n\tv_and_b32_e32 v217, %[hi], v220\n\tv_lshlrev_b32_e32 v218, 16, v221\n\tv_and_b32_e32 v219, %[hi], v221\n\t"
        PGS "v_lshlrev_b32_e32 v220, 16, v222\n\tv_and_b32_e32 v221, %[hi], v222\n\tv_lshlrev_b32_e32 v222, 16, v223\n\tv_and_b32_e32 v223, %[hi], v223\n\t"
        ".Lb5_%=:\n\t"
        "s_bitcmp1_b32 %[fr], 6\n\ts_cbranch_scc0 .Lb6_%=\n\t"
        PGS "v_lshlrev_b32_e32 v224, 16, v232\n\tv_and_b32_e32 v225, %[hi], v232\n\tv_lshlrev_b32_e32 v226, 16, v233\n\tv_and_b32_e32 v227, %[hi], v233\n\t"
        PGS "v_lshlrev_b32_e32 v228, 16, v234\n\tv_and_b32_e32 v229, %[hi], v234\n\tv_lshlrev_b32_e32 v230, 16, v235\n\tv_and_b32_e32 v231, %[hi], v235\n\t"
        PGS "v_lshlrev_b32_e32 v232, 16, v236\n\tv_and_b32_e32 v233, %[hi], v236\n\tv_lshlrev_b32_e32 v234, 16, v237\n\tv_and_b32_e32 v235, %[hi], v237\n\t"
        PGS "v_lshlrev_b32_e32 v236, 16, v238\n\tv_and_b32_e32 v237, %[hi], v238\n\tv_lshlrev_b32_e32 v238, 16, v239\n\tv_and_b32_e32 v239, %[hi], v239\n\t"
        ".Lb6_%=:\n\t"
        "s_bitcmp1_b32 %[fr], 7\n\ts_cbranch_scc0 .Lb7_%=\n\t"
        PGS "v_lshlrev_b32_e32 v240, 16, v248\n\tv_and_b32_e32 v241, %[hi], v248\n\tv_lshlrev_b32_e32 v242, 16, v249\n\tv_and_b32_e32 v243, %[hi], v249\n\t"
        PGS "v_lshlrev_b32_e32 v244, 16, v250\n\tv_and_b32_e32 v245, %[hi], v250\n\tv_lshlrev_b32_e32 v246, 16, v251\n\tv_and_b32_e32 v247, %[hi], v251\n\t"
        PGS "v_lshlrev_b32_e32 v248, 16, v252\n\tv_and_b32_e32 v249, %[hi], v252\n\tv_lshlrev_b32_e32 v250, 16, v253\n\tv_and_b32_e32 v251, %[hi], v253\n\t"
        PGS "v_lshlrev_b32_e32 v252, 16, v254\n\tv_and_b32_e32 v253, %[hi], v254\n\tv_lshlrev_b32_e32 v254, 16, v255\n\tv_and_b32_e32 v255, %[hi], v255\n\t"
        ".Lb7_%=:\n\t"
        "s_cmp_eq_u32 %[got], -1\n\ts_cbranch_scc0 .Lidle_%=\n"
        ".Ldone_%=:"
        : "=&{v[144:159]}"(f[1]), "=&{v[160:175]}"(f[2]), "=&{v[176:191]}"(f[3]), "=&{v[192:207]}"(f[4]), "=&{v[208:223]}"(f[5]), "=&{v[224:239]}"(f[6]), "=&{v[240:255]}"(f[7]),
          [fl] "+v"(fl), [f2] "=&v"(f2), [got] "+s"(got), [spin] "+s"(spin), [fr] "=&s"(fresh), [so] "=&s"(so)
        : [tau] "s"(tau), [l16] "v"(l16), [l8] "v"(l8), [lr] "v"(lr), [sp] "s"(pb.srd), [sh] "s"(srdH), [ro] "s"(xg * 73728u + w * 2048u), [tok] "s"(tok_off),
          [flg] "s"(flg_off + xg * 128u), [hi] "s"(0xffff0000u), [slp] "i"(isA ? 8 : 0)
        : "vcc", "scc", "memory");
#undef POLL
#undef SEEN
    const f32x16 t = ((f[0] + f[1]) + (f[2] + f[3])) + ((f[4] + f[5]) + (f[6] + f[7]));
#pragma unroll
    for (int s = 0; s < 2; ++s)
    {
      const u32x4 ov = natural8(u32x4{cvtpk(t[8 * s], t[8 * s + 1]), cvtpk(t[8 * s + 2], t[8 * s + 3]), cvtpk(t[8 * s + 4], t[8 * s + 5]), cvtpk(t[8 * s + 6], t[8 * s + 7])});
      __builtin_amdgcn_raw_buffer_store_b128(ov, mk_srd(out, 2097152u), oo + s * 64, 0, 17);
    }
    asm volatile("s_waitcnt vmcnt(2)" : "+v"(fl) :: "memory");
    if ((lane >> 1) == w && fl != toks) asm volatile("buffer_store_dword %0, %1, %2, %3 offen sc1" :: "v"(fl), "v"(fo), "s"(srdH), "s"(flg_off) : "memory");
  }
}

__global__ __launch_bounds__(256, 1) __attribute__((aligned(4096)))
void fused_attn(const char* __restrict__ Ax, const char* __restrict__ Ak, const char* __restrict__ Av, const char* __restrict__ Awq, const char* __restrict__ Awo,
                const char* __restrict__ Bx, const char* __restrict__ Bk, const char* __restrict__ Bv, const char* __restrict__ Bwq, const char* __restrict__ Bwo,
                char* __restrict__ part, char* __restrict__ out) {
  extern __shared__ __attribute__((aligned(16))) char lds[];
  const unsigned bid = blockIdx.x;
  unsigned long long tnow;
  {
    const unsigned xg = bid & 7u, b = (bid >> 3) & 15u;
    const char *px, *pq, *pc; unsigned left;
    asm volatile(PG_DEFS "s_load_dwordx2 %0, %5, %6\n\ts_load_dwordx2 %1, %5, 0x28\n\ts_memrealtime %2\n\ts_getpc_b64 %3\n.Lpc_%=:\n\ts_mov_b32 %4, .Lhot_end-.Lpc_%="
                 : "=&s"(pq), "=&s"(px), "=&s"(tnow), "=&s"(pc), "=&s"(left) : "s"(__builtin_amdgcn_kernarg_segment_ptr()), "s"(bid >= 128u ? 0x18u : 0x40u));
    unsigned tid = threadIdx.x;
    PINV(tid);
    const unsigned lane = tid & 63u, w = rfl(tid >> 6), zq = ((b & 3u) << 14) | ((b >> 2) << 8);
    unsigned q0 = q_soff(zq), q1 = q_soff(zq + 0x1300u), r01 = 65536u + w * 16384u, r2 = 131072u + w * 8192u, xs = (b & 3u) * 256u, xd = w * 4096u + (b & 3u) * 16384u;
    asm volatile("s_mov_b32 m0, %2\n\ts_nop 0\n\tbuffer_load_dword %0, %1, 0 offen lds"
                 :: "v"(bid >= 128u && tid - 192u < WARM ? (b * WARM + tid - 192u) * 128u : 0x80000000u), "s"(mk_srd(pc, left)), "s"(r01 + SLOT) : "memory");
    Src sQ, sX;
    mk_w<false>(sQ, (xg * 4u + w) * 256u, 8192u, lane);
    mk_rows<false>(sX, b * 65536u + w * 16384u, 1024u, lane);
#pragma unroll
    for (int i = 0; i < 4; ++i) PINV(sX.v[i]);
    PINS(xs); PINS(xd);
#pragma unroll
    for (int i = 0; i < 4; ++i) PINV(sQ.v[i]);
    PINS(q0); PINS(q1); PINS(r01); PINS(r2);
    GAP();
    asm volatile("s_waitcnt lgkmcnt(0)" : "+s"(pq), "+s"(px), "+s"(tnow));
    GAP();
    sQ.srd = mk_srd(pq, 4194304u); sX.srd = mk_srd(px, 1048576u);
    dma_unit<1>(sQ, q0, r2);
    dma_unit<W_NT>(sQ, q1, r01);
    asm volatile("s_cmp_ge_u32 %0, 0x80\n\ts_cbranch_scc1 .Lx_%=\n\ts_mov_b32 m0, %1\n\ts_nop 0\n\tbuffer_load_dwordx4 %2, %6, %7 offen lds\n\tbuffer_load_dwordx4 %3, %6, %7 offen offset:1024 lds\n\t"
                 "buffer_load_dwordx4 %4, %6, %7 offen offset:2048 lds\n\tbuffer_load_dwordx4 %5, %6, %7 offen offset:3072 lds\n.Lx_%=:"
                 :: "s"(bid), "s"(xd), "v"(sX.v[0]), "v"(sX.v[1]), "v"(sX.v[2]), "v"(sX.v[3]), "s"(sX.srd), "s"(xs) : "scc", "memory");
    dma_unit<1>(sQ, q0 + 128u, r2 + 4096u);
    dma_unit<W_NT>(sQ, q1 + 128u, r01 + 4096u);
    asm volatile("" :: "v"(lds));
  }
  if (bid < 128u) { body<false>(Bx, Bk, Bv, Bwq, Bwo, Bwo, tnow, part, out); asm volatile(PG_END ".Lhot_end:"); __builtin_amdgcn_endpgm(); }
  body<true>(Ax, Ak, Av, Awq, Awo, Bwo, tnow, part, out);
  asm volatile(PG_END ".rept %0\n\ts_nop 0\n.endr" :: "i"(ASHIFT));
}

extern "C" void kernel_launch(void* const* d_in, const int* in_sizes, int n_in, void* d_out, int out_size, void* d_ws, size_t ws_size, hipStream_t stream) {
  (void)in_sizes; (void)n_in; (void)out_size; (void)ws_size;
  (void)hipFuncSetAttribute((const void*)fused_attn, hipFuncAttributeMaxDynamicSharedMemorySize, (int)LDS_BYTES);
  fused_attn<<<256, 256, LDS_BYTES, stream>>>((const char*)d_in[0], (const char*)d_in[1], (const char*)d_in[2], (const char*)d_in[3], (const char*)d_in[4],
                                              (const char*)d_in[5], (const char*)d_in[6], (const char*)d_in[7], (const char*)d_in[8], (const char*)d_in[9], (char*)d_ws, (char*)d_out);
}
```

```cpp
#include <hip/hip_runtime.h>
#include <hip/hip_bf16.h>
#include <utility>
#include <type_traits>

typedef __bf16 bf16x8 __attribute__((ext_vector_type(8)));
typedef __bf16 bf16x4 __attribute__((ext_vector_type(4)));
typedef __bf16 bf16x2 __attribute__((ext_vector_type(2)));
typedef float f32x4 __attribute__((ext_vector_type(4)));
typedef float f32x2 __attribute__((ext_vector_type(2)));
typedef float f32x16 __attribute__((ext_vector_type(16)));
typedef unsigned u32x4 __attribute__((ext_vector_type(4)));
#define LAS __attribute__((address_space(3)))
#define DI __device__ __forceinline__
typedef __amdgpu_buffer_rsrc_t srd_t;
template <int... Is> using iseq = std::integer_sequence<int, Is...>;
template <int N> using make_iseq = std::make_integer_sequence<int, N>;

constexpr int KV_NT = 1, W_NT = 0, X_NT = 0;
constexpr int SK = 4, SV = 4, SP = 5;
constexpr unsigned EASY_T = 1350, EASY_L = 1000, EASY2_F = 2000, EASY2_T = 3100;
constexpr int EASY = 6, PACE_ATT = 1, EASY2 = 10;
constexpr unsigned WARM = 40;
constexpr int PGB = 9, AWSH = 36, BSHIFT = 0, ASHIFT = 0;

constexpr unsigned SLOT = 8192, LDS_BYTES = 20 * SLOT;
constexpr unsigned HOFF = 16u << 20;
constexpr float SC = 0.12751743082f;
constexpr float LMAX = 1048576.0f;

#define GAP() __builtin_amdgcn_sched_barrier(0)
#define PINS(x) asm volatile("" : "+s"(x))
#define PINV(x) asm volatile("" : "+v"(x))
DI unsigned rfl(unsigned x) { return __builtin_amdgcn_readfirstlane(x); }
DI unsigned cvtpk(float lo, float hi) { f32x2 v = {lo, hi}; bf16x2 b = __builtin_convertvector(v, bf16x2); return __builtin_bit_cast(unsigned, b); }
DI u32x4 pack2(const f32x4& a, const f32x4& b) { return u32x4{cvtpk(a[0], a[1]), cvtpk(a[2], a[3]), cvtpk(b[0], b[1]), cvtpk(b[2], b[3])}; }
DI u32x4 natural8(u32x4 w) {
  auto s0 = __builtin_amdgcn_permlane32_swap(w[0], w[2], false, false);
  auto s1 = __builtin_amdgcn_permlane32_swap(w[1], w[3], false, false);
  auto t0 = __builtin_amdgcn_permlane16_swap(s0[0], s0[1], false, false);
  auto t1 = __builtin_amdgcn_permlane16_swap(s1[0], s1[1], false, false);
  return u32x4{t0[0], t1[0], t0[1], t1[1]};
}
#define KSYM "_Z10fused_attnPKcS0_S0_S0_S0_S0_S0_S0_S0_S0_PcS1_"
#define PGL(c) ".if (((. - " KSYM ") >> 12) & 3) == " #c "\n777" #c ": s_cbranch_execz 777" #c "f\n.endif\n"
#define PG_DEFS ".set pgnext, 4096\n.macro pgstop\n.if (. - " KSYM ") >= pgnext\n.set pgnext, ((. - " KSYM ") | 4095) + 1\n" PGL(0) PGL(1) PGL(2) PGL(3) ".endif\n.endm\n" \
                ".macro mfp a:vararg\nv_mfma_f32_16x16x32_bf16 \\a\npgstop\n.endm\n"
#define PG_END "s_endpgm\n7770:\n7771:\n7772:\n7773:\n\ts_setpc_b64 vcc\n"
#define MF "mfp "
#define PGS "pgstop\n\t"
template <int SH = 0> DI void pg_walk(unsigned w) {
  asm volatile("s_mov_b64 exec, 0\n\ts_call_b64 vcc, .Lw1_%=\n\ts_branch .Lw2_%=\n.Lw1_%=:\n\ts_cmp_eq_u32 %0, 0\n\ts_cbranch_scc1 7770f\n\ts_cmp_eq_u32 %0, 1\n\ts_cbranch_scc1 7771f\n\t"
               "s_cmp_eq_u32 %0, 2\n\ts_cbranch_scc1 7772f\n\ts_branch 7773f\n.rept %1\n\ts_nop 0\n.endr\n.Lw2_%=:\n\ts_mov_b64 exec, -1" :: "s"(w), "i"(SH) : "vcc", "scc", "memory");
}
template <bool Z> DI void mm_vvv(f32x4& c, const bf16x8& a, const bf16x8& b) {
  if constexpr (Z) asm volatile(MF "%0, %1, %2, 0" : "=&v"(c) : "v"(a), "v"(b));
  else             asm volatile(MF "%0, %1, %2, %0" : "+v"(c) : "v"(a), "v"(b));
}
template <bool Z> DI void mm_vva(f32x4& c, const bf16x8& a, const bf16x8& b) {
  if constexpr (Z) asm volatile(MF "%0, %1, %2, 0" : "=&v"(c) : "v"(a), "a"(b));
  else             asm volatile(MF "%0, %1, %2, %0" : "+v"(c) : "v"(a), "a"(b));
}
DI void mm_vca(f32x4& d, const bf16x8& a, const bf16x8& b, const f32x4& c) { asm volatile(MF "%0, %1, %2, %3" : "=&v"(d) : "v"(a), "a"(b), "v"(c)); }
template <bool Z = false> DI void mm_avv(f32x4& c, const bf16x8& a, const bf16x8& b) {
  if constexpr (Z) asm volatile(MF "%0, %1, %2, 0" : "=a"(c) : "v"(a), "v"(b));
  else             asm volatile(MF "%0, %1, %2, %0" : "+a"(c) : "v"(a), "v"(b));
}
DI void set_m0(unsigned v) { asm volatile("s_mov_b32 m0, %0" :: "s"(v) : "memory"); }
template <int NT, int P> DI void dma_p(unsigned voff, srd_t srd, unsigned soff) {
  if constexpr (NT) asm volatile("buffer_load_dwordx4 %0, %1, %2 offen offset:%3 nt lds" :: "v"(voff), "s"(srd), "s"(soff), "i"(1024 * P) : "memory");
  else              asm volatile("buffer_load_dwordx4 %0, %1, %2 offen offset:%3 lds" :: "v"(voff), "s"(srd), "s"(soff), "i"(1024 * P) : "memory");
}
struct Src { unsigned v[4]; srd_t srd; };
template <int NT> DI void dma_unit(const Src& s, unsigned soff, unsigned ldsaddr) {
  asm volatile("s_mov_b32 m0, %0\n\ts_nop 0" :: "s"(ldsaddr) : "memory");
  dma_p<NT, 0>(s.v[0], s.srd, soff); dma_p<NT, 1>(s.v[1], s.srd, soff); dma_p<NT, 2>(s.v[2], s.srd, soff); dma_p<NT, 3>(s.v[3], s.srd, soff);
}
template <int NT> DI void dma_elem(const Src& s, unsigned soff, unsigned ustep, unsigned ldsaddr) { dma_unit<NT>(s, soff, ldsaddr); dma_unit<NT>(s, soff + ustep, ldsaddr + 4096u); }
template <int N> DI void wait_vm() { asm volatile("s_waitcnt vmcnt(%0)" :: "i"(N) : "memory"); }
template <int N, int L> DI void wait_vm_lgkm() { asm volatile("" ::: "memory"); __builtin_amdgcn_s_waitcnt((N & 15) | ((N >> 4) << 14) | 0x70 | (L << 8)); asm volatile("" ::: "memory"); }
DI void wait_lgkm0() { wait_vm_lgkm<63, 0>(); }
DI void barrier() { asm volatile("s_barrier" ::: "memory"); }
DI bf16x8 lds128(unsigned a) { return *(const LAS bf16x8*)(size_t)(a); }
DI bf16x4 lds_tr1(unsigned a) { return __builtin_amdgcn_ds_read_tr16_b64_v4bf16((LAS bf16x4*)(size_t)(a)); }
DI bf16x8 cat(bf16x4 lo, bf16x4 hi) { return __builtin_shufflevector(lo, hi, 0, 1, 2, 3, 4, 5, 6, 7); }
DI bf16x8 lds_tr(unsigned a0, unsigned a1) { return cat(lds_tr1(a0), lds_tr1(a1)); }
DI srd_t mk_srd(const void* base, unsigned bytes) { return __builtin_amdgcn_make_buffer_rsrc((void*)base, (short)0, (int)bytes, 0x00020000); }
template <bool VX> DI void mk_rows(Src& s, unsigned base, unsigned stride, unsigned lane) {
  const unsigned row = (lane >> 2) & 3u, cb = lane >> 4, sl = lane & 3u;
#pragma unroll
  for (int p = 0; p < 4; ++p) s.v[p] = base + row * stride + cb * 64u + ((sl ^ (VX ? 2 * (p & 1) : (p & 2))) << 4) + (4u * stride - 1024u) * p;
}
template <bool VX> DI void mk_w(Src& s, unsigned base, unsigned stride, unsigned lane) {
  const unsigned sub = lane >> 5, cb = (lane >> 4) & 1u, row = (lane >> 2) & 3u, sl = lane & 3u;
#pragma unroll
  for (int p = 0; p < 4; ++p) s.v[p] = base + (4u * sub + row) * stride + cb * 64u + ((sl ^ (VX ? 2u * sub : 2u * (p & 1))) << 4) + (8u * stride - 1024u) * p;
}
DI void touch(unsigned& d, unsigned& voff, const char* base) {
  asm volatile("global_load_dword %0, %1, %2 nt\n\tglobal_load_dword %0, %1, %2\n\tv_add_u32_e32 %1, 0x1000, %1" : "+v"(d), "+v"(voff) : "s"(base) : "memory");
}
struct Geo { int r0, r1, p0, p1, u, t0, t1, t2, t3; };
constexpr Geo GB = {8, 15, 16, 12, 11, 13, 14, 10, 9}, GA = {15, 8, 16, 9, 10, 14, 11, 12, 13};
constexpr unsigned KUS = 1u << GB.u;
template <bool isA> DI unsigned ksoffg(unsigned m, unsigned rk) {
  constexpr Geo G = isA ? GA : GB;
  const unsigned t = m + rk;
  return ((t & 1u) << G.t0) | (((t >> 1) & 1u) << G.t1) | (((t >> 2) & 1u) << G.t2) | (((t >> 3) & 1u) << G.t3);
}
DI unsigned ksoff(unsigned m, unsigned rk) { return ksoffg<false>(m, rk); }
template <bool isA, bool VX> DI void mk_src_kv(Src& s, const char* base, unsigned lane, unsigned w) {
  constexpr Geo G = isA ? GA : GB;
  s.srd = mk_srd(base, 131072u);
  const unsigned row = (lane >> 2) & 3u, sl = lane & 3u, o = ((row & 1u) << G.r0) + ((row >> 1) << G.r1) + (lane >> 4) * 64u;
#pragma unroll
  for (unsigned p = 0; p < (isA ? 2u : 4u); ++p)
    s.v[p] = o + ((sl ^ (VX ? 2u * (p & 1u) : isA ? 2u * (w & 1u) : (p & 2u))) << 4) + ((p & 1u) << G.p0) + ((p >> 1) << G.p1) - (isA ? 0u : 1024u * p);
}

struct PC {
  unsigned r01, r2;
  unsigned w01[2], w2[2];
  unsigned xb, xmask, xa;
  unsigned z, soff;
};
template <unsigned TS_, unsigned QS_, unsigned ZI_, unsigned ZM_, unsigned ZO_, unsigned US_, unsigned CS_, bool ROT_> struct PK {
  static constexpr unsigned TS = TS_, QS = QS_, ZI = ZI_, ZM = ZM_, ZO = ZO_, US = US_, CS = CS_; static constexpr bool ROT = ROT_; };
DI unsigned q_soff(unsigned z) { return ((z & 0xC300u) * 0x440u) & 0x3C0000u; }
template <int POS> DI unsigned ring_lds(const PC& c) { return POS == 2 ? c.r2 : c.r01 + POS * SLOT; }
template <int POS> DI unsigned ring_rd(const PC& c, int d) { return (POS == 2 ? c.w2[d & 1] : c.w01[d & 1] + POS * SLOT) + (d >> 1) * 256u; }
struct NoX { static constexpr bool ACT = true; template <int G> DI void run() const {} };
template <int SL, unsigned LEN> struct EasyX {
  static constexpr bool ACT = true;
  unsigned long long& t; unsigned from;
  template <int G> DI void run() const {
    if constexpr (G == 8) asm volatile("s_memrealtime %0" : "=s"(t));
    if constexpr (G == 16) PINS(t);
    if constexpr (G == 16) asm volatile("s_cmp_ge_u32 %0, %2\n\ts_cbranch_scc1 .Le_%=\n\ts_sleep %1\n.Le_%=:" :: "s"((unsigned)t - from), "i"(SL), "i"(LEN) : "scc");
  }
};
template <class K, int POS, int S, bool Z, bool RD, bool DM, int NT, bool XT, int M, class X>
DI void proj_gap(f32x4 (&acc)[8][4], const bf16x8 (&cur)[4], bf16x8 (&nxt)[4], bf16x4 (&lo)[4], bf16x8 (&bf)[4], PC& c, unsigned soff, const Src& src, const Src& xs, unsigned xsoff, const X& ex) {
  constexpr int qb = M >> 2, db = M & 3;
  mm_vvv<Z>(acc[4 * S + db][qb], cur[db], bf[qb]);
  if constexpr (RD && M < 8) {
    constexpr int d = M >> 1; const unsigned a = S == 0 ? ring_rd<POS>(c, d) + 4096u : ring_rd<(POS + 1) % 3>(c, d);
    if constexpr (M & 1) nxt[d] = cat(lo[d], lds_tr1(a + K::TS)); else lo[d] = lds_tr1(a);
  }
  if constexpr (X::ACT && RD && S == 1 && db == 3) bf[qb] = lds128(c.xa + qb * K::QS);
  if constexpr (XT) {
    if constexpr (M < 8 && !(M & 1)) dma_p<X_NT, M / 2>(xs.v[M / 2], xs.srd, xsoff);
    if constexpr (M == 7) set_m0(ring_lds<POS>(c) + S * 4096u);
    if constexpr (M >= 8 && !(M & 1)) dma_p<NT, (M - 8) / 2>(src.v[(M - 8) / 2], src.srd, soff);
  } else if constexpr (DM && M >= 8 && !(M & 1)) dma_p<NT, (M - 8) / 2>(src.v[(M - 8) / 2], src.srd, soff);
  if constexpr (X::ACT && S == 0 && M == 9) { PINS(c.z); c.z = (c.z + K::ZI) & K::ZM; PINS(c.z); }
  if constexpr (X::ACT && S == 0 && M == 11) { c.xa = (c.z & c.xmask) | c.xb; PINV(c.xa); }
  if constexpr (S == 1 && DM && !K::ROT && M == 9) { PINS(c.soff); c.soff = (c.soff + K::CS) & (16u * K::CS - 1u); PINS(c.soff); }
  if constexpr (S == 1 && DM && K::ROT && M == 9) { PINS(c.z); c.soff = (c.z + 0x3100u) & 0xC300u; PINS(c.soff); }
  if constexpr (S == 1 && DM && K::ROT && M == 13) { PINS(c.soff); c.soff = (c.soff * 0x440u) & 0x3C0000u; PINS(c.soff); }
  ex.template run<16 * S + M>();
  GAP();
}
template <class K, int POS, int S, bool Z, bool RD, bool DM, int NT, int VM, bool BAR, bool XT, class X, int... Ms>
DI void proj_unit(f32x4 (&acc)[8][4], const bf16x8 (&cur)[4], bf16x8 (&nxt)[4], bf16x8 (&bf)[4], PC& c, const Src& src, const Src& xs, unsigned xsoff, unsigned xdst, const X& ex, iseq<Ms...>) {
  wait_vm_lgkm<(RD ? VM : 63), (S == 0 ? 2 : 0)>();
  if constexpr (BAR) barrier();
  const unsigned soff = c.soff + S * K::US;
  if constexpr (XT) set_m0(xdst); else if constexpr (DM) set_m0(ring_lds<POS>(c) + S * 4096u);
  GAP();
  bf16x4 lo[4];
  (proj_gap<K, POS, S, Z, RD, DM, NT, XT, Ms>(acc, cur, nxt, lo, bf, c, soff, src, xs, xsoff, ex), ...);
}
template <class K, int POS, bool Z, bool LAST, bool DM, int NT, int VM0, int VM1, bool BAR1 = false, bool XT0 = false, bool XT1 = false, class X = NoX>
DI void proj_elem(f32x4 (&acc)[8][4], bf16x8 (&a0)[4], bf16x8 (&a1)[4], bf16x8 (&bf)[4], PC& c, const Src& src, const X& ex = X{},
                  const Src* xs = nullptr, unsigned xso0 = 0, unsigned xd0 = 0, unsigned xso1 = 0, unsigned xd1 = 0) {
  proj_unit<K, POS, 0, Z, true, DM, NT, VM0, false, XT0>(acc, a0, a1, bf, c, src, xs ? *xs : src, xso0, xd0, ex, make_iseq<16>{});
  proj_unit<K, POS, 1, Z, !LAST, DM, NT, VM1, BAR1, XT1>(acc, a1, a0, bf, c, src, xs ? *xs : src, xso1, xd1, ex, make_iseq<16>{});
}
template <class K> DI void proj_setup(PC& c, unsigned tb, unsigned xb, unsigned z0, unsigned s3) {
  c.w01[0] = c.r01 + tb; c.w01[1] = c.w01[0] ^ 32u; c.w2[0] = c.r2 + tb; c.w2[1] = c.w2[0] ^ 32u;
  PINV(c.w01[0]); PINV(c.w01[1]); PINV(c.w2[0]); PINV(c.w2[1]);
  c.xb = xb; c.xmask = K::ZO; PINV(c.xmask);
  c.z = z0; c.soff = s3;
}
template <class K, int POS> DI void proj_start(bf16x8 (&a0)[4], bf16x8 (&bf)[4], PC& c, unsigned tb, unsigned xb, unsigned z0, unsigned s3) {
  proj_setup<K>(c, tb, xb, z0, s3);
  c.xa = (c.z & K::ZO) | xb; PINV(c.xa);
#pragma unroll
  for (int d = 0; d < 4; ++d) { const unsigned a = ring_rd<POS>(c, d); a0[d] = lds_tr(a, a + K::TS); }
  GAP();
#pragma unroll
  for (int q = 0; q < 4; ++q) bf[q] = lds128(c.xa + q * K::QS);
  GAP();
}
#define ACC_ALL(C, a) C(a[0][0]), C(a[0][1]), C(a[0][2]), C(a[0][3]), C(a[1][0]), C(a[1][1]), C(a[1][2]), C(a[1][3]), C(a[2][0]), C(a[2][1]), C(a[2][2]), C(a[2][3]), C(a[3][0]), C(a[3][1]), C(a[3][2]), C(a[3][3])
DI void acc_fence_v(f32x4 (&acc)[8][4]) {
  asm volatile("s_nop 7\n\ts_nop 3" : ACC_ALL("+v", acc) :: "memory");
  f32x4 (&hi)[4][4] = *(f32x4 (*)[4][4])&acc[4];
  asm volatile("" : ACC_ALL("+v", hi) :: "memory");
  GAP();
}
DI void acc_fence_a(f32x4 (&acc)[8][4]) {
  asm volatile("s_nop 7\n\ts_nop 3" : ACC_ALL("+a", acc) :: "memory");
  f32x4 (&hi)[4][4] = *(f32x4 (*)[4][4])&acc[4];
  asm volatile("" : ACC_ALL("+a", hi) :: "memory");
  GAP();
}

struct Stash { u32x4 p[64]; unsigned base[3]; };
template <int I> DI void stash_load1(Stash& st, const Src& sK, const Src& sV, unsigned& rk, unsigned wo_, unsigned (&sm)[16]) {
  constexpr int m = I >> 2, isV = (I >> 1) & 1, pc_ = I & 1;
  if constexpr (!isV && !pc_) { PINS(rk); sm[m] = ksoffg<true>(m, rk) + wo_; }
  const unsigned so = sm[m];
  if constexpr (I < 56) asm volatile("buffer_load_dwordx4 %0, %1, %2, %3 offen nt" : "=a"(st.p[I]) : "v"(isV ? sV.v[pc_] : sK.v[pc_]), "s"(isV ? sV.srd : sK.srd), "s"(so) : "memory");
  else                  asm volatile("buffer_load_dwordx4 %0, %1, %2, %3 offen nt" : "=v"(st.p[I]) : "v"(isV ? sV.v[pc_] : sK.v[pc_]), "s"(isV ? sV.srd : sK.srd), "s"(so) : "memory");
}
constexpr int stash_idx(int j) { return 4 * ((j >> 1) & 15) + 2 * (j >> 5) + (j & 1); }
template <int... Is> DI void stash_load(Stash& st, const Src& sK, const Src& sV, unsigned rk, unsigned wo_, iseq<Is...>) { unsigned sm[16]; (stash_load1<stash_idx(Is)>(st, sK, sV, rk, wo_, sm), ...); }
constexpr int kv_pair(int m) { constexpr int t[10] = {0, 1, 2, 3, 8, 9, 4, 5, 6, 7}; return t[m % 10]; }
constexpr unsigned long long kv_tab() { unsigned long long v = 0; for (int i = 0; i < 16; ++i) v |= (unsigned long long)kv_pair(i) << (4 * i); return v; }
template <int I> DI void stash_write1(const Stash& st) {
  constexpr int m = I >> 2, isV = (I >> 1) & 1, pc_ = I & 1, off = kv_pair(m) * 16384 + isV * 8192 + pc_ * 1024;
  if constexpr (I < 56) asm volatile("ds_write_b128 %0, %1 offset:%2" :: "v"(st.base[off >> 16]), "a"(st.p[I]), "i"(off & 65535) : "memory");
  else                  asm volatile("ds_write_b128 %0, %1 offset:%2" :: "v"(st.base[off >> 16]), "v"(st.p[I]), "i"(off & 65535) : "memory");
}

struct AC {
  f32x4 o[8][4];
  bf16x8 q[4][4];
  bf16x8 kf[8], vf[8], pf[4];
  f32x4 negm[4];
  f32x4 l[4];
  bf16x8 ones;
  unsigned ra[2];
};
typedef f32x4 ST[2][4];
constexpr int VR = 32;
constexpr int NEB = 22;
template <int E> DI float sm_val(ST& S) { return S[(E >> 2) & 1][E >> 3][E & 3]; }
template <int E> DI void sm_exp1(ST& S) { float a; asm volatile("v_exp_f32_e32 %0, %1" : "=v"(a) : "v"(sm_val<E>(S))); S[(E >> 2) & 1][E >> 3][E & 3] = a; }
template <int QB, int P> DI void sm_cvt(ST& S, AC& c) {
  u32x4 w = __builtin_bit_cast(u32x4, c.pf[QB]);
  unsigned r;
  asm volatile("v_cvt_pk_bf16_f32 %0, %1, %2" : "=v"(r) : "v"(sm_val<8 * QB + 2 * P>(S)), "v"(sm_val<8 * QB + 2 * P + 1>(S)));
  w[P] = r;
  c.pf[QB] = __builtin_bit_cast(bf16x8, w);
}
constexpr bool wide(int nt) { return nt == W_NT; }
template <bool DM, int NT, int VM, int R, int M> DI void mem_pre(unsigned ldsaddr) {
  if constexpr (M == 6) wait_lgkm0();
  constexpr int F = wide(NT) ? 2 : 6, H = wide(NT) ? 18 : 14;
  if constexpr (DM && (M == F || M == H)) set_m0(ldsaddr + (M == H ? 4096u : 0u));
  if constexpr (M == R && VM < 63) wait_vm<VM>();
}
template <bool DM, int NT, int M> DI void mem_post(const Src& src, unsigned soff, unsigned ustep) {
  if constexpr (wide(NT)) { if constexpr (DM && M < 32 && (M & 3) == 2) dma_p<NT, (M >> 2) & 3>(src.v[(M >> 2) & 3], src.srd, soff + (M >= 16 ? ustep : 0u)); }
  else if constexpr (DM && M >= 6 && M <= 20 && !(M & 1)) dma_p<NT, ((M - 6) / 2) & 3>(src.v[((M - 6) / 2) & 3], src.srd, soff + (M >= 14 ? ustep : 0u));
}
template <int WR, int M> DI void mem_wr(const Stash& st) {
  if constexpr (WR >= 32 && M >= 7 && M < 19 && (M & 3) == 3) stash_write1<WR + (M - 7) / 4>(st);
  if constexpr (WR >= 0 && WR < 32 && M >= 8 && M < 16)
    asm volatile("ds_write_b128 %0, %1 offset:%2" :: "v"(st.base[0]), "a"(st.p[WR + M - 8]), "i"((M - 8) * 1024) : "memory");
}
template <int N> using IC = std::integral_constant<int, N>;
struct PA { unsigned rd0, rd1, soff, fill; };
template <bool QK, bool DM, int NT, int VM, int WR, int M, class F>
DI void gapA(AC& c, ST& S, ST& Sn, PA& pa, F& addr, const Src& src, unsigned ustep, const Stash& st, bf16x4& lo) {
  constexpr int R = QK ? 16 : 0;
  if constexpr ((DM || !QK) && M == 0) addr(pa, IC<0>{});
  if constexpr (QK) mem_pre<DM, NT, VM, 16, M>(pa.fill); else if constexpr (M == 0 && VM < 63) wait_vm<VM>();
  if constexpr (QK) {
    constexpr int f = M >> 2, s = f >> 1, kb = f & 1, qb = M & 3;
    if constexpr (s == 0) mm_vca(Sn[kb][qb], c.kf[f], c.q[qb][0], c.negm[qb]); else mm_vva<false>(Sn[kb][qb], c.kf[f], c.q[qb][s]);
  }
  mem_post<DM, NT, M>(src, pa.soff, ustep);
  mem_wr<WR, M>(st);
  if constexpr (QK && !DM && M >= 10 && M < 16) addr(pa, IC<M - 10>{});
  if constexpr (QK && M == 12) asm volatile("" :: "v"(c.negm[0]), "v"(c.negm[1]), "v"(c.negm[2]), "v"(c.negm[3]));
  if constexpr (M >= R && M < R + 16) {
    constexpr int db = (M - R) >> 1; const unsigned a = ((db & 1) ? pa.rd1 : pa.rd0) + (db >> 1) * 256u;
    if constexpr (M & 1) c.vf[db] = cat(lo, lds_tr1(a + 4096u)); else lo = lds_tr1(a);
  }
  if constexpr (NEB + M < 32) sm_exp1<NEB + M>(S);
  if constexpr (M >= 16 && (M & 1)) sm_cvt<2 + ((M - 16) >> 3), ((M - 16) >> 1) & 3>(S, c);
  GAP();
}
template <bool QK, bool DM, int NT, int VM, int WR, class F, int... Ms>
DI void phaseA_(AC& c, ST& S, ST& Sn, F& addr, const Src& src, unsigned ustep, const Stash& st, iseq<Ms...>) {
  PA pa; bf16x4 lo;
  (gapA<QK, DM, NT, VM, WR, Ms>(c, S, Sn, pa, addr, src, ustep, st, lo), ...);
}
template <bool QK, bool DM, int NT, int VM, int WR = -1, class F>
DI void phaseA(AC& c, ST& S, ST& Sn, F addr, const Src& src, unsigned ustep, const Stash& st) {
  phaseA_<QK, DM, NT, VM, WR>(c, S, Sn, addr, src, ustep, st, make_iseq<32>{});
}
template <bool MAX> DI float xlane(float v) {
  auto a = __builtin_amdgcn_permlane32_swap(__float_as_uint(v), __float_as_uint(v), false, false);
  v = MAX ? fmaxf(__uint_as_float(a[0]), __uint_as_float(a[1])) : __uint_as_float(a[0]) + __uint_as_float(a[1]);
  auto b = __builtin_amdgcn_permlane16_swap(__float_as_uint(v), __float_as_uint(v), false, false);
  return MAX ? fmaxf(__uint_as_float(b[0]), __uint_as_float(b[1])) : __uint_as_float(b[0]) + __uint_as_float(b[1]);
}
DI float row_max(const f32x4& x, const f32x4& y) {
  return xlane<true>(fmaxf(fmaxf(fmaxf(x[0], x[1]), fmaxf(x[2], x[3])), fmaxf(fmaxf(y[0], y[1]), fmaxf(y[2], y[3]))));
}
template <int SM, bool KR, bool DM, int NT, int VM, int WR, int M, class F>
DI void gapB(AC& c, ST& Sn, PA& pa, F& addr, const Src& src, unsigned ustep, const Stash& st, bf16x4& lo) {
  mem_pre<DM, NT, VM, (KR ? 18 : VR), M>(pa.fill);
  {
    constexpr int h = M / 18, r = M % 18, qb = 2 * h + (r & 1);
    if constexpr (r < 2) mm_vvv<SM == 3>(c.l[qb], c.ones, c.pf[qb]);
    else if constexpr (SM == 2 && r < 6) mm_vvv<false>(c.o[(r - 2) >> 1][qb], c.vf[(r - 2) >> 1], c.pf[qb]);
    else mm_avv<SM == 3>(c.o[(r - 2) >> 1][qb], c.vf[(r - 2) >> 1], c.pf[qb]);
  }
  mem_post<DM, NT, M>(src, pa.soff, ustep);
  mem_wr<WR, M>(st);
  if constexpr (M < 4) addr(pa, IC<M>{});
  if constexpr (KR && M >= 18 && M < 34 && !(M & 1)) { constexpr int f = (M - 18) >> 1; c.kf[f] = lds128(pa.rd0 + (f & 1) * 4096u + (f >> 1) * 256u); }
  constexpr int R = VM < 63 ? VR : 20;
  if constexpr (!KR && M >= R) {
    c.ra[0] = pa.rd0; c.ra[1] = pa.rd1;
    constexpr int db = (M - R) >> 1; const unsigned a = ((db & 1) ? pa.rd1 : pa.rd0) + (db >> 1) * 256u;
    if constexpr (M & 1) c.kf[db] = cat(lo, lds_tr1(a + 4096u)); else lo = lds_tr1(a);
  }
  if constexpr (SM) {
    if constexpr (M >= 4 && M < 18) sm_exp1<M - 4>(Sn);
    if constexpr (M >= 19 && M < 35 && (M & 1)) sm_exp1<14 + (M - 19) / 2>(Sn);
    if constexpr (M >= 20 && !(M & 1)) sm_cvt<((M - 20) >> 3), ((M - 20) >> 1) & 3>(Sn, c);
  }
  GAP();
}
template <int SM, bool KR, bool DM, int NT, int VM, int WR, class F, int... Ms>
DI void phaseB_(AC& c, ST& Sn, F& addr, const Src& src, unsigned ustep, const Stash& st, iseq<Ms...>) {
  PA pa; bf16x4 lo;
  (gapB<SM, KR, DM, NT, VM, WR, Ms>(c, Sn, pa, addr, src, ustep, st, lo), ...);
}
template <int SM, bool KR, bool DM, int NT, int VM, int WR = -1, class F>
DI void phaseB(AC& c, ST& Sn, F addr, const Src& src, unsigned ustep, const Stash& st) {
  phaseB_<SM, KR, DM, NT, VM, WR>(c, Sn, addr, src, ustep, st, make_iseq<36>{});
}
DI void k_read(AC& ac, unsigned ka) {
#pragma unroll
  for (int f = 0; f < 8; ++f) ac.kf[f] = lds128(ka + (f & 1) * 4096u + (f >> 1) * 256u);
}
constexpr int NW = 29;
template <bool DM, int M> DI void qk0_gap(AC& ac, ST& SA, const Src& src, unsigned soff, unsigned fill, const Stash& st) {
  constexpr int f = M >> 2, qb = M & 3;
  if constexpr (DM && (M == 0 || M == 16)) set_m0(fill + M * 256u);
  mm_vva<(f < 2)>(SA[f & 1][qb], ac.kf[f], ac.q[qb][f >> 1]);
  if constexpr (DM && qb == 0) dma_p<KV_NT, f & 3>(src.v[f & 3], src.srd, soff + (f >> 2) * KUS);
  if constexpr (!DM && qb == 0) stash_write1<NW + f>(st);
  GAP();
}
template <bool DM, int... Ms> DI void qk0(AC& ac, ST& SA, const Src& src, unsigned soff, unsigned fill, const Stash& st, iseq<Ms...>) { (qk0_gap<DM, Ms>(ac, SA, src, soff, fill, st), ...); }
template <int... Js> DI void exp_first(ST& SA, iseq<Js...>) { (sm_exp1<Js>(SA), ...); }
template <int... Js> DI void cvt_first(ST& SA, AC& c, iseq<Js...>) { (sm_cvt<(Js >> 2), (Js & 3)>(SA, c), ...); }
DI float max3(float a, float b, float c) { float r; asm("v_max3_f32 %0, %1, %2, %3" : "=v"(r) : "v"(a), "v"(b), "v"(c)); return r; }
DI float max2(float a, float b) { float r; asm("v_max_f32_e32 %0, %1, %2" : "=v"(r) : "v"(a), "v"(b)); return r; }
template <bool ZL> DI void sm_first(AC& ac, ST& SA) {
  asm volatile("s_nop 7\n\ts_nop 3" : "+v"(SA[0][0]), "+v"(SA[0][1]), "+v"(SA[0][2]), "+v"(SA[0][3]), "+v"(SA[1][0]), "+v"(SA[1][1]), "+v"(SA[1][2]), "+v"(SA[1][3]) :: "memory");
  GAP();
  float m[4], n[4];
#pragma unroll
  for (int q = 0; q < 4; ++q) { m[q] = max3(SA[0][q][0], SA[0][q][1], SA[0][q][2]); n[q] = max3(SA[1][q][0], SA[1][q][1], SA[1][q][2]); }
  GAP();
#pragma unroll
  for (int q = 0; q < 4; ++q) m[q] = max3(m[q], SA[0][q][3], SA[1][q][3]);
  GAP();
#pragma unroll
  for (int k = 0; k < 2; ++k) {
#pragma unroll
    for (int q = 0; q < 4; ++q) { const float a = m[q], b = n[q]; m[q] = max2(a, b); n[q] = max2(b, a); }
    GAP();
#pragma unroll
    for (int q = 0; q < 4; ++q) {
      auto a = k ? __builtin_amdgcn_permlane16_swap(__float_as_uint(m[q]), __float_as_uint(n[q]), false, false)
                 : __builtin_amdgcn_permlane32_swap(__float_as_uint(m[q]), __float_as_uint(n[q]), false, false);
      m[q] = __uint_as_float(a[0]); n[q] = __uint_as_float(a[1]);
    }
    GAP();
  }
#pragma unroll
  for (int q = 0; q < 4; ++q) m[q] = max2(m[q], n[q]);
  GAP();
#pragma unroll
  for (int q = 0; q < 4; ++q) {
    SA[0][q] -= m[q]; SA[1][q] -= m[q];
    ac.negm[q] = f32x4{-m[q], -m[q], -m[q], -m[q]};
    if constexpr (ZL) { ac.l[q] = f32x4{}; PINV(ac.l[q]); }
    ac.pf[q] = bf16x8{};
  }
  ac.ones = __builtin_bit_cast(bf16x8, u32x4{0x3f803f80u, 0x3f803f80u, 0x3f803f80u, 0x3f803f80u});
  asm volatile("" : "+v"(ac.negm[0]), "+v"(ac.negm[1]), "+v"(ac.negm[2]), "+v"(ac.negm[3]), "+v"(ac.ones));
  exp_first(SA, make_iseq<NEB>{});
  cvt_first(SA, ac, make_iseq<8>{});
  GAP();
}
struct BS { unsigned p[4], v[6], k[2], rk, wo, l16, w; };
constexpr bool b_dma(int f) { return f != 2 * (SK - 3) && f != 2 * (SV - 2) + 1 && f != 2 * (SP - 2) + 1 && f < 30 && f != 26; }
constexpr int b_vm(int f) {
  const int s = f == 29 ? 27 : f - 3;
  if (!b_dma(s)) return 63;
  int n = !b_dma(f) ? 0 : f == 28 ? 4 : f == 29 ? 8 : 5 + (f & 1);
  for (int i = s + 1; i < f; ++i) n += b_dma(i) ? 8 : 0;
  return n;
}
template <class KO, int I> DI void b_step(AC& ac, ST& S, ST& Sn, BS& b, const Src& sK, const Src& sV, const Src& sO, Stash& st) {
  constexpr int fa = 2 * I, fb = 2 * I + 1, vi = I == SP ? 4 : (I & 1) ? 0 : 2;
  const unsigned pa = b.p[(I & 1) ? 3 : 1], pb = b.p[(I & 1) ? 0 : 2];
  if constexpr (I == PGB) pg_walk<BSHIFT>(b.w);
  if constexpr (I >= 1 && I <= 13) barrier();
  if constexpr (I == SK - 3) st.base[0] = pa + b.l16;
  phaseA<true, b_dma(fa), (I < 14 ? KV_NT : W_NT), b_vm(fa), (I == SK - 3 ? 0 : -1)>(ac, S, Sn, [&](PA& p, auto k) __attribute__((always_inline)) {
    if (k.value) return;
    PINS(b.rk);
    p.soff = I < 14 ? ksoff(I < 13 ? I + 3 : 15, b.rk) : b.wo; p.fill = I == 14 ? b.p[0] : pa;
    p.rd0 = b.v[vi] + (I == SP ? SLOT : 0u); p.rd1 = b.v[vi + 1] + (I == SP ? SLOT : 0u); }, I < 13 ? sK : I < 14 ? sV : sO, I < 14 ? KUS : KO::US, st);
  if constexpr (I == SV - 2) st.base[0] = pb + b.l16;
  phaseB<2, (I < 14), b_dma(fb), (I < 14 ? KV_NT : W_NT), b_vm(fb), (I == SV - 2 ? 8 : -1)>(ac, Sn, [&](PA& p, auto k) __attribute__((always_inline)) {
      if (k.value) return;
      PINS(b.rk);
      p.soff = I < 14 ? ksoff(I + 2, b.rk) : b.wo + KO::CS; p.fill = I == 14 ? b.p[1] : I == 13 ? pa : pb; p.rd0 = I == 14 ? b.v[4] : b.k[(I & 1) ? 0 : 1]; p.rd1 = b.v[5]; }, I < 14 ? sV : sO, I < 14 ? KUS : KO::US, st);
}
template <int CH, int Q, int J, bool V = false> DI void o_word(const AC& c, const float (&inv)[5], bf16x8 (&nb)[4]) {
  u32x4 w = __builtin_bit_cast(u32x4, nb[Q]);
  unsigned r;
  if constexpr (V)
    asm volatile("v_mul_f32_e32 %0, %1, %3\n\tv_mul_f32_e32 %4, %2, %3\n\tv_cvt_pk_bf16_f32 %0, %0, %4"
                 : "=&v"(r) : "v"(c.o[2 * CH + (J >> 1)][Q][2 * (J & 1)]), "v"(c.o[2 * CH + (J >> 1)][Q][2 * (J & 1) + 1]), "v"(inv[Q]), "v"(inv[4]));
  else
  asm volatile("v_accvgpr_read_b32 %0, %1\n\tv_accvgpr_read_b32 %4, %2\n\tv_mul_f32_e32 %0, %0, %3\n\tv_mul_f32_e32 %4, %4, %3\n\tv_cvt_pk_bf16_f32 %0, %0, %4"
               : "=&v"(r) : "a"(c.o[2 * CH + (J >> 1)][Q][2 * (J & 1)]), "a"(c.o[2 * CH + (J >> 1)][Q][2 * (J & 1) + 1]), "v"(inv[Q]), "v"(inv[4]));
  w[J] = r;
  nb[Q] = __builtin_bit_cast(bf16x8, w);
}
template <int CH, int Q> DI void o_put(const bf16x8 (&nb)[4], unsigned ow) { asm volatile("ds_write_b128 %0, %1 offset:%2" :: "v"(ow), "v"(nb[Q]), "i"(CH * 4096 + Q * 1024) : "memory"); }
template <int E> struct CvX {
  static constexpr bool ACT = E == 3;
  const AC& c; const float (&inv)[5]; const bf16x8 (&cur)[4]; bf16x8 (&nb)[4]; unsigned ow;
  template <int G> DI void run() const {
    if constexpr (E < 3 && (G & 1)) o_word<E + 1, (G >> 3), (G >> 1) & 3>(c, inv, nb);
    if constexpr (G == 8 || G == 10) o_put<E, 2 + ((G >> 1) & 1)>(cur, ow);
    if constexpr (E < 3 && (G == 24 || G == 26)) o_put<E + 1, ((G >> 1) & 1)>(nb, ow);
  }
};
template <class K, int VM, int DM, int M> DI void gapE(AC& c, ST& S, float (&inv)[5], bf16x8 (&nb)[4], bf16x8 (&a0)[4], bf16x4& lo, const PC& pc, const Src& src, unsigned soff, unsigned long long& bad, unsigned ow) {
  constexpr int h = M / 18, r = M % 18, qb = 2 * h + (r & 1);
  if constexpr (M == 24) wait_vm<VM>();
  constexpr int D = M - (DM == 2 ? 4 : 0);
  constexpr int NR = DM == 2 ? 0 : VR - 20;
  if constexpr (M == NR + 4) wait_lgkm0();
  if constexpr (M == 6 && DM == 2) barrier();
  if constexpr (DM && (D == 3 || D == 19)) set_m0(pc.r2 + (D == 19 ? 4096u : 0u));
  if constexpr (r < 2) mm_vvv<false>(c.l[qb], c.ones, c.pf[qb]);
  else if constexpr (DM == 1 && r < 6) mm_vvv<false>(c.o[(r - 2) >> 1][qb], c.kf[(r - 2) >> 1], c.pf[qb]);
  else mm_avv(c.o[(r - 2) >> 1][qb], c.kf[(r - 2) >> 1], c.pf[qb]);
  if constexpr (DM && D > 0 && D < 32 && (D & 3) == 3) dma_p<W_NT, (D >> 2) & 3>(src.v[(D >> 2) & 3], src.srd, soff + (D >= 16 ? K::US : 0u));
  if constexpr (M < NR) {
    constexpr int db = (M + 16 - NR) >> 1; const unsigned a = c.ra[db & 1] + (db >> 1) * 256u;
    if constexpr (M & 1) c.kf[db] = cat(lo, lds_tr1(a + 4096u)); else lo = lds_tr1(a);
  }
  if constexpr (M >= 24 && M < 32) {
    constexpr int d = (M - 24) >> 1; const unsigned a = ring_rd<0>(pc, d);
    if constexpr (M & 1) a0[d] = cat(lo, lds_tr1(a + K::TS)); else lo = lds_tr1(a);
  }
  if constexpr (NEB + M < 32) sm_exp1<NEB + M>(S);
  if constexpr (M >= 10 && M < 14) { sm_cvt<2 + ((M - 10) >> 1), 2 * (M & 1)>(S, c); sm_cvt<2 + ((M - 10) >> 1), 2 * (M & 1) + 1>(S, c); }
  if constexpr (M == 14 || M == 15 || M == 22 || M == 23) asm volatile("v_rcp_f32_e32 %0, %1" : "=v"(inv[qb]) : "v"(c.l[qb][0]));
  if constexpr (M >= 16 && M < 22) o_word<0, ((M - 16) >> 2), (M & 3), (DM == 1)>(c, inv, nb);
  if constexpr (M >= 24 && M < 34) o_word<0, ((M - 18) >> 2), ((M - 18) & 3), (DM == 1)>(c, inv, nb);
  if constexpr (M == 32) o_put<0, 0>(nb, ow);
  if constexpr (M == 33) o_put<0, 1>(nb, ow);
  if constexpr (r == 4) asm volatile("" :: "v"(c.l[2 * h]), "v"(c.l[2 * h + 1]));
  if constexpr (M == 34)
    asm volatile("v_add_f32_e32 %1, %2, %3\n\tv_add_f32_e32 %1, %1, %4\n\tv_add_f32_e32 %1, %1, %5\n\tv_cmp_nle_f32_e64 %0, %1, %6"
                 : "=s"(bad) : "v"(inv[4]), "v"(c.l[0][0]), "v"(c.l[1][0]), "v"(c.l[2][0]), "v"(c.l[3][0]), "s"(LMAX));
  GAP();
}
template <class K, int VM, int DM, int... Ms> DI void phaseE(AC& c, ST& S, float (&inv)[5], bf16x8 (&nb)[4], bf16x8 (&a0)[4], const PC& pc, const Src& src, unsigned soff, unsigned long long& bad, unsigned ow, iseq<Ms...>) {
  bf16x4 lo;
#pragma unroll
  for (int q = 0; q < 4; ++q) nb[q] = bf16x8{};
  (gapE<K, VM, DM, Ms>(c, S, inv, nb, a0, lo, pc, src, soff, bad, ow), ...);
}
struct Pub { u32x4 pk[16]; srd_t srd; unsigned v[4], so; };
struct TL { bf16x8 w[2][3][4], x[2][3][4]; bf16x4 lo[2]; unsigned xa[3], xs[3], drop[2], l16; };
template <int S, int QB, int J> DI void pub_word(const f32x4 (&acc)[8][4], Pub& p) {
  const f32x4& a = acc[4 * S + (J >> 1)][QB];
  unsigned r;
  asm volatile("v_cvt_pk_bf16_f32 %0, %1, %2" : "=v"(r) : "v"(a[2 * (J & 1)]), "v"(a[2 * (J & 1) + 1]));
  p.pk[8 * S + 2 * QB + (J >> 2)][J & 3] = r;
}
template <int F> DI void pub_store(const Pub& p) {
  asm volatile("buffer_store_dwordx4 %0, %1, %2, %3 offen offset:%4 sc0 sc1" :: "v"(p.pk[F]), "v"(p.v[F >> 2]), "s"(p.srd), "s"(p.so), "i"((F & 3) * 1024) : "memory");
}
template <class K, int S, int E, int R> DI void tl_w(TL& t, const PC& c) {
  const unsigned a = ring_rd<(E + 1) % 3>(c, R >> 1) + S * 4096u;
  if constexpr (R & 1) t.w[S][E][R >> 1] = cat(t.lo[S], lds_tr1(a + K::TS)); else t.lo[S] = lds_tr1(a);
}
template <class K, int G> DI void tl_gap(f32x4 (&acc)[8][4], TL& t, const PC& c, Pub& p) {
  constexpr int S = G >= 48, qb = G < 16 ? G >> 2 : G < 48 ? (G - 16) >> 3 : (G - 48) / 12, e = G < 16 ? 0 : G < 48 ? 1 + ((G >> 2) & 1) : G % 3, db = G < 48 ? G & 3 : (G % 12) / 3;
  if constexpr (G == 10) wait_vm<4>();
  if constexpr (G == 27) wait_vm<0>();
  mm_vvv<false>(acc[4 * S + db][qb], t.w[S][e][db], t.x[S][e][qb]);
  if constexpr (G >= 28 && G <= 56 && !(G & 3)) pub_store<(G - 28) / 4>(p);
  if constexpr (G >= 59 && G % 6 == 5) pub_store<8 + (G - 59) / 6>(p);
  if constexpr (G < 8) tl_w<K, 0, 1, G>(t, c);
  if constexpr (G >= 10 && G < 18) tl_w<K, 0, 2, G - 10>(t, c);
  if constexpr (G >= 16 && G < 24) tl_w<K, 1, 0, G - 16>(t, c);
  if constexpr (G >= 25 && G < 56 && (G & 1)) tl_w<K, 1, 1 + (((G - 25) >> 2) & 1), 2 * ((G - 25) >> 3) + (((G - 25) >> 1) & 1)>(t, c);
  if constexpr (G < 34 && (G & 3) == 2) t.x[0][1 + ((G >> 2) & 1)][G >> 3] = lds128(t.xa[1 + ((G >> 2) & 1)] + (G >> 3) * K::QS);
  if constexpr (G >= 34 && G < 82 && (G & 3) == 2) t.x[1][((G - 34) % 12) >> 2][(G - 34) / 12] = lds128(t.xa[((G - 34) % 12) >> 2] + ((G - 34) / 12) * K::QS);
  if constexpr (G >= 24 && G < 56) pub_word<0, ((G - 24) >> 3), (G & 7)>(acc, p);
  if constexpr (G >= 54 && G % 3 < 2) pub_word<1, (G - 54) / 12, 2 * (((G - 54) % 12) / 3) + G % 3>(acc, p);
  if constexpr (G == 0 || G == 1 || G == 3) { t.xa[(G + 1) & 3] = t.xs[(G + 1) & 3] | t.l16; PINV(t.xa[(G + 1) & 3]); }
  if constexpr (G == 4 || G == 5) { p.v[2 * (G & 1)] = t.drop[G & 1] | t.l16; PINV(p.v[2 * (G & 1)]); }
  if constexpr (G == 7 || G == 8) { p.v[2 * (G & 1) + 1] = p.v[2 * (G & 1)] | 4096u; PINV(p.v[2 * (G & 1) + 1]); }
  GAP();
}
template <class K, int... Gs> DI void tl_run(f32x4 (&acc)[8][4], TL& t, const PC& c, Pub& p, iseq<Gs...>) {
  wait_vm_lgkm<12, 2>();
  GAP();
  (tl_gap<K, Gs>(acc, t, c, p), ...);
  pub_word<1, 3, 4>(acc, p); pub_word<1, 3, 5>(acc, p);
  asm volatile("s_nop 4");
  pub_word<1, 3, 6>(acc, p); pub_word<1, 3, 7>(acc, p); pub_store<15>(p);
}
template <int I> DI void own_put1(const u32x4& v, unsigned a) { asm volatile("ds_write_b128 %0, %1 offset:%2 ; %3" :: "v"(a), "v"(v), "i"((I & 7) * 1024), "i"(I) : "memory"); }
template <int B, int... Is> DI void own_put(const Pub& p, unsigned a, iseq<Is...>) { (own_put1<B + Is>(p.pk[B + Is], a), ...); }
DI void redo_head(const AC& ac, const char* __restrict__ Kp, const char* __restrict__ Vp, unsigned lane, unsigned ow) {
  const unsigned r16 = lane & 15u, g = lane >> 4;
#pragma unroll 1
  for (unsigned qb = 0; qb < 4; ++qb) {
    bf16x8 qq[4];
#pragma unroll
    for (int s = 0; s < 4; ++s) qq[s] = qb == 0 ? ac.q[0][s] : qb == 1 ? ac.q[1][s] : qb == 2 ? ac.q[2][s] : ac.q[3][s];
    f32x4 o[8];
#pragma unroll
    for (int d = 0; d < 8; ++d) o[d] = f32x4{};
    float m = -__builtin_inff(), l = 0.f;
#pragma unroll 1
    for (unsigned t = 0; t < 16; ++t) {
      f32x4 s0 = {}, s1 = {};
      const char* kp = Kp + (t * 32u + r16) * 256u + g * 16u;
#pragma unroll
      for (int s = 0; s < 4; ++s) {
        s0 = __builtin_amdgcn_mfma_f32_16x16x32_bf16(*(const bf16x8*)(kp + s * 64), qq[s], s0, 0, 0, 0);
        s1 = __builtin_amdgcn_mfma_f32_16x16x32_bf16(*(const bf16x8*)(kp + s * 64 + 4096), qq[s], s1, 0, 0, 0);
      }
      const float mn = fmaxf(m, row_max(s0, s1)), al = __builtin_amdgcn_exp2f(m - mn);
      m = mn;
#pragma unroll
      for (int j = 0; j < 4; ++j) { s0[j] = __builtin_amdgcn_exp2f(s0[j] - mn); s1[j] = __builtin_amdgcn_exp2f(s1[j] - mn); }
      l = l * al + ((s0[0] + s0[1]) + (s0[2] + s0[3])) + ((s1[0] + s1[1]) + (s1[2] + s1[3]));
      const bf16x8 pf = __builtin_bit_cast(bf16x8, pack2(s0, s1));
      const __bf16* vp = (const __bf16*)(Vp + (t * 32u + g * 4u) * 256u) + r16;
#pragma unroll
      for (int d = 0; d < 8; ++d) {
        bf16x8 vf;
#pragma unroll
        for (int j = 0; j < 8; ++j) vf[j] = vp[((j & 3) + (j >> 2) * 16) * 128 + d * 16];
        o[d] = __builtin_amdgcn_mfma_f32_16x16x32_bf16(vf, pf, o[d] * al, 0, 0, 0);
      }
    }
    const float inv = 1.0f / xlane<false>(l);
#pragma unroll
    for (int s = 0; s < 4; ++s) *(LAS u32x4*)(size_t)(ow + s * 4096u + qb * 1024u) = pack2(o[2 * s] * inv, o[2 * s + 1] * inv);
  }
}
template <bool isA, int J> DI void q_conv1(AC& ac, f32x4 (&acc)[8][4], Stash& st, const Src& sK, const Src& sV, unsigned rk, unsigned p3, u32x4 (&t)[2]) {
  constexpr int s = J >> 2, qb = J & 3;
  if constexpr (isA) { if constexpr (2 * J < NW) stash_write1<2 * J>(st); }
  else {
    constexpr int p = 2 * (J & 1);
    const Src& src = s < 2 ? sK : sV;
    const unsigned so = ksoff(s == 0 ? 2 : s == 1 ? SK : s == 2 ? SV : SP, rk) + ((J >> 1) & 1) * KUS;
    if constexpr (s == 0 || s == 3) {
      if constexpr (p == 0) asm volatile("s_mov_b32 m0, %0\n\ts_nop 0" :: "s"(p3 + (s == 3 ? SLOT : 0u) + (J & 2) * 2048u) : "memory");
      dma_p<KV_NT, p>(src.v[p], src.srd, so); dma_p<KV_NT, p + 1>(src.v[p + 1], src.srd, so);
    } else {
      asm volatile("buffer_load_dwordx4 %0, %1, %2, %3 offen offset:%4 nt" : "=a"(st.p[2 * J - 8]) : "v"(src.v[p]), "s"(src.srd), "s"(so), "i"(1024 * p) : "memory");
      asm volatile("buffer_load_dwordx4 %0, %1, %2, %3 offen offset:%4 nt" : "=a"(st.p[2 * J - 7]) : "v"(src.v[p + 1]), "s"(src.srd), "s"(so), "i"(1024 * p + 1024) : "memory");
    }
  }
  t[J & 1] = pack2(acc[2 * s][qb] * SC, acc[2 * s + 1][qb] * SC);
  PINV(t[J & 1]);
  GAP();
  if constexpr (J & 1) {
#pragma unroll
    for (int k = 0; k < 2; ++k) {
#pragma unroll
      for (int i = 0; i < 4; ++i) {
        u32x4& w = t[i >> 1];
        auto a = k ? __builtin_amdgcn_permlane16_swap(w[i & 1], w[2 + (i & 1)], false, false) : __builtin_amdgcn_permlane32_swap(w[i & 1], w[2 + (i & 1)], false, false);
        w[i & 1] = a[0]; w[2 + (i & 1)] = a[1];
      }
      if constexpr (isA) { if (k == 0) { if constexpr (2 * J - 1 < NW) stash_write1<2 * J - 1>(st); } else { if constexpr (2 * J + 1 < NW) stash_write1<2 * J + 1>(st); } }
      GAP();
    }
    ac.q[qb - 1][s] = __builtin_bit_cast(bf16x8, t[0]); ac.q[qb][s] = __builtin_bit_cast(bf16x8, t[1]);
    asm volatile("" : "+a"(ac.q[qb - 1][s]), "+a"(ac.q[qb][s]));
    GAP();
  }
}
template <bool isA, int... Js> DI void q_conv(AC& ac, f32x4 (&acc)[8][4], Stash& st, const Src& sK, const Src& sV, unsigned rk, unsigned p3, iseq<Js...>) { u32x4 t[2]; (q_conv1<isA, Js>(ac, acc, st, sK, sV, rk, p3, t), ...); }
template <int N, int N0 = 0> DI void zero_o(AC& ac) {
#pragma unroll
  for (int i = N0; i < N; ++i)
#pragma unroll
    for (int q = 0; q < 4; ++q) { ac.o[i][q] = f32x4{}; asm volatile("" : "+a"(ac.o[i][q])); }
}

template <bool isA>
DI void body(const char* __restrict__ X, const char* __restrict__ K, const char* __restrict__ V, const char* __restrict__ Wq, const char* __restrict__ Wo,
             const char* __restrict__ Wo2, unsigned long long tnow, char* __restrict__ part, char* __restrict__ out) {
  const unsigned tlim = (unsigned)tnow + EASY_T;
  unsigned tid = threadIdx.x;
  PINV(tid);
  const unsigned lane = tid & 63u, w = rfl(tid >> 6);
  const unsigned bid = blockIdx.x, xg = bid & 7u, rr = bid >> 3, b = rr & 15u, h = xg * 4u + w;
  const unsigned r16 = lane & 15u, g = lane >> 4;
  const size_t kvo = isA ? (size_t)(b * 8u + xg) * 131072u : (size_t)(b * 32u + h) * 131072u;
  const unsigned rk = isA ? 7u * b : 5u * b + 8u * w;
  typedef PK<512u, 4096u, 0x1300u, 0xF300u, 0xC300u, 128u, 32u * 8192u, true> KQ;
  typedef PK<512u, 4096u, 0x1300u, 0xF300u, 0xC300u, KUS, 8192u, false> KT;
  typedef PK<2048u, 1024u, 0x1000u, 0xF000u, 0xF000u, 128u, 32u * 1024u, false> KO;
  PC pc;
  pc.r01 = 65536u + w * 16384u; pc.r2 = 131072u + w * 8192u;
  const unsigned zq = ((b & 3u) << 14) | ((b >> 2) << 8), co = 4u * w;
  const srd_t srdH = mk_srd(part + HOFF, 4096u + 256u * 128u);
  const unsigned tok_off = rr * 128u, flg_off = 4096u + rr * 1024u;
  const unsigned l8 = (lane & 7u) * 4u;
  unsigned entry_flags, dum0 = lane;
  Src sQ, sX, sK, sV, sO;
  const unsigned p0 = b & 3u, p1 = (p0 + 1u) & 3u, p2 = (p0 + 2u) & 3u, p3 = (p0 + 3u) & 3u, xw = w * 4096u;
  Stash st;
  mk_src_kv<isA, false>(sK, K + kvo, lane, w);
  mk_src_kv<isA, true>(sV, V + kvo, lane, w);
  if constexpr (isA) { stash_load(st, sK, sV, rk, ((w & 1u) << GA.p1) + ((w >> 1) << GA.u), make_iseq<64>{}); GAP(); }
  asm volatile("buffer_load_dword %0, %1, %2, %3 offen sc1" : "=v"(entry_flags) : "v"(l8), "s"(srdH), "s"(flg_off + xg * 128u) : "memory");
  sQ.srd = mk_srd(Wq, 4194304u); sX.srd = mk_srd(X, 1048576u);
  mk_w<false>(sQ, h * 256u, 8192u, lane);
  mk_rows<false>(sX, b * 65536u + w * 16384u, 1024u, lane);
  sO.srd = mk_srd(Wo + (size_t)xg * 524288u, 524288u);
  mk_w<true>(sO, w * 256u, 1024u, lane);
  if constexpr (isA) {
    dma_unit<X_NT>(sX, p0 * 256u, xw + p0 * 16384u);
    dma_elem<W_NT>(sQ, q_soff(zq + 0x2200u), KQ::US, pc.r01 + SLOT);
    dma_unit<X_NT>(sX, p1 * 256u, xw + p1 * 16384u);
    dma_unit<X_NT>(sX, p2 * 256u, xw + p2 * 16384u);
    dma_unit<X_NT>(sX, p3 * 256u, xw + p3 * 16384u);
  }
  srd_t pb_srd = mk_srd(part + (size_t)rr * 524288u, 524288u);
  unsigned pb_so = xg * 65536u + w * 16384u, drop0 = 2u * w == xg ? 0x80000000u : 0u, drop1 = 2u * w + 1u == xg ? 0x80002000u : 0x2000u;
  unsigned xs0 = ((co + 13u) & 15u) << 12, xs1 = ((co + 14u) & 15u) << 12, xs2 = ((co + 15u) & 15u) << 12, oz = (co + 3u) << 12, os3 = (co + 3u) * KO::CS;
  unsigned ks0 = ksoff(0u, rk), ks1 = ksoff(1u, rk);
  {
    PINS(pb_srd); PINS(pb_so); PINS(drop0); PINS(drop1); PINS(xs0); PINS(xs1); PINS(xs2); PINS(oz); PINS(os3); PINS(sO.srd);
    if constexpr (!isA) {
      PINS(sK.srd); PINS(sV.srd); PINS(ks0); PINS(ks1);
#pragma unroll
      for (int i = 0; i < 4; ++i) { PINV(sK.v[i]); PINV(sV.v[i]); PINV(sO.v[i]); }
    }
  }
  const unsigned rb = 1024u * (r16 >> 2) + 64u * (r16 & 3u) + 16u * (g ^ ((r16 >> 2) & 2u));
  const unsigned tb = 64u * ((lane >> 2) & 3u) + 32u * (g & 1u) + 8u * (lane & 3u);
  const unsigned tbV = tb + 1024u * g;
  const unsigned sb0 = w * 2048u + lane * 16u;
  st.base[0] = sb0; st.base[1] = sb0 + 65536u; st.base[2] = sb0 + 131072u;
  AC ac;
  if constexpr (!isA) zero_o<8, 2>(ac);
  {
    f32x4 acc[8][4];
    bf16x8 a0[4], a1[4], bf[4];
    if constexpr (isA) pg_walk<AWSH>(w);
    wait_vm<(isA ? 20 : 9)>();
    barrier();
    proj_start<KQ, 2>(a0, bf, pc, tbV, rb, zq, q_soff(zq + 0x3100u));
    if constexpr (isA) {
      proj_elem<KQ, 2, true,  false, true, W_NT, 63, 63>(acc, a0, a1, bf, pc, sQ);
      proj_elem<KQ, 0, false, false, true, W_NT, 63, 28>(acc, a0, a1, bf, pc, sQ);
      proj_elem<KQ, 1, false, false, true, W_NT, 28, 16>(acc, a0, a1, bf, pc, sQ);
      proj_elem<KQ, 2, false, false, true, W_NT, 16, 16, true>(acc, a0, a1, bf, pc, sQ);
    } else {
      dma_elem<W_NT>(sQ, q_soff(zq + 0x2200u), KQ::US, pc.r01 + SLOT);
      proj_elem<KQ, 2, true,  false, true, W_NT, 13, 20, false, true, true>(acc, a0, a1, bf, pc, sQ, NoX{}, &sX, p1 * 256u, xw + p1 * 16384u, p2 * 256u, xw + p2 * 16384u);
      proj_elem<KQ, 0, false, false, true, W_NT, 24, 28, false, true, false>(acc, a0, a1, bf, pc, sQ, NoX{}, &sX, p3 * 256u, xw + p3 * 16384u);
      proj_elem<KQ, 1, false, false, true, W_NT, 28, 24>(acc, a0, a1, bf, pc, sQ);
      proj_elem<KQ, 2, false, false, true, W_NT, 20, 16, true>(acc, a0, a1, bf, pc, sQ);
    }
    typedef std::conditional_t<isA, EasyX<EASY, EASY_L>, NoX> PX;
    const PX px = [&] { if constexpr (isA) return EasyX<EASY, EASY_L>{tnow, tlim}; else return NoX{}; }();
#pragma unroll 1
    for (unsigned e = 0; e < 3; ++e) {
      proj_elem<KQ, 0, false, false, true, W_NT, 16, 16>(acc, a0, a1, bf, pc, sQ, px);
      proj_elem<KQ, 1, false, false, true, W_NT, 16, 16>(acc, a0, a1, bf, pc, sQ, px);
      proj_elem<KQ, 2, false, false, true, W_NT, 16, 16>(acc, a0, a1, bf, pc, sQ, px);
    }
    if constexpr (isA) {
      proj_elem<KQ, 0, false, false, false, W_NT, 16, 12>(acc, a0, a1, bf, pc, sQ, px);
      proj_elem<KQ, 1, false, false, false, W_NT, 8, 4>(acc, a0, a1, bf, pc, sQ, px);
      proj_elem<KQ, 2, false, true, false, W_NT, 0, 0>(acc, a0, a1, bf, pc, sQ, px);
    } else {
      pc.soff = ks0;
      proj_elem<KT, 0, false, false, true, KV_NT, 16, 16>(acc, a0, a1, bf, pc, sK);
      pc.soff = ks1;
      proj_elem<KT, 1, false, false, true, KV_NT, 16, 16>(acc, a0, a1, bf, pc, sK);
      pc.soff = ks0;
      proj_elem<KT, 2, false, true, true, KV_NT, 16, 16>(acc, a0, a1, bf, pc, sV);
    }
    PINV(entry_flags);
    if (w == 0) {
      const unsigned long long mk = __builtin_amdgcn_ballot_w64(((entry_flags >> (lane & 7u)) & 1u) != 0u);
      const unsigned tok = 0x5a5a5a00u | (~(unsigned)mk & 0xffu);
      if (lane == 0) asm volatile("buffer_store_dword %0, off, %1, %2 sc1" :: "v"(tok), "s"(srdH), "s"(tok_off + xg * 4u) : "memory");
    }
    barrier();
    acc_fence_v(acc);
    q_conv<isA>(ac, acc, st, sK, sV, rk, w * 16384u, make_iseq<16>{});
  }
  ST SA, SB;
  if constexpr (isA) {
    const unsigned kb = rb, vb = SLOT + tbV;
    unsigned dum = lane, to = ((lane & 8u) * 16u + (lane & 7u) * 4u + (lane >> 4) * 32768u) | (((lane >> 4) ^ w) << 8);
    const unsigned tbo = (b & 3u) * 1024u + (b >> 2) * 131072u;
    PINV(to);
    const char* ta = Wo + (size_t)xg * 524288u + tbo;
    const char* tb2 = Wo2 + (size_t)xg * 524288u + tbo;
    touch(dum, to, ta);
    asm volatile("s_waitcnt lgkmcnt(0)" ::: "memory");
    barrier();
    k_read(ac, kb + kv_pair(0) * 16384u);
    wait_lgkm0();
    GAP();
    qk0<false>(ac, SA, sK, 0u, 0u, st, make_iseq<32>{});
    k_read(ac, kb + kv_pair(1) * 16384u);
    touch(dum, to, ta);
    sm_first<!isA>(ac, SA);
    unsigned i4 = 0;
    unsigned blk; const char* tp;
    auto ak = [&](PA& p, auto k) __attribute__((always_inline)) {
      if constexpr (k.value == 0) { __builtin_amdgcn_s_sleep(PACE_ATT); PINS(i4); blk = (unsigned)((kv_tab() >> 8) >> i4) << 14; i4 += 4u; PINS(blk); PINS(i4); }
      if constexpr (k.value == 1) { p.rd0 = kb + (blk & 0x3c000u); PINV(p.rd0); }
    };
    auto at = [&](PA& p, auto k) __attribute__((always_inline)) {
      if constexpr (k.value == 0) { PINS(i4); blk = (unsigned)(kv_tab() >> i4) << 14; PINS(blk); }
      if constexpr (k.value == 1) { p.rd0 = vb + (blk & 0x3c000u); PINV(p.rd0); }
      if constexpr (k.value == 2) asm volatile("s_cmp_lt_u32 %1, 24\n\ts_cselect_b64 %0, %2, %3" : "=s"(tp) : "s"(i4), "s"(ta), "s"(tb2 - 32768) : "scc");
      if constexpr (k.value == 3) { p.rd1 = p.rd0 ^ 32u; PINV(p.rd1); asm volatile("global_load_dword %0, %1, %2 nt" : "+v"(dum) : "v"(to), "s"(tp) : "memory"); }
      if constexpr (k.value == 4) asm volatile("global_load_dword %0, %1, %2\n\tv_add_u32_e32 %1, %3, %1" : "+v"(dum), "+v"(to) : "s"(tp), "i"(4096) : "memory");
    };
    phaseA<true, false, KV_NT, 63, NW + 8>(ac, SA, SB, at, sK, 0u, st);
    phaseB<3, true, false, KV_NT, 63>(ac, SB, ak, sK, 0u, st);
    phaseA<true, false, KV_NT, 63>(ac, SB, SA, at, sK, 0u, st);
    phaseB<true, true, false, KV_NT, 63>(ac, SA, ak, sK, 0u, st);
    barrier();
    while (i4 < 24u) {
      phaseA<true, false, KV_NT, 63>(ac, SA, SB, at, sK, 0u, st);
      phaseB<true, true, false, KV_NT, 63>(ac, SB, ak, sK, 0u, st);
      phaseA<true, false, KV_NT, 63>(ac, SB, SA, at, sK, 0u, st);
      phaseB<true, true, false, KV_NT, 63>(ac, SA, ak, sK, 0u, st);
    }
    barrier();
    phaseA<true, false, KV_NT, 63, 40>(ac, SA, SB, at, sK, 0u, st);
    phaseB<true, true, false, KV_NT, 63, 43>(ac, SB, ak, sK, 0u, st);
    phaseA<true, false, KV_NT, 63, 46>(ac, SB, SA, at, sK, 0u, st);
    phaseB<true, true, false, KV_NT, 63, 49>(ac, SA, ak, sK, 0u, st);
    wait_vm_lgkm<8, 8>(); barrier();
    phaseA<true, false, KV_NT, 63, 52>(ac, SA, SB, at, sK, 0u, st);
    phaseB<true, true, false, KV_NT, 63, 55>(ac, SB, ak, sK, 0u, st);
    phaseA<true, false, KV_NT, 63, 58>(ac, SB, SA, at, sK, 0u, st);
    phaseB<true, true, false, KV_NT, 63, 61>(ac, SA, ak, sK, 0u, st);
    wait_vm_lgkm<63, 8>(); barrier();
    while (i4 < 56u) {
      phaseA<true, false, KV_NT, 63>(ac, SA, SB, at, sK, 0u, st);
      phaseB<true, true, false, KV_NT, 63>(ac, SB, ak, sK, 0u, st);
      phaseA<true, false, KV_NT, 63>(ac, SB, SA, at, sK, 0u, st);
      phaseB<true, true, false, KV_NT, 63>(ac, SA, ak, sK, 0u, st);
    }
    barrier();
    unsigned v14 = vb + kv_pair(14) * 16384u, x14 = v14 ^ 32u; PINV(v14); PINV(x14);
    phaseA<true, true, W_NT, 63>(ac, SA, SB, [&](PA& p, auto) { p.soff = co * KO::CS; p.fill = pc.r01; p.rd0 = v14; p.rd1 = x14; }, sO, KO::US, st);
    phaseB<true, false, true, W_NT, 63>(ac, SB, [&](PA& p, auto k) __attribute__((always_inline)) {
      p.soff = (co + 1u) * KO::CS; p.fill = pc.r01 + SLOT; p.rd0 = v14 + 16384u; p.rd1 = x14 + 16384u;
      if constexpr (k.value == 1) proj_setup<KO>(pc, tb + 512u * g, lane * 16u, oz, os3); }, sO, KO::US, st);
    dum0 = dum;
  } else {
    const unsigned P0 = pc.r01, P1 = pc.r01 + SLOT, P2 = pc.r2, P3 = w * 16384u;
    unsigned v0 = tbV + P0, v2 = tbV + P2, x0 = v0 ^ 32u, x2 = v2 ^ 32u, k1 = rb + P1, k3 = rb + P3;
    PINV(v0); PINV(v2); PINV(x0); PINV(x2); PINV(k1); PINV(k3);
    wait_vm<48>();
    k_read(ac, k1 - SLOT);
#pragma unroll
    for (int i = 0; i < 8; ++i) { ac.o[i >> 2][i & 3] = f32x4{}; PINV(ac.o[i >> 2][i & 3]); }
    wait_lgkm0();
    GAP();
    qk0<true>(ac, SA, sV, ks1, P0, st, make_iseq<32>{});
    wait_vm<48>();
    GAP();
    k_read(ac, k1);
    sm_first<!isA>(ac, SA);
    BS bs{{P0, P1, P2, P3}, {v0, x0, v2, x2, tbV + P3, (tbV + P3) ^ 32u}, {k1, k3}, rk, co * KO::CS, lane * 16u, w};
    PINV(bs.v[4]); PINV(bs.v[5]);
    proj_setup<KO>(pc, tb + 512u * g, lane * 16u, oz, os3);
#define B2(I) b_step<KO, I>(ac, SA, SB, bs, sK, sV, sO, st); b_step<KO, I + 1>(ac, SB, SA, bs, sK, sV, sO, st);
    B2(0) B2(2) B2(4) B2(6) B2(8) B2(10)
    B2(12) b_step<KO, 14>(ac, SA, SB, bs, sK, sV, sO, st);
#undef B2
  }
  {
    const unsigned l16 = lane * 16u;
    unsigned ow = w * 16384u + l16;
    PINV(ow);
    f32x4 acc[8][4];
    bf16x8 a0[4], a1[4], b0[4], bf[4];
    float inv[5];
    asm volatile("" : "=v"(inv[4]));
    unsigned toks;
    asm volatile("buffer_load_dword %0, %1, %2, %3 offen sc1" : "=v"(toks) : "v"(l8), "s"(srdH), "s"(tok_off) : "memory");
    GAP();
    unsigned long long bad;
    phaseE<KO, (isA ? 18 : 19), (isA ? 2 : 1)>(ac, SB, inv, b0, a0, pc, sO, os3 - KO::CS, bad, ow, make_iseq<36>{});
    if (__builtin_expect(bad != 0ull, 0)) {
      asm volatile("s_waitcnt vmcnt(0)" : "+v"(toks) :: "memory");
      acc_fence_a(ac.o);
      redo_head(ac, K + kvo, V + kvo, lane, ow);
#pragma unroll
      for (int q = 0; q < 4; ++q) {
        inv[q] = 1.0f;
        b0[q] = lds128(ow + q * 1024u);
#pragma unroll
        for (int s = 1; s < 4; ++s) {
          const u32x4 t = __builtin_bit_cast(u32x4, lds128(ow + s * 4096u + q * 1024u));
          ac.o[2 * s][q] = f32x4{__uint_as_float(t[0] << 16), __uint_as_float(t[0] & 0xffff0000u), __uint_as_float(t[1] << 16), __uint_as_float(t[1] & 0xffff0000u)};
          ac.o[2 * s + 1][q] = f32x4{__uint_as_float(t[2] << 16), __uint_as_float(t[2] & 0xffff0000u), __uint_as_float(t[3] << 16), __uint_as_float(t[3] & 0xffff0000u)};
        }
      }
      wait_lgkm0();
      acc_fence_a(ac.o);
    }
#pragma unroll
    for (int q = 0; q < 4; ++q) bf[q] = bf16x8{};
    GAP();
    proj_elem<KO, 0, true, false, true, W_NT, 16, 16>(acc, a0, a1, b0, pc, sO, CvX<0>{ac, inv, b0, bf, ow});
    proj_elem<KO, 1, false, false, true, W_NT, 16, 16>(acc, a0, a1, bf, pc, sO, CvX<1>{ac, inv, bf, b0, ow});
    proj_elem<KO, 2, false, false, true, W_NT, 16, 16>(acc, a0, a1, b0, pc, sO, CvX<2>{ac, inv, b0, bf, ow});
    if constexpr (isA) PINV(dum0);
    proj_elem<KO, 0, false, false, true, W_NT, 16, 16, true>(acc, a0, a1, bf, pc, sO, CvX<3>{ac, inv, bf, b0, ow});
    typedef std::conditional_t<isA, EasyX<EASY2, EASY2_T - EASY2_F>, NoX> PX;
    const PX px = [&] { if constexpr (isA) return EasyX<EASY2, EASY2_T - EASY2_F>{tnow, tlim + (EASY2_F - EASY_T)}; else return NoX{}; }();
#pragma unroll 1
    for (unsigned e = 0; e < 3; ++e) {
      proj_elem<KO, 1, false, false, true, W_NT, 16, 16>(acc, a0, a1, bf, pc, sO, px);
      proj_elem<KO, 2, false, false, true, W_NT, 16, 16>(acc, a0, a1, bf, pc, sO, px);
      proj_elem<KO, 0, false, false, true, W_NT, 16, 16>(acc, a0, a1, bf, pc, sO, px);
    }
    Pub pb;
    pb.srd = pb_srd; pb.so = pb_so;
#pragma unroll
    for (int i = 0; i < 16; ++i) pb.pk[i] = u32x4{};
    TL tl;
#pragma unroll
    for (int i = 0; i < 4; ++i) { tl.w[0][0][i] = a0[i]; tl.x[0][0][i] = bf[i]; }
    tl.xs[0] = xs0; tl.xs[1] = xs1; tl.xs[2] = xs2; tl.drop[0] = drop0; tl.drop[1] = drop1; tl.l16 = l16;
    tl_run<KO>(acc, tl, pc, pb, make_iseq<96>{});
    const unsigned lr = ((lane + xg) & 7u) * 4u;
    unsigned fl;
    asm volatile("buffer_load_dword %0, %1, %2, %3 offen sc1" : "=v"(fl) : "v"(lr), "s"(srdH), "s"(flg_off + xg * 128u) : "memory");
    PINV(toks);
    if (w == (xg >> 1)) {
      const unsigned a = pc.r01 + SLOT + l16;
      if (xg & 1u) own_put<8>(pb, a, make_iseq<8>{}); else own_put<0>(pb, a, make_iseq<8>{});
    }
    const unsigned fo = lane * 128u + xg * 4u, tau = __builtin_amdgcn_readlane(toks, xg);
    unsigned oo = ((b * 64u + 16u * w + r16) * 1024u + (isA ? 0u : 512u) + 64u * xg + 8u * g) * 2u;
    PINV(oo);
    wait_lgkm0();
    barrier();
    const unsigned oa = 65536u + SLOT + (xg >> 1) * 16384u + w * 2048u + l16;
    const u32x4 r0 = __builtin_bit_cast(u32x4, lds128(oa)), r1 = __builtin_bit_cast(u32x4, lds128(oa + 1024u));
    GAP();
    wait_vm<9>();
    if (lane == 2u * w) asm volatile("buffer_store_dword %0, %1, %2, %3 offen sc1" :: "v"(toks), "v"(fo), "s"(srdH), "s"(flg_off) : "memory");
    wait_vm<2>();
    if (lane == 2u * w + 1u) asm volatile("buffer_store_dword %0, %1, %2, %3 offen sc1" :: "v"(toks), "v"(fo), "s"(srdH), "s"(flg_off) : "memory");
    f32x16 f[8];
#pragma unroll
    for (int j = 0; j < 8; ++j) { const unsigned t = j < 4 ? r0[j & 3] : r1[j & 3]; f[0][2 * j] = __uint_as_float(t << 16); f[0][2 * j + 1] = __uint_as_float(t & 0xffff0000u); }
    PINV(f[0]);
    asm volatile("s_waitcnt vmcnt(2)" : "+v"(fl) : "v"(pb.pk[0]), "v"(pb.pk[1]), "v"(pb.pk[2]), "v"(pb.pk[3]), "v"(pb.pk[4]), "v"(pb.pk[5]), "v"(pb.pk[6]), "v"(pb.pk[7]),
                 "v"(pb.pk[8]), "v"(pb.pk[9]), "v"(pb.pk[10]), "v"(pb.pk[11]), "v"(pb.pk[12]), "v"(pb.pk[13]), "v"(pb.pk[14]), "v"(pb.pk[15]) : "memory");
    unsigned got = 0xffffff01u, spin = 1u << 21, fresh, so, f2;
#define POLL(R) "buffer_load_dword %[" R "], %[lr], %[sh], %[flg] offen sc1\n\t"
#define SEEN(R) "v_cmp_eq_u32_e32 vcc, %[tau], %[" R "]\n\ts_andn2_b32 %[fr], vcc_lo, %[got]\n\ts_cbranch_scc1 .Lproc_%=\n\t"
    asm volatile(
        SEEN("fl") POLL("fl") "s_sleep 4\n"
        ".Lidle_%=:\n\t" POLL("f2") "s_waitcnt vmcnt(1)\n\t" SEEN("fl") "s_sleep %[slp]\n\t" POLL("fl") "s_waitcnt vmcnt(1)\n\t" SEEN("f2")
        "s_sleep %[slp]\n\ts_sub_u32 %[spin], %[spin], 1\n\ts_cbranch_scc0 .Lidle_%=\n\ts_branch .Ldone_%=\n"
        ".Lproc_%=:\n\ts_or_b32 %[got], %[got], %[fr]\n\t"
        PGS "s_bitcmp1_b32 %[fr], 1\n\ts_cbranch_scc0 .La1_%=\n\ts_add_u32 %[so], %[ro], 65536\n\ts_bitset0_b32 %[so], 19\n\t"
        "buffer_load_dwordx4 v[152:155], %[l16], %[sp], %[so] offen sc1\n\tbuffer_load_dwordx4 v[156:159], %[l16], %[sp], %[so] offen offset:1024 sc1\n.La1_%=:\n\t"
        PGS "s_bitcmp1_b32 %[fr], 2\n\ts_cbranch_scc0 .La2_%=\n\ts_add_u32 %[so], %[ro], 131072\n\ts_bitset0_b32 %[so], 19\n\t"
        "buffer_load_dwordx4 v[168:171], %[l16], %[sp], %[so] offen sc1\n\tbuffer_load_dwordx4 v[172:175], %[l16], %[sp], %[so] offen offset:1024 sc1\n.La2_%=:\n\t"
        PGS "s_bitcmp1_b32 %[fr], 3\n\ts_cbranch_scc0 .La3_%=\n\ts_add_u32 %[so], %[ro], 196608\n\ts_bitset0_b32 %[so], 19\n\t"
        "buffer_load_dwordx4 v[184:187], %[l16], %[sp], %[so] offen sc1\n\tbuffer_load_dwordx4 v[188:191], %[l16], %[sp], %[so] offen offset:1024 sc1\n.La3_%=:\n\t"
        PGS "s_bitcmp1_b32 %[fr], 4\n\ts_cbranch_scc0 .La4_%=\n\ts_add_u32 %[so], %[ro], 262144\n\ts_bitset0_b32 %[so], 19\n\t"
        "buffer_load_dwordx4 v[200:203], %[l16], %[sp], %[so] offen sc1\n\tbuffer_load_dwordx4 v[204:207], %[l16], %[sp], %[so] offen offset:1024 sc1\n.La4_%=:\n\t"
        PGS "s_bitcmp1_b32 %[fr], 5\n\ts_cbranch_scc0 .La5_%=\n\ts_add_u32 %[so], %[ro], 327680\n\ts_bitset0_b32 %[so], 19\n\t"
        "buffer_load_dwordx4 v[216:219], %[l16], %[sp], %[so] offen sc1\n\tbuffer_load_dwordx4 v[220:223], %[l16], %[sp], %[so] offen offset:1024 sc1\n.La5_%=:\n\t"
        PGS "s_bitcmp1_b32 %[fr], 6\n\ts_cbranch_scc0 .La6_%=\n\ts_add_u32 %[so], %[ro], 393216\n\ts_bitset0_b32 %[so], 19\n\t"
        "buffer_load_dwordx4 v[232:235], %[l16], %[sp], %[so] offen sc1\n\tbuffer_load_dwordx4 v[236:239], %[l16], %[sp], %[so] offen offset:1024 sc1\n.La6_%=:\n\t"
        PGS "s_bitcmp1_b32 %[fr], 7\n\ts_cbranch_scc0 .La7_%=\n\ts_add_u32 %[so], %[ro], 458752\n\ts_bitset0_b32 %[so], 19\n\t"
        "buffer_load_dwordx4 v[248:251], %[l16], %[sp], %[so] offen sc1\n\tbuffer_load_dwordx4 v[252:255], %[l16], %[sp], %[so] offen offset:1024 sc1\n.La7_%=:\n\t"
        "s_waitcnt vmcnt(0)\n\ts_cmp_eq_u32 %[got], -1\n\ts_cbranch_scc1 .Ltok_%=\n\t" POLL("fl") "s_branch .Lwide_%=\n"
        ".Ltok_%=:\n\tbuffer_load_dword %[fl], %[l8], %[sh], %[tok] offen sc1\n"
        ".Lwide_%=:\n\t"
        "s_bitcmp1_b32 %[fr], 1\n\ts_cbranch_scc0 .Lb1_%=\n\t"
        PGS "v_lshlrev_b32_e32 v144, 16, v152\n\tv_and_b32_e32 v145, %[hi], v152\n\tv_lshlrev_b32_e32 v146, 16, v153\n\tv_and_b32_e32 v147, %[hi], v153\n\t"
        PGS "v_lshlrev_b32_e32 v148, 16, v154\n\tv_and_b32_e32 v149, %[hi], v154\n\tv_lshlrev_b32_e32 v150, 16, v155\n\tv_and_b32_e32 v151, %[hi], v155\n\t"
        PGS "v_lshlrev_b32_e32 v152, 16, v156\n\tv_and_b32_e32 v153, %[hi], v156\n\tv_lshlrev_b32_e32 v154, 16, v157\n\tv_and_b32_e32 v155, %[hi], v157\n\t"
        PGS "v_lshlrev_b32_e32 v156, 16, v158\n\tv_and_b32_e32 v157, %[hi], v158\n\tv_lshlrev_b32_e32 v158, 16, v159\n\tv_and_b32_e32 v159, %[hi], v159\n\t"
        ".Lb1_%=:\n\t"
        "s_bitcmp1_b32 %[fr], 2\n\ts_cbranch_scc0 .Lb2_%=\n\t"
        PGS "v_lshlrev_b32_e32 v160, 16, v168\n\tv_and_b32_e32 v161, %[hi], v168\n\tv_lshlrev_b32_e32 v162, 16, v169\n\tv_and_b32_e32 v163, %[hi], v169\n\t"
        PGS "v_lshlrev_b32_e32 v164, 16, v170\n\tv_and_b32_e32 v165, %[hi], v170\n\tv_lshlrev_b32_e32 v166, 16, v171\n\tv_and_b32_e32 v167, %[hi], v171\n\t"
        PGS "v_lshlrev_b32_e32 v168, 16, v172\n\tv_and_b32_e32 v169, %[hi], v172\n\tv_lshlrev_b32_e32 v170, 16, v173\n\tv_and_b32_e32 v171, %[hi], v173\n\t"
        PGS "v_lshlrev_b32_e32 v172, 16, v174\n\tv_and_b32_e32 v173, %[hi], v174\n\tv_lshlrev_b32_e32 v174, 16, v175\n\tv_and_b32_e32 v175, %[hi], v175\n\t"
        ".Lb2_%=:\n\t"
        "s_bitcmp1_b32 %[fr], 3\n\ts_cbranch_scc0 .Lb3_%=\n\t"
        PGS "v_lshlrev_b32_e32 v176, 16, v184\n\tv_and_b32_e32 v177, %[hi], v184\n\tv_lshlrev_b32_e32 v178, 16, v185\n\tv_and_b32_e32 v179, %[hi], v185\n\t"
        PGS "v_lshlrev_b32_e32 v180, 16, v186\n\tv_and_b32_e32 v181, %[hi], v186\n\tv_lshlrev_b32_e32 v182, 16, v187\n\tv_and_b32_e32 v183, %[hi], v187\n\t"
        PGS "v_lshlrev_b32_e32 v184, 16, v188\n\tv_and_b32_e32 v185, %[hi], v188\n\tv_lshlrev_b32_e32 v186, 16, v189\n\tv_and_b32_e32 v187, %[hi], v189\n\t"
        PGS "v_lshlrev_b32_e32 v188, 16, v190\n\tv_and_b32_e32 v189, %[hi], v190\n\tv_lshlrev_b32_e32 v190, 16, v191\n\tv_and_b32_e32 v191, %[hi], v191\n\t"
        ".Lb3_%=:\n\t"
        "s_bitcmp1_b32 %[fr], 4\n\ts_cbranch_scc0 .Lb4_%=\n\t"
        PGS "v_lshlrev_b32_e32 v192, 16, v200\n\tv_and_b32_e32 v193, %[hi], v200\n\tv_lshlrev_b32_e32 v194, 16, v201\n\tv_and_b32_e32 v195, %[hi], v201\n\t"
        PGS "v_lshlrev_b32_e32 v196, 16, v202\n\tv_and_b32_e32 v197, %[hi], v202\n\tv_lshlrev_b32_e32 v198, 16, v203\n\tv_and_b32_e32 v199, %[hi], v203\n\t"
        PGS "v_lshlrev_b32_e32 v200, 16, v204\n\tv_and_b32_e32 v201, %[hi], v204\n\tv_lshlrev_b32_e32 v202, 16, v205\n\tv_and_b32_e32 v203, %[hi], v205\n\t"
        PGS "v_lshlrev_b32_e32 v204, 16, v206\n\tv_and_b32_e32 v205, %[hi], v206\n\tv_lshlrev_b32_e32 v206, 16, v207\n\tv_and_b32_e32 v207, %[hi], v207\n\t"
        ".Lb4_%=:\n\t"
        "s_bitcmp1_b32 %[fr], 5\n\ts_cbranch_scc0 .Lb5_%=\n\t"
        PGS "v_lshlrev_b32_e32 v208, 16, v216\n\tv_and_b32_e32 v209, %[hi], v216\n\tv_lshlrev_b32_e32 v210, 16, v217\n\tv_and_b32_e32 v211, %[hi], v217\n\t"
        PGS "v_lshlrev_b32_e32 v212, 16, v218\n\tv_and_b32_e32 v213, %[hi], v218\n\tv_lshlrev_b32_e32 v214, 16, v219\n\tv_and_b32_e32 v215, %[hi], v219\n\t"
        PGS "v_lshlrev_b32_e32 v216, 16, v220\n\tv_and_b32_e32 v217, %[hi], v220\n\tv_lshlrev_b32_e32 v218, 16, v221\n\tv_and_b32_e32 v219, %[hi], v221\n\t"
        PGS "v_lshlrev_b32_e32 v220, 16, v222\n\tv_and_b32_e32 v221, %[hi], v222\n\tv_lshlrev_b32_e32 v222, 16, v223\n\tv_and_b32_e32 v223, %[hi], v223\n\t"
        ".Lb5_%=:\n\t"
        "s_bitcmp1_b32 %[fr], 6\n\ts_cbranch_scc0 .Lb6_%=\n\t"
        PGS "v_lshlrev_b32_e32 v224, 16, v232\n\tv_and_b32_e32 v225, %[hi], v232\n\tv_lshlrev_b32_e32 v226, 16, v233\n\tv_and_b32_e32 v227, %[hi], v233\n\t"
        PGS "v_lshlrev_b32_e32 v228, 16, v234\n\tv_and_b32_e32 v229, %[hi], v234\n\tv_lshlrev_b32_e32 v230, 16, v235\n\tv_and_b32_e32 v231, %[hi], v235\n\t"
        PGS "v_lshlrev_b32_e32 v232, 16, v236\n\tv_and_b32_e32 v233, %[hi], v236\n\tv_lshlrev_b32_e32 v234, 16, v237\n\tv_and_b32_e32 v235, %[hi], v237\n\t"
        PGS "v_lshlrev_b32_e32 v236, 16, v238\n\tv_and_b32_e32 v237, %[hi], v238\n\tv_lshlrev_b32_e32 v238, 16, v239\n\tv_and_b32_e32 v239, %[hi], v239\n\t"
        ".Lb6_%=:\n\t"
        "s_bitcmp1_b32 %[fr], 7\n\ts_cbranch_scc0 .Lb7_%=\n\t"
        PGS "v_lshlrev_b32_e32 v240, 16, v248\n\tv_and_b32_e32 v241, %[hi], v248\n\tv_lshlrev_b32_e32 v242, 16, v249\n\tv_and_b32_e32 v243, %[hi], v249\n\t"
        PGS "v_lshlrev_b32_e32 v244, 16, v250\n\tv_and_b32_e32 v245, %[hi], v250\n\tv_lshlrev_b32_e32 v246, 16, v251\n\tv_and_b32_e32 v247, %[hi], v251\n\t"
        PGS "v_lshlrev_b32_e32 v248, 16, v252\n\tv_and_b32_e32 v249, %[hi], v252\n\tv_lshlrev_b32_e32 v250, 16, v253\n\tv_and_b32_e32 v251, %[hi], v253\n\t"
        PGS "v_lshlrev_b32_e32 v252, 16, v254\n\tv_and_b32_e32 v253, %[hi], v254\n\tv_lshlrev_b32_e32 v254, 16, v255\n\tv_and_b32_e32 v255, %[hi], v255\n\t"
        ".Lb7_%=:\n\t"
        "s_cmp_eq_u32 %[got], -1\n\ts_cbranch_scc0 .Lidle_%=\n"
        ".Ldone_%=:"
        : "=&{v[144:159]}"(f[1]), "=&{v[160:175]}"(f[2]), "=&{v[176:191]}"(f[3]), "=&{v[192:207]}"(f[4]), "=&{v[208:223]}"(f[5]), "=&{v[224:239]}"(f[6]), "=&{v[240:255]}"(f[7]),
          [fl] "+v"(fl), [f2] "=&v"(f2), [got] "+s"(got), [spin] "+s"(spin), [fr] "=&s"(fresh), [so] "=&s"(so)
        : [tau] "s"(tau), [l16] "v"(l16), [l8] "v"(l8), [lr] "v"(lr), [sp] "s"(pb.srd), [sh] "s"(srdH), [ro] "s"(xg * 73728u + w * 2048u), [tok] "s"(tok_off),
          [flg] "s"(flg_off + xg * 128u), [hi] "s"(0xffff0000u), [slp] "i"(isA ? 8 : 0)
        : "vcc", "scc", "memory");
#undef POLL
#undef SEEN
    const f32x16 t = ((f[0] + f[1]) + (f[2] + f[3])) + ((f[4] + f[5]) + (f[6] + f[7]));
#pragma unroll
    for (int s = 0; s < 2; ++s)
    {
      const u32x4 ov = natural8(u32x4{cvtpk(t[8 * s], t[8 * s + 1]), cvtpk(t[8 * s + 2], t[8 * s + 3]), cvtpk(t[8 * s + 4], t[8 * s + 5]), cvtpk(t[8 * s + 6], t[8 * s + 7])});
      __builtin_amdgcn_raw_buffer_store_b128(ov, mk_srd(out, 2097152u), oo + s * 64, 0, 17);
    }
    asm volatile("s_waitcnt vmcnt(2)" : "+v"(fl) :: "memory");
    if ((lane >> 1) == w && fl != toks) asm volatile("buffer_store_dword %0, %1, %2, %3 offen sc1" :: "v"(fl), "v"(fo), "s"(srdH), "s"(flg_off) : "memory");
  }
}

__global__ __launch_bounds__(256, 1) __attribute__((aligned(4096)))
void fused_attn(const char* __restrict__ Ax, const char* __restrict__ Ak, const char* __restrict__ Av, const char* __restrict__ Awq, const char* __restrict__ Awo,
                const char* __restrict__ Bx, const char* __restrict__ Bk, const char* __restrict__ Bv, const char* __restrict__ Bwq, const char* __restrict__ Bwo,
                char* __restrict__ part, char* __restrict__ out) {
  extern __shared__ __attribute__((aligned(16))) char lds[];
  const unsigned bid = blockIdx.x;
  unsigned long long tnow;
  {
    const unsigned xg = bid & 7u, b = (bid >> 3) & 15u;
    const char *px, *pq, *pc; unsigned left;
    asm volatile(PG_DEFS "s_load_dwordx2 %0, %5, %6\n\ts_load_dwordx2 %1, %5, 0x28\n\ts_memrealtime %2\n\ts_getpc_b64 %3\n.Lpc_%=:\n\ts_mov_b32 %4, .Lhot_end-.Lpc_%="
                 : "=&s"(pq), "=&s"(px), "=&s"(tnow), "=&s"(pc), "=&s"(left) : "s"(__builtin_amdgcn_kernarg_segment_ptr()), "s"(bid >= 128u ? 0x18u : 0x40u));
    unsigned tid = threadIdx.x;
    PINV(tid);
    const unsigned lane = tid & 63u, w = rfl(tid >> 6), zq = ((b & 3u) << 14) | ((b >> 2) << 8);
    unsigned q0 = q_soff(zq), q1 = q_soff(zq + 0x1300u), r01 = 65536u + w * 16384u, r2 = 131072u + w * 8192u, xs = (b & 3u) * 256u, xd = w * 4096u + (b & 3u) * 16384u;
    asm volatile("s_mov_b32 m0, %2\n\ts_nop 0\n\tbuffer_load_dword %0, %1, 0 offen lds"
                 :: "v"(bid >= 128u && tid - 192u < WARM ? (b * WARM + tid - 192u) * 128u : 0x80000000u), "s"(mk_srd(pc, left)), "s"(r01 + SLOT) : "memory");
    Src sQ, sX;
    mk_w<false>(sQ, (xg * 4u + w) * 256u, 8192u, lane);
    mk_rows<false>(sX, b * 65536u + w * 16384u, 1024u, lane);
#pragma unroll
    for (int i = 0; i < 4; ++i) PINV(sX.v[i]);
    PINS(xs); PINS(xd);
#pragma unroll
    for (int i = 0; i < 4; ++i) PINV(sQ.v[i]);
    PINS(q0); PINS(q1); PINS(r01); PINS(r2);
    GAP();
    asm volatile("s_waitcnt lgkmcnt(0)" : "+s"(pq), "+s"(px), "+s"(tnow));
    GAP();
    sQ.srd = mk_srd(pq, 4194304u); sX.srd = mk_srd(px, 1048576u);
    dma_unit<1>(sQ, q0, r2);
    dma_unit<W_NT>(sQ, q1, r01);
    asm volatile("s_cmp_ge_u32 %0, 0x80\n\ts_cbranch_scc1 .Lx_%=\n\ts_mov_b32 m0, %1\n\ts_nop 0\n\tbuffer_load_dwordx4 %2, %6, %7 offen lds\n\tbuffer_load_dwordx4 %3, %6, %7 offen offset:1024 lds\n\t"
                 "buffer_load_dwordx4 %4, %6, %7 offen offset:2048 lds\n\tbuffer_load_dwordx4 %5, %6, %7 offen offset:3072 lds\n.Lx_%=:"
                 :: "s"(bid), "s"(xd), "v"(sX.v[0]), "v"(sX.v[1]), "v"(sX.v[2]), "v"(sX.v[3]), "s"(sX.srd), "s"(xs) : "scc", "memory");
    dma_unit<1>(sQ, q0 + 128u, r2 + 4096u);
    dma_unit<W_NT>(sQ, q1 + 128u, r01 + 4096u);
    asm volatile("" :: "v"(lds));
  }
  if (bid < 128u) { body<false>(Bx, Bk, Bv, Bwq, Bwo, Bwo, tnow, part, out); asm volatile(PG_END ".Lhot_end:" ::: "memory"); __builtin_amdgcn_endpgm(); }
  body<true>(Ax, Ak, Av, Awq, Awo, Bwo, tnow, part, out);
  asm volatile(PG_END ".rept %0\n\ts_nop 0\n.endr" :: "i"(ASHIFT) : "memory");
}

extern "C" void kernel_launch(void* const* d_in, const int* in_sizes, int n_in, void* d_out, int out_size, void* d_ws, size_t ws_size, hipStream_t stream) {
  (void)in_sizes; (void)n_in; (void)out_size; (void)ws_size;
  (void)hipFuncSetAttribute((const void*)fused_attn, hipFuncAttributeMaxDynamicSharedMemorySize, (int)LDS_BYTES);
  fused_attn<<<256, 256, LDS_BYTES, stream>>>((const char*)d_in[0], (const char*)d_in[1], (const char*)d_in[2], (const char*)d_in[3], (const char*)d_in[4],
                                              (const char*)d_in[5], (const char*)d_in[6], (const char*)d_in[7], (const char*)d_in[8], (const char*)d_in[9], (char*)d_ws, (char*)d_out);
}
```

```cpp
#include <hip/hip_runtime.h>
#include <hip/hip_bf16.h>
#include <utility>
#include <type_traits>

typedef __bf16 bf16x8 __attribute__((ext_vector_type(8)));
typedef __bf16 bf16x4 __attribute__((ext_vector_type(4)));
typedef __bf16 bf16x2 __attribute__((ext_vector_type(2)));
typedef float f32x4 __attribute__((ext_vector_type(4)));
typedef float f32x2 __attribute__((ext_vector_type(2)));
typedef float f32x16 __attribute__((ext_vector_type(16)));
typedef unsigned u32x4 __attribute__((ext_vector_type(4)));
#define LAS __attribute__((address_space(3)))
#define DI __device__ __forceinline__
typedef __amdgpu_buffer_rsrc_t srd_t;
template <int... Is> using iseq = std::integer_sequence<int, Is...>;
template <int N> using make_iseq = std::make_integer_sequence<int, N>;

constexpr int KV_NT = 1, W_NT = 0, X_NT = 0;
constexpr int SK = 4, SV = 4, SP = 5;
constexpr unsigned EASY_T = 1350, EASY_L = 1000, EASY2_F = 2000, EASY2_T = 3150;
constexpr int EASY = 6, PACE_ATT = 2, EASY2 = 8;
constexpr unsigned WARM = 40;
constexpr int PGB = 9, AWSH = 36, BSHIFT = 0, ASHIFT = 0;

constexpr unsigned SLOT = 8192, LDS_BYTES = 20 * SLOT;
constexpr unsigned HOFF = 16u << 20;
constexpr float SC = 0.12751743082f;
constexpr float LMAX = 1048576.0f;

#define GAP() __builtin_amdgcn_sched_barrier(0)
#define PINS(x) asm volatile("" : "+s"(x))
#define PINV(x) asm volatile("" : "+v"(x))
DI unsigned rfl(unsigned x) { return __builtin_amdgcn_readfirstlane(x); }
DI unsigned cvtpk(float lo, float hi) { f32x2 v = {lo, hi}; bf16x2 b = __builtin_convertvector(v, bf16x2); return __builtin_bit_cast(unsigned, b); }
DI u32x4 pack2(const f32x4& a, const f32x4& b) { return u32x4{cvtpk(a[0], a[1]), cvtpk(a[2], a[3]), cvtpk(b[0], b[1]), cvtpk(b[2], b[3])}; }
DI u32x4 natural8(u32x4 w) {
  auto s0 = __builtin_amdgcn_permlane32_swap(w[0], w[2], false, false);
  auto s1 = __builtin_amdgcn_permlane32_swap(w[1], w[3], false, false);
  auto t0 = __builtin_amdgcn_permlane16_swap(s0[0], s0[1], false, false);
  auto t1 = __builtin_amdgcn_permlane16_swap(s1[0], s1[1], false, false);
  return u32x4{t0[0], t1[0], t0[1], t1[1]};
}
#define KSYM "_Z10fused_attnPKcS0_S0_S0_S0_S0_S0_S0_S0_S0_PcS1_"
#define PGL(c) ".if (((. - " KSYM ") >> 12) & 3) == " #c "\n777" #c ": s_cbranch_execz 777" #c "f\n.endif\n"
#define PG_DEFS ".set pgnext, 4096\n.macro pgstop\n.if (. - " KSYM ") >= pgnext\n.set pgnext, ((. - " KSYM ") | 4095) + 1\n" PGL(0) PGL(1) PGL(2) PGL(3) ".endif\n.endm\n" \
                ".macro mfp a:vararg\nv_mfma_f32_16x16x32_bf16 \\a\npgstop\n.endm\n"
#define PG_END "s_endpgm\n7770:\n7771:\n7772:\n7773:\n\ts_setpc_b64 vcc\n"
#define MF "mfp "
#define PGS "pgstop\n\t"
template <int SH = 0> DI void pg_walk(unsigned w) {
  asm volatile("s_mov_b64 exec, 0\n\ts_call_b64 vcc, .Lw1_%=\n\ts_branch .Lw2_%=\n.Lw1_%=:\n\ts_cmp_eq_u32 %0, 0\n\ts_cbranch_scc1 7770f\n\ts_cmp_eq_u32 %0, 1\n\ts_cbranch_scc1 7771f\n\t"
               "s_cmp_eq_u32 %0, 2\n\ts_cbranch_scc1 7772f\n\ts_branch 7773f\n.rept %1\n\ts_nop 0\n.endr\n.Lw2_%=:\n\ts_mov_b64 exec, -1" :: "s"(w), "i"(SH) : "vcc", "scc", "memory");
}
template <bool Z> DI void mm_vvv(f32x4& c, const bf16x8& a, const bf16x8& b) {
  if constexpr (Z) asm volatile(MF "%0, %1, %2, 0" : "=&v"(c) : "v"(a), "v"(b));
  else             asm volatile(MF "%0, %1, %2, %0" : "+v"(c) : "v"(a), "v"(b));
}
template <bool Z> DI void mm_vva(f32x4& c, const bf16x8& a, const bf16x8& b) {
  if constexpr (Z) asm volatile(MF "%0, %1, %2, 0" : "=&v"(c) : "v"(a), "a"(b));
  else             asm volatile(MF "%0, %1, %2, %0" : "+v"(c) : "v"(a), "a"(b));
}
DI void mm_vca(f32x4& d, const bf16x8& a, const bf16x8& b, const f32x4& c) { asm volatile(MF "%0, %1, %2, %3" : "=&v"(d) : "v"(a), "a"(b), "v"(c)); }
template <bool Z = false> DI void mm_avv(f32x4& c, const bf16x8& a, const bf16x8& b) {
  if constexpr (Z) asm volatile(MF "%0, %1, %2, 0" : "=a"(c) : "v"(a), "v"(b));
  else             asm volatile(MF "%0, %1, %2, %0" : "+a"(c) : "v"(a), "v"(b));
}
DI void set_m0(unsigned v) { asm volatile("s_mov_b32 m0, %0" :: "s"(v) : "memory"); }
template <int NT, int P> DI void dma_p(unsigned voff, srd_t srd, unsigned soff) {
  if constexpr (NT) asm volatile("buffer_load_dwordx4 %0, %1, %2 offen offset:%3 nt lds" :: "v"(voff), "s"(srd), "s"(soff), "i"(1024 * P) : "memory");
  else              asm volatile("buffer_load_dwordx4 %0, %1, %2 offen offset:%3 lds" :: "v"(voff), "s"(srd), "s"(soff), "i"(1024 * P) : "memory");
}
struct Src { unsigned v[4]; srd_t srd; };
template <int NT> DI void dma_unit(const Src& s, unsigned soff, unsigned ldsaddr) {
  asm volatile("s_mov_b32 m0, %0\n\ts_nop 0" :: "s"(ldsaddr) : "memory");
  dma_p<NT, 0>(s.v[0], s.srd, soff); dma_p<NT, 1>(s.v[1], s.srd, soff); dma_p<NT, 2>(s.v[2], s.srd, soff); dma_p<NT, 3>(s.v[3], s.srd, soff);
}
template <int NT> DI void dma_elem(const Src& s, unsigned soff, unsigned ustep, unsigned ldsaddr) { dma_unit<NT>(s, soff, ldsaddr); dma_unit<NT>(s, soff + ustep, ldsaddr + 4096u); }
template <int N> DI void wait_vm() { asm volatile("s_waitcnt vmcnt(%0)" :: "i"(N) : "memory"); }
template <int N, int L> DI void wait_vm_lgkm() { asm volatile("" ::: "memory"); __builtin_amdgcn_s_waitcnt((N & 15) | ((N >> 4) << 14) | 0x70 | (L << 8)); asm volatile("" ::: "memory"); }
DI void wait_lgkm0() { wait_vm_lgkm<63, 0>(); }
DI void barrier() { asm volatile("s_barrier" ::: "memory"); }
DI bf16x8 lds128(unsigned a) { return *(const LAS bf16x8*)(size_t)(a); }
DI bf16x4 lds_tr1(unsigned a) { return __builtin_amdgcn_ds_read_tr16_b64_v4bf16((LAS bf16x4*)(size_t)(a)); }
DI bf16x8 cat(bf16x4 lo, bf16x4 hi) { return __builtin_shufflevector(lo, hi, 0, 1, 2, 3, 4, 5, 6, 7); }
DI bf16x8 lds_tr(unsigned a0, unsigned a1) { return cat(lds_tr1(a0), lds_tr1(a1)); }
DI srd_t mk_srd(const void* base, unsigned bytes) { return __builtin_amdgcn_make_buffer_rsrc((void*)base, (short)0, (int)bytes, 0x00020000); }
template <bool VX> DI void mk_rows(Src& s, unsigned base, unsigned stride, unsigned lane) {
  const unsigned row = (lane >> 2) & 3u, cb = lane >> 4, sl = lane & 3u;
#pragma unroll
  for (int p = 0; p < 4; ++p) s.v[p] = base + row * stride + cb * 64u + ((sl ^ (VX ? 2 * (p & 1) : (p & 2))) << 4) + (4u * stride - 1024u) * p;
}
template <bool VX> DI void mk_w(Src& s, unsigned base, unsigned stride, unsigned lane) {
  const unsigned sub = lane >> 5, cb = (lane >> 4) & 1u, row = (lane >> 2) & 3u, sl = lane & 3u;
#pragma unroll
  for (int p = 0; p < 4; ++p) s.v[p] = base + (4u * sub + row) * stride + cb * 64u + ((sl ^ (VX ? 2u * sub : 2u * (p & 1))) << 4) + (8u * stride - 1024u) * p;
}
DI void touch(unsigned& d, unsigned& voff, const char* base) {
  asm volatile("global_load_dword %0, %1, %2 nt\n\tglobal_load_dword %0, %1, %2\n\tv_add_u32_e32 %1, 0x1000, %1" : "+v"(d), "+v"(voff) : "s"(base) : "memory");
}
struct Geo { int r0, r1, p0, p1, u, t0, t1, t2, t3; };
constexpr Geo GB = {8, 15, 16, 12, 11, 13, 14, 10, 9}, GA = {15, 8, 16, 9, 10, 14, 11, 12, 13};
constexpr unsigned KUS = 1u << GB.u;
template <bool isA> DI unsigned ksoffg(unsigned m, unsigned rk) {
  constexpr Geo G = isA ? GA : GB;
  const unsigned t = m + rk;
  return ((t & 1u) << G.t0) | (((t >> 1) & 1u) << G.t1) | (((t >> 2) & 1u) << G.t2) | (((t >> 3) & 1u) << G.t3);
}
DI unsigned ksoff(unsigned m, unsigned rk) { return ksoffg<false>(m, rk); }
template <bool isA, bool VX> DI void mk_src_kv(Src& s, const char* base, unsigned lane, unsigned w) {
  constexpr Geo G = isA ? GA : GB;
  s.srd = mk_srd(base, 131072u);
  const unsigned row = (lane >> 2) & 3u, sl = lane & 3u, o = ((row & 1u) << G.r0) + ((row >> 1) << G.r1) + (lane >> 4) * 64u;
#pragma unroll
  for (unsigned p = 0; p < (isA ? 2u : 4u); ++p)
    s.v[p] = o + ((sl ^ (VX ? 2u * (p & 1u) : isA ? 2u * (w & 1u) : (p & 2u))) << 4) + ((p & 1u) << G.p0) + ((p >> 1) << G.p1) - (isA ? 0u : 1024u * p);
}

struct PC {
  unsigned r01, r2;
  unsigned w01[2], w2[2];
  unsigned xb, xmask, xa;
  unsigned z, soff;
};
template <unsigned TS_, unsigned QS_, unsigned ZI_, unsigned ZM_, unsigned ZO_, unsigned US_, unsigned CS_, bool ROT_> struct PK {
  static constexpr unsigned TS = TS_, QS = QS_, ZI = ZI_, ZM = ZM_, ZO = ZO_, US = US_, CS = CS_; static constexpr bool ROT = ROT_; };
DI unsigned q_soff(unsigned z) { return ((z & 0xC300u) * 0x440u) & 0x3C0000u; }
template <int POS> DI unsigned ring_lds(const PC& c) { return POS == 2 ? c.r2 : c.r01 + POS * SLOT; }
template <int POS> DI unsigned ring_rd(const PC& c, int d) { return (POS == 2 ? c.w2[d & 1] : c.w01[d & 1] + POS * SLOT) + (d >> 1) * 256u; }
struct NoX { static constexpr bool ACT = true; template <int G> DI void run() const {} };
template <int SL, unsigned LEN> struct EasyX {
  static constexpr bool ACT = true;
  unsigned long long& t; unsigned from;
  template <int G> DI void run() const {
    if constexpr (G == 8) asm volatile("s_memrealtime %0" : "=s"(t));
    if constexpr (G == 16) PINS(t);
    if constexpr (G == 16) asm volatile("s_cmp_ge_u32 %0, %2\n\ts_cbranch_scc1 .Le_%=\n\ts_sleep %1\n.Le_%=:" :: "s"((unsigned)t - from), "i"(SL), "i"(LEN) : "scc");
  }
};
template <class K, int POS, int S, bool Z, bool RD, bool DM, int NT, bool XT, int M, class X>
DI void proj_gap(f32x4 (&acc)[8][4], const bf16x8 (&cur)[4], bf16x8 (&nxt)[4], bf16x4 (&lo)[4], bf16x8 (&bf)[4], PC& c, unsigned soff, const Src& src, const Src& xs, unsigned xsoff, const X& ex) {
  constexpr int qb = M >> 2, db = M & 3;
  mm_vvv<Z>(acc[4 * S + db][qb], cur[db], bf[qb]);
  if constexpr (RD && M < 8) {
    constexpr int d = M >> 1; const unsigned a = S == 0 ? ring_rd<POS>(c, d) + 4096u : ring_rd<(POS + 1) % 3>(c, d);
    if constexpr (M & 1) nxt[d] = cat(lo[d], lds_tr1(a + K::TS)); else lo[d] = lds_tr1(a);
  }
  if constexpr (X::ACT && RD && S == 1 && db == 3) bf[qb] = lds128(c.xa + qb * K::QS);
  if constexpr (XT) {
    if constexpr (M < 8 && !(M & 1)) dma_p<X_NT, M / 2>(xs.v[M / 2], xs.srd, xsoff);
    if constexpr (M == 7) set_m0(ring_lds<POS>(c) + S * 4096u);
    if constexpr (M >= 8 && !(M & 1)) dma_p<NT, (M - 8) / 2>(src.v[(M - 8) / 2], src.srd, soff);
  } else if constexpr (DM && M >= 8 && !(M & 1)) dma_p<NT, (M - 8) / 2>(src.v[(M - 8) / 2], src.srd, soff);
  if constexpr (X::ACT && S == 0 && M == 9) { PINS(c.z); c.z = (c.z + K::ZI) & K::ZM; PINS(c.z); }
  if constexpr (X::ACT && S == 0 && M == 11) { c.xa = (c.z & c.xmask) | c.xb; PINV(c.xa); }
  if constexpr (S == 1 && DM && !K::ROT && M == 9) { PINS(c.soff); c.soff = (c.soff + K::CS) & (16u * K::CS - 1u); PINS(c.soff); }
  if constexpr (S == 1 && DM && K::ROT && M == 9) { PINS(c.z); c.soff = (c.z + 0x3100u) & 0xC300u; PINS(c.soff); }
  if constexpr (S == 1 && DM && K::ROT && M == 13) { PINS(c.soff); c.soff = (c.soff * 0x440u) & 0x3C0000u; PINS(c.soff); }
  ex.template run<16 * S + M>();
  GAP();
}
template <class K, int POS, int S, bool Z, bool RD, bool DM, int NT, int VM, bool BAR, bool XT, class X, int... Ms>
DI void proj_unit(f32x4 (&acc)[8][4], const bf16x8 (&cur)[4], bf16x8 (&nxt)[4], bf16x8 (&bf)[4], PC& c, const Src& src, const Src& xs, unsigned xsoff, unsigned xdst, const X& ex, iseq<Ms...>) {
  wait_vm_lgkm<(RD ? VM : 63), (S == 0 ? 2 : 0)>();
  if constexpr (BAR) barrier();
  const unsigned soff = c.soff + S * K::US;
  if constexpr (XT) set_m0(xdst); else if constexpr (DM) set_m0(ring_lds<POS>(c) + S * 4096u);
  GAP();
  bf16x4 lo[4];
  (proj_gap<K, POS, S, Z, RD, DM, NT, XT, Ms>(acc, cur, nxt, lo, bf, c, soff, src, xs, xsoff, ex), ...);
}
template <class K, int POS, bool Z, bool LAST, bool DM, int NT, int VM0, int VM1, bool BAR1 = false, bool XT0 = false, bool XT1 = false, class X = NoX>
DI void proj_elem(f32x4 (&acc)[8][4], bf16x8 (&a0)[4], bf16x8 (&a1)[4], bf16x8 (&bf)[4], PC& c, const Src& src, const X& ex = X{},
                  const Src* xs = nullptr, unsigned xso0 = 0, unsigned xd0 = 0, unsigned xso1 = 0, unsigned xd1 = 0) {
  proj_unit<K, POS, 0, Z, true, DM, NT, VM0, false, XT0>(acc, a0, a1, bf, c, src, xs ? *xs : src, xso0, xd0, ex, make_iseq<16>{});
  proj_unit<K, POS, 1, Z, !LAST, DM, NT, VM1, BAR1, XT1>(acc, a1, a0, bf, c, src, xs ? *xs : src, xso1, xd1, ex, make_iseq<16>{});
}
template <class K> DI void proj_setup(PC& c, unsigned tb, unsigned xb, unsigned z0, unsigned s3) {
  c.w01[0] = c.r01 + tb; c.w01[1] = c.w01[0] ^ 32u; c.w2[0] = c.r2 + tb; c.w2[1] = c.w2[0] ^ 32u;
  PINV(c.w01[0]); PINV(c.w01[1]); PINV(c.w2[0]); PINV(c.w2[1]);
  c.xb = xb; c.xmask = K::ZO; PINV(c.xmask);
  c.z = z0; c.soff = s3;
}
template <class K, int POS> DI void proj_start(bf16x8 (&a0)[4], bf16x8 (&bf)[4], PC& c, unsigned tb, unsigned xb, unsigned z0, unsigned s3) {
  proj_setup<K>(c, tb, xb, z0, s3);
  c.xa = (c.z & K::ZO) | xb; PINV(c.xa);
#pragma unroll
  for (int d = 0; d < 4; ++d) { const unsigned a = ring_rd<POS>(c, d); a0[d] = lds_tr(a, a + K::TS); }
  GAP();
#pragma unroll
  for (int q = 0; q < 4; ++q) bf[q] = lds128(c.xa + q * K::QS);
  GAP();
}
#define ACC_ALL(C, a) C(a[0][0]), C(a[0][1]), C(a[0][2]), C(a[0][3]), C(a[1][0]), C(a[1][1]), C(a[1][2]), C(a[1][3]), C(a[2][0]), C(a[2][1]), C(a[2][2]), C(a[2][3]), C(a[3][0]), C(a[3][1]), C(a[3][2]), C(a[3][3])
DI void acc_fence_v(f32x4 (&acc)[8][4]) {
  asm volatile("s_nop 7\n\ts_nop 3" : ACC_ALL("+v", acc) :: "memory");
  f32x4 (&hi)[4][4] = *(f32x4 (*)[4][4])&acc[4];
  asm volatile("" : ACC_ALL("+v", hi) :: "memory");
  GAP();
}
DI void acc_fence_a(f32x4 (&acc)[8][4]) {
  asm volatile("s_nop 7\n\ts_nop 3" : ACC_ALL("+a", acc) :: "memory");
  f32x4 (&hi)[4][4] = *(f32x4 (*)[4][4])&acc[4];
  asm volatile("" : ACC_ALL("+a", hi) :: "memory");
  GAP();
}

struct Stash { u32x4 p[64]; unsigned base[3]; };
template <int I> DI void stash_load1(Stash& st, const Src& sK, const Src& sV, unsigned& rk, unsigned wo_, unsigned (&sm)[16]) {
  constexpr int m = I >> 2, isV = (I >> 1) & 1, pc_ = I & 1;
  if constexpr (!isV && !pc_) { PINS(rk); sm[m] = ksoffg<true>(m, rk) + wo_; }
  const unsigned so = sm[m];
  if constexpr (I < 56) asm volatile("buffer_load_dwordx4 %0, %1, %2, %3 offen nt" : "=a"(st.p[I]) : "v"(isV ? sV.v[pc_] : sK.v[pc_]), "s"(isV ? sV.srd : sK.srd), "s"(so) : "memory");
  else                  asm volatile("buffer_load_dwordx4 %0, %1, %2, %3 offen nt" : "=v"(st.p[I]) : "v"(isV ? sV.v[pc_] : sK.v[pc_]), "s"(isV ? sV.srd : sK.srd), "s"(so) : "memory");
}
constexpr int stash_idx(int j) { return 4 * ((j >> 1) & 15) + 2 * (j >> 5) + (j & 1); }
template <int... Is> DI void stash_load(Stash& st, const Src& sK, const Src& sV, unsigned rk, unsigned wo_, iseq<Is...>) { unsigned sm[16]; (stash_load1<stash_idx(Is)>(st, sK, sV, rk, wo_, sm), ...); }
constexpr int kv_pair(int m) { constexpr int t[10] = {0, 1, 2, 3, 8, 9, 4, 5, 6, 7}; return t[m % 10]; }
constexpr unsigned long long kv_tab() { unsigned long long v = 0; for (int i = 0; i < 16; ++i) v |= (unsigned long long)kv_pair(i) << (4 * i); return v; }
template <int I> DI void stash_write1(const Stash& st) {
  constexpr int m = I >> 2, isV = (I >> 1) & 1, pc_ = I & 1, off = kv_pair(m) * 16384 + isV * 8192 + pc_ * 1024;
  if constexpr (I < 56) asm volatile("ds_write_b128 %0, %1 offset:%2" :: "v"(st.base[off >> 16]), "a"(st.p[I]), "i"(off & 65535) : "memory");
  else                  asm volatile("ds_write_b128 %0, %1 offset:%2" :: "v"(st.base[off >> 16]), "v"(st.p[I]), "i"(off & 65535) : "memory");
}

struct AC {
  f32x4 o[8][4];
  bf16x8 q[4][4];
  bf16x8 kf[8], vf[8], pf[4];
  f32x4 negm[4];
  f32x4 l[4];
  bf16x8 ones;
  unsigned ra[2];
};
typedef f32x4 ST[2][4];
constexpr int VR = 32;
constexpr int NEB = 22;
template <int E> DI float sm_val(ST& S) { return S[(E >> 2) & 1][E >> 3][E & 3]; }
template <int E> DI void sm_exp1(ST& S) { float a; asm volatile("v_exp_f32_e32 %0, %1" : "=v"(a) : "v"(sm_val<E>(S))); S[(E >> 2) & 1][E >> 3][E & 3] = a; }
template <int QB, int P> DI void sm_cvt(ST& S, AC& c) {
  u32x4 w = __builtin_bit_cast(u32x4, c.pf[QB]);
  unsigned r;
  asm volatile("v_cvt_pk_bf16_f32 %0, %1, %2" : "=v"(r) : "v"(sm_val<8 * QB + 2 * P>(S)), "v"(sm_val<8 * QB + 2 * P + 1>(S)));
  w[P] = r;
  c.pf[QB] = __builtin_bit_cast(bf16x8, w);
}
constexpr bool wide(int nt) { return nt == W_NT; }
template <bool DM, int NT, int VM, int R, int M> DI void mem_pre(unsigned ldsaddr) {
  if constexpr (M == 6) wait_lgkm0();
  constexpr int F = wide(NT) ? 2 : 6, H = wide(NT) ? 18 : 14;
  if constexpr (DM && (M == F || M == H)) set_m0(ldsaddr + (M == H ? 4096u : 0u));
  if constexpr (M == R && VM < 63) wait_vm<VM>();
}
template <bool DM, int NT, int M> DI void mem_post(const Src& src, unsigned soff, unsigned ustep) {
  if constexpr (wide(NT)) { if constexpr (DM && M < 32 && (M & 3) == 2) dma_p<NT, (M >> 2) & 3>(src.v[(M >> 2) & 3], src.srd, soff + (M >= 16 ? ustep : 0u)); }
  else if constexpr (DM && M >= 6 && M <= 20 && !(M & 1)) dma_p<NT, ((M - 6) / 2) & 3>(src.v[((M - 6) / 2) & 3], src.srd, soff + (M >= 14 ? ustep : 0u));
}
template <int WR, int M> DI void mem_wr(const Stash& st) {
  if constexpr (WR >= 32 && M >= 7 && M < 19 && (M & 3) == 3) stash_write1<WR + (M - 7) / 4>(st);
  if constexpr (WR >= 0 && WR < 32 && M >= 8 && M < 16)
    asm volatile("ds_write_b128 %0, %1 offset:%2" :: "v"(st.base[0]), "a"(st.p[WR + M - 8]), "i"((M - 8) * 1024) : "memory");
}
template <int N> using IC = std::integral_constant<int, N>;
struct PA { unsigned rd0, rd1, soff, fill; };
template <bool QK, bool DM, int NT, int VM, int WR, int M, class F>
DI void gapA(AC& c, ST& S, ST& Sn, PA& pa, F& addr, const Src& src, unsigned ustep, const Stash& st, bf16x4& lo) {
  constexpr int R = QK ? 16 : 0;
  if constexpr ((DM || !QK) && M == 0) addr(pa, IC<0>{});
  if constexpr (QK) mem_pre<DM, NT, VM, 16, M>(pa.fill); else if constexpr (M == 0 && VM < 63) wait_vm<VM>();
  if constexpr (QK) {
    constexpr int f = M >> 2, s = f >> 1, kb = f & 1, qb = M & 3;
    if constexpr (s == 0) mm_vca(Sn[kb][qb], c.kf[f], c.q[qb][0], c.negm[qb]); else mm_vva<false>(Sn[kb][qb], c.kf[f], c.q[qb][s]);
  }
  mem_post<DM, NT, M>(src, pa.soff, ustep);
  mem_wr<WR, M>(st);
  if constexpr (QK && !DM && M >= 10 && M < 16) addr(pa, IC<M - 10>{});
  if constexpr (QK && M == 12) asm volatile("" :: "v"(c.negm[0]), "v"(c.negm[1]), "v"(c.negm[2]), "v"(c.negm[3]));
  if constexpr (M >= R && M < R + 16) {
    constexpr int db = (M - R) >> 1; const unsigned a = ((db & 1) ? pa.rd1 : pa.rd0) + (db >> 1) * 256u;
    if constexpr (M & 1) c.vf[db] = cat(lo, lds_tr1(a + 4096u)); else lo = lds_tr1(a);
  }
  if constexpr (NEB + M < 32) sm_exp1<NEB + M>(S);
  if constexpr (M >= 16 && (M & 1)) sm_cvt<2 + ((M - 16) >> 3), ((M - 16) >> 1) & 3>(S, c);
  GAP();
}
template <bool QK, bool DM, int NT, int VM, int WR, class F, int... Ms>
DI void phaseA_(AC& c, ST& S, ST& Sn, F& addr, const Src& src, unsigned ustep, const Stash& st, iseq<Ms...>) {
  PA pa; bf16x4 lo;
  (gapA<QK, DM, NT, VM, WR, Ms>(c, S, Sn, pa, addr, src, ustep, st, lo), ...);
}
template <bool QK, bool DM, int NT, int VM, int WR = -1, class F>
DI void phaseA(AC& c, ST& S, ST& Sn, F addr, const Src& src, unsigned ustep, const Stash& st) {
  phaseA_<QK, DM, NT, VM, WR>(c, S, Sn, addr, src, ustep, st, make_iseq<32>{});
}
template <bool MAX> DI float xlane(float v) {
  auto a = __builtin_amdgcn_permlane32_swap(__float_as_uint(v), __float_as_uint(v), false, false);
  v = MAX ? fmaxf(__uint_as_float(a[0]), __uint_as_float(a[1])) : __uint_as_float(a[0]) + __uint_as_float(a[1]);
  auto b = __builtin_amdgcn_permlane16_swap(__float_as_uint(v), __float_as_uint(v), false, false);
  return MAX ? fmaxf(__uint_as_float(b[0]), __uint_as_float(b[1])) : __uint_as_float(b[0]) + __uint_as_float(b[1]);
}
DI float row_max(const f32x4& x, const f32x4& y) {
  return xlane<true>(fmaxf(fmaxf(fmaxf(x[0], x[1]), fmaxf(x[2], x[3])), fmaxf(fmaxf(y[0], y[1]), fmaxf(y[2], y[3]))));
}
template <int SM, bool KR, bool DM, int NT, int VM, int WR, int M, class F>
DI void gapB(AC& c, ST& Sn, PA& pa, F& addr, const Src& src, unsigned ustep, const Stash& st, bf16x4& lo) {
  mem_pre<DM, NT, VM, (KR ? 18 : VR), M>(pa.fill);
  {
    constexpr int h = M / 18, r = M % 18, qb = 2 * h + (r & 1);
    if constexpr (r < 2) mm_vvv<SM == 3>(c.l[qb], c.ones, c.pf[qb]);
    else if constexpr (SM == 2 && r < 6) mm_vvv<false>(c.o[(r - 2) >> 1][qb], c.vf[(r - 2) >> 1], c.pf[qb]);
    else mm_avv<SM == 3>(c.o[(r - 2) >> 1][qb], c.vf[(r - 2) >> 1], c.pf[qb]);
  }
  mem_post<DM, NT, M>(src, pa.soff, ustep);
  mem_wr<WR, M>(st);
  if constexpr (M < 4) addr(pa, IC<M>{});
  if constexpr (KR && M >= 18 && M < 34 && !(M & 1)) { constexpr int f = (M - 18) >> 1; c.kf[f] = lds128(pa.rd0 + (f & 1) * 4096u + (f >> 1) * 256u); }
  constexpr int R = VM < 63 ? VR : 20;
  if constexpr (!KR && M >= R) {
    c.ra[0] = pa.rd0; c.ra[1] = pa.rd1;
    constexpr int db = (M - R) >> 1; const unsigned a = ((db & 1) ? pa.rd1 : pa.rd0) + (db >> 1) * 256u;
    if constexpr (M & 1) c.kf[db] = cat(lo, lds_tr1(a + 4096u)); else lo = lds_tr1(a);
  }
  if constexpr (SM) {
    if constexpr (M >= 4 && M < 18) sm_exp1<M - 4>(Sn);
    if constexpr (M >= 19 && M < 35 && (M & 1)) sm_exp1<14 + (M - 19) / 2>(Sn);
    if constexpr (M >= 20 && !(M & 1)) sm_cvt<((M - 20) >> 3), ((M - 20) >> 1) & 3>(Sn, c);
  }
  GAP();
}
template <int SM, bool KR, bool DM, int NT, int VM, int WR, class F, int... Ms>
DI void phaseB_(AC& c, ST& Sn, F& addr, const Src& src, unsigned ustep, const Stash& st, iseq<Ms...>) {
  PA pa; bf16x4 lo;
  (gapB<SM, KR, DM, NT, VM, WR, Ms>(c, Sn, pa, addr, src, ustep, st, lo), ...);
}
template <int SM, bool KR, bool DM, int NT, int VM, int WR = -1, class F>
DI void phaseB(AC& c, ST& Sn, F addr, const Src& src, unsigned ustep, const Stash& st) {
  phaseB_<SM, KR, DM, NT, VM, WR>(c, Sn, addr, src, ustep, st, make_iseq<36>{});
}
DI void k_read(AC& ac, unsigned ka) {
#pragma unroll
  for (int f = 0; f < 8; ++f) ac.kf[f] = lds128(ka + (f & 1) * 4096u + (f >> 1) * 256u);
}
constexpr int NW = 29;
template <bool DM, int M> DI void qk0_gap(AC& ac, ST& SA, const Src& src, unsigned soff, unsigned fill, const Stash& st) {
  constexpr int f = M >> 2, qb = M & 3;
  if constexpr (DM && (M == 0 || M == 16)) set_m0(fill + M * 256u);
  mm_vva<(f < 2)>(SA[f & 1][qb], ac.kf[f], ac.q[qb][f >> 1]);
  if constexpr (DM && qb == 0) dma_p<KV_NT, f & 3>(src.v[f & 3], src.srd, soff + (f >> 2) * KUS);
  if constexpr (!DM && qb == 0) stash_write1<NW + f>(st);
  GAP();
}
template <bool DM, int... Ms> DI void qk0(AC& ac, ST& SA, const Src& src, unsigned soff, unsigned fill, const Stash& st, iseq<Ms...>) { (qk0_gap<DM, Ms>(ac, SA, src, soff, fill, st), ...); }
template <int... Js> DI void exp_first(ST& SA, iseq<Js...>) { (sm_exp1<Js>(SA), ...); }
template <int... Js> DI void cvt_first(ST& SA, AC& c, iseq<Js...>) { (sm_cvt<(Js >> 2), (Js & 3)>(SA, c), ...); }
DI float max3(float a, float b, float c) { float r; asm("v_max3_f32 %0, %1, %2, %3" : "=v"(r) : "v"(a), "v"(b), "v"(c)); return r; }
DI float max2(float a, float b) { float r; asm("v_max_f32_e32 %0, %1, %2" : "=v"(r) : "v"(a), "v"(b)); return r; }
template <bool ZL> DI void sm_first(AC& ac, ST& SA) {
  asm volatile("s_nop 7\n\ts_nop 3" : "+v"(SA[0][0]), "+v"(SA[0][1]), "+v"(SA[0][2]), "+v"(SA[0][3]), "+v"(SA[1][0]), "+v"(SA[1][1]), "+v"(SA[1][2]), "+v"(SA[1][3]) :: "memory");
  GAP();
  float m[4], n[4];
#pragma unroll
  for (int q = 0; q < 4; ++q) { m[q] = max3(SA[0][q][0], SA[0][q][1], SA[0][q][2]); n[q] = max3(SA[1][q][0], SA[1][q][1], SA[1][q][2]); }
  GAP();
#pragma unroll
  for (int q = 0; q < 4; ++q) m[q] = max3(m[q], SA[0][q][3], SA[1][q][3]);
  GAP();
#pragma unroll
  for (int k = 0; k < 2; ++k) {
#pragma unroll
    for (int q = 0; q < 4; ++q) { const float a = m[q], b = n[q]; m[q] = max2(a, b); n[q] = max2(b, a); }
    GAP();
#pragma unroll
    for (int q = 0; q < 4; ++q) {
      auto a = k ? __builtin_amdgcn_permlane16_swap(__float_as_uint(m[q]), __float_as_uint(n[q]), false, false)
                 : __builtin_amdgcn_permlane32_swap(__float_as_uint(m[q]), __float_as_uint(n[q]), false, false);
      m[q] = __uint_as_float(a[0]); n[q] = __uint_as_float(a[1]);
    }
    GAP();
  }
#pragma unroll
  for (int q = 0; q < 4; ++q) m[q] = max2(m[q], n[q]);
  GAP();
#pragma unroll
  for (int q = 0; q < 4; ++q) {
    SA[0][q] -= m[q]; SA[1][q] -= m[q];
    ac.negm[q] = f32x4{-m[q], -m[q], -m[q], -m[q]};
    if constexpr (ZL) { ac.l[q] = f32x4{}; PINV(ac.l[q]); }
    ac.pf[q] = bf16x8{};
  }
  ac.ones = __builtin_bit_cast(bf16x8, u32x4{0x3f803f80u, 0x3f803f80u, 0x3f803f80u, 0x3f803f80u});
  asm volatile("" : "+v"(ac.negm[0]), "+v"(ac.negm[1]), "+v"(ac.negm[2]), "+v"(ac.negm[3]), "+v"(ac.ones));
  exp_first(SA, make_iseq<NEB>{});
  cvt_first(SA, ac, make_iseq<8>{});
  GAP();
}
struct BS { unsigned p[4], v[6], k[2], rk, wo, l16, w; };
constexpr bool b_dma(int f) { return f != 2 * (SK - 3) && f != 2 * (SV - 2) + 1 && f != 2 * (SP - 2) + 1 && f < 30 && f != 26; }
constexpr int b_vm(int f) {
  const int s = f == 29 ? 27 : f - 3;
  if (!b_dma(s)) return 63;
  int n = !b_dma(f) ? 0 : f == 28 ? 4 : f == 29 ? 8 : 5 + (f & 1);
  for (int i = s + 1; i < f; ++i) n += b_dma(i) ? 8 : 0;
  return n;
}
template <class KO, int I> DI void b_step(AC& ac, ST& S, ST& Sn, BS& b, const Src& sK, const Src& sV, const Src& sO, Stash& st) {
  constexpr int fa = 2 * I, fb = 2 * I + 1, vi = I == SP ? 4 : (I & 1) ? 0 : 2;
  const unsigned pa = b.p[(I & 1) ? 3 : 1], pb = b.p[(I & 1) ? 0 : 2];
  if constexpr (I == PGB) pg_walk<BSHIFT>(b.w);
  if constexpr (I >= 1 && I <= 13) barrier();
  if constexpr (I == SK - 3) st.base[0] = pa + b.l16;
  phaseA<true, b_dma(fa), (I < 14 ? KV_NT : W_NT), b_vm(fa), (I == SK - 3 ? 0 : -1)>(ac, S, Sn, [&](PA& p, auto k) __attribute__((always_inline)) {
    if (k.value) return;
    PINS(b.rk);
    p.soff = I < 14 ? ksoff(I < 13 ? I + 3 : 15, b.rk) : b.wo; p.fill = I == 14 ? b.p[0] : pa;
    p.rd0 = b.v[vi] + (I == SP ? SLOT : 0u); p.rd1 = b.v[vi + 1] + (I == SP ? SLOT : 0u); }, I < 13 ? sK : I < 14 ? sV : sO, I < 14 ? KUS : KO::US, st);
  if constexpr (I == SV - 2) st.base[0] = pb + b.l16;
  phaseB<2, (I < 14), b_dma(fb), (I < 14 ? KV_NT : W_NT), b_vm(fb), (I == SV - 2 ? 8 : -1)>(ac, Sn, [&](PA& p, auto k) __attribute__((always_inline)) {
      if (k.value) return;
      PINS(b.rk);
      p.soff = I < 14 ? ksoff(I + 2, b.rk) : b.wo + KO::CS; p.fill = I == 14 ? b.p[1] : I == 13 ? pa : pb; p.rd0 = I == 14 ? b.v[4] : b.k[(I & 1) ? 0 : 1]; p.rd1 = b.v[5]; }, I < 14 ? sV : sO, I < 14 ? KUS : KO::US, st);
}
template <int CH, int Q, int J, bool V = false> DI void o_word(const AC& c, const float (&inv)[5], bf16x8 (&nb)[4]) {
  u32x4 w = __builtin_bit_cast(u32x4, nb[Q]);
  unsigned r;
  if constexpr (V)
    asm volatile("v_mul_f32_e32 %0, %1, %3\n\tv_mul_f32_e32 %4, %2, %3\n\tv_cvt_pk_bf16_f32 %0, %0, %4"
                 : "=&v"(r) : "v"(c.o[2 * CH + (J >> 1)][Q][2 * (J & 1)]), "v"(c.o[2 * CH + (J >> 1)][Q][2 * (J & 1) + 1]), "v"(inv[Q]), "v"(inv[4]));
  else
  asm volatile("v_accvgpr_read_b32 %0, %1\n\tv_accvgpr_read_b32 %4, %2\n\tv_mul_f32_e32 %0, %0, %3\n\tv_mul_f32_e32 %4, %4, %3\n\tv_cvt_pk_bf16_f32 %0, %0, %4"
               : "=&v"(r) : "a"(c.o[2 * CH + (J >> 1)][Q][2 * (J & 1)]), "a"(c.o[2 * CH + (J >> 1)][Q][2 * (J & 1) + 1]), "v"(inv[Q]), "v"(inv[4]));
  w[J] = r;
  nb[Q] = __builtin_bit_cast(bf16x8, w);
}
template <int CH, int Q> DI void o_put(const bf16x8 (&nb)[4], unsigned ow) { asm volatile("ds_write_b128 %0, %1 offset:%2" :: "v"(ow), "v"(nb[Q]), "i"(CH * 4096 + Q * 1024) : "memory"); }
template <int E> struct CvX {
  static constexpr bool ACT = E == 3;
  const AC& c; const float (&inv)[5]; const bf16x8 (&cur)[4]; bf16x8 (&nb)[4]; unsigned ow;
  template <int G> DI void run() const {
    if constexpr (E < 3 && (G & 1)) o_word<E + 1, (G >> 3), (G >> 1) & 3>(c, inv, nb);
    if constexpr (G == 8 || G == 10) o_put<E, 2 + ((G >> 1) & 1)>(cur, ow);
    if constexpr (E < 3 && (G == 24 || G == 26)) o_put<E + 1, ((G >> 1) & 1)>(nb, ow);
  }
};
template <class K, int VM, int DM, int M> DI void gapE(AC& c, ST& S, float (&inv)[5], bf16x8 (&nb)[4], bf16x8 (&a0)[4], bf16x4& lo, const PC& pc, const Src& src, unsigned soff, unsigned long long& bad, unsigned ow) {
  constexpr int h = M / 18, r = M % 18, qb = 2 * h + (r & 1);
  if constexpr (M == 24) wait_vm<VM>();
  constexpr int D = M - (DM == 2 ? 4 : 0);
  constexpr int NR = DM == 2 ? 0 : VR - 20;
  if constexpr (M == NR + 4) wait_lgkm0();
  if constexpr (M == 6 && DM == 2) barrier();
  if constexpr (DM && (D == 3 || D == 19)) set_m0(pc.r2 + (D == 19 ? 4096u : 0u));
  if constexpr (r < 2) mm_vvv<false>(c.l[qb], c.ones, c.pf[qb]);
  else if constexpr (DM == 1 && r < 6) mm_vvv<false>(c.o[(r - 2) >> 1][qb], c.kf[(r - 2) >> 1], c.pf[qb]);
  else mm_avv(c.o[(r - 2) >> 1][qb], c.kf[(r - 2) >> 1], c.pf[qb]);
  if constexpr (DM && D > 0 && D < 32 && (D & 3) == 3) dma_p<W_NT, (D >> 2) & 3>(src.v[(D >> 2) & 3], src.srd, soff + (D >= 16 ? K::US : 0u));
  if constexpr (M < NR) {
    constexpr int db = (M + 16 - NR) >> 1; const unsigned a = c.ra[db & 1] + (db >> 1) * 256u;
    if constexpr (M & 1) c.kf[db] = cat(lo, lds_tr1(a + 4096u)); else lo = lds_tr1(a);
  }
  if constexpr (M >= 24 && M < 32) {
    constexpr int d = (M - 24) >> 1; const unsigned a = ring_rd<0>(pc, d);
    if constexpr (M & 1) a0[d] = cat(lo, lds_tr1(a + K::TS)); else lo = lds_tr1(a);
  }
  if constexpr (NEB + M < 32) sm_exp1<NEB + M>(S);
  if constexpr (M >= 10 && M < 14) { sm_cvt<2 + ((M - 10) >> 1), 2 * (M & 1)>(S, c); sm_cvt<2 + ((M - 10) >> 1), 2 * (M & 1) + 1>(S, c); }
  if constexpr (M == 14 || M == 15 || M == 22 || M == 23) asm volatile("v_rcp_f32_e32 %0, %1" : "=v"(inv[qb]) : "v"(c.l[qb][0]));
  if constexpr (M >= 16 && M < 22) o_word<0, ((M - 16) >> 2), (M & 3), (DM == 1)>(c, inv, nb);
  if constexpr (M >= 24 && M < 34) o_word<0, ((M - 18) >> 2), ((M - 18) & 3), (DM == 1)>(c, inv, nb);
  if constexpr (M == 32) o_put<0, 0>(nb, ow);
  if constexpr (M == 33) o_put<0, 1>(nb, ow);
  if constexpr (r == 4) asm volatile("" :: "v"(c.l[2 * h]), "v"(c.l[2 * h + 1]));
  if constexpr (M == 34)
    asm volatile("v_add_f32_e32 %1, %2, %3\n\tv_add_f32_e32 %1, %1, %4\n\tv_add_f32_e32 %1, %1, %5\n\tv_cmp_nle_f32_e64 %0, %1, %6"
                 : "=s"(bad) : "v"(inv[4]), "v"(c.l[0][0]), "v"(c.l[1][0]), "v"(c.l[2][0]), "v"(c.l[3][0]), "s"(LMAX));
  GAP();
}
template <class K, int VM, int DM, int... Ms> DI void phaseE(AC& c, ST& S, float (&inv)[5], bf16x8 (&nb)[4], bf16x8 (&a0)[4], const PC& pc, const Src& src, unsigned soff, unsigned long long& bad, unsigned ow, iseq<Ms...>) {
  bf16x4 lo;
#pragma unroll
  for (int q = 0; q < 4; ++q) nb[q] = bf16x8{};
  (gapE<K, VM, DM, Ms>(c, S, inv, nb, a0, lo, pc, src, soff, bad, ow), ...);
}
struct Pub { u32x4 pk[16]; srd_t srd; unsigned v[4], so; };
struct TL { bf16x8 w[2][3][4], x[2][3][4]; bf16x4 lo[2]; unsigned xa[3], xs[3], drop[2], l16; };
template <int S, int QB, int J> DI void pub_word(const f32x4 (&acc)[8][4], Pub& p) {
  const f32x4& a = acc[4 * S + (J >> 1)][QB];
  unsigned r;
  asm volatile("v_cvt_pk_bf16_f32 %0, %1, %2" : "=v"(r) : "v"(a[2 * (J & 1)]), "v"(a[2 * (J & 1) + 1]));
  p.pk[8 * S + 2 * QB + (J >> 2)][J & 3] = r;
}
template <int F> DI void pub_store(const Pub& p) {
  asm volatile("buffer_store_dwordx4 %0, %1, %2, %3 offen offset:%4 sc0 sc1" :: "v"(p.pk[F]), "v"(p.v[F >> 2]), "s"(p.srd), "s"(p.so), "i"((F & 3) * 1024) : "memory");
}
template <class K, int S, int E, int R> DI void tl_w(TL& t, const PC& c) {
  const unsigned a = ring_rd<(E + 1) % 3>(c, R >> 1) + S * 4096u;
  if constexpr (R & 1) t.w[S][E][R >> 1] = cat(t.lo[S], lds_tr1(a + K::TS)); else t.lo[S] = lds_tr1(a);
}
template <class K, int G> DI void tl_gap(f32x4 (&acc)[8][4], TL& t, const PC& c, Pub& p) {
  constexpr int S = G >= 48, qb = G < 16 ? G >> 2 : G < 48 ? (G - 16) >> 3 : (G - 48) / 12, e = G < 16 ? 0 : G < 48 ? 1 + ((G >> 2) & 1) : G % 3, db = G < 48 ? G & 3 : (G % 12) / 3;
  if constexpr (G == 10) wait_vm<4>();
  if constexpr (G == 27) wait_vm<0>();
  mm_vvv<false>(acc[4 * S + db][qb], t.w[S][e][db], t.x[S][e][qb]);
  if constexpr (G >= 28 && G <= 56 && !(G & 3)) pub_store<(G - 28) / 4>(p);
  if constexpr (G >= 59 && G % 6 == 5) pub_store<8 + (G - 59) / 6>(p);
  if constexpr (G < 8) tl_w<K, 0, 1, G>(t, c);
  if constexpr (G >= 10 && G < 18) tl_w<K, 0, 2, G - 10>(t, c);
  if constexpr (G >= 16 && G < 24) tl_w<K, 1, 0, G - 16>(t, c);
  if constexpr (G >= 25 && G < 56 && (G & 1)) tl_w<K, 1, 1 + (((G - 25) >> 2) & 1), 2 * ((G - 25) >> 3) + (((G - 25) >> 1) & 1)>(t, c);
  if constexpr (G < 34 && (G & 3) == 2) t.x[0][1 + ((G >> 2) & 1)][G >> 3] = lds128(t.xa[1 + ((G >> 2) & 1)] + (G >> 3) * K::QS);
  if constexpr (G >= 34 && G < 82 && (G & 3) == 2) t.x[1][((G - 34) % 12) >> 2][(G - 34) / 12] = lds128(t.xa[((G - 34) % 12) >> 2] + ((G - 34) / 12) * K::QS);
  if constexpr (G >= 24 && G < 56) pub_word<0, ((G - 24) >> 3), (G & 7)>(acc, p);
  if constexpr (G >= 54 && G % 3 < 2) pub_word<1, (G - 54) / 12, 2 * (((G - 54) % 12) / 3) + G % 3>(acc, p);
  if constexpr (G == 0 || G == 1 || G == 3) { t.xa[(G + 1) & 3] = t.xs[(G + 1) & 3] | t.l16; PINV(t.xa[(G + 1) & 3]); }
  if constexpr (G == 4 || G == 5) { p.v[2 * (G & 1)] = t.drop[G & 1] | t.l16; PINV(p.v[2 * (G & 1)]); }
  if constexpr (G == 7 || G == 8) { p.v[2 * (G & 1) + 1] = p.v[2 * (G & 1)] | 4096u; PINV(p.v[2 * (G & 1) + 1]); }
  GAP();
}
template <class K, int... Gs> DI void tl_run(f32x4 (&acc)[8][4], TL& t, const PC& c, Pub& p, iseq<Gs...>) {
  wait_vm_lgkm<12, 2>();
  GAP();
  (tl_gap<K, Gs>(acc, t, c, p), ...);
  pub_word<1, 3, 4>(acc, p); pub_word<1, 3, 5>(acc, p);
  asm volatile("s_nop 4");
  pub_word<1, 3, 6>(acc, p); pub_word<1, 3, 7>(acc, p); pub_store<15>(p);
}
template <int I> DI void own_put1(const u32x4& v, unsigned a) { asm volatile("ds_write_b128 %0, %1 offset:%2 ; %3" :: "v"(a), "v"(v), "i"((I & 7) * 1024), "i"(I) : "memory"); }
template <int B, int... Is> DI void own_put(const Pub& p, unsigned a, iseq<Is...>) { (own_put1<B + Is>(p.pk[B + Is], a), ...); }
DI void redo_head(const AC& ac, const char* __restrict__ Kp, const char* __restrict__ Vp, unsigned lane, unsigned ow) {
  const unsigned r16 = lane & 15u, g = lane >> 4;
#pragma unroll 1
  for (unsigned qb = 0; qb < 4; ++qb) {
    bf16x8 qq[4];
#pragma unroll
    for (int s = 0; s < 4; ++s) qq[s] = qb == 0 ? ac.q[0][s] : qb == 1 ? ac.q[1][s] : qb == 2 ? ac.q[2][s] : ac.q[3][s];
    f32x4 o[8];
#pragma unroll
    for (int d = 0; d < 8; ++d) o[d] = f32x4{};
    float m = -__builtin_inff(), l = 0.f;
#pragma unroll 1
    for (unsigned t = 0; t < 16; ++t) {
      f32x4 s0 = {}, s1 = {};
      const char* kp = Kp + (t * 32u + r16) * 256u + g * 16u;
#pragma unroll
      for (int s = 0; s < 4; ++s) {
        s0 = __builtin_amdgcn_mfma_f32_16x16x32_bf16(*(const bf16x8*)(kp + s * 64), qq[s], s0, 0, 0, 0);
        s1 = __builtin_amdgcn_mfma_f32_16x16x32_bf16(*(const bf16x8*)(kp + s * 64 + 4096), qq[s], s1, 0, 0, 0);
      }
      const float mn = fmaxf(m, row_max(s0, s1)), al = __builtin_amdgcn_exp2f(m - mn);
      m = mn;
#pragma unroll
      for (int j = 0; j < 4; ++j) { s0[j] = __builtin_amdgcn_exp2f(s0[j] - mn); s1[j] = __builtin_amdgcn_exp2f(s1[j] - mn); }
      l = l * al + ((s0[0] + s0[1]) + (s0[2] + s0[3])) + ((s1[0] + s1[1]) + (s1[2] + s1[3]));
      const bf16x8 pf = __builtin_bit_cast(bf16x8, pack2(s0, s1));
      const __bf16* vp = (const __bf16*)(Vp + (t * 32u + g * 4u) * 256u) + r16;
#pragma unroll
      for (int d = 0; d < 8; ++d) {
        bf16x8 vf;
#pragma unroll
        for (int j = 0; j < 8; ++j) vf[j] = vp[((j & 3) + (j >> 2) * 16) * 128 + d * 16];
        o[d] = __builtin_amdgcn_mfma_f32_16x16x32_bf16(vf, pf, o[d] * al, 0, 0, 0);
      }
    }
    const float inv = 1.0f / xlane<false>(l);
#pragma unroll
    for (int s = 0; s < 4; ++s) *(LAS u32x4*)(size_t)(ow + s * 4096u + qb * 1024u) = pack2(o[2 * s] * inv, o[2 * s + 1] * inv);
  }
}
template <bool isA, int J> DI void q_conv1(AC& ac, f32x4 (&acc)[8][4], Stash& st, const Src& sK, const Src& sV, unsigned rk, unsigned p3, u32x4 (&t)[2]) {
  constexpr int s = J >> 2, qb = J & 3;
  if constexpr (isA) { if constexpr (2 * J < NW) stash_write1<2 * J>(st); }
  else {
    constexpr int p = 2 * (J & 1);
    const Src& src = s < 2 ? sK : sV;
    const unsigned so = ksoff(s == 0 ? 2 : s == 1 ? SK : s == 2 ? SV : SP, rk) + ((J >> 1) & 1) * KUS;
    if constexpr (s == 0 || s == 3) {
      if constexpr (p == 0) asm volatile("s_mov_b32 m0, %0\n\ts_nop 0" :: "s"(p3 + (s == 3 ? SLOT : 0u) + (J & 2) * 2048u) : "memory");
      dma_p<KV_NT, p>(src.v[p], src.srd, so); dma_p<KV_NT, p + 1>(src.v[p + 1], src.srd, so);
    } else {
      asm volatile("buffer_load_dwordx4 %0, %1, %2, %3 offen offset:%4 nt" : "=a"(st.p[2 * J - 8]) : "v"(src.v[p]), "s"(src.srd), "s"(so), "i"(1024 * p) : "memory");
      asm volatile("buffer_load_dwordx4 %0, %1, %2, %3 offen offset:%4 nt" : "=a"(st.p[2 * J - 7]) : "v"(src.v[p + 1]), "s"(src.srd), "s"(so), "i"(1024 * p + 1024) : "memory");
    }
  }
  t[J & 1] = pack2(acc[2 * s][qb] * SC, acc[2 * s + 1][qb] * SC);
  PINV(t[J & 1]);
  GAP();
  if constexpr (J & 1) {
#pragma unroll
    for (int k = 0; k < 2; ++k) {
#pragma unroll
      for (int i = 0; i < 4; ++i) {
        u32x4& w = t[i >> 1];
        auto a = k ? __builtin_amdgcn_permlane16_swap(w[i & 1], w[2 + (i & 1)], false, false) : __builtin_amdgcn_permlane32_swap(w[i & 1], w[2 + (i & 1)], false, false);
        w[i & 1] = a[0]; w[2 + (i & 1)] = a[1];
      }
      if constexpr (isA) { if (k == 0) { if constexpr (2 * J - 1 < NW) stash_write1<2 * J - 1>(st); } else { if constexpr (2 * J + 1 < NW) stash_write1<2 * J + 1>(st); } }
      GAP();
    }
    ac.q[qb - 1][s] = __builtin_bit_cast(bf16x8, t[0]); ac.q[qb][s] = __builtin_bit_cast(bf16x8, t[1]);
    asm volatile("" : "+a"(ac.q[qb - 1][s]), "+a"(ac.q[qb][s]));
    GAP();
  }
}
template <bool isA, int... Js> DI void q_conv(AC& ac, f32x4 (&acc)[8][4], Stash& st, const Src& sK, const Src& sV, unsigned rk, unsigned p3, iseq<Js...>) { u32x4 t[2]; (q_conv1<isA, Js>(ac, acc, st, sK, sV, rk, p3, t), ...); }
template <int N, int N0 = 0> DI void zero_o(AC& ac) {
#pragma unroll
  for (int i = N0; i < N; ++i)
#pragma unroll
    for (int q = 0; q < 4; ++q) { ac.o[i][q] = f32x4{}; asm volatile("" : "+a"(ac.o[i][q])); }
}

template <bool isA>
DI void body(const char* __restrict__ X, const char* __restrict__ K, const char* __restrict__ V, const char* __restrict__ Wq, const char* __restrict__ Wo,
             const char* __restrict__ Wo2, unsigned long long tnow, char* __restrict__ part, char* __restrict__ out) {
  const unsigned tlim = (unsigned)tnow + EASY_T;
  unsigned tid = threadIdx.x;
  PINV(tid);
  const unsigned lane = tid & 63u, w = rfl(tid >> 6);
  const unsigned bid = blockIdx.x, xg = bid & 7u, rr = bid >> 3, b = rr & 15u, h = xg * 4u + w;
  const unsigned r16 = lane & 15u, g = lane >> 4;
  const size_t kvo = isA ? (size_t)(b * 8u + xg) * 131072u : (size_t)(b * 32u + h) * 131072u;
  const unsigned rk = isA ? 7u * b : 5u * b + 8u * w;
  typedef PK<512u, 4096u, 0x1300u, 0xF300u, 0xC300u, 128u, 32u * 8192u, true> KQ;
  typedef PK<512u, 4096u, 0x1300u, 0xF300u, 0xC300u, KUS, 8192u, false> KT;
  typedef PK<2048u, 1024u, 0x1000u, 0xF000u, 0xF000u, 128u, 32u * 1024u, false> KO;
  PC pc;
  pc.r01 = 65536u + w * 16384u; pc.r2 = 131072u + w * 8192u;
  const unsigned zq = ((b & 3u) << 14) | ((b >> 2) << 8), co = 4u * w;
  const srd_t srdH = mk_srd(part + HOFF, 4096u + 256u * 128u);
  const unsigned tok_off = rr * 128u, flg_off = 4096u + rr * 1024u;
  const unsigned l8 = (lane & 7u) * 4u;
  unsigned entry_flags, dum0 = lane;
  Src sQ, sX, sK, sV, sO;
  const unsigned p0 = b & 3u, p1 = (p0 + 1u) & 3u, p2 = (p0 + 2u) & 3u, p3 = (p0 + 3u) & 3u, xw = w * 4096u;
  Stash st;
  mk_src_kv<isA, false>(sK, K + kvo, lane, w);
  mk_src_kv<isA, true>(sV, V + kvo, lane, w);
  if constexpr (isA) { stash_load(st, sK, sV, rk, ((w & 1u) << GA.p1) + ((w >> 1) << GA.u), make_iseq<64>{}); GAP(); }
  asm volatile("buffer_load_dword %0, %1, %2, %3 offen sc1" : "=v"(entry_flags) : "v"(l8), "s"(srdH), "s"(flg_off + xg * 128u) : "memory");
  sQ.srd = mk_srd(Wq, 4194304u); sX.srd = mk_srd(X, 1048576u);
  mk_w<false>(sQ, h * 256u, 8192u, lane);
  mk_rows<false>(sX, b * 65536u + w * 16384u, 1024u, lane);
  sO.srd = mk_srd(Wo + (size_t)xg * 524288u, 524288u);
  mk_w<true>(sO, w * 256u, 1024u, lane);
  if constexpr (isA) {
    dma_unit<X_NT>(sX, p0 * 256u, xw + p0 * 16384u);
    dma_elem<W_NT>(sQ, q_soff(zq + 0x2200u), KQ::US, pc.r01 + SLOT);
    dma_unit<X_NT>(sX, p1 * 256u, xw + p1 * 16384u);
    dma_unit<X_NT>(sX, p2 * 256u, xw + p2 * 16384u);
    dma_unit<X_NT>(sX, p3 * 256u, xw + p3 * 16384u);
  }
  srd_t pb_srd = mk_srd(part + (size_t)rr * 524288u, 524288u);
  unsigned pb_so = xg * 65536u + w * 16384u, drop0 = 2u * w == xg ? 0x80000000u : 0u, drop1 = 2u * w + 1u == xg ? 0x80002000u : 0x2000u;
  unsigned xs0 = ((co + 13u) & 15u) << 12, xs1 = ((co + 14u) & 15u) << 12, xs2 = ((co + 15u) & 15u) << 12, oz = (co + 3u) << 12, os3 = (co + 3u) * KO::CS;
  unsigned ks0 = ksoff(0u, rk), ks1 = ksoff(1u, rk);
  {
    PINS(pb_srd); PINS(pb_so); PINS(drop0); PINS(drop1); PINS(xs0); PINS(xs1); PINS(xs2); PINS(oz); PINS(os3); PINS(sO.srd);
    if constexpr (!isA) {
      PINS(sK.srd); PINS(sV.srd); PINS(ks0); PINS(ks1);
#pragma unroll
      for (int i = 0; i < 4; ++i) { PINV(sK.v[i]); PINV(sV.v[i]); PINV(sO.v[i]); }
    }
  }
  const unsigned rb = 1024u * (r16 >> 2) + 64u * (r16 & 3u) + 16u * (g ^ ((r16 >> 2) & 2u));
  const unsigned tb = 64u * ((lane >> 2) & 3u) + 32u * (g & 1u) + 8u * (lane & 3u);
  const unsigned tbV = tb + 1024u * g;
  const unsigned sb0 = w * 2048u + lane * 16u;
  st.base[0] = sb0; st.base[1] = sb0 + 65536u; st.base[2] = sb0 + 131072u;
  AC ac;
  if constexpr (!isA) zero_o<8, 2>(ac);
  {
    f32x4 acc[8][4];
    bf16x8 a0[4], a1[4], bf[4];
    if constexpr (isA) pg_walk<AWSH>(w);
    wait_vm<(isA ? 20 : 9)>();
    barrier();
    proj_start<KQ, 2>(a0, bf, pc, tbV, rb, zq, q_soff(zq + 0x3100u));
    if constexpr (isA) {
      proj_elem<KQ, 2, true,  false, true, W_NT, 63, 63>(acc, a0, a1, bf, pc, sQ);
      proj_elem<KQ, 0, false, false, true, W_NT, 63, 28>(acc, a0, a1, bf, pc, sQ);
      proj_elem<KQ, 1, false, false, true, W_NT, 28, 16>(acc, a0, a1, bf, pc, sQ);
      proj_elem<KQ, 2, false, false, true, W_NT, 16, 16, true>(acc, a0, a1, bf, pc, sQ);
    } else {
      dma_elem<W_NT>(sQ, q_soff(zq + 0x2200u), KQ::US, pc.r01 + SLOT);
      proj_elem<KQ, 2, true,  false, true, W_NT, 13, 20, false, true, true>(acc, a0, a1, bf, pc, sQ, NoX{}, &sX, p1 * 256u, xw + p1 * 16384u, p2 * 256u, xw + p2 * 16384u);
      proj_elem<KQ, 0, false, false, true, W_NT, 24, 28, false, true, false>(acc, a0, a1, bf, pc, sQ, NoX{}, &sX, p3 * 256u, xw + p3 * 16384u);
      proj_elem<KQ, 1, false, false, true, W_NT, 28, 24>(acc, a0, a1, bf, pc, sQ);
      proj_elem<KQ, 2, false, false, true, W_NT, 20, 16, true>(acc, a0, a1, bf, pc, sQ);
    }
    typedef std::conditional_t<isA, EasyX<EASY, EASY_L>, NoX> PX;
    const PX px = [&] { if constexpr (isA) return EasyX<EASY, EASY_L>{tnow, tlim}; else return NoX{}; }();
#pragma unroll 1
    for (unsigned e = 0; e < 3; ++e) {
      proj_elem<KQ, 0, false, false, true, W_NT, 16, 16>(acc, a0, a1, bf, pc, sQ, px);
      proj_elem<KQ, 1, false, false, true, W_NT, 16, 16>(acc, a0, a1, bf, pc, sQ, px);
      proj_elem<KQ, 2, false, false, true, W_NT, 16, 16>(acc, a0, a1, bf, pc, sQ, px);
    }
    if constexpr (isA) {
      proj_elem<KQ, 0, false, false, false, W_NT, 16, 12>(acc, a0, a1, bf, pc, sQ, px);
      proj_elem<KQ, 1, false, false, false, W_NT, 8, 4>(acc, a0, a1, bf, pc, sQ, px);
      proj_elem<KQ, 2, false, true, false, W_NT, 0, 0>(acc, a0, a1, bf, pc, sQ, px);
    } else {
      pc.soff = ks0;
      proj_elem<KT, 0, false, false, true, KV_NT, 16, 16>(acc, a0, a1, bf, pc, sK);
      pc.soff = ks1;
      proj_elem<KT, 1, false, false, true, KV_NT, 16, 16>(acc, a0, a1, bf, pc, sK);
      pc.soff = ks0;
      proj_elem<KT, 2, false, true, true, KV_NT, 16, 16>(acc, a0, a1, bf, pc, sV);
    }
    PINV(entry_flags);
    if (w == 0) {
      const unsigned long long mk = __builtin_amdgcn_ballot_w64(((entry_flags >> (lane & 7u)) & 1u) != 0u);
      const unsigned tok = 0x5a5a5a00u | (~(unsigned)mk & 0xffu);
      if (lane == 0) asm volatile("buffer_store_dword %0, off, %1, %2 sc1" :: "v"(tok), "s"(srdH), "s"(tok_off + xg * 4u) : "memory");
    }
    barrier();
    acc_fence_v(acc);
    q_conv<isA>(ac, acc, st, sK, sV, rk, w * 16384u, make_iseq<16>{});
  }
  ST SA, SB;
  if constexpr (isA) {
    const unsigned kb = rb, vb = SLOT + tbV;
    unsigned dum = lane, to = ((lane & 8u) * 16u + (lane & 7u) * 4u + (lane >> 4) * 32768u) | (((lane >> 4) ^ w) << 8);
    const unsigned tbo = (b & 3u) * 1024u + (b >> 2) * 131072u;
    PINV(to);
    const char* ta = Wo + (size_t)xg * 524288u + tbo;
    const char* tb2 = Wo2 + (size_t)xg * 524288u + tbo;
    touch(dum, to, ta);
    asm volatile("s_waitcnt lgkmcnt(0)" ::: "memory");
    barrier();
    k_read(ac, kb + kv_pair(0) * 16384u);
    wait_lgkm0();
    GAP();
    qk0<false>(ac, SA, sK, 0u, 0u, st, make_iseq<32>{});
    k_read(ac, kb + kv_pair(1) * 16384u);
    touch(dum, to, ta);
    sm_first<!isA>(ac, SA);
    unsigned i4 = 0;
    unsigned blk; const char* tp;
    auto aw = [&](PA& p, auto k) __attribute__((always_inline)) {
      if constexpr (k.value == 0) { PINS(i4); blk = (unsigned)((kv_tab() >> 8) >> i4) << 14; i4 += 4u; PINS(blk); PINS(i4); }
      if constexpr (k.value == 1) { p.rd0 = kb + (blk & 0x3c000u); PINV(p.rd0); }
    };
    auto ak = [&](PA& p, auto k) __attribute__((always_inline)) { if constexpr (k.value == 0) __builtin_amdgcn_s_sleep(PACE_ATT); aw(p, k); };
    auto at = [&](PA& p, auto k) __attribute__((always_inline)) {
      if constexpr (k.value == 0) { PINS(i4); blk = (unsigned)(kv_tab() >> i4) << 14; PINS(blk); }
      if constexpr (k.value == 1) { p.rd0 = vb + (blk & 0x3c000u); PINV(p.rd0); }
      if constexpr (k.value == 2) asm volatile("s_cmp_lt_u32 %1, 24\n\ts_cselect_b64 %0, %2, %3" : "=s"(tp) : "s"(i4), "s"(ta), "s"(tb2 - 32768) : "scc");
      if constexpr (k.value == 3) { p.rd1 = p.rd0 ^ 32u; PINV(p.rd1); asm volatile("global_load_dword %0, %1, %2 nt" : "+v"(dum) : "v"(to), "s"(tp) : "memory"); }
      if constexpr (k.value == 4) asm volatile("global_load_dword %0, %1, %2\n\tv_add_u32_e32 %1, %3, %1" : "+v"(dum), "+v"(to) : "s"(tp), "i"(4096) : "memory");
    };
    phaseA<true, false, KV_NT, 63, NW + 8>(ac, SA, SB, at, sK, 0u, st);
    phaseB<3, true, false, KV_NT, 63>(ac, SB, ak, sK, 0u, st);
    phaseA<true, false, KV_NT, 63>(ac, SB, SA, at, sK, 0u, st);
    phaseB<true, true, false, KV_NT, 63>(ac, SA, ak, sK, 0u, st);
    barrier();
    while (i4 < 24u) {
      phaseA<true, false, KV_NT, 63>(ac, SA, SB, at, sK, 0u, st);
      phaseB<true, true, false, KV_NT, 63>(ac, SB, ak, sK, 0u, st);
      phaseA<true, false, KV_NT, 63>(ac, SB, SA, at, sK, 0u, st);
      phaseB<true, true, false, KV_NT, 63>(ac, SA, ak, sK, 0u, st);
    }
    barrier();
    phaseA<true, false, KV_NT, 63, 40>(ac, SA, SB, at, sK, 0u, st);
    phaseB<true, true, false, KV_NT, 63, 43>(ac, SB, aw, sK, 0u, st);
    phaseA<true, false, KV_NT, 63, 46>(ac, SB, SA, at, sK, 0u, st);
    phaseB<true, true, false, KV_NT, 63, 49>(ac, SA, aw, sK, 0u, st);
    wait_vm_lgkm<8, 8>(); barrier();
    phaseA<true, false, KV_NT, 63, 52>(ac, SA, SB, at, sK, 0u, st);
    phaseB<true, true, false, KV_NT, 63, 55>(ac, SB, aw, sK, 0u, st);
    phaseA<true, false, KV_NT, 63, 58>(ac, SB, SA, at, sK, 0u, st);
    phaseB<true, true, false, KV_NT, 63, 61>(ac, SA, aw, sK, 0u, st);
    wait_vm_lgkm<63, 8>(); barrier();
    while (i4 < 56u) {
      phaseA<true, false, KV_NT, 63>(ac, SA, SB, at, sK, 0u, st);
      phaseB<true, true, false, KV_NT, 63>(ac, SB, ak, sK, 0u, st);
      phaseA<true, false, KV_NT, 63>(ac, SB, SA, at, sK, 0u, st);
      phaseB<true, true, false, KV_NT, 63>(ac, SA, ak, sK, 0u, st);
    }
    barrier();
    unsigned v14 = vb + kv_pair(14) * 16384u, x14 = v14 ^ 32u; PINV(v14); PINV(x14);
    phaseA<true, true, W_NT, 63>(ac, SA, SB, [&](PA& p, auto) { p.soff = co * KO::CS; p.fill = pc.r01; p.rd0 = v14; p.rd1 = x14; }, sO, KO::US, st);
    phaseB<true, false, true, W_NT, 63>(ac, SB, [&](PA& p, auto k) __attribute__((always_inline)) {
      p.soff = (co + 1u) * KO::CS; p.fill = pc.r01 + SLOT; p.rd0 = v14 + 16384u; p.rd1 = x14 + 16384u;
      if constexpr (k.value == 1) proj_setup<KO>(pc, tb + 512u * g, lane * 16u, oz, os3); }, sO, KO::US, st);
    dum0 = dum;
  } else {
    const unsigned P0 = pc.r01, P1 = pc.r01 + SLOT, P2 = pc.r2, P3 = w * 16384u;
    unsigned v0 = tbV + P0, v2 = tbV + P2, x0 = v0 ^ 32u, x2 = v2 ^ 32u, k1 = rb + P1, k3 = rb + P3;
    PINV(v0); PINV(v2); PINV(x0); PINV(x2); PINV(k1); PINV(k3);
    wait_vm<48>();
    k_read(ac, k1 - SLOT);
#pragma unroll
    for (int i = 0; i < 8; ++i) { ac.o[i >> 2][i & 3] = f32x4{}; PINV(ac.o[i >> 2][i & 3]); }
    wait_lgkm0();
    GAP();
    qk0<true>(ac, SA, sV, ks1, P0, st, make_iseq<32>{});
    wait_vm<48>();
    GAP();
    k_read(ac, k1);
    sm_first<!isA>(ac, SA);
    BS bs{{P0, P1, P2, P3}, {v0, x0, v2, x2, tbV + P3, (tbV + P3) ^ 32u}, {k1, k3}, rk, co * KO::CS, lane * 16u, w};
    PINV(bs.v[4]); PINV(bs.v[5]);
    proj_setup<KO>(pc, tb + 512u * g, lane * 16u, oz, os3);
#define B2(I) b_step<KO, I>(ac, SA, SB, bs, sK, sV, sO, st); b_step<KO, I + 1>(ac, SB, SA, bs, sK, sV, sO, st);
    B2(0) B2(2) B2(4) B2(6) B2(8) B2(10)
    B2(12) b_step<KO, 14>(ac, SA, SB, bs, sK, sV, sO, st);
#undef B2
  }
  {
    const unsigned l16 = lane * 16u;
    unsigned ow = w * 16384u + l16;
    PINV(ow);
    f32x4 acc[8][4];
    bf16x8 a0[4], a1[4], b0[4], bf[4];
    float inv[5];
    asm volatile("" : "=v"(inv[4]));
    unsigned toks;
    asm volatile("buffer_load_dword %0, %1, %2, %3 offen sc1" : "=v"(toks) : "v"(l8), "s"(srdH), "s"(tok_off) : "memory");
    GAP();
    unsigned long long bad;
    phaseE<KO, (isA ? 18 : 19), (isA ? 2 : 1)>(ac, SB, inv, b0, a0, pc, sO, os3 - KO::CS, bad, ow, make_iseq<36>{});
    if (__builtin_expect(bad != 0ull, 0)) {
      asm volatile("s_waitcnt vmcnt(0)" : "+v"(toks) :: "memory");
      acc_fence_a(ac.o);
      redo_head(ac, K + kvo, V + kvo, lane, ow);
#pragma unroll
      for (int q = 0; q < 4; ++q) {
        inv[q] = 1.0f;
        b0[q] = lds128(ow + q * 1024u);
#pragma unroll
        for (int s = 1; s < 4; ++s) {
          const u32x4 t = __builtin_bit_cast(u32x4, lds128(ow + s * 4096u + q * 1024u));
          ac.o[2 * s][q] = f32x4{__uint_as_float(t[0] << 16), __uint_as_float(t[0] & 0xffff0000u), __uint_as_float(t[1] << 16), __uint_as_float(t[1] & 0xffff0000u)};
          ac.o[2 * s + 1][q] = f32x4{__uint_as_float(t[2] << 16), __uint_as_float(t[2] & 0xffff0000u), __uint_as_float(t[3] << 16), __uint_as_float(t[3] & 0xffff0000u)};
        }
      }
      wait_lgkm0();
      acc_fence_a(ac.o);
    }
#pragma unroll
    for (int q = 0; q < 4; ++q) bf[q] = bf16x8{};
    GAP();
    proj_elem<KO, 0, true, false, true, W_NT, 16, 16>(acc, a0, a1, b0, pc, sO, CvX<0>{ac, inv, b0, bf, ow});
    proj_elem<KO, 1, false, false, true, W_NT, 16, 16>(acc, a0, a1, bf, pc, sO, CvX<1>{ac, inv, bf, b0, ow});
    proj_elem<KO, 2, false, false, true, W_NT, 16, 16>(acc, a0, a1, b0, pc, sO, CvX<2>{ac, inv, b0, bf, ow});
    if constexpr (isA) PINV(dum0);
    proj_elem<KO, 0, false, false, true, W_NT, 16, 16, true>(acc, a0, a1, bf, pc, sO, CvX<3>{ac, inv, bf, b0, ow});
    typedef std::conditional_t<isA, EasyX<EASY2, EASY2_T - EASY2_F>, NoX> PX;
    const PX px = [&] { if constexpr (isA) return EasyX<EASY2, EASY2_T - EASY2_F>{tnow, tlim + (EASY2_F - EASY_T)}; else return NoX{}; }();
#pragma unroll 1
    for (unsigned e = 0; e < 3; ++e) {
      proj_elem<KO, 1, false, false, true, W_NT, 16, 16>(acc, a0, a1, bf, pc, sO, px);
      proj_elem<KO, 2, false, false, true, W_NT, 16, 16>(acc, a0, a1, bf, pc, sO, px);
      proj_elem<KO, 0, false, false, true, W_NT, 16, 16>(acc, a0, a1, bf, pc, sO, px);
    }
    Pub pb;
    pb.srd = pb_srd; pb.so = pb_so;
#pragma unroll
    for (int i = 0; i < 16; ++i) pb.pk[i] = u32x4{};
    TL tl;
#pragma unroll
    for (int i = 0; i < 4; ++i) { tl.w[0][0][i] = a0[i]; tl.x[0][0][i] = bf[i]; }
    tl.xs[0] = xs0; tl.xs[1] = xs1; tl.xs[2] = xs2; tl.drop[0] = drop0; tl.drop[1] = drop1; tl.l16 = l16;
    tl_run<KO>(acc, tl, pc, pb, make_iseq<96>{});
    const unsigned lr = ((lane + xg) & 7u) * 4u;
    unsigned fl;
    asm volatile("buffer_load_dword %0, %1, %2, %3 offen sc1" : "=v"(fl) : "v"(lr), "s"(srdH), "s"(flg_off + xg * 128u) : "memory");
    PINV(toks);
    if (w == (xg >> 1)) {
      const unsigned a = pc.r01 + SLOT + l16;
      if (xg & 1u) own_put<8>(pb, a, make_iseq<8>{}); else own_put<0>(pb, a, make_iseq<8>{});
    }
    const unsigned fo = lane * 128u + xg * 4u, tau = __builtin_amdgcn_readlane(toks, xg);
    unsigned oo = ((b * 64u + 16u * w + r16) * 1024u + (isA ? 0u : 512u) + 64u * xg + 8u * g) * 2u;
    PINV(oo);
    wait_lgkm0();
    barrier();
    const unsigned oa = 65536u + SLOT + (xg >> 1) * 16384u + w * 2048u + l16;
    const u32x4 r0 = __builtin_bit_cast(u32x4, lds128(oa)), r1 = __builtin_bit_cast(u32x4, lds128(oa + 1024u));
    GAP();
    wait_vm<9>();
    if (lane == 2u * w) asm volatile("buffer_store_dword %0, %1, %2, %3 offen sc1" :: "v"(toks), "v"(fo), "s"(srdH), "s"(flg_off) : "memory");
    wait_vm<2>();
    if (lane == 2u * w + 1u) asm volatile("buffer_store_dword %0, %1, %2, %3 offen sc1" :: "v"(toks), "v"(fo), "s"(srdH), "s"(flg_off) : "memory");
    f32x16 f[8];
#pragma unroll
    for (int j = 0; j < 8; ++j) { const unsigned t = j < 4 ? r0[j & 3] : r1[j & 3]; f[0][2 * j] = __uint_as_float(t << 16); f[0][2 * j + 1] = __uint_as_float(t & 0xffff0000u); }
    PINV(f[0]);
    asm volatile("s_waitcnt vmcnt(2)" : "+v"(fl) : "v"(pb.pk[0]), "v"(pb.pk[1]), "v"(pb.pk[2]), "v"(pb.pk[3]), "v"(pb.pk[4]), "v"(pb.pk[5]), "v"(pb.pk[6]), "v"(pb.pk[7]),
                 "v"(pb.pk[8]), "v"(pb.pk[9]), "v"(pb.pk[10]), "v"(pb.pk[11]), "v"(pb.pk[12]), "v"(pb.pk[13]), "v"(pb.pk[14]), "v"(pb.pk[15]) : "memory");
    unsigned got = 0xffffff01u, spin = 1u << 21, fresh, so, f2;
#define POLL(R) "buffer_load_dword %[" R "], %[lr], %[sh], %[flg] offen sc1\n\t"
#define SEEN(R) "v_cmp_eq_u32_e32 vcc, %[tau], %[" R "]\n\ts_andn2_b32 %[fr], vcc_lo, %[got]\n\ts_cbranch_scc1 .Lproc_%=\n\t"
    asm volatile(
        SEEN("fl") POLL("fl") "s_sleep 4\n"
        ".Lidle_%=:\n\t" POLL("f2") "s_waitcnt vmcnt(1)\n\t" SEEN("fl") "s_sleep %[slp]\n\t" POLL("fl") "s_waitcnt vmcnt(1)\n\t" SEEN("f2")
        "s_sleep %[slp]\n\ts_sub_u32 %[spin], %[spin], 1\n\ts_cbranch_scc0 .Lidle_%=\n\ts_branch .Ldone_%=\n"
        ".Lproc_%=:\n\ts_or_b32 %[got], %[got], %[fr]\n\t"
        PGS "s_bitcmp1_b32 %[fr], 1\n\ts_cbranch_scc0 .La1_%=\n\ts_add_u32 %[so], %[ro], 65536\n\ts_bitset0_b32 %[so], 19\n\t"
        "buffer_load_dwordx4 v[152:155], %[l16], %[sp], %[so] offen sc1\n\tbuffer_load_dwordx4 v[156:159], %[l16], %[sp], %[so] offen offset:1024 sc1\n.La1_%=:\n\t"
        PGS "s_bitcmp1_b32 %[fr], 2\n\ts_cbranch_scc0 .La2_%=\n\ts_add_u32 %[so], %[ro], 131072\n\ts_bitset0_b32 %[so], 19\n\t"
        "buffer_load_dwordx4 v[168:171], %[l16], %[sp], %[so] offen sc1\n\tbuffer_load_dwordx4 v[172:175], %[l16], %[sp], %[so] offen offset:1024 sc1\n.La2_%=:\n\t"
        PGS "s_bitcmp1_b32 %[fr], 3\n\ts_cbranch_scc0 .La3_%=\n\ts_add_u32 %[so], %[ro], 196608\n\ts_bitset0_b32 %[so], 19\n\t"
        "buffer_load_dwordx4 v[184:187], %[l16], %[sp], %[so] offen sc1\n\tbuffer_load_dwordx4 v[188:191], %[l16], %[sp], %[so] offen offset:1024 sc1\n.La3_%=:\n\t"
        PGS "s_bitcmp1_b32 %[fr], 4\n\ts_cbranch_scc0 .La4_%=\n\ts_add_u32 %[so], %[ro], 262144\n\ts_bitset0_b32 %[so], 19\n\t"
        "buffer_load_dwordx4 v[200:203], %[l16], %[sp], %[so] offen sc1\n\tbuffer_load_dwordx4 v[204:207], %[l16], %[sp], %[so] offen offset:1024 sc1\n.La4_%=:\n\t"
        PGS "s_bitcmp1_b32 %[fr], 5\n\ts_cbranch_scc0 .La5_%=\n\ts_add_u32 %[so], %[ro], 327680\n\ts_bitset0_b32 %[so], 19\n\t"
        "buffer_load_dwordx4 v[216:219], %[l16], %[sp], %[so] offen sc1\n\tbuffer_load_dwordx4 v[220:223], %[l16], %[sp], %[so] offen offset:1024 sc1\n.La5_%=:\n\t"
        PGS "s_bitcmp1_b32 %[fr], 6\n\ts_cbranch_scc0 .La6_%=\n\ts_add_u32 %[so], %[ro], 393216\n\ts_bitset0_b32 %[so], 19\n\t"
        "buffer_load_dwordx4 v[232:235], %[l16], %[sp], %[so] offen sc1\n\tbuffer_load_dwordx4 v[236:239], %[l16], %[sp], %[so] offen offset:1024 sc1\n.La6_%=:\n\t"
        PGS "s_bitcmp1_b32 %[fr], 7\n\ts_cbranch_scc0 .La7_%=\n\ts_add_u32 %[so], %[ro], 458752\n\ts_bitset0_b32 %[so], 19\n\t"
        "buffer_load_dwordx4 v[248:251], %[l16], %[sp], %[so] offen sc1\n\tbuffer_load_dwordx4 v[252:255], %[l16], %[sp], %[so] offen offset:1024 sc1\n.La7_%=:\n\t"
        "s_waitcnt vmcnt(0)\n\ts_cmp_eq_u32 %[got], -1\n\ts_cbranch_scc1 .Ltok_%=\n\t" POLL("fl") "s_branch .Lwide_%=\n"
        ".Ltok_%=:\n\tbuffer_load_dword %[fl], %[l8], %[sh], %[tok] offen sc1\n"
        ".Lwide_%=:\n\t"
        "s_bitcmp1_b32 %[fr], 1\n\ts_cbranch_scc0 .Lb1_%=\n\t"
        PGS "v_lshlrev_b32_e32 v144, 16, v152\n\tv_and_b32_e32 v145, %[hi], v152\n\tv_lshlrev_b32_e32 v146, 16, v153\n\tv_and_b32_e32 v147, %[hi], v153\n\t"
        PGS "v_lshlrev_b32_e32 v148, 16, v154\n\tv_and_b32_e32 v149, %[hi], v154\n\tv_lshlrev_b32_e32 v150, 16, v155\n\tv_and_b32_e32 v151, %[hi], v155\n\t"
        PGS "v_lshlrev_b32_e32 v152, 16, v156\n\tv_and_b32_e32 v153, %[hi], v156\n\tv_lshlrev_b32_e32 v154, 16, v157\n\tv_and_b32_e32 v155, %[hi], v157\n\t"
        PGS "v_lshlrev_b32_e32 v156, 16, v158\n\tv_and_b32_e32 v157, %[hi], v158\n\tv_lshlrev_b32_e32 v158, 16, v159\n\tv_and_b32_e32 v159, %[hi], v159\n\t"
        ".Lb1_%=:\n\t"
        "s_bitcmp1_b32 %[fr], 2\n\ts_cbranch_scc0 .Lb2_%=\n\t"
        PGS "v_lshlrev_b32_e32 v160, 16, v168\n\tv_and_b32_e32 v161, %[hi], v168\n\tv_lshlrev_b32_e32 v162, 16, v169\n\tv_and_b32_e32 v163, %[hi], v169\n\t"
        PGS "v_lshlrev_b32_e32 v164, 16, v170\n\tv_and_b32_e32 v165, %[hi], v170\n\tv_lshlrev_b32_e32 v166, 16, v171\n\tv_and_b32_e32 v167, %[hi], v171\n\t"
        PGS "v_lshlrev_b32_e32 v168, 16, v172\n\tv_and_b32_e32 v169, %[hi], v172\n\tv_lshlrev_b32_e32 v170, 16, v173\n\tv_and_b32_e32 v171, %[hi], v173\n\t"
        PGS "v_lshlrev_b32_e32 v172, 16, v174\n\tv_and_b32_e32 v173, %[hi], v174\n\tv_lshlrev_b32_e32 v174, 16, v175\n\tv_and_b32_e32 v175, %[hi], v175\n\t"
        ".Lb2_%=:\n\t"
        "s_bitcmp1_b32 %[fr], 3\n\ts_cbranch_scc0 .Lb3_%=\n\t"
        PGS "v_lshlrev_b32_e32 v176, 16, v184\n\tv_and_b32_e32 v177, %[hi], v184\n\tv_lshlrev_b32_e32 v178, 16, v185\n\tv_and_b32_e32 v179, %[hi], v185\n\t"
        PGS "v_lshlrev_b32_e32 v180, 16, v186\n\tv_and_b32_e32 v181, %[hi], v186\n\tv_lshlrev_b32_e32 v182, 16, v187\n\tv_and_b32_e32 v183, %[hi], v187\n\t"
        PGS "v_lshlrev_b32_e32 v184, 16, v188\n\tv_and_b32_e32 v185, %[hi], v188\n\tv_lshlrev_b32_e32 v186, 16, v189\n\tv_and_b32_e32 v187, %[hi], v189\n\t"
        PGS "v_lshlrev_b32_e32 v188, 16, v190\n\tv_and_b32_e32 v189, %[hi], v190\n\tv_lshlrev_b32_e32 v190, 16, v191\n\tv_and_b32_e32 v191, %[hi], v191\n\t"
        ".Lb3_%=:\n\t"
        "s_bitcmp1_b32 %[fr], 4\n\ts_cbranch_scc0 .Lb4_%=\n\t"
        PGS "v_lshlrev_b32_e32 v192, 16, v200\n\tv_and_b32_e32 v193, %[hi], v200\n\tv_lshlrev_b32_e32 v194, 16, v201\n\tv_and_b32_e32 v195, %[hi], v201\n\t"
        PGS "v_lshlrev_b32_e32 v196, 16, v202\n\tv_and_b32_e32 v197, %[hi], v202\n\tv_lshlrev_b32_e32 v198, 16, v203\n\tv_and_b32_e32 v199, %[hi], v203\n\t"
        PGS "v_lshlrev_b32_e32 v200, 16, v204\n\tv_and_b32_e32 v201, %[hi], v204\n\tv_lshlrev_b32_e32 v202, 16, v205\n\tv_and_b32_e32 v203, %[hi], v205\n\t"
        PGS "v_lshlrev_b32_e32 v204, 16, v206\n\tv_and_b32_e32 v205, %[hi], v206\n\tv_lshlrev_b32_e32 v206, 16, v207\n\tv_and_b32_e32 v207, %[hi], v207\n\t"
        ".Lb4_%=:\n\t"
        "s_bitcmp1_b32 %[fr], 5\n\ts_cbranch_scc0 .Lb5_%=\n\t"
        PGS "v_lshlrev_b32_e32 v208, 16, v216\n\tv_and_b32_e32 v209, %[hi], v216\n\tv_lshlrev_b32_e32 v210, 16, v217\n\tv_and_b32_e32 v211, %[hi], v217\n\t"
        PGS "v_lshlrev_b32_e32 v212, 16, v218\n\tv_and_b32_e32 v213, %[hi], v218\n\tv_lshlrev_b32_e32 v214, 16, v219\n\tv_and_b32_e32 v215, %[hi], v219\n\t"
        PGS "v_lshlrev_b32_e32 v216, 16, v220\n\tv_and_b32_e32 v217, %[hi], v220\n\tv_lshlrev_b32_e32 v218, 16, v221\n\tv_and_b32_e32 v219, %[hi], v221\n\t"
        PGS "v_lshlrev_b32_e32 v220, 16, v222\n\tv_and_b32_e32 v221, %[hi], v222\n\tv_lshlrev_b32_e32 v222, 16, v223\n\tv_and_b32_e32 v223, %[hi], v223\n\t"
        ".Lb5_%=:\n\t"
        "s_bitcmp1_b32 %[fr], 6\n\ts_cbranch_scc0 .Lb6_%=\n\t"
        PGS "v_lshlrev_b32_e32 v224, 16, v232\n\tv_and_b32_e32 v225, %[hi], v232\n\tv_lshlrev_b32_e32 v226, 16, v233\n\tv_and_b32_e32 v227, %[hi], v233\n\t"
        PGS "v_lshlrev_b32_e32 v228, 16, v234\n\tv_and_b32_e32 v229, %[hi], v234\n\tv_lshlrev_b32_e32 v230, 16, v235\n\tv_and_b32_e32 v231, %[hi], v235\n\t"
        PGS "v_lshlrev_b32_e32 v232, 16, v236\n\tv_and_b32_e32 v233, %[hi], v236\n\tv_lshlrev_b32_e32 v234, 16, v237\n\tv_and_b32_e32 v235, %[hi], v237\n\t"
        PGS "v_lshlrev_b32_e32 v236, 16, v238\n\tv_and_b32_e32 v237, %[hi], v238\n\tv_lshlrev_b32_e32 v238, 16, v239\n\tv_and_b32_e32 v239, %[hi], v239\n\t"
        ".Lb6_%=:\n\t"
        "s_bitcmp1_b32 %[fr], 7\n\ts_cbranch_scc0 .Lb7_%=\n\t"
        PGS "v_lshlrev_b32_e32 v240, 16, v248\n\tv_and_b32_e32 v241, %[hi], v248\n\tv_lshlrev_b32_e32 v242, 16, v249\n\tv_and_b32_e32 v243, %[hi], v249\n\t"
        PGS "v_lshlrev_b32_e32 v244, 16, v250\n\tv_and_b32_e32 v245, %[hi], v250\n\tv_lshlrev_b32_e32 v246, 16, v251\n\tv_and_b32_e32 v247, %[hi], v251\n\t"
        PGS "v_lshlrev_b32_e32 v248, 16, v252\n\tv_and_b32_e32 v249, %[hi], v252\n\tv_lshlrev_b32_e32 v250, 16, v253\n\tv_and_b32_e32 v251, %[hi], v253\n\t"
        PGS "v_lshlrev_b32_e32 v252, 16, v254\n\tv_and_b32_e32 v253, %[hi], v254\n\tv_lshlrev_b32_e32 v254, 16, v255\n\tv_and_b32_e32 v255, %[hi], v255\n\t"
        ".Lb7_%=:\n\t"
        "s_cmp_eq_u32 %[got], -1\n\ts_cbranch_scc0 .Lidle_%=\n"
        ".Ldone_%=:"
        : "=&{v[144:159]}"(f[1]), "=&{v[160:175]}"(f[2]), "=&{v[176:191]}"(f[3]), "=&{v[192:207]}"(f[4]), "=&{v[208:223]}"(f[5]), "=&{v[224:239]}"(f[6]), "=&{v[240:255]}"(f[7]),
          [fl] "+v"(fl), [f2] "=&v"(f2), [got] "+s"(got), [spin] "+s"(spin), [fr] "=&s"(fresh), [so] "=&s"(so)
        : [tau] "s"(tau), [l16] "v"(l16), [l8] "v"(l8), [lr] "v"(lr), [sp] "s"(pb.srd), [sh] "s"(srdH), [ro] "s"(xg * 73728u + w * 2048u), [tok] "s"(tok_off),
          [flg] "s"(flg_off + xg * 128u), [hi] "s"(0xffff0000u), [slp] "i"(isA ? 8 : 0)
        : "vcc", "scc", "memory");
#undef POLL
#undef SEEN
    const f32x16 t = ((f[0] + f[1]) + (f[2] + f[3])) + ((f[4] + f[5]) + (f[6] + f[7]));
#pragma unroll
    for (int s = 0; s < 2; ++s)
    {
      const u32x4 ov = natural8(u32x4{cvtpk(t[8 * s], t[8 * s + 1]), cvtpk(t[8 * s + 2], t[8 * s + 3]), cvtpk(t[8 * s + 4], t[8 * s + 5]), cvtpk(t[8 * s + 6], t[8 * s + 7])});
      __builtin_amdgcn_raw_buffer_store_b128(ov, mk_srd(out, 2097152u), oo + s * 64, 0, 17);
    }
    asm volatile("s_waitcnt vmcnt(2)" : "+v"(fl) :: "memory");
    if ((lane >> 1) == w && fl != toks) asm volatile("buffer_store_dword %0, %1, %2, %3 offen sc1" :: "v"(fl), "v"(fo), "s"(srdH), "s"(flg_off) : "memory");
  }
}

__global__ __launch_bounds__(256, 1) __attribute__((aligned(4096)))
void fused_attn(const char* __restrict__ Ax, const char* __restrict__ Ak, const char* __restrict__ Av, const char* __restrict__ Awq, const char* __restrict__ Awo,
                const char* __restrict__ Bx, const char* __restrict__ Bk, const char* __restrict__ Bv, const char* __restrict__ Bwq, const char* __restrict__ Bwo,
                char* __restrict__ part, char* __restrict__ out) {
  extern __shared__ __attribute__((aligned(16))) char lds[];
  const unsigned bid = blockIdx.x;
  unsigned long long tnow;
  {
    const unsigned xg = bid & 7u, b = (bid >> 3) & 15u;
    const char *px, *pq, *pc; unsigned left;
    asm volatile(PG_DEFS "s_load_dwordx2 %0, %5, %6\n\ts_load_dwordx2 %1, %5, 0x28\n\ts_memrealtime %2\n\ts_getpc_b64 %3\n.Lpc_%=:\n\ts_mov_b32 %4, .Lhot_end-.Lpc_%="
                 : "=&s"(pq), "=&s"(px), "=&s"(tnow), "=&s"(pc), "=&s"(left) : "s"(__builtin_amdgcn_kernarg_segment_ptr()), "s"(bid >= 128u ? 0x18u : 0x40u));
    unsigned tid = threadIdx.x;
    PINV(tid);
    const unsigned lane = tid & 63u, w = rfl(tid >> 6), zq = ((b & 3u) << 14) | ((b >> 2) << 8);
    unsigned q0 = q_soff(zq), q1 = q_soff(zq + 0x1300u), r01 = 65536u + w * 16384u, r2 = 131072u + w * 8192u, xs = (b & 3u) * 256u, xd = w * 4096u + (b & 3u) * 16384u;
    asm volatile("s_mov_b32 m0, %2\n\ts_nop 0\n\tbuffer_load_dword %0, %1, 0 offen lds"
                 :: "v"(bid >= 128u && tid - 192u < WARM ? (b * WARM + tid - 192u) * 128u : 0x80000000u), "s"(mk_srd(pc, left)), "s"(r01 + SLOT) : "memory");
    Src sQ, sX;
    mk_w<false>(sQ, (xg * 4u + w) * 256u, 8192u, lane);
    mk_rows<false>(sX, b * 65536u + w * 16384u, 1024u, lane);
#pragma unroll
    for (int i = 0; i < 4; ++i) PINV(sX.v[i]);
    PINS(xs); PINS(xd);
#pragma unroll
    for (int i = 0; i < 4; ++i) PINV(sQ.v[i]);
    PINS(q0); PINS(q1); PINS(r01); PINS(r2);
    GAP();
    asm volatile("s_waitcnt lgkmcnt(0)" : "+s"(pq), "+s"(px), "+s"(tnow));
    GAP();
    sQ.srd = mk_srd(pq, 4194304u); sX.srd = mk_srd(px, 1048576u);
    dma_unit<1>(sQ, q0, r2);
    dma_unit<W_NT>(sQ, q1, r01);
    asm volatile("s_cmp_ge_u32 %0, 0x80\n\ts_cbranch_scc1 .Lx_%=\n\ts_mov_b32 m0, %1\n\ts_nop 0\n\tbuffer_load_dwordx4 %2, %6, %7 offen lds\n\tbuffer_load_dwordx4 %3, %6, %7 offen offset:1024 lds\n\t"
                 "buffer_load_dwordx4 %4, %6, %7 offen offset:2048 lds\n\tbuffer_load_dwordx4 %5, %6, %7 offen offset:3072 lds\n.Lx_%=:"
                 :: "s"(bid), "s"(xd), "v"(sX.v[0]), "v"(sX.v[1]), "v"(sX.v[2]), "v"(sX.v[3]), "s"(sX.srd), "s"(xs) : "scc", "memory");
    dma_unit<1>(sQ, q0 + 128u, r2 + 4096u);
    dma_unit<W_NT>(sQ, q1 + 128u, r01 + 4096u);
    asm volatile("" :: "v"(lds));
  }
  if (bid < 128u) { body<false>(Bx, Bk, Bv, Bwq, Bwo, Bwo, tnow, part, out); asm volatile(PG_END ".Lhot_end:" ::: "memory"); __builtin_amdgcn_endpgm(); }
  body<true>(Ax, Ak, Av, Awq, Awo, Bwo, tnow, part, out);
  asm volatile(PG_END ".rept %0\n\ts_nop 0\n.endr" :: "i"(ASHIFT) : "memory");
}

extern "C" void kernel_launch(void* const* d_in, const int* in_sizes, int n_in, void* d_out, int out_size, void* d_ws, size_t ws_size, hipStream_t stream) {
  (void)in_sizes; (void)n_in; (void)out_size; (void)ws_size;
  (void)hipFuncSetAttribute((const void*)fused_attn, hipFuncAttributeMaxDynamicSharedMemorySize, (int)LDS_BYTES);
  fused_attn<<<256, 256, LDS_BYTES, stream>>>((const char*)d_in[0], (const char*)d_in[1], (const char*)d_in[2], (const char*)d_in[3], (const char*)d_in[4],
                                              (const char*)d_in[5], (const char*)d_in[6], (const char*)d_in[7], (const char*)d_in[8], (const char*)d_in[9], (char*)d_ws, (char*)d_out);
}
```
